```python
import torch

def _softmax(x, axis=-1):
    return torch.softmax(x, dim=axis)
def _gelu(x):
    return torch.nn.functional.gelu(x, approximate="tanh")
def _silu(x):
    return torch.nn.functional.silu(x)
def _sigmoid(x):
    return torch.sigmoid(x)
def _layernorm(x, axis=-1, eps=1e-5):
    m = x.mean(dim=axis, keepdim=True)
    v = x.var(dim=axis, keepdim=True, unbiased=False)
    return (x - m) / torch.sqrt(v + eps)
def _scatter_add(tgt, idx, src, axis=0):
    return tgt.clone().index_add_(axis, idx, src)
def _segment_sum(src, seg, n):
    out = torch.zeros((n,) + src.shape[1:], dtype=src.dtype)
    return out.index_add_(0, seg, src)
def _pad(x, pw):
    flat = tuple(v for pair in reversed(pw) for v in pair)
    return torch.nn.functional.pad(x, flat)

kernel_name = 'concat_heads__gqa_block_cached__gqa_block_cached__decode'

def setup_inputs():

    _w = (lambda a: (lambda t: t.to(torch.bfloat16) if t.is_floating_point() else t)(torch.as_tensor(a)))
    def _A_setup():
        B, Sq, D = 1, 1, 576
        Hq, Hkv, Sk, Dh = 32, 16, 16384, 128
        assert Hq % Hkv == 0, (Hq, Hkv)
        return {
            "x": _w((lambda *s: torch.randn(*s, dtype=torch.bfloat16) * 0.8)(B, Sq, D)),
            "k_cache": _w((lambda *s: torch.randn(*s, dtype=torch.bfloat16) * 0.8)(B, Hkv, Sk, Dh)),
            "v_cache": _w((lambda *s: torch.randn(*s, dtype=torch.bfloat16) * 0.05)(B, Hkv, Sk, Dh)),
            "wq": _w((lambda *s: torch.randn(*s, dtype=torch.bfloat16) * 0.8)(D, Hq * Dh) * (D ** -0.5)),
            "wo": _w((lambda *s: torch.randn(*s, dtype=torch.bfloat16) * 0.05)(Hq * Dh, D) * ((Hq * Dh) ** -0.5)),
        }
    _A_in = _A_setup()

    def _B_setup():
        B, Sq, D = 1, 1, 576
        Hq, Hkv, Sk, Dh = 32, 16, 16384, 128
        assert Hq % Hkv == 0, (Hq, Hkv)
        return {
            "x": _w((lambda *s: torch.randn(*s, dtype=torch.bfloat16) * 0.8)(B, Sq, D)),
            "k_cache": _w((lambda *s: torch.randn(*s, dtype=torch.bfloat16) * 0.8)(B, Hkv, Sk, Dh)),
            "v_cache": _w((lambda *s: torch.randn(*s, dtype=torch.bfloat16) * 0.05)(B, Hkv, Sk, Dh)),
            "wq": _w((lambda *s: torch.randn(*s, dtype=torch.bfloat16) * 0.8)(D, Hq * Dh) * (D ** -0.5)),
            "wo": _w((lambda *s: torch.randn(*s, dtype=torch.bfloat16) * 0.05)(Hq * Dh, D) * ((Hq * Dh) ** -0.5)),
        }
    _B_in = _B_setup()

    return {
        "A_x": _A_in["x"],
        "A_k_cache": _A_in["k_cache"],
        "A_v_cache": _A_in["v_cache"],
        "A_wq": _A_in["wq"],
        "A_wo": _A_in["wo"],
        "B_x": _B_in["x"],
        "B_k_cache": _B_in["k_cache"],
        "B_v_cache": _B_in["v_cache"],
        "B_wq": _B_in["wq"],
        "B_wo": _B_in["wo"],
    }

def reference(A_x, A_k_cache, A_v_cache, A_wq, A_wo, B_x, B_k_cache, B_v_cache, B_wq, B_wo):
    def _A_ref(x, k_cache, v_cache, wq, wo):
        B, Sq, D = x.shape
        Hq, Dh = 32, 128
        Hkv = k_cache.shape[1]
        rep = Hq // Hkv
        q = (lambda x, *d: x.permute(*d))((x @ wq).reshape(B, Sq, Hq, Dh), 0, 2, 1, 3)
        k = (lambda x, n, axis: torch.repeat_interleave(x, n, dim=axis))(k_cache, rep, axis=1)
        v = (lambda x, n, axis: torch.repeat_interleave(x, n, dim=axis))(v_cache, rep, axis=1)
        scores = torch.einsum("bhqd,bhkd->bhqk", q, k) * (Dh ** -0.5)
        p = _softmax(scores, axis=-1)
        o = torch.einsum("bhqk,bhkd->bhqd", p, v)
        return (lambda x, *d: x.permute(*d))(o, 0, 2, 1, 3).reshape(B, Sq, Hq * Dh) @ wo

    def _B_ref(x, k_cache, v_cache, wq, wo):
        B, Sq, D = x.shape
        Hq, Dh = 32, 128
        Hkv = k_cache.shape[1]
        rep = Hq // Hkv
        q = (lambda x, *d: x.permute(*d))((x @ wq).reshape(B, Sq, Hq, Dh), 0, 2, 1, 3)
        k = (lambda x, n, axis: torch.repeat_interleave(x, n, dim=axis))(k_cache, rep, axis=1)
        v = (lambda x, n, axis: torch.repeat_interleave(x, n, dim=axis))(v_cache, rep, axis=1)
        scores = torch.einsum("bhqd,bhkd->bhqk", q, k) * (Dh ** -0.5)
        p = _softmax(scores, axis=-1)
        o = torch.einsum("bhqk,bhkd->bhqd", p, v)
        return (lambda x, *d: x.permute(*d))(o, 0, 2, 1, 3).reshape(B, Sq, Hq * Dh) @ wo

    a = _A_ref(A_x, A_k_cache, A_v_cache, A_wq, A_wo)
    b = _B_ref(B_x, B_k_cache, B_v_cache, B_wq, B_wo)
    return torch.concatenate([a, b], axis=-1)
```

```cpp
#include <hip/hip_runtime.h>
#include <hip/hip_bf16.h>
#include <cstdint>

typedef unsigned v4u __attribute__((ext_vector_type(4)));
typedef float f32x4 __attribute__((ext_vector_type(4)));
typedef __bf16 bf16x2 __attribute__((ext_vector_type(2)));

constexpr int D = 576, HQ = 32, HKV = 16, SK = 16384, DH = 128, NQ = HQ * DH;
constexpr int NSPLIT = 16;
constexpr int KEYS = SK / NSPLIT;

__device__ __forceinline__ float bflo(unsigned u) { return __builtin_bit_cast(float, u << 16); }
__device__ __forceinline__ float bfhi(unsigned u) { return __builtin_bit_cast(float, u & 0xffff0000u); }
__device__ __forceinline__ float dot2(unsigned k, unsigned q, float acc) {
  return __builtin_amdgcn_fdot2_f32_bf16(__builtin_bit_cast(bf16x2, k), __builtin_bit_cast(bf16x2, q), acc, false);
}
__device__ __forceinline__ unsigned short f2bf(float f) {
  __bf16 b = (__bf16)f; return __builtin_bit_cast(unsigned short, b);
}

__global__ void __launch_bounds__(256) k_q(const unsigned short* xA, const unsigned short* wqA, const unsigned short* xB, const unsigned short* wqB, unsigned short* q) {
  int b = blockIdx.x >> 6, cb = blockIdx.x & 63;
  const unsigned short* x = b ? xB : xA; const unsigned short* wq = b ? wqB : wqA;
  int t = threadIdx.x, cg = t & 7, rg = t >> 3;
  float acc[8] = {0,0,0,0,0,0,0,0};
  for (int r = rg; r < D; r += 32) {
    v4u w = *(const v4u*)(wq + (size_t)r * NQ + cb * 64 + cg * 8);
    float xv = bflo(x[r]);
    for (int k = 0; k < 4; ++k) { acc[2*k] += xv * bflo(w[k]); acc[2*k+1] += xv * bfhi(w[k]); }
  }
  __shared__ float red[32][65];
  for (int k = 0; k < 8; ++k) red[rg][cg*8+k] = acc[k];
  __syncthreads();
  if (t < 64) { float s = 0; for (int r = 0; r < 32; ++r) s += red[r][t]; q[b * NQ + cb * 64 + t] = f2bf(s); }
}

__global__ void __launch_bounds__(256) k_attn(const unsigned short* kA, const unsigned short* vA, const unsigned short* kB, const unsigned short* vB, const unsigned short* q, float* part) {
  int s = blockIdx.x % NSPLIT, kvh = (blockIdx.x / NSPLIT) % HKV, b = blockIdx.x / (NSPLIT * HKV);
  const unsigned short* K = (b ? kB : kA) + ((size_t)kvh * SK + (size_t)s * KEYS) * DH;
  const unsigned short* V = (b ? vB : vA) + ((size_t)kvh * SK + (size_t)s * KEYS) * DH;
  int t = threadIdx.x, lane = t & 63, wave = t >> 6, r = lane >> 4, c = lane & 15;
  __shared__ float sc[2][KEYS];
  __shared__ float red[4][4][2][128];
  __shared__ float ml[4][2][2];
  v4u q0 = *(const v4u*)(q + b * NQ + (2 * kvh) * DH + 8 * c);
  v4u q1 = *(const v4u*)(q + b * NQ + (2 * kvh + 1) * DH + 8 * c);
  const float C2 = 0.088388347648318440f * 1.4426950408889634f;
  for (int k0 = wave * 4; k0 < KEYS; k0 += 16) {
    v4u kk = __builtin_nontemporal_load((const v4u*)(K + (size_t)(k0 + r) * DH + 8 * c));
    float d0 = 0, d1 = 0;
    for (int i = 0; i < 4; ++i) { d0 = dot2(kk[i], q0[i], d0); d1 = dot2(kk[i], q1[i], d1); }
    for (int o = 1; o < 16; o <<= 1) { d0 += __shfl_xor(d0, o); d1 += __shfl_xor(d1, o); }
    if (c == 0) { sc[0][k0 + r] = d0 * C2; sc[1][k0 + r] = d1 * C2; }
  }
  __syncthreads();
  float m0 = -INFINITY, m1 = -INFINITY;
  for (int k = t; k < KEYS; k += 256) { m0 = fmaxf(m0, sc[0][k]); m1 = fmaxf(m1, sc[1][k]); }
  for (int o = 1; o < 64; o <<= 1) { m0 = fmaxf(m0, __shfl_xor(m0, o)); m1 = fmaxf(m1, __shfl_xor(m1, o)); }
  if (lane == 0) { ml[wave][0][0] = m0; ml[wave][1][0] = m1; }
  __syncthreads();
  m0 = fmaxf(fmaxf(ml[0][0][0], ml[1][0][0]), fmaxf(ml[2][0][0], ml[3][0][0]));
  m1 = fmaxf(fmaxf(ml[0][1][0], ml[1][1][0]), fmaxf(ml[2][1][0], ml[3][1][0]));
  float l0 = 0, l1 = 0;
  for (int k = t; k < KEYS; k += 256) { float p0 = exp2f(sc[0][k] - m0), p1 = exp2f(sc[1][k] - m1); sc[0][k] = p0; sc[1][k] = p1; l0 += p0; l1 += p1; }
  for (int o = 1; o < 64; o <<= 1) { l0 += __shfl_xor(l0, o); l1 += __shfl_xor(l1, o); }
  if (lane == 0) { ml[wave][0][1] = l0; ml[wave][1][1] = l1; }
  __syncthreads();
  float a0[8] = {0,0,0,0,0,0,0,0}, a1[8] = {0,0,0,0,0,0,0,0};
  for (int k0 = wave * 4; k0 < KEYS; k0 += 16) {
    v4u vv = __builtin_nontemporal_load((const v4u*)(V + (size_t)(k0 + r) * DH + 8 * c));
    float p0 = sc[0][k0 + r], p1 = sc[1][k0 + r];
    for (int i = 0; i < 4; ++i) {
      float lo = bflo(vv[i]), hi = bfhi(vv[i]);
      a0[2*i] += p0 * lo; a0[2*i+1] += p0 * hi; a1[2*i] += p1 * lo; a1[2*i+1] += p1 * hi;
    }
  }
  for (int i = 0; i < 8; ++i) { red[wave][r][0][8*c+i] = a0[i]; red[wave][r][1][8*c+i] = a1[i]; }
  __syncthreads();
  int h = t >> 7, d = t & 127;
  float o = 0;
  for (int w = 0; w < 4; ++w) for (int rr = 0; rr < 4; ++rr) o += red[w][rr][h][d];
  float* P = part + ((size_t)(b * HQ + 2 * kvh + h) * NSPLIT + s) * 130;
  P[2 + d] = o;
  if (d == 0) { P[0] = h ? m1 : m0; P[1] = ml[0][h][1] + ml[1][h][1] + ml[2][h][1] + ml[3][h][1]; }
}

__global__ void __launch_bounds__(256) k_out(const float* part, const unsigned short* woA, const unsigned short* woB, unsigned short* out) {
  int b = blockIdx.x / 72, cb = blockIdx.x % 72;
  const unsigned short* wo = b ? woB : woA;
  __shared__ float o[NQ];
  __shared__ float w[HQ][NSPLIT];
  int t = threadIdx.x;
  if (t < HQ) {
    const float* P = part + (size_t)(b * HQ + t) * NSPLIT * 130;
    float M = -INFINITY;
    for (int s = 0; s < NSPLIT; ++s) M = fmaxf(M, P[s * 130]);
    float L = 0;
    for (int s = 0; s < NSPLIT; ++s) { float e = exp2f(P[s * 130] - M); w[t][s] = e; L += e * P[s * 130 + 1]; }
    for (int s = 0; s < NSPLIT; ++s) w[t][s] /= L;
  }
  __syncthreads();
  for (int i = t; i < NQ; i += 256) {
    int h = i >> 7, d = i & 127;
    const float* P = part + (size_t)(b * HQ + h) * NSPLIT * 130 + 2 + d;
    float s = 0;
    for (int sp = 0; sp < NSPLIT; ++sp) s += w[h][sp] * P[sp * 130];
    o[i] = bflo(f2bf(s));
  }
  __syncthreads();
  float acc[8] = {0,0,0,0,0,0,0,0};
  for (int r = t; r < NQ; r += 256) {
    v4u ww = *(const v4u*)(wo + (size_t)r * D + cb * 8);
    float ov = o[r];
    for (int k = 0; k < 4; ++k) { acc[2*k] += ov * bflo(ww[k]); acc[2*k+1] += ov * bfhi(ww[k]); }
  }
  for (int k = 0; k < 8; ++k) for (int of = 1; of < 64; of <<= 1) acc[k] += __shfl_xor(acc[k], of);
  __shared__ float red[4][8];
  if ((t & 63) == 0) for (int k = 0; k < 8; ++k) red[t >> 6][k] = acc[k];
  __syncthreads();
  if (t < 8) out[b * D + cb * 8 + t] = f2bf(red[0][t] + red[1][t] + red[2][t] + red[3][t]);
}

extern "C" void kernel_launch(void* const* d_in, const int* in_sizes, int n_in, void* d_out, int out_size, void* d_ws, size_t ws_size, hipStream_t stream) {
  const unsigned short* xA = (const unsigned short*)d_in[0]; const unsigned short* kA = (const unsigned short*)d_in[1];
  const unsigned short* vA = (const unsigned short*)d_in[2]; const unsigned short* wqA = (const unsigned short*)d_in[3];
  const unsigned short* woA = (const unsigned short*)d_in[4];
  const unsigned short* xB = (const unsigned short*)d_in[5]; const unsigned short* kB = (const unsigned short*)d_in[6];
  const unsigned short* vB = (const unsigned short*)d_in[7]; const unsigned short* wqB = (const unsigned short*)d_in[8];
  const unsigned short* woB = (const unsigned short*)d_in[9];
  unsigned short* q = (unsigned short*)d_ws;
  float* part = (float*)((char*)d_ws + 65536);
  k_q<<<128, 256, 0, stream>>>(xA, wqA, xB, wqB, q);
  k_attn<<<2 * HKV * NSPLIT, 256, 0, stream>>>(kA, vA, kB, vB, q, part);
  k_out<<<144, 256, 0, stream>>>(part, woA, woB, (unsigned short*)d_out);
}
```

```cpp
#include <hip/hip_runtime.h>
#include <cstdint>

typedef unsigned v4u __attribute__((ext_vector_type(4)));
typedef unsigned v2u __attribute__((ext_vector_type(2)));
typedef float f32x4 __attribute__((ext_vector_type(4)));
typedef __bf16 bf16x8 __attribute__((ext_vector_type(8)));
typedef __bf16 bf16x2 __attribute__((ext_vector_type(2)));
typedef float f32x2 __attribute__((ext_vector_type(2)));
#define GAS __attribute__((address_space(1)))
typedef GAS unsigned gu32;
typedef GAS unsigned long long gu64;
#define RLX_AGENT __ATOMIC_RELAXED, __HIP_MEMORY_SCOPE_AGENT
#define RLX_WG __ATOMIC_RELAXED, __HIP_MEMORY_SCOPE_WORKGROUP

#define STAMPS 0
#define NSLOT 4
#define WFULL 28
#define DELAY0 0
#define DELAY1 32
#define ABL 0
#define WOAT 7u
#define PACEN 4
#define STATIC 0
#define MAPV 0

constexpr int DM = 576, SK = 16384, NQ = 4096;
constexpr int NWG = 256, NTHR = 512;
constexpr int NT = 128;
constexpr int AUX_NT = 2, AUX_SC1 = 16;
constexpr int RSRC3 = 0x00020000;
constexpr int OOB = 0x7fff0000;
constexpr int WS_EPOCH = 0;
constexpr int WS_QG = 4096;
constexpr int PG_ROW = 288 * 8;
constexpr int WS_PG = WS_QG + 32 * 4096;
constexpr int YG_ROW = 640 * 8;
constexpr int WS_YG = WS_PG + 256 * PG_ROW;
constexpr int WS_ST = WS_YG + 256 * YG_ROW;
constexpr unsigned TAG_STEP = 0x9E3779B9u;
constexpr unsigned SPIN_CAP = 1u << 17;

struct Args {
  const void* x[2]; const void* k[2]; const void* v[2]; const void* wq[2]; const void* wo[2];
  void* out; void* ws;
};

#define SB() __builtin_amdgcn_sched_barrier(0)
__device__ __forceinline__ f32x4 mfma(v4u a, v4u b, f32x4 c) {
  return __builtin_amdgcn_mfma_f32_16x16x32_bf16(__builtin_bit_cast(bf16x8, a), __builtin_bit_cast(bf16x8, b), c, 0, 0, 0);
}
__device__ __forceinline__ unsigned dup_bf16(float f) {
  f32x2 v = {f, f};
  return __builtin_bit_cast(unsigned, __builtin_convertvector(v, bf16x2));
}
__device__ __forceinline__ unsigned short f2bf(float f) { return __builtin_bit_cast(unsigned short, (__bf16)f); }
__device__ __forceinline__ v4u diag(unsigned pp, v4u mask) { v4u r = {pp & mask.x, pp & mask.y, pp & mask.z, pp & mask.w}; return r; }
template <int CTRL> __device__ __forceinline__ float dpp(float x) {
  return __builtin_bit_cast(float, __builtin_amdgcn_mov_dpp(__builtin_bit_cast(int, x), CTRL, 0xf, 0xf, true));
}
constexpr int XOR1 = 0xB1, XOR2 = 0x4E, HMIR = 0x141, ROR8 = 0x128, ROR4 = 0x124, BCAST = 0x150;
__device__ __forceinline__ float fmax_(float a, float b) { return __builtin_fmaxf(a, b); }
__device__ __forceinline__ float xg_max(float x) {
  auto s = __builtin_amdgcn_permlane16_swap(__float_as_uint(x), __float_as_uint(x), false, false);
  x = fmax_(__uint_as_float(s[0]), __uint_as_float(s[1]));
  auto t = __builtin_amdgcn_permlane32_swap(__float_as_uint(x), __float_as_uint(x), false, false);
  return fmax_(__uint_as_float(t[0]), __uint_as_float(t[1]));
}
__device__ __forceinline__ float xg_sum(float x) {
  auto s = __builtin_amdgcn_permlane16_swap(__float_as_uint(x), __float_as_uint(x), false, false);
  x = __uint_as_float(s[0]) + __uint_as_float(s[1]);
  auto t = __builtin_amdgcn_permlane32_swap(__float_as_uint(x), __float_as_uint(x), false, false);
  return __uint_as_float(t[0]) + __uint_as_float(t[1]);
}
__device__ __forceinline__ void st_gran(unsigned char* ws, int off, unsigned tag, float v) {
  __hip_atomic_store((gu64*)(ws + off), ((unsigned long long)tag << 32) | __float_as_uint(v), RLX_AGENT);
}
__device__ __forceinline__ v2u ldg(__amdgpu_buffer_rsrc_t rs, int off) { return __builtin_amdgcn_raw_buffer_load_b64(rs, off, 0, AUX_SC1); }
template <int N, int SLEEP> __device__ __forceinline__ void sweep(__amdgpu_buffer_rsrc_t rs, const int (&off)[N], unsigned tag, v2u (&x)[N]) {
#pragma nounroll
  for (unsigned spins = 0; spins < SPIN_CAP; ++spins) {
    bool ok = true;
#pragma unroll
    for (int k = 0; k < N; ++k) ok &= x[k].y == tag;
    if (__all(ok)) break;
    __builtin_amdgcn_s_sleep(SLEEP);
    v2u y[N];
#pragma unroll
    for (int k = 0; k < N; ++k) y[k] = ldg(rs, x[k].y == tag ? OOB : off[k]);
    SB();
#pragma unroll
    for (int k = 0; k < N; ++k) x[k] = x[k].y == tag ? x[k] : y[k];
  }
}
__device__ __forceinline__ v4u mkrs(const void* p, unsigned bytes) {
  const unsigned long long u = (unsigned long long)p;
  v4u r = {(unsigned)u, (unsigned)(u >> 32) & 0xffffu, bytes, (unsigned)RSRC3};
  return r;
}
template <int IMM> __device__ __forceinline__ void bl128(v4u& d, int voff, v4u rs, int soff) {
  asm volatile("buffer_load_dwordx4 %0, %1, %2, %3 offen offset:%4 nt" : "=v"(d) : "v"(voff), "s"(rs), "s"(soff), "i"(IMM));
}
template <int STEP> __device__ __forceinline__ void bl4(v4u (&d)[4], int voff, v4u rs, int soff, unsigned long long em) {
  asm volatile("s_mov_b64 exec, %7\n\t"
               "buffer_load_dwordx4 %0, %4, %5, %6 offen nt\n\t"
               "buffer_load_dwordx4 %1, %4, %5, %6 offen offset:%8 nt\n\t"
               "buffer_load_dwordx4 %2, %4, %5, %6 offen offset:%9 nt\n\t"
               "buffer_load_dwordx4 %3, %4, %5, %6 offen offset:%10 nt\n\t"
               "s_mov_b64 exec, -1"
               : "=&v"(d[0]), "=&v"(d[1]), "=&v"(d[2]), "=&v"(d[3]) : "v"(voff), "s"(rs), "s"(soff), "s"(em), "i"(STEP), "i"(2 * STEP), "i"(3 * STEP));
}
__device__ __forceinline__ void bl4s(v4u (&d)[4], int voff, v4u rs, int s0, int s1, int s2, int s3, unsigned long long em) {
  asm volatile("s_mov_b64 exec, %10\n\t"
               "buffer_load_dwordx4 %0, %4, %5, %6 offen nt\n\t"
               "buffer_load_dwordx4 %1, %4, %5, %7 offen nt\n\t"
               "buffer_load_dwordx4 %2, %4, %5, %8 offen nt\n\t"
               "buffer_load_dwordx4 %3, %4, %5, %9 offen nt\n\t"
               "s_mov_b64 exec, -1"
               : "=&v"(d[0]), "=&v"(d[1]), "=&v"(d[2]), "=&v"(d[3]) : "v"(voff), "s"(rs), "s"(s0), "s"(s1), "s"(s2), "s"(s3), "s"(em));
}
#define WAIT4(N, R) WAIT4_(N, R)
#define WAIT4_(N, R) asm volatile("s_waitcnt vmcnt(" #N ")" : "+v"(R[0]), "+v"(R[1]), "+v"(R[2]), "+v"(R[3]))
#define KEEP4(R) asm volatile("" :: "v"(R[0]), "v"(R[1]), "v"(R[2]), "v"(R[3]))
__device__ __forceinline__ void lds_barrier() {
  asm volatile("s_waitcnt lgkmcnt(0)" ::: "memory");
  __builtin_amdgcn_s_barrier();
  asm volatile("" ::: "memory");
}
__device__ __forceinline__ void lds_arrive(unsigned* c, int lane) {
  asm volatile("s_waitcnt lgkmcnt(0)" ::: "memory");
  if (lane == 0) __hip_atomic_fetch_add(c, 1u, RLX_WG);
}
__device__ __forceinline__ void lds_wait(unsigned* c, unsigned want) {
#pragma nounroll
  for (unsigned spins = 0; spins < (1u << 24); ++spins) {
    if (__builtin_amdgcn_readfirstlane(__hip_atomic_load(c, RLX_WG)) >= want) break;
    __builtin_amdgcn_s_sleep(1);
  }
  asm volatile("" ::: "memory");
}
#define STAMP(i) do {} while (0)
#define STAMP_OUT() do {} while (0)

__global__ void __launch_bounds__(NTHR, 2) gqa2_fused(Args a) {
  __shared__ __attribute__((aligned(16))) float lds[8 * 256 + 64 + 16 * 64 + 128 + 8];
  float* const l_o = lds;
  float* const l_ml = lds + 2048;
  float* const l_qp = lds + 2048 + 64;
  unsigned short* const l_q = (unsigned short*)(lds + 2048 + 64 + 1024);
  unsigned* const l_cnt = (unsigned*)(lds + 2048 + 64 + 1024 + 128);

  const int tid = threadIdx.x, lane = tid & 63, wave = __builtin_amdgcn_readfirstlane(tid >> 6);
  const int g = lane >> 4, n = lane & 15;
  const int wg = blockIdx.x;
  const int G = MAPV == 0 ? wg >> 3 : wg & 31, s = MAPV == 0 ? wg & 7 : wg >> 5;
  const int b = G >> 4, kvh = G & 15;
  unsigned char* const ws = (unsigned char*)a.ws;
  STAMP(0);

  const v4u rk = mkrs((unsigned char*)a.k[b] + ((size_t)kvh * SK + s * 2048) * 256, 2048 * 256);
  const v4u rv = mkrs((unsigned char*)a.v[b] + ((size_t)kvh * SK + s * 2048) * 256, 2048 * 256);
  const int cg = s & 3, kh = s >> 2;
  const v4u rq = mkrs((unsigned char*)a.wq[b] + ((size_t)kh * 288 * NQ + kvh * 256 + cg * 64) * 2, 287 * NQ * 2 + 128);
  const v4u rx = mkrs((unsigned char*)a.x[b] + kh * 288 * 2, 288 * 2);
  const v4u rw4 = mkrs(ws, WS_ST);
  const __amdgpu_buffer_rsrc_t rw = __builtin_amdgcn_make_buffer_rsrc(ws, 0, WS_ST, RSRC3);
  const void* wop = a.wo[b];
  asm volatile("" : "+s"(wop));
  const __amdgpu_buffer_rsrc_t ro = __builtin_amdgcn_make_buffer_rsrc((unsigned char*)wop + ((size_t)(kvh * 256 + s * 32) * DM) * 2, 0, 32 * DM * 2, RSRC3);
  GAS unsigned short* outp = (GAS unsigned short*)a.out;
  asm volatile("" : "+s"(outp));

  unsigned ep;
  v4u wqv[5]; unsigned xv[5];
  {
    const int row = 8 * wave + 2 * g + (n >> 3);
    const int vo = (ABL & 1) ? OOB : row * (NQ * 2) + (n & 7) * 16;
    bl128<0>(wqv[0], vo, rq, 0);
    bl128<0>(wqv[1], vo, rq, 1 * 64 * NQ * 2);
    bl128<0>(wqv[2], vo, rq, 2 * 64 * NQ * 2);
    bl128<0>(wqv[3], vo, rq, 3 * 64 * NQ * 2);
    bl128<0>(wqv[4], wave >= 4 ? OOB : vo, rq, 4 * 64 * NQ * 2);
#define XLOAD(i) asm volatile("buffer_load_ushort %0, %1, %2, 0 offen offset:%3" : "=v"(xv[i]) : "v"(row * 2), "s"(rx), "i"(i * 128))
    XLOAD(0); XLOAD(1); XLOAD(2); XLOAD(3); XLOAD(4);
    asm volatile("s_mov_b64 exec, 1\n\tbuffer_load_dword %0, off, %1, %2 sc1\n\ts_mov_b64 exec, -1" : "=v"(ep) : "s"(rw4), "s"(wave == 0 ? WS_EPOCH + b * 256 : OOB));
  }
  const int vk = n * 256 + g * 16, vv = g * 1024 + n * 16;
  v4u Kr[NSLOT][4], Vr[NSLOT][4];
#define ISSUE_K(u, t) { const int so_ = (t) * 4096; bl128<0>(Kr[u][0], vk, rk, so_); bl128<64>(Kr[u][1], vk, rk, so_); bl128<128>(Kr[u][2], vk, rk, so_); bl128<192>(Kr[u][3], vk, rk, so_); }
#define ISSUE_V(u, t) { const int so_ = (t) * 4096; bl128<0>(Vr[u][0], vv, rv, so_); bl128<256>(Vr[u][1], vv, rv, so_); bl128<512>(Vr[u][2], vv, rv, so_); bl128<768>(Vr[u][3], vv, rv, so_); }
  if (tid < 8) l_cnt[tid] = tid == 4 ? 8u * NSLOT : 0u;
  SB();
  lds_barrier();
  ISSUE_K(0, wave); ISSUE_V(0, wave);
  SB();
  v4u mask;
  {
    const unsigned half = (n & 1) ? 0xffff0000u : 0x0000ffffu; const int r = (n & 7) >> 1;
    mask.x = r == 0 ? half : 0u; mask.y = r == 1 ? half : 0u; mask.z = r == 2 ? half : 0u; mask.w = r == 3 ? half : 0u;
  }
  const bool own = (g >> 1) == (n >> 3);
  {
#define W0 "8"
    asm volatile("s_waitcnt vmcnt(" W0 ")" : "+v"(wqv[0]), "+v"(wqv[1]), "+v"(wqv[2]), "+v"(wqv[3]), "+v"(wqv[4]),
                 "+v"(xv[0]), "+v"(xv[1]), "+v"(xv[2]), "+v"(xv[3]), "+v"(xv[4]), "+v"(ep));
    f32x4 aq = {0.f, 0.f, 0.f, 0.f};
#pragma unroll
    for (int i = 0; i < 5; ++i) { const unsigned xx = xv[i] * 0x10001u; aq = mfma(diag(xx, mask), wqv[i], aq); }
    if (own) *(f32x4*)(l_qp + (wave * 2 + (n >> 3)) * 64 + 8 * (n & 7) + 4 * (g & 1)) = aq;
  }
  unsigned tag = __builtin_amdgcn_readfirstlane(ep) + TAG_STEP;
  STAMP(1);
  lds_arrive(l_cnt + 0, lane);
  if (wave == 0) {
    lds_wait(l_cnt + 0, 8u);
    float q = 0.f;
#pragma unroll
    for (int i = 0; i < 16; ++i) q += l_qp[i * 64 + lane];
    st_gran(ws, WS_QG + (G * 512 + kh * 256 + cg * 64 + lane) * 8, tag, q);
    STAMP(2);
    __builtin_amdgcn_s_sleep(DELAY1);
    const int qo = WS_QG + (G * 512 + lane) * 8;
    const int off[8] = {qo, qo + 512, qo + 1024, qo + 1536, qo + 2048, qo + 2560, qo + 3072, qo + 3584};
    v2u x[8];
#pragma unroll
    for (int k = 0; k < 8; ++k) x[k] = ldg(rw, off[k]);
    sweep<8, 1>(rw, off, tag, x);
#pragma unroll
    for (int k = 0; k < 4; ++k) l_q[k * 64 + lane] = f2bf(__uint_as_float(x[k].x) + __uint_as_float(x[k + 4].x));
    if (lane == 0) l_cnt[5] = tag;
    lds_arrive(l_cnt + 1, lane);
    STAMP(3);
  }
#define PACE() PACE_(PACEN)
#define PACE_(N) PACE__(N)
#define PACE__(N) asm volatile("s_cmp_lg_u32 %0, 0\n\ts_cbranch_scc1 .Lp%=\n\ts_waitcnt vmcnt(" #N ")\n.Lp%=:" :: "s"(__builtin_amdgcn_readfirstlane(__hip_atomic_load(l_cnt + 1, RLX_WG))) : "scc", "memory")
  SB();
  PACE(); ISSUE_K(1, wave + 8); SB();
  PACE(); ISSUE_V(1, wave + 8); SB();
  PACE(); ISSUE_K(2, wave + 16); SB();
  PACE(); ISSUE_V(2, wave + 16); SB();
  PACE(); ISSUE_K(3, wave + 24); SB();
  PACE(); ISSUE_V(3, wave + 24); SB();
  STAMP(4);
  lds_wait(l_cnt + 1, 1u);
  tag = __builtin_amdgcn_readfirstlane(l_cnt[5]);
  v4u Q[4];
#pragma unroll
  for (int i = 0; i < 4; ++i) Q[i] = *(const v4u*)(l_q + (n >> 3) * 128 + 32 * i + 8 * g);
  STAMP(5);

  constexpr float C2 = 0.088388347648318440f * 1.4426950408889634f;
  float m = -INFINITY, l = 0.f;
  f32x4 O = {0.f, 0.f, 0.f, 0.f};
  const int wcol = wave * 128 + n * 8;
  const int vwo = (!(ABL & 4) && wcol < DM) ? g * (DM * 2) + wcol * 2 : OOB;
  asm volatile("" : "+v"(Q[0]), "+v"(Q[1]), "+v"(Q[2]), "+v"(Q[3]));
#define TILE_QK(u, W) \
    f32x4 S = {0.f, 0.f, 0.f, 0.f}; \
    WAIT4(W, Kr[u]); \
    if (!(ABL & 8)) { _Pragma("unroll") for (int j_ = 0; j_ < 4; ++j_) S = mfma(Kr[u][j_], Q[j_], S); } \
    asm volatile("" : "+v"(S) :: "memory"); SB();
#define TILE_PV(u, W) { \
    const float tm = xg_max(fmax_(fmax_(S.x, S.y), fmax_(S.z, S.w))) * C2; \
    const float mn = fmax_(m, tm); \
    const float al = __builtin_amdgcn_exp2f(m - mn); \
    m = mn; \
    const float p0 = __builtin_amdgcn_exp2f(fmaf(S.x, C2, -mn)), p1 = __builtin_amdgcn_exp2f(fmaf(S.y, C2, -mn)); \
    const float p2 = __builtin_amdgcn_exp2f(fmaf(S.z, C2, -mn)), p3 = __builtin_amdgcn_exp2f(fmaf(S.w, C2, -mn)); \
    l = fmaf(l, al, (p0 + p1) + (p2 + p3)); \
    const float a8 = dpp<ROR8>(al); \
    O *= own ? al : a8; \
    WAIT4(W, Vr[u]); \
    O = mfma(diag(dup_bf16(p0), mask), Vr[u][0], O); \
    O = mfma(diag(dup_bf16(p1), mask), Vr[u][1], O); \
    O = mfma(diag(dup_bf16(p2), mask), Vr[u][2], O); \
    O = mfma(diag(dup_bf16(p3), mask), Vr[u][3], O); \
    asm volatile("" : "+v"(O), "+v"(l), "+v"(m) :: "memory"); SB(); }
#pragma nounroll
  for (int t = wave + 32; t < NT; t += 32) {
#define SLOT(u) { TILE_QK(u, 28); ISSUE_K(u, t + 8 * u); SB(); TILE_PV(u, 28); ISSUE_V(u, t + 8 * u); SB(); }
    SLOT(0) SLOT(1) SLOT(2) SLOT(3)
  }
  STAMP(6);
  { TILE_QK(0, 28); TILE_PV(0, 24); }
  { TILE_QK(1, 20); TILE_PV(1, 16); }
  { TILE_QK(2, 12); TILE_PV(2, 8); }
  { TILE_QK(3, 4); TILE_PV(3, 0); }
  STAMP(7);
  l = xg_sum(l);
  *(f32x4*)(l_o + wave * 256 + (g >> 1) * 128 + 8 * n + 4 * (g & 1)) = O;
  if (g == 0 && (n & 7) == 0) { l_ml[(n >> 3) * 8 + wave] = m; l_ml[16 + (n >> 3) * 8 + wave] = l; }
  lds_arrive(l_cnt + 2, lane);
  const int pidx = (G & 15) * 8 + s;
  if (wave < 5) {
    const int cb = WS_PG + G * 8 * PG_ROW;
    const int po = cb + (n & 4) * PG_ROW + (32 * s + g + 4 * ((n & 3) + 4 * (n >> 3))) * 8;
    const int off[5] = {po, po + PG_ROW, po + 2 * PG_ROW, po + 3 * PG_ROW, cb + (n & 7) * PG_ROW + (256 + 16 * (s >> 2) + (n >> 3)) * 8};
    v2u x[5];
    lds_wait(l_cnt + 2, WOAT);
    v4u Wo[8];
#pragma unroll
    for (int i = 0; i < 8; ++i) Wo[i] = __builtin_amdgcn_raw_buffer_load_b128(ro, vwo, i * 4 * DM * 2, AUX_NT);
    SB();
    lds_wait(l_cnt + 2, 8u);
#pragma unroll
    for (int k = 0; k < 5; ++k) x[k] = ldg(rw, off[k]);
    sweep<5, 2>(rw, off, tag, x);
    STAMP(10);
    const float v = __uint_as_float(x[4].x), v8 = dpp<ROR8>(v);
    const float Ms = n < 8 ? v : v8, Ls = n < 8 ? v8 : v;
    float Mx = fmax_(Ms, dpp<XOR1>(Ms)); Mx = fmax_(Mx, dpp<XOR2>(Mx)); Mx = fmax_(Mx, dpp<HMIR>(Mx));
    const float w = __builtin_amdgcn_exp2f(Ms - Mx);
    float Lt = w * Ls; Lt += dpp<XOR1>(Lt); Lt += dpp<XOR2>(Lt); Lt += dpp<HMIR>(Lt);
    float o = dpp<0x00>(w) * __uint_as_float(x[0].x);
    o = fmaf(dpp<0x55>(w), __uint_as_float(x[1].x), o);
    o = fmaf(dpp<0xAA>(w), __uint_as_float(x[2].x), o);
    o = fmaf(dpp<0xFF>(w), __uint_as_float(x[3].x), o);
    o += dpp<ROR4>(o);
    o *= __builtin_amdgcn_rcpf(Lt);
    const unsigned ob = dup_bf16(o);
    f32x4 Y0 = {0.f, 0.f, 0.f, 0.f}, Y1 = {0.f, 0.f, 0.f, 0.f};
#define YA(i) diag((unsigned)__builtin_amdgcn_mov_dpp((int)ob, BCAST + (i), 0xf, 0xf, true), mask)
    Y0 = mfma(YA(4), Wo[0], Y0); Y1 = mfma(YA(12), Wo[4], Y1);
    Y0 = mfma(YA(5), Wo[1], Y0); Y1 = mfma(YA(13), Wo[5], Y1);
    Y0 = mfma(YA(6), Wo[2], Y0); Y1 = mfma(YA(14), Wo[6], Y1);
    Y0 = mfma(YA(7), Wo[3], Y0); Y1 = mfma(YA(15), Wo[7], Y1);
    const f32x4 Y = Y0 + Y1;
    if (wcol < DM) {
      const int dst = WS_YG + (b * 128 + pidx) * YG_ROW + (wave * 128 + lane) * 8;
      st_gran(ws, dst, tag, g < 2 ? Y.x : Y.z);
      st_gran(ws, dst + 512, tag, g < 2 ? Y.y : Y.w);
    }
    if (wave == 0) lds_arrive(l_cnt + 3, lane);
    STAMP(11);
  } else {
    if (wave < 7) {
      const int h = wave - 5;
      lds_wait(l_cnt + 2, 8u);
      STAMP(8);
      const f32x4 m0 = *(const f32x4*)(l_ml + h * 8), m1 = *(const f32x4*)(l_ml + h * 8 + 4);
      const f32x4 l0 = *(const f32x4*)(l_ml + 16 + h * 8), l1 = *(const f32x4*)(l_ml + 16 + h * 8 + 4);
      float ov[8][2];
#pragma unroll
      for (int w = 0; w < 8; ++w) { ov[w][0] = l_o[w * 256 + h * 128 + lane]; ov[w][1] = l_o[w * 256 + h * 128 + 64 + lane]; }
      const float M = fmax_(fmax_(fmax_(m0.x, m0.y), fmax_(m0.z, m0.w)), fmax_(fmax_(m1.x, m1.y), fmax_(m1.z, m1.w)));
      float o0 = 0.f, o1 = 0.f, L = 0.f;
#pragma unroll
      for (int w = 0; w < 8; ++w) {
        const float e = __builtin_amdgcn_exp2f((w < 4 ? m0[w & 3] : m1[w & 3]) - M);
        o0 = fmaf(e, ov[w][0], o0); o1 = fmaf(e, ov[w][1], o1);
        L = fmaf(e, w < 4 ? l0[w & 3] : l1[w & 3], L);
      }
      const int dst = WS_PG + (G * 8 + s) * PG_ROW;
      st_gran(ws, dst + (h * 128 + lane) * 8, tag, o0);
      st_gran(ws, dst + (h * 128 + 64 + lane) * 8, tag, o1);
      if (lane < 16) st_gran(ws, dst + (256 + h * 16 + lane) * 8, tag, (lane & 1) ? L : M);
      STAMP(9);
    }
    if (wave == 7 || (wave == 6 && pidx < 16)) {
      const int pos = wave == 7 ? pidx * 4 : 512 + (pidx >> 1) * 16 + (pidx & 1) * 4;
      const int so = WS_YG + (b * 128 + (lane >> 2)) * YG_ROW + (pos + (lane & 3)) * 8;
      const int off[8] = {so, so + 16 * YG_ROW, so + 32 * YG_ROW, so + 48 * YG_ROW, so + 64 * YG_ROW, so + 80 * YG_ROW, so + 96 * YG_ROW, so + 112 * YG_ROW};
      const int p = pos + (lane & 3), pl = p & 63;
      const int col = (p & ~127) + 8 * (pl & 15) + 4 * ((pl >> 4) & 1) + 2 * (pl >> 5) + ((p >> 6) & 1);
      v2u x[8];
      lds_wait(l_cnt + 3, 1u);
#pragma unroll
      for (int k = 0; k < 8; ++k) x[k] = ldg(rw, off[k]);
      sweep<8, 2>(rw, off, tag, x);
      STAMP(12);
      float y = ((__uint_as_float(x[0].x) + __uint_as_float(x[1].x)) + (__uint_as_float(x[2].x) + __uint_as_float(x[3].x)))
              + ((__uint_as_float(x[4].x) + __uint_as_float(x[5].x)) + (__uint_as_float(x[6].x) + __uint_as_float(x[7].x)));
      y += dpp<ROR4>(y); y += dpp<ROR8>(y);
      y = xg_sum(y);
      if (lane < 4) outp[b * DM + col] = f2bf(y);
      if (pos == 0 && lane == 0) __hip_atomic_store((gu32*)(ws + WS_EPOCH + b * 256), tag, RLX_AGENT);
      STAMP(13);
    }
  }
  STAMP_OUT();
}

extern "C" void kernel_launch(void* const* d_in, const int* in_sizes, int n_in, void* d_out, int out_size, void* d_ws, size_t ws_size, hipStream_t stream) {
  Args a;
  a.x[0] = d_in[0]; a.k[0] = d_in[1]; a.v[0] = d_in[2]; a.wq[0] = d_in[3]; a.wo[0] = d_in[4];
  a.x[1] = d_in[5]; a.k[1] = d_in[6]; a.v[1] = d_in[7]; a.wq[1] = d_in[8]; a.wo[1] = d_in[9];
  a.out = d_out; a.ws = d_ws;
  gqa2_fused<<<NWG, NTHR, 0, stream>>>(a);
}
```

```cpp
#include <hip/hip_runtime.h>
#include <cstdint>
#include <cstdio>

typedef unsigned v4u __attribute__((ext_vector_type(4)));
typedef unsigned v2u __attribute__((ext_vector_type(2)));
typedef float f32x4 __attribute__((ext_vector_type(4)));
typedef __bf16 bf16x8 __attribute__((ext_vector_type(8)));
typedef __bf16 bf16x2 __attribute__((ext_vector_type(2)));
typedef float f32x2 __attribute__((ext_vector_type(2)));
#define GAS __attribute__((address_space(1)))
typedef GAS unsigned gu32;
typedef GAS unsigned long long gu64;
#define RLX_AGENT __ATOMIC_RELAXED, __HIP_MEMORY_SCOPE_AGENT
#define RLX_WG __ATOMIC_RELAXED, __HIP_MEMORY_SCOPE_WORKGROUP

#define STAMPS 0
#define NSLOT 4
#define WFULL 28
#define DELAY0 0
#define DELAY1 32
#define ABL 0
#define WOAT 7u
#define POL "nt"
#define PACEN 4
#define STATIC 0
#define MAPV 0

constexpr int DM = 576, SK = 16384, NQ = 4096;
constexpr int NWG = 256, NTHR = 512;
constexpr int NT = 128;
constexpr int AUX_NT = 2, AUX_SC1 = 16;
constexpr int RSRC3 = 0x00020000;
constexpr int OOB = 0x7fff0000;
constexpr int WS_EPOCH = 0;
constexpr int WS_QG = 4096;
constexpr int PG_ROW = 288 * 8;
constexpr int WS_PG = WS_QG + 32 * 4096;
constexpr int YG_ROW = 640 * 8;
constexpr int WS_YG = WS_PG + 256 * PG_ROW;
constexpr int WS_ST = WS_YG + 256 * YG_ROW;
constexpr unsigned TAG_STEP = 0x9E3779B9u;
constexpr unsigned SPIN_CAP = 1u << 17;

struct Args {
  const void* x[2]; const void* k[2]; const void* v[2]; const void* wq[2]; const void* wo[2];
  void* out; void* ws;
};

#define SB() __builtin_amdgcn_sched_barrier(0)
__device__ __forceinline__ f32x4 mfma(v4u a, v4u b, f32x4 c) {
  return __builtin_amdgcn_mfma_f32_16x16x32_bf16(__builtin_bit_cast(bf16x8, a), __builtin_bit_cast(bf16x8, b), c, 0, 0, 0);
}
__device__ __forceinline__ unsigned dup_bf16(float f) {
  f32x2 v = {f, f};
  return __builtin_bit_cast(unsigned, __builtin_convertvector(v, bf16x2));
}
__device__ __forceinline__ unsigned short f2bf(float f) { return __builtin_bit_cast(unsigned short, (__bf16)f); }
__device__ __forceinline__ v4u diag(unsigned pp, v4u mask) { v4u r = {pp & mask.x, pp & mask.y, pp & mask.z, pp & mask.w}; return r; }
template <int CTRL> __device__ __forceinline__ float dpp(float x) {
  return __builtin_bit_cast(float, __builtin_amdgcn_mov_dpp(__builtin_bit_cast(int, x), CTRL, 0xf, 0xf, true));
}
constexpr int XOR1 = 0xB1, XOR2 = 0x4E, HMIR = 0x141, ROR8 = 0x128, ROR4 = 0x124, BCAST = 0x150;
__device__ __forceinline__ float fmax_(float a, float b) { return __builtin_fmaxf(a, b); }
__device__ __forceinline__ float xg_max(float x) {
  auto s = __builtin_amdgcn_permlane16_swap(__float_as_uint(x), __float_as_uint(x), false, false);
  x = fmax_(__uint_as_float(s[0]), __uint_as_float(s[1]));
  auto t = __builtin_amdgcn_permlane32_swap(__float_as_uint(x), __float_as_uint(x), false, false);
  return fmax_(__uint_as_float(t[0]), __uint_as_float(t[1]));
}
__device__ __forceinline__ float xg_sum(float x) {
  auto s = __builtin_amdgcn_permlane16_swap(__float_as_uint(x), __float_as_uint(x), false, false);
  x = __uint_as_float(s[0]) + __uint_as_float(s[1]);
  auto t = __builtin_amdgcn_permlane32_swap(__float_as_uint(x), __float_as_uint(x), false, false);
  return __uint_as_float(t[0]) + __uint_as_float(t[1]);
}
__device__ __forceinline__ void st_gran(unsigned char* ws, int off, unsigned tag, float v) {
  __hip_atomic_store((gu64*)(ws + off), ((unsigned long long)tag << 32) | __float_as_uint(v), RLX_AGENT);
}
__device__ __forceinline__ v2u ldg(__amdgpu_buffer_rsrc_t rs, int off) { return __builtin_amdgcn_raw_buffer_load_b64(rs, off, 0, AUX_SC1); }
template <int N, int SLEEP> __device__ __forceinline__ void sweep(__amdgpu_buffer_rsrc_t rs, const int (&off)[N], unsigned tag, v2u (&x)[N]) {
#pragma nounroll
  for (unsigned spins = 0; spins < SPIN_CAP; ++spins) {
    bool ok = true;
#pragma unroll
    for (int k = 0; k < N; ++k) ok &= x[k].y == tag;
    if (__all(ok)) break;
    __builtin_amdgcn_s_sleep(SLEEP);
    v2u y[N];
#pragma unroll
    for (int k = 0; k < N; ++k) y[k] = ldg(rs, x[k].y == tag ? OOB : off[k]);
    SB();
#pragma unroll
    for (int k = 0; k < N; ++k) x[k] = x[k].y == tag ? x[k] : y[k];
  }
}
__device__ __forceinline__ v4u mkrs(const void* p, unsigned bytes) {
  const unsigned long long u = (unsigned long long)p;
  v4u r = {(unsigned)u, (unsigned)(u >> 32) & 0xffffu, bytes, (unsigned)RSRC3};
  return r;
}
template <int IMM> __device__ __forceinline__ void bl128(v4u& d, int voff, v4u rs, int soff) {
  asm volatile("buffer_load_dwordx4 %0, %1, %2, %3 offen offset:%4 " POL : "=v"(d) : "v"(voff), "s"(rs), "s"(soff), "i"(IMM));
}
#define WAIT4(N, R) WAIT4_(N, R)
#define WAIT4_(N, R) asm volatile("s_waitcnt vmcnt(" #N ")" : "+v"(R[0]), "+v"(R[1]), "+v"(R[2]), "+v"(R[3]))
#define KEEP4(R) asm volatile("" :: "v"(R[0]), "v"(R[1]), "v"(R[2]), "v"(R[3]))
__device__ __forceinline__ void lds_barrier() {
  asm volatile("s_waitcnt lgkmcnt(0)" ::: "memory");
  __builtin_amdgcn_s_barrier();
  asm volatile("" ::: "memory");
}
__device__ __forceinline__ void lds_arrive(unsigned* c, int lane) {
  asm volatile("s_waitcnt lgkmcnt(0)" ::: "memory");
  if (lane == 0) __hip_atomic_fetch_add(c, 1u, RLX_WG);
}
__device__ __forceinline__ void lds_wait(unsigned* c, unsigned want) {
#pragma nounroll
  for (unsigned spins = 0; spins < (1u << 24); ++spins) {
    if (__builtin_amdgcn_readfirstlane(__hip_atomic_load(c, RLX_WG)) >= want) break;
    __builtin_amdgcn_s_sleep(1);
  }
  asm volatile("" ::: "memory");
}
#define STAMP(i) do {} while (0)
#define STAMP_OUT() do {} while (0)

__global__ void __launch_bounds__(NTHR, 2) gqa2_fused(Args a) {
  __shared__ __attribute__((aligned(16))) float lds[8 * 256 + 64 + 16 * 64 + 128 + 8];
  float* const l_o = lds;
  float* const l_ml = lds + 2048;
  float* const l_qp = lds + 2048 + 64;
  unsigned short* const l_q = (unsigned short*)(lds + 2048 + 64 + 1024);
  unsigned* const l_cnt = (unsigned*)(lds + 2048 + 64 + 1024 + 128);

  const int tid = threadIdx.x, lane = tid & 63, wave = __builtin_amdgcn_readfirstlane(tid >> 6);
  const int g = lane >> 4, n = lane & 15;
  const int wg = blockIdx.x;
  const int G = MAPV == 0 ? wg >> 3 : wg & 31, s = MAPV == 0 ? wg & 7 : wg >> 5;
  const int b = G >> 4, kvh = G & 15;
  unsigned char* const ws = (unsigned char*)a.ws;
  STAMP(0);

  const v4u rk = mkrs((unsigned char*)a.k[b] + ((size_t)kvh * SK + s * 2048) * 256, 2048 * 256);
  const v4u rv = mkrs((unsigned char*)a.v[b] + ((size_t)kvh * SK + s * 2048) * 256, 2048 * 256);
  const int cg = s & 3, kh = s >> 2;
  const v4u rq = mkrs((unsigned char*)a.wq[b] + ((size_t)kh * 288 * NQ + kvh * 256 + cg * 64) * 2, 287 * NQ * 2 + 128);
  const v4u rx = mkrs((unsigned char*)a.x[b] + kh * 288 * 2, 288 * 2);
  const v4u rw4 = mkrs(ws, WS_ST);
  const __amdgpu_buffer_rsrc_t rw = __builtin_amdgcn_make_buffer_rsrc(ws, 0, WS_ST, RSRC3);
  const void* wop = a.wo[b];
  asm volatile("" : "+s"(wop));
  const __amdgpu_buffer_rsrc_t ro = __builtin_amdgcn_make_buffer_rsrc((unsigned char*)wop + ((size_t)(kvh * 256 + s * 32) * DM) * 2, 0, 32 * DM * 2, RSRC3);
  GAS unsigned short* outp = (GAS unsigned short*)a.out;
  asm volatile("" : "+s"(outp));

  unsigned ep;
  v4u wqv[5]; unsigned xv[5];
  {
    const int row = 8 * wave + 2 * g + (n >> 3);
    const int vo = (ABL & 1) ? OOB : row * (NQ * 2) + (n & 7) * 16;
    bl128<0>(wqv[0], vo, rq, 0);
    bl128<0>(wqv[1], vo, rq, 1 * 64 * NQ * 2);
    bl128<0>(wqv[2], vo, rq, 2 * 64 * NQ * 2);
    bl128<0>(wqv[3], vo, rq, 3 * 64 * NQ * 2);
    bl128<0>(wqv[4], wave >= 4 ? OOB : vo, rq, 4 * 64 * NQ * 2);
#define XLOAD(i) asm volatile("buffer_load_ushort %0, %1, %2, 0 offen offset:%3" : "=v"(xv[i]) : "v"(row * 2), "s"(rx), "i"(i * 128))
    XLOAD(0); XLOAD(1); XLOAD(2); XLOAD(3); XLOAD(4);
    asm volatile("s_mov_b64 exec, 1\n\tbuffer_load_dword %0, off, %1, %2 sc1\n\ts_mov_b64 exec, -1" : "=v"(ep) : "s"(rw4), "s"(wave == 0 ? WS_EPOCH + b * 256 : OOB));
  }
  const int vk = n * 256 + g * 16, vv = g * 1024 + n * 16;
  v4u Kr[NSLOT][4], Vr[NSLOT][4];
#define ISSUE_K(u, t) { const int so_ = (t) * 4096; bl128<0>(Kr[u][0], vk, rk, so_); bl128<64>(Kr[u][1], vk, rk, so_); bl128<128>(Kr[u][2], vk, rk, so_); bl128<192>(Kr[u][3], vk, rk, so_); }
#define ISSUE_V(u, t) { const int so_ = (t) * 4096; bl128<0>(Vr[u][0], vv, rv, so_); bl128<256>(Vr[u][1], vv, rv, so_); bl128<512>(Vr[u][2], vv, rv, so_); bl128<768>(Vr[u][3], vv, rv, so_); }
  if (tid < 8) l_cnt[tid] = tid == 4 ? 8u * NSLOT : 0u;
  SB();
  lds_barrier();
  ISSUE_K(0, wave); ISSUE_V(0, wave);
  SB();
  v4u mask;
  {
    const unsigned half = (n & 1) ? 0xffff0000u : 0x0000ffffu; const int r = (n & 7) >> 1;
    mask.x = r == 0 ? half : 0u; mask.y = r == 1 ? half : 0u; mask.z = r == 2 ? half : 0u; mask.w = r == 3 ? half : 0u;
  }
  const bool own = (g >> 1) == (n >> 3);
  {
#define W0 "8"
    asm volatile("s_waitcnt vmcnt(" W0 ")" : "+v"(wqv[0]), "+v"(wqv[1]), "+v"(wqv[2]), "+v"(wqv[3]), "+v"(wqv[4]),
                 "+v"(xv[0]), "+v"(xv[1]), "+v"(xv[2]), "+v"(xv[3]), "+v"(xv[4]), "+v"(ep));
    f32x4 aq = {0.f, 0.f, 0.f, 0.f};
#pragma unroll
    for (int i = 0; i < 5; ++i) { const unsigned xx = xv[i] * 0x10001u; aq = mfma(diag(xx, mask), wqv[i], aq); }
    if (own) *(f32x4*)(l_qp + (wave * 2 + (n >> 3)) * 64 + 8 * (n & 7) + 4 * (g & 1)) = aq;
  }
  unsigned tag = __builtin_amdgcn_readfirstlane(ep) + TAG_STEP;
  STAMP(1);
  lds_arrive(l_cnt + 0, lane);
  if (wave == 0) {
    lds_wait(l_cnt + 0, 8u);
    float q = 0.f;
#pragma unroll
    for (int i = 0; i < 16; ++i) q += l_qp[i * 64 + lane];
    st_gran(ws, WS_QG + (G * 512 + kh * 256 + cg * 64 + lane) * 8, tag, q);
    STAMP(2);
    __builtin_amdgcn_s_sleep(DELAY1);
    const int qo = WS_QG + (G * 512 + lane) * 8;
    const int off[8] = {qo, qo + 512, qo + 1024, qo + 1536, qo + 2048, qo + 2560, qo + 3072, qo + 3584};
    v2u x[8];
#pragma unroll
    for (int k = 0; k < 8; ++k) x[k] = ldg(rw, off[k]);
    sweep<8, 1>(rw, off, tag, x);
#pragma unroll
    for (int k = 0; k < 4; ++k) l_q[k * 64 + lane] = f2bf(__uint_as_float(x[k].x) + __uint_as_float(x[k + 4].x));
    if (lane == 0) l_cnt[5] = tag;
    lds_arrive(l_cnt + 1, lane);
    STAMP(3);
  }
#define PACE() PACE_(PACEN)
#define PACE_(N) PACE__(N)
#define PACE__(N) asm volatile("s_cmp_lg_u32 %0, 0\n\ts_cbranch_scc1 .Lp%=\n\ts_waitcnt vmcnt(" #N ")\n.Lp%=:" :: "s"(__builtin_amdgcn_readfirstlane(__hip_atomic_load(l_cnt + 1, RLX_WG))) : "scc", "memory")
  SB();
  PACE(); ISSUE_K(1, wave + 8); SB();
  PACE(); ISSUE_V(1, wave + 8); SB();
  PACE(); ISSUE_K(2, wave + 16); SB();
  PACE(); ISSUE_V(2, wave + 16); SB();
  PACE(); ISSUE_K(3, wave + 24); SB();
  PACE(); ISSUE_V(3, wave + 24); SB();
  STAMP(4);
  lds_wait(l_cnt + 1, 1u);
  tag = __builtin_amdgcn_readfirstlane(l_cnt[5]);
  v4u Q[4];
#pragma unroll
  for (int i = 0; i < 4; ++i) Q[i] = *(const v4u*)(l_q + (n >> 3) * 128 + 32 * i + 8 * g);
  STAMP(5);

  constexpr float C2 = 0.088388347648318440f * 1.4426950408889634f;
  float m = -INFINITY, l = 0.f;
  f32x4 O = {0.f, 0.f, 0.f, 0.f};
  const int wcol = wave * 128 + n * 8;
  const int vwo = (!(ABL & 4) && wcol < DM) ? g * (DM * 2) + wcol * 2 : OOB;
  asm volatile("" : "+v"(Q[0]), "+v"(Q[1]), "+v"(Q[2]), "+v"(Q[3]));
#define TILE_QK(u, W) \
    f32x4 S = {0.f, 0.f, 0.f, 0.f}; \
    WAIT4(W, Kr[u]); \
    if (!(ABL & 8)) { _Pragma("unroll") for (int j_ = 0; j_ < 4; ++j_) S = mfma(Kr[u][j_], Q[j_], S); } \
    asm volatile("" : "+v"(S) :: "memory"); SB();
#define TILE_PV(u, W) { \
    const float tm = xg_max(fmax_(fmax_(S.x, S.y), fmax_(S.z, S.w))) * C2; \
    const float mn = fmax_(m, tm); \
    const float al = __builtin_amdgcn_exp2f(m - mn); \
    m = mn; \
    const float p0 = __builtin_amdgcn_exp2f(fmaf(S.x, C2, -mn)), p1 = __builtin_amdgcn_exp2f(fmaf(S.y, C2, -mn)); \
    const float p2 = __builtin_amdgcn_exp2f(fmaf(S.z, C2, -mn)), p3 = __builtin_amdgcn_exp2f(fmaf(S.w, C2, -mn)); \
    l = fmaf(l, al, (p0 + p1) + (p2 + p3)); \
    const float a8 = dpp<ROR8>(al); \
    O *= own ? al : a8; \
    WAIT4(W, Vr[u]); \
    O = mfma(diag(dup_bf16(p0), mask), Vr[u][0], O); \
    O = mfma(diag(dup_bf16(p1), mask), Vr[u][1], O); \
    O = mfma(diag(dup_bf16(p2), mask), Vr[u][2], O); \
    O = mfma(diag(dup_bf16(p3), mask), Vr[u][3], O); \
    asm volatile("" : "+v"(O), "+v"(l), "+v"(m) :: "memory"); SB(); }
#pragma nounroll
  for (int t = wave + 32; t < NT; t += 32) {
#define SLOT(u) { TILE_QK(u, 28); ISSUE_K(u, t + 8 * u); SB(); TILE_PV(u, 28); ISSUE_V(u, t + 8 * u); SB(); }
    SLOT(0) SLOT(1) SLOT(2) SLOT(3)
  }
  STAMP(6);
  { TILE_QK(0, 28); TILE_PV(0, 24); }
  { TILE_QK(1, 20); TILE_PV(1, 16); }
  { TILE_QK(2, 12); TILE_PV(2, 8); }
  { TILE_QK(3, 4); TILE_PV(3, 0); }
  STAMP(7);
  l = xg_sum(l);
  *(f32x4*)(l_o + wave * 256 + (g >> 1) * 128 + 8 * n + 4 * (g & 1)) = O;
  if (g == 0 && (n & 7) == 0) { l_ml[(n >> 3) * 8 + wave] = m; l_ml[16 + (n >> 3) * 8 + wave] = l; }
  lds_arrive(l_cnt + 2, lane);
  const int pidx = (G & 15) * 8 + s;
  if (wave < 5) {
    const int cb = WS_PG + G * 8 * PG_ROW;
    const int po = cb + (n & 4) * PG_ROW + (32 * s + g + 4 * ((n & 3) + 4 * (n >> 3))) * 8;
    const int off[5] = {po, po + PG_ROW, po + 2 * PG_ROW, po + 3 * PG_ROW, cb + (n & 7) * PG_ROW + (256 + 16 * (s >> 2) + (n >> 3)) * 8};
    v2u x[5];
    lds_wait(l_cnt + 2, WOAT);
    v4u Wo[8];
#pragma unroll
    for (int i = 0; i < 8; ++i) Wo[i] = __builtin_amdgcn_raw_buffer_load_b128(ro, vwo, i * 4 * DM * 2, AUX_NT);
    SB();
    lds_wait(l_cnt + 2, 8u);
#pragma unroll
    for (int k = 0; k < 5; ++k) x[k] = ldg(rw, off[k]);
    sweep<5, 2>(rw, off, tag, x);
    STAMP(10);
    const float v = __uint_as_float(x[4].x), v8 = dpp<ROR8>(v);
    const float Ms = n < 8 ? v : v8, Ls = n < 8 ? v8 : v;
    float Mx = fmax_(Ms, dpp<XOR1>(Ms)); Mx = fmax_(Mx, dpp<XOR2>(Mx)); Mx = fmax_(Mx, dpp<HMIR>(Mx));
    const float w = __builtin_amdgcn_exp2f(Ms - Mx);
    float Lt = w * Ls; Lt += dpp<XOR1>(Lt); Lt += dpp<XOR2>(Lt); Lt += dpp<HMIR>(Lt);
    float o = dpp<0x00>(w) * __uint_as_float(x[0].x);
    o = fmaf(dpp<0x55>(w), __uint_as_float(x[1].x), o);
    o = fmaf(dpp<0xAA>(w), __uint_as_float(x[2].x), o);
    o = fmaf(dpp<0xFF>(w), __uint_as_float(x[3].x), o);
    o += dpp<ROR4>(o);
    o *= __builtin_amdgcn_rcpf(Lt);
    const unsigned ob = dup_bf16(o);
    f32x4 Y0 = {0.f, 0.f, 0.f, 0.f}, Y1 = {0.f, 0.f, 0.f, 0.f};
#define YA(i) diag((unsigned)__builtin_amdgcn_mov_dpp((int)ob, BCAST + (i), 0xf, 0xf, true), mask)
    Y0 = mfma(YA(4), Wo[0], Y0); Y1 = mfma(YA(12), Wo[4], Y1);
    Y0 = mfma(YA(5), Wo[1], Y0); Y1 = mfma(YA(13), Wo[5], Y1);
    Y0 = mfma(YA(6), Wo[2], Y0); Y1 = mfma(YA(14), Wo[6], Y1);
    Y0 = mfma(YA(7), Wo[3], Y0); Y1 = mfma(YA(15), Wo[7], Y1);
    const f32x4 Y = Y0 + Y1;
    if (wcol < DM) {
      const int dst = WS_YG + (b * 128 + pidx) * YG_ROW + (wave * 128 + lane) * 8;
      st_gran(ws, dst, tag, g < 2 ? Y.x : Y.z);
      st_gran(ws, dst + 512, tag, g < 2 ? Y.y : Y.w);
    }
    if (wave == 0) lds_arrive(l_cnt + 3, lane);
    STAMP(11);
  } else {
    if (wave < 7) {
      const int h = wave - 5;
      lds_wait(l_cnt + 2, 8u);
      STAMP(8);
      const f32x4 m0 = *(const f32x4*)(l_ml + h * 8), m1 = *(const f32x4*)(l_ml + h * 8 + 4);
      const f32x4 l0 = *(const f32x4*)(l_ml + 16 + h * 8), l1 = *(const f32x4*)(l_ml + 16 + h * 8 + 4);
      float ov[8][2];
#pragma unroll
      for (int w = 0; w < 8; ++w) { ov[w][0] = l_o[w * 256 + h * 128 + lane]; ov[w][1] = l_o[w * 256 + h * 128 + 64 + lane]; }
      const float M = fmax_(fmax_(fmax_(m0.x, m0.y), fmax_(m0.z, m0.w)), fmax_(fmax_(m1.x, m1.y), fmax_(m1.z, m1.w)));
      float o0 = 0.f, o1 = 0.f, L = 0.f;
#pragma unroll
      for (int w = 0; w < 8; ++w) {
        const float e = __builtin_amdgcn_exp2f((w < 4 ? m0[w & 3] : m1[w & 3]) - M);
        o0 = fmaf(e, ov[w][0], o0); o1 = fmaf(e, ov[w][1], o1);
        L = fmaf(e, w < 4 ? l0[w & 3] : l1[w & 3], L);
      }
      const int dst = WS_PG + (G * 8 + s) * PG_ROW;
      st_gran(ws, dst + (h * 128 + lane) * 8, tag, o0);
      st_gran(ws, dst + (h * 128 + 64 + lane) * 8, tag, o1);
      if (lane < 16) st_gran(ws, dst + (256 + h * 16 + lane) * 8, tag, (lane & 1) ? L : M);
      STAMP(9);
    }
    if (wave == 7 || (wave == 6 && pidx < 16)) {
      const int pos = wave == 7 ? pidx * 4 : 512 + (pidx >> 1) * 16 + (pidx & 1) * 4;
      const int so = WS_YG + (b * 128 + (lane >> 2)) * YG_ROW + (pos + (lane & 3)) * 8;
      const int off[8] = {so, so + 16 * YG_ROW, so + 32 * YG_ROW, so + 48 * YG_ROW, so + 64 * YG_ROW, so + 80 * YG_ROW, so + 96 * YG_ROW, so + 112 * YG_ROW};
      const int p = pos + (lane & 3), pl = p & 63;
      const int col = (p & ~127) + 8 * (pl & 15) + 4 * ((pl >> 4) & 1) + 2 * (pl >> 5) + ((p >> 6) & 1);
      v2u x[8];
      lds_wait(l_cnt + 3, 1u);
#pragma unroll
      for (int k = 0; k < 8; ++k) x[k] = ldg(rw, off[k]);
      sweep<8, 2>(rw, off, tag, x);
      STAMP(12);
      float y = ((__uint_as_float(x[0].x) + __uint_as_float(x[1].x)) + (__uint_as_float(x[2].x) + __uint_as_float(x[3].x)))
              + ((__uint_as_float(x[4].x) + __uint_as_float(x[5].x)) + (__uint_as_float(x[6].x) + __uint_as_float(x[7].x)));
      y += dpp<ROR4>(y); y += dpp<ROR8>(y);
      y = xg_sum(y);
      if (lane < 4) outp[b * DM + col] = f2bf(y);
      if (pos == 0 && lane == 0) __hip_atomic_store((gu32*)(ws + WS_EPOCH + b * 256), tag, RLX_AGENT);
      STAMP(13);
    }
  }
  STAMP_OUT();
}

extern "C" void kernel_launch(void* const* d_in, const int* in_sizes, int n_in, void* d_out, int out_size, void* d_ws, size_t ws_size, hipStream_t stream) {
  Args a;
  a.x[0] = d_in[0]; a.k[0] = d_in[1]; a.v[0] = d_in[2]; a.wq[0] = d_in[3]; a.wo[0] = d_in[4];
  a.x[1] = d_in[5]; a.k[1] = d_in[6]; a.v[1] = d_in[7]; a.wq[1] = d_in[8]; a.wo[1] = d_in[9];
  a.out = d_out; a.ws = d_ws;
  gqa2_fused<<<NWG, NTHR, 0, stream>>>(a);
}
```

```cpp
#define PMASK 8
#include <hip/hip_runtime.h>
#include <cstdint>
#include <cstdio>

typedef unsigned v4u __attribute__((ext_vector_type(4)));
typedef unsigned v2u __attribute__((ext_vector_type(2)));
typedef float f32x4 __attribute__((ext_vector_type(4)));
typedef __bf16 bf16x8 __attribute__((ext_vector_type(8)));
typedef __bf16 bf16x2 __attribute__((ext_vector_type(2)));
typedef float f32x2 __attribute__((ext_vector_type(2)));
#define GAS __attribute__((address_space(1)))
typedef GAS unsigned gu32;
typedef GAS unsigned long long gu64;
#define RLX_AGENT __ATOMIC_RELAXED, __HIP_MEMORY_SCOPE_AGENT
#define RLX_WG __ATOMIC_RELAXED, __HIP_MEMORY_SCOPE_WORKGROUP

#define STAMPS 0
#define NSLOT 4
#define WFULL 28
#define DELAY0 0
#define DELAY1 32
#define ABL 0
#define WOAT 7u
#define POL "nt"
#define PACEN 4
#define STATIC 0
#define MAPV 0

constexpr int DM = 576, SK = 16384, NQ = 4096;
constexpr int NWG = 256, NTHR = 512;
constexpr int NT = 128;
constexpr int AUX_NT = 2, AUX_SC1 = 16;
constexpr int RSRC3 = 0x00020000;
constexpr int OOB = 0x7fff0000;
constexpr int WS_EPOCH = 0;
constexpr int WS_QG = 4096;
constexpr int PG_ROW = 288 * 8;
constexpr int WS_PG = WS_QG + 32 * 4096;
constexpr int YG_ROW = 640 * 8;
constexpr int WS_YG = WS_PG + 256 * PG_ROW;
constexpr int WS_ST = WS_YG + 256 * YG_ROW;
constexpr unsigned TAG_STEP = 0x9E3779B9u;
constexpr unsigned SPIN_CAP = 1u << 17;

struct Args {
  const void* x[2]; const void* k[2]; const void* v[2]; const void* wq[2]; const void* wo[2];
  void* out; void* ws;
};

#define SB() __builtin_amdgcn_sched_barrier(0)
__device__ __forceinline__ f32x4 mfma(v4u a, v4u b, f32x4 c) {
  return __builtin_amdgcn_mfma_f32_16x16x32_bf16(__builtin_bit_cast(bf16x8, a), __builtin_bit_cast(bf16x8, b), c, 0, 0, 0);
}
__device__ __forceinline__ unsigned dup_bf16(float f) {
  f32x2 v = {f, f};
  return __builtin_bit_cast(unsigned, __builtin_convertvector(v, bf16x2));
}
__device__ __forceinline__ unsigned short f2bf(float f) { return __builtin_bit_cast(unsigned short, (__bf16)f); }
__device__ __forceinline__ v4u diag(unsigned pp, v4u mask) { v4u r = {pp & mask.x, pp & mask.y, pp & mask.z, pp & mask.w}; return r; }
template <int CTRL> __device__ __forceinline__ float dpp(float x) {
  return __builtin_bit_cast(float, __builtin_amdgcn_mov_dpp(__builtin_bit_cast(int, x), CTRL, 0xf, 0xf, true));
}
constexpr int XOR1 = 0xB1, XOR2 = 0x4E, HMIR = 0x141, ROR8 = 0x128, ROR4 = 0x124, BCAST = 0x150;
__device__ __forceinline__ float fmax_(float a, float b) { return __builtin_fmaxf(a, b); }
__device__ __forceinline__ float xg_max(float x) {
  auto s = __builtin_amdgcn_permlane16_swap(__float_as_uint(x), __float_as_uint(x), false, false);
  x = fmax_(__uint_as_float(s[0]), __uint_as_float(s[1]));
  auto t = __builtin_amdgcn_permlane32_swap(__float_as_uint(x), __float_as_uint(x), false, false);
  return fmax_(__uint_as_float(t[0]), __uint_as_float(t[1]));
}
__device__ __forceinline__ float xg_sum(float x) {
  auto s = __builtin_amdgcn_permlane16_swap(__float_as_uint(x), __float_as_uint(x), false, false);
  x = __uint_as_float(s[0]) + __uint_as_float(s[1]);
  auto t = __builtin_amdgcn_permlane32_swap(__float_as_uint(x), __float_as_uint(x), false, false);
  return __uint_as_float(t[0]) + __uint_as_float(t[1]);
}
__device__ __forceinline__ void st_gran(unsigned char* ws, int off, unsigned tag, float v) {
  __hip_atomic_store((gu64*)(ws + off), ((unsigned long long)tag << 32) | __float_as_uint(v), RLX_AGENT);
}
__device__ __forceinline__ v2u ldg(__amdgpu_buffer_rsrc_t rs, int off) { return __builtin_amdgcn_raw_buffer_load_b64(rs, off, 0, AUX_SC1); }
template <int N, int SLEEP> __device__ __forceinline__ void sweep(__amdgpu_buffer_rsrc_t rs, const int (&off)[N], unsigned tag, v2u (&x)[N]) {
#pragma nounroll
  for (unsigned spins = 0; spins < SPIN_CAP; ++spins) {
    bool ok = true;
#pragma unroll
    for (int k = 0; k < N; ++k) ok &= x[k].y == tag;
    if (__all(ok)) break;
    __builtin_amdgcn_s_sleep(SLEEP);
    v2u y[N];
#pragma unroll
    for (int k = 0; k < N; ++k) y[k] = ldg(rs, x[k].y == tag ? OOB : off[k]);
    SB();
#pragma unroll
    for (int k = 0; k < N; ++k) x[k] = x[k].y == tag ? x[k] : y[k];
  }
}
__device__ __forceinline__ v4u mkrs(const void* p, unsigned bytes) {
  const unsigned long long u = (unsigned long long)p;
  v4u r = {(unsigned)u, (unsigned)(u >> 32) & 0xffffu, bytes, (unsigned)RSRC3};
  return r;
}
template <int STEP> __device__ __forceinline__ void bl4p(v4u (&d)[4], int voff, v4u rs, int soff, int def) {
  asm volatile("s_cmp_lg_u32 %7, 0\n\ts_cbranch_scc1 .Ld%=\n\t"
               "buffer_load_dwordx4 %0, %4, %5, %6 offen nt\n\t"
               "buffer_load_dwordx4 %1, %4, %5, %6 offen offset:%8 nt\n\t"
               "buffer_load_dwordx4 %2, %4, %5, %6 offen offset:%9 nt\n\t"
               "buffer_load_dwordx4 %3, %4, %5, %6 offen offset:%10 nt\n\t"
               "s_branch .Le%=\n.Ld%=:\n\t"
               "buffer_load_dwordx4 %0, %4, %5, %6 offen\n\t"
               "buffer_load_dwordx4 %1, %4, %5, %6 offen offset:%8\n\t"
               "buffer_load_dwordx4 %2, %4, %5, %6 offen offset:%9\n\t"
               "buffer_load_dwordx4 %3, %4, %5, %6 offen offset:%10\n.Le%=:"
               : "=&v"(d[0]), "=&v"(d[1]), "=&v"(d[2]), "=&v"(d[3]) : "v"(voff), "s"(rs), "s"(soff), "s"(def), "i"(STEP), "i"(2 * STEP), "i"(3 * STEP) : "scc");
}
template <int IMM> __device__ __forceinline__ void bl128(v4u& d, int voff, v4u rs, int soff) {
  asm volatile("buffer_load_dwordx4 %0, %1, %2, %3 offen offset:%4 " POL : "=v"(d) : "v"(voff), "s"(rs), "s"(soff), "i"(IMM));
}
#define WAIT4(N, R) WAIT4_(N, R)
#define WAIT4_(N, R) asm volatile("s_waitcnt vmcnt(" #N ")" : "+v"(R[0]), "+v"(R[1]), "+v"(R[2]), "+v"(R[3]))
#define KEEP4(R) asm volatile("" :: "v"(R[0]), "v"(R[1]), "v"(R[2]), "v"(R[3]))
__device__ __forceinline__ void lds_barrier() {
  asm volatile("s_waitcnt lgkmcnt(0)" ::: "memory");
  __builtin_amdgcn_s_barrier();
  asm volatile("" ::: "memory");
}
__device__ __forceinline__ void lds_arrive(unsigned* c, int lane) {
  asm volatile("s_waitcnt lgkmcnt(0)" ::: "memory");
  if (lane == 0) __hip_atomic_fetch_add(c, 1u, RLX_WG);
}
__device__ __forceinline__ void lds_wait(unsigned* c, unsigned want) {
#pragma nounroll
  for (unsigned spins = 0; spins < (1u << 24); ++spins) {
    if (__builtin_amdgcn_readfirstlane(__hip_atomic_load(c, RLX_WG)) >= want) break;
    __builtin_amdgcn_s_sleep(1);
  }
  asm volatile("" ::: "memory");
}
#define STAMP(i) do {} while (0)
#define STAMP_OUT() do {} while (0)

__global__ void __launch_bounds__(NTHR, 2) gqa2_fused(Args a) {
  __shared__ __attribute__((aligned(16))) float lds[8 * 256 + 64 + 16 * 64 + 128 + 8];
  float* const l_o = lds;
  float* const l_ml = lds + 2048;
  float* const l_qp = lds + 2048 + 64;
  unsigned short* const l_q = (unsigned short*)(lds + 2048 + 64 + 1024);
  unsigned* const l_cnt = (unsigned*)(lds + 2048 + 64 + 1024 + 128);

  const int tid = threadIdx.x, lane = tid & 63, wave = __builtin_amdgcn_readfirstlane(tid >> 6);
  const int g = lane >> 4, n = lane & 15;
  const int wg = blockIdx.x;
  const int G = MAPV == 0 ? wg >> 3 : wg & 31, s = MAPV == 0 ? wg & 7 : wg >> 5;
  const int b = G >> 4, kvh = G & 15;
  unsigned char* const ws = (unsigned char*)a.ws;
  STAMP(0);

  const v4u rk = mkrs((unsigned char*)a.k[b] + ((size_t)kvh * SK + s * 2048) * 256, 2048 * 256);
  const v4u rv = mkrs((unsigned char*)a.v[b] + ((size_t)kvh * SK + s * 2048) * 256, 2048 * 256);
  const int cg = s & 3, kh = s >> 2;
  const v4u rq = mkrs((unsigned char*)a.wq[b] + ((size_t)kh * 288 * NQ + kvh * 256 + cg * 64) * 2, 287 * NQ * 2 + 128);
  const v4u rx = mkrs((unsigned char*)a.x[b] + kh * 288 * 2, 288 * 2);
  const v4u rw4 = mkrs(ws, WS_ST);
  const __amdgpu_buffer_rsrc_t rw = __builtin_amdgcn_make_buffer_rsrc(ws, 0, WS_ST, RSRC3);
  const void* wop = a.wo[b];
  asm volatile("" : "+s"(wop));
  const __amdgpu_buffer_rsrc_t ro = __builtin_amdgcn_make_buffer_rsrc((unsigned char*)wop + ((size_t)(kvh * 256 + s * 32) * DM) * 2, 0, 32 * DM * 2, RSRC3);
  GAS unsigned short* outp = (GAS unsigned short*)a.out;
  asm volatile("" : "+s"(outp));

  unsigned ep;
  v4u wqv[5]; unsigned xv[5];
  {
    const int row = 8 * wave + 2 * g + (n >> 3);
    const int vo = (ABL & 1) ? OOB : row * (NQ * 2) + (n & 7) * 16;
    bl128<0>(wqv[0], vo, rq, 0);
    bl128<0>(wqv[1], vo, rq, 1 * 64 * NQ * 2);
    bl128<0>(wqv[2], vo, rq, 2 * 64 * NQ * 2);
    bl128<0>(wqv[3], vo, rq, 3 * 64 * NQ * 2);
    bl128<0>(wqv[4], wave >= 4 ? OOB : vo, rq, 4 * 64 * NQ * 2);
#define XLOAD(i) asm volatile("buffer_load_ushort %0, %1, %2, 0 offen offset:%3" : "=v"(xv[i]) : "v"(row * 2), "s"(rx), "i"(i * 128))
    XLOAD(0); XLOAD(1); XLOAD(2); XLOAD(3); XLOAD(4);
    asm volatile("s_mov_b64 exec, 1\n\tbuffer_load_dword %0, off, %1, %2 sc1\n\ts_mov_b64 exec, -1" : "=v"(ep) : "s"(rw4), "s"(wave == 0 ? WS_EPOCH + b * 256 : OOB));
  }
  const int vk = n * 256 + g * 16, vv = g * 1024 + n * 16;
  v4u Kr[NSLOT][4], Vr[NSLOT][4];
  int pm = PMASK; asm volatile("" : "+s"(pm));
  const int defk = (pm >> (2 * b)) & 1, defv = (pm >> (2 * b + 1)) & 1;
#define ISSUE_K(u, t) bl4p<64>(Kr[u], vk, rk, (t) * 4096, defk);
#define ISSUE_V(u, t) bl4p<256>(Vr[u], vv, rv, (t) * 4096, defv);
  if (tid < 8) l_cnt[tid] = tid == 4 ? 8u * NSLOT : 0u;
  SB();
  lds_barrier();
  ISSUE_K(0, wave); ISSUE_V(0, wave);
  SB();
  v4u mask;
  {
    const unsigned half = (n & 1) ? 0xffff0000u : 0x0000ffffu; const int r = (n & 7) >> 1;
    mask.x = r == 0 ? half : 0u; mask.y = r == 1 ? half : 0u; mask.z = r == 2 ? half : 0u; mask.w = r == 3 ? half : 0u;
  }
  const bool own = (g >> 1) == (n >> 3);
  {
#define W0 "8"
    asm volatile("s_waitcnt vmcnt(" W0 ")" : "+v"(wqv[0]), "+v"(wqv[1]), "+v"(wqv[2]), "+v"(wqv[3]), "+v"(wqv[4]),
                 "+v"(xv[0]), "+v"(xv[1]), "+v"(xv[2]), "+v"(xv[3]), "+v"(xv[4]), "+v"(ep));
    f32x4 aq = {0.f, 0.f, 0.f, 0.f};
#pragma unroll
    for (int i = 0; i < 5; ++i) { const unsigned xx = xv[i] * 0x10001u; aq = mfma(diag(xx, mask), wqv[i], aq); }
    if (own) *(f32x4*)(l_qp + (wave * 2 + (n >> 3)) * 64 + 8 * (n & 7) + 4 * (g & 1)) = aq;
  }
  unsigned tag = __builtin_amdgcn_readfirstlane(ep) + TAG_STEP;
  STAMP(1);
  lds_arrive(l_cnt + 0, lane);
  if (wave == 0) {
    lds_wait(l_cnt + 0, 8u);
    float q = 0.f;
#pragma unroll
    for (int i = 0; i < 16; ++i) q += l_qp[i * 64 + lane];
    st_gran(ws, WS_QG + (G * 512 + kh * 256 + cg * 64 + lane) * 8, tag, q);
    STAMP(2);
    __builtin_amdgcn_s_sleep(DELAY1);
    const int qo = WS_QG + (G * 512 + lane) * 8;
    const int off[8] = {qo, qo + 512, qo + 1024, qo + 1536, qo + 2048, qo + 2560, qo + 3072, qo + 3584};
    v2u x[8];
#pragma unroll
    for (int k = 0; k < 8; ++k) x[k] = ldg(rw, off[k]);
    sweep<8, 1>(rw, off, tag, x);
#pragma unroll
    for (int k = 0; k < 4; ++k) l_q[k * 64 + lane] = f2bf(__uint_as_float(x[k].x) + __uint_as_float(x[k + 4].x));
    if (lane == 0) l_cnt[5] = tag;
    lds_arrive(l_cnt + 1, lane);
    STAMP(3);
  }
#define PACE() PACE_(PACEN)
#define PACE_(N) PACE__(N)
#define PACE__(N) asm volatile("s_cmp_lg_u32 %0, 0\n\ts_cbranch_scc1 .Lp%=\n\ts_waitcnt vmcnt(" #N ")\n.Lp%=:" :: "s"(__builtin_amdgcn_readfirstlane(__hip_atomic_load(l_cnt + 1, RLX_WG))) : "scc", "memory")
  SB();
  PACE(); ISSUE_K(1, wave + 8); SB();
  PACE(); ISSUE_V(1, wave + 8); SB();
  PACE(); ISSUE_K(2, wave + 16); SB();
  PACE(); ISSUE_V(2, wave + 16); SB();
  PACE(); ISSUE_K(3, wave + 24); SB();
  PACE(); ISSUE_V(3, wave + 24); SB();
  STAMP(4);
  lds_wait(l_cnt + 1, 1u);
  tag = __builtin_amdgcn_readfirstlane(l_cnt[5]);
  v4u Q[4];
#pragma unroll
  for (int i = 0; i < 4; ++i) Q[i] = *(const v4u*)(l_q + (n >> 3) * 128 + 32 * i + 8 * g);
  STAMP(5);

  constexpr float C2 = 0.088388347648318440f * 1.4426950408889634f;
  float m = -INFINITY, l = 0.f;
  f32x4 O = {0.f, 0.f, 0.f, 0.f};
  const int wcol = wave * 128 + n * 8;
  const int vwo = (!(ABL & 4) && wcol < DM) ? g * (DM * 2) + wcol * 2 : OOB;
  asm volatile("" : "+v"(Q[0]), "+v"(Q[1]), "+v"(Q[2]), "+v"(Q[3]));
#define TILE_QK(u, W) \
    f32x4 S = {0.f, 0.f, 0.f, 0.f}; \
    WAIT4(W, Kr[u]); \
    if (!(ABL & 8)) { _Pragma("unroll") for (int j_ = 0; j_ < 4; ++j_) S = mfma(Kr[u][j_], Q[j_], S); } \
    asm volatile("" : "+v"(S) :: "memory"); SB();
#define TILE_PV(u, W) { \
    const float tm = xg_max(fmax_(fmax_(S.x, S.y), fmax_(S.z, S.w))) * C2; \
    const float mn = fmax_(m, tm); \
    const float al = __builtin_amdgcn_exp2f(m - mn); \
    m = mn; \
    const float p0 = __builtin_amdgcn_exp2f(fmaf(S.x, C2, -mn)), p1 = __builtin_amdgcn_exp2f(fmaf(S.y, C2, -mn)); \
    const float p2 = __builtin_amdgcn_exp2f(fmaf(S.z, C2, -mn)), p3 = __builtin_amdgcn_exp2f(fmaf(S.w, C2, -mn)); \
    l = fmaf(l, al, (p0 + p1) + (p2 + p3)); \
    const float a8 = dpp<ROR8>(al); \
    O *= own ? al : a8; \
    WAIT4(W, Vr[u]); \
    O = mfma(diag(dup_bf16(p0), mask), Vr[u][0], O); \
    O = mfma(diag(dup_bf16(p1), mask), Vr[u][1], O); \
    O = mfma(diag(dup_bf16(p2), mask), Vr[u][2], O); \
    O = mfma(diag(dup_bf16(p3), mask), Vr[u][3], O); \
    asm volatile("" : "+v"(O), "+v"(l), "+v"(m) :: "memory"); SB(); }
#pragma nounroll
  for (int t = wave + 32; t < NT; t += 32) {
#define SLOT(u) { TILE_QK(u, 28); ISSUE_K(u, t + 8 * u); SB(); TILE_PV(u, 28); ISSUE_V(u, t + 8 * u); SB(); }
    SLOT(0) SLOT(1) SLOT(2) SLOT(3)
  }
  STAMP(6);
  { TILE_QK(0, 28); TILE_PV(0, 24); }
  { TILE_QK(1, 20); TILE_PV(1, 16); }
  { TILE_QK(2, 12); TILE_PV(2, 8); }
  { TILE_QK(3, 4); TILE_PV(3, 0); }
  STAMP(7);
  l = xg_sum(l);
  *(f32x4*)(l_o + wave * 256 + (g >> 1) * 128 + 8 * n + 4 * (g & 1)) = O;
  if (g == 0 && (n & 7) == 0) { l_ml[(n >> 3) * 8 + wave] = m; l_ml[16 + (n >> 3) * 8 + wave] = l; }
  lds_arrive(l_cnt + 2, lane);
  const int pidx = (G & 15) * 8 + s;
  if (wave < 5) {
    const int cb = WS_PG + G * 8 * PG_ROW;
    const int po = cb + (n & 4) * PG_ROW + (32 * s + g + 4 * ((n & 3) + 4 * (n >> 3))) * 8;
    const int off[5] = {po, po + PG_ROW, po + 2 * PG_ROW, po + 3 * PG_ROW, cb + (n & 7) * PG_ROW + (256 + 16 * (s >> 2) + (n >> 3)) * 8};
    v2u x[5];
    lds_wait(l_cnt + 2, WOAT);
    v4u Wo[8];
#pragma unroll
    for (int i = 0; i < 8; ++i) Wo[i] = __builtin_amdgcn_raw_buffer_load_b128(ro, vwo, i * 4 * DM * 2, AUX_NT);
    SB();
    lds_wait(l_cnt + 2, 8u);
#pragma unroll
    for (int k = 0; k < 5; ++k) x[k] = ldg(rw, off[k]);
    sweep<5, 2>(rw, off, tag, x);
    STAMP(10);
    const float v = __uint_as_float(x[4].x), v8 = dpp<ROR8>(v);
    const float Ms = n < 8 ? v : v8, Ls = n < 8 ? v8 : v;
    float Mx = fmax_(Ms, dpp<XOR1>(Ms)); Mx = fmax_(Mx, dpp<XOR2>(Mx)); Mx = fmax_(Mx, dpp<HMIR>(Mx));
    const float w = __builtin_amdgcn_exp2f(Ms - Mx);
    float Lt = w * Ls; Lt += dpp<XOR1>(Lt); Lt += dpp<XOR2>(Lt); Lt += dpp<HMIR>(Lt);
    float o = dpp<0x00>(w) * __uint_as_float(x[0].x);
    o = fmaf(dpp<0x55>(w), __uint_as_float(x[1].x), o);
    o = fmaf(dpp<0xAA>(w), __uint_as_float(x[2].x), o);
    o = fmaf(dpp<0xFF>(w), __uint_as_float(x[3].x), o);
    o += dpp<ROR4>(o);
    o *= __builtin_amdgcn_rcpf(Lt);
    const unsigned ob = dup_bf16(o);
    f32x4 Y0 = {0.f, 0.f, 0.f, 0.f}, Y1 = {0.f, 0.f, 0.f, 0.f};
#define YA(i) diag((unsigned)__builtin_amdgcn_mov_dpp((int)ob, BCAST + (i), 0xf, 0xf, true), mask)
    Y0 = mfma(YA(4), Wo[0], Y0); Y1 = mfma(YA(12), Wo[4], Y1);
    Y0 = mfma(YA(5), Wo[1], Y0); Y1 = mfma(YA(13), Wo[5], Y1);
    Y0 = mfma(YA(6), Wo[2], Y0); Y1 = mfma(YA(14), Wo[6], Y1);
    Y0 = mfma(YA(7), Wo[3], Y0); Y1 = mfma(YA(15), Wo[7], Y1);
    const f32x4 Y = Y0 + Y1;
    if (wcol < DM) {
      const int dst = WS_YG + (b * 128 + pidx) * YG_ROW + (wave * 128 + lane) * 8;
      st_gran(ws, dst, tag, g < 2 ? Y.x : Y.z);
      st_gran(ws, dst + 512, tag, g < 2 ? Y.y : Y.w);
    }
    if (wave == 0) lds_arrive(l_cnt + 3, lane);
    STAMP(11);
  } else {
    if (wave < 7) {
      const int h = wave - 5;
      lds_wait(l_cnt + 2, 8u);
      STAMP(8);
      const f32x4 m0 = *(const f32x4*)(l_ml + h * 8), m1 = *(const f32x4*)(l_ml + h * 8 + 4);
      const f32x4 l0 = *(const f32x4*)(l_ml + 16 + h * 8), l1 = *(const f32x4*)(l_ml + 16 + h * 8 + 4);
      float ov[8][2];
#pragma unroll
      for (int w = 0; w < 8; ++w) { ov[w][0] = l_o[w * 256 + h * 128 + lane]; ov[w][1] = l_o[w * 256 + h * 128 + 64 + lane]; }
      const float M = fmax_(fmax_(fmax_(m0.x, m0.y), fmax_(m0.z, m0.w)), fmax_(fmax_(m1.x, m1.y), fmax_(m1.z, m1.w)));
      float o0 = 0.f, o1 = 0.f, L = 0.f;
#pragma unroll
      for (int w = 0; w < 8; ++w) {
        const float e = __builtin_amdgcn_exp2f((w < 4 ? m0[w & 3] : m1[w & 3]) - M);
        o0 = fmaf(e, ov[w][0], o0); o1 = fmaf(e, ov[w][1], o1);
        L = fmaf(e, w < 4 ? l0[w & 3] : l1[w & 3], L);
      }
      const int dst = WS_PG + (G * 8 + s) * PG_ROW;
      st_gran(ws, dst + (h * 128 + lane) * 8, tag, o0);
      st_gran(ws, dst + (h * 128 + 64 + lane) * 8, tag, o1);
      if (lane < 16) st_gran(ws, dst + (256 + h * 16 + lane) * 8, tag, (lane & 1) ? L : M);
      STAMP(9);
    }
    if (wave == 7 || (wave == 6 && pidx < 16)) {
      const int pos = wave == 7 ? pidx * 4 : 512 + (pidx >> 1) * 16 + (pidx & 1) * 4;
      const int so = WS_YG + (b * 128 + (lane >> 2)) * YG_ROW + (pos + (lane & 3)) * 8;
      const int off[8] = {so, so + 16 * YG_ROW, so + 32 * YG_ROW, so + 48 * YG_ROW, so + 64 * YG_ROW, so + 80 * YG_ROW, so + 96 * YG_ROW, so + 112 * YG_ROW};
      const int p = pos + (lane & 3), pl = p & 63;
      const int col = (p & ~127) + 8 * (pl & 15) + 4 * ((pl >> 4) & 1) + 2 * (pl >> 5) + ((p >> 6) & 1);
      v2u x[8];
      lds_wait(l_cnt + 3, 1u);
#pragma unroll
      for (int k = 0; k < 8; ++k) x[k] = ldg(rw, off[k]);
      sweep<8, 2>(rw, off, tag, x);
      STAMP(12);
      float y = ((__uint_as_float(x[0].x) + __uint_as_float(x[1].x)) + (__uint_as_float(x[2].x) + __uint_as_float(x[3].x)))
              + ((__uint_as_float(x[4].x) + __uint_as_float(x[5].x)) + (__uint_as_float(x[6].x) + __uint_as_float(x[7].x)));
      y += dpp<ROR4>(y); y += dpp<ROR8>(y);
      y = xg_sum(y);
      if (lane < 4) outp[b * DM + col] = f2bf(y);
      if (pos == 0 && lane == 0) __hip_atomic_store((gu32*)(ws + WS_EPOCH + b * 256), tag, RLX_AGENT);
      STAMP(13);
    }
  }
  STAMP_OUT();
}

extern "C" void kernel_launch(void* const* d_in, const int* in_sizes, int n_in, void* d_out, int out_size, void* d_ws, size_t ws_size, hipStream_t stream) {
  Args a;
  a.x[0] = d_in[0]; a.k[0] = d_in[1]; a.v[0] = d_in[2]; a.wq[0] = d_in[3]; a.wo[0] = d_in[4];
  a.x[1] = d_in[5]; a.k[1] = d_in[6]; a.v[1] = d_in[7]; a.wq[1] = d_in[8]; a.wo[1] = d_in[9];
  a.out = d_out; a.ws = d_ws;
  gqa2_fused<<<NWG, NTHR, 0, stream>>>(a);
}
```

```cpp
#define OFF16 4400
#include <hip/hip_runtime.h>
#include <cstdint>

typedef unsigned v4u __attribute__((ext_vector_type(4)));
typedef unsigned v2u __attribute__((ext_vector_type(2)));
typedef float f32x4 __attribute__((ext_vector_type(4)));
typedef __bf16 bf16x8 __attribute__((ext_vector_type(8)));
typedef __bf16 bf16x2 __attribute__((ext_vector_type(2)));
typedef float f32x2 __attribute__((ext_vector_type(2)));
#define GAS __attribute__((address_space(1)))
typedef GAS unsigned gu32;
typedef GAS unsigned long long gu64;
#define RLX_AGENT __ATOMIC_RELAXED, __HIP_MEMORY_SCOPE_AGENT
#define RLX_WG __ATOMIC_RELAXED, __HIP_MEMORY_SCOPE_WORKGROUP

#define STAMPS 0
#define MAPV 2
#define DT16 1792
#define PRE 8
#define DEP 3
#define LIM 8
#define WO16 0
#define PTH16 400
#define SLK16 800
#define WOS16 600
#define NOWO 0
#define PREW 2
#define H1D 60
#define SL1 2
#define SL2 2
#define NCL 1
#define LIM0 0
#define H2D 80
#define QG16 1200

constexpr int DM = 576, NQ = 4096;
constexpr int NWG = 256, NTHR = 640;
constexpr int AUX_NT = 2, AUX_SC1 = 16;
constexpr int RSRC3 = 0x00020000;
constexpr int OOB = 0x7fff0000;
constexpr int WS_EPOCH = 0;
constexpr int WS_QG = 4096;
constexpr int PG_ROW = 288 * 8;
constexpr int WS_PG = WS_QG + 32 * 4096;
constexpr int YG_ROW = 640 * 8;
constexpr int WS_YG = WS_PG + 512 * PG_ROW;
constexpr int WS_ST = WS_YG + 256 * YG_ROW;
constexpr unsigned TAG_STEP = 0x9E3779B9u;
constexpr unsigned SPIN_CAP = 1u << 17;
constexpr float C2 = 0.088388347648318440f * 1.4426950408889634f;

struct Args {
  const void* x[2]; const void* k[2]; const void* v[2]; const void* wq[2]; const void* wo[2];
  void* out; void* ws;
};

#define SB() __builtin_amdgcn_sched_barrier(0)
__device__ __forceinline__ f32x4 mfma(v4u a, v4u b, f32x4 c) {
  return __builtin_amdgcn_mfma_f32_16x16x32_bf16(__builtin_bit_cast(bf16x8, a), __builtin_bit_cast(bf16x8, b), c, 0, 0, 0);
}
__device__ __forceinline__ unsigned dup_bf16(float f) {
  f32x2 v = {f, f};
  return __builtin_bit_cast(unsigned, __builtin_convertvector(v, bf16x2));
}
__device__ __forceinline__ unsigned short f2bf(float f) { return __builtin_bit_cast(unsigned short, (__bf16)f); }
__device__ __forceinline__ v4u diag(unsigned pp, v4u mask) { v4u r = {pp & mask.x, pp & mask.y, pp & mask.z, pp & mask.w}; return r; }
template <int CTRL> __device__ __forceinline__ float dpp(float x) {
  return __builtin_bit_cast(float, __builtin_amdgcn_mov_dpp(__builtin_bit_cast(int, x), CTRL, 0xf, 0xf, true));
}
constexpr int XOR1 = 0xB1, XOR2 = 0x4E, HMIR = 0x141, MIR = 0x140, ROR8 = 0x128, ROR4 = 0x124;
__device__ __forceinline__ float fmax_(float a, float b) { return __builtin_fmaxf(a, b); }
__device__ __forceinline__ float xg_max(float x) {
  auto s = __builtin_amdgcn_permlane16_swap(__float_as_uint(x), __float_as_uint(x), false, false);
  x = fmax_(__uint_as_float(s[0]), __uint_as_float(s[1]));
  auto t = __builtin_amdgcn_permlane32_swap(__float_as_uint(x), __float_as_uint(x), false, false);
  return fmax_(__uint_as_float(t[0]), __uint_as_float(t[1]));
}
__device__ __forceinline__ float x32_sum(float x) {
  auto t = __builtin_amdgcn_permlane32_swap(__float_as_uint(x), __float_as_uint(x), false, false);
  return __uint_as_float(t[0]) + __uint_as_float(t[1]);
}
__device__ __forceinline__ float xg_sum(float x) {
  auto s = __builtin_amdgcn_permlane16_swap(__float_as_uint(x), __float_as_uint(x), false, false);
  return x32_sum(__uint_as_float(s[0]) + __uint_as_float(s[1]));
}
__device__ __forceinline__ void st_gran(unsigned char* ws, int off, unsigned tag, float v) {
  __hip_atomic_store((gu64*)(ws + off), ((unsigned long long)tag << 32) | __float_as_uint(v), RLX_AGENT);
}
__device__ __forceinline__ v2u ldg(__amdgpu_buffer_rsrc_t rs, int off) { return __builtin_amdgcn_raw_buffer_load_b64(rs, off, 0, AUX_SC1); }
template <int N, int SLEEP> __device__ __forceinline__ void sweep(__amdgpu_buffer_rsrc_t rs, const int (&off)[N], unsigned tag, v2u (&x)[N]) {
#pragma unroll
  for (int k = 0; k < N; ++k) x[k] = ldg(rs, off[k]);
#pragma nounroll
  for (unsigned spins = 0; spins < SPIN_CAP; ++spins) {
    bool ok = true;
#pragma unroll
    for (int k = 0; k < N; ++k) ok &= x[k].y == tag;
    if (__all(ok)) break;
    __builtin_amdgcn_s_sleep(SLEEP);
    v2u y[N];
#pragma unroll
    for (int k = 0; k < N; ++k) y[k] = ldg(rs, x[k].y == tag ? OOB : off[k]);
    SB();
#pragma unroll
    for (int k = 0; k < N; ++k) x[k] = x[k].y == tag ? x[k] : y[k];
  }
}
__device__ __forceinline__ v4u mkrs(const void* p, unsigned bytes) {
  const unsigned long long u = (unsigned long long)p;
  v4u r = {(unsigned)u, (unsigned)(u >> 32) & 0xffffu, bytes, (unsigned)RSRC3};
  return r;
}
__device__ __forceinline__ void tile_load(v4u (&d)[4], int voff, v4u rs, int soff) {
  asm volatile("buffer_load_dwordx4 %0, %4, %5, %6 offen nt\n\t"
               "buffer_load_dwordx4 %1, %4, %5, %6 offen offset:1024 nt\n\t"
               "buffer_load_dwordx4 %2, %4, %5, %6 offen offset:2048 nt\n\t"
               "buffer_load_dwordx4 %3, %4, %5, %6 offen offset:3072 nt"
               : "=&v"(d[0]), "=&v"(d[1]), "=&v"(d[2]), "=&v"(d[3]) : "v"(voff), "s"(rs), "s"(soff));
}
template <int N> __device__ __forceinline__ void tile_wait(v4u (&d)[4]) {
  asm volatile("s_waitcnt vmcnt(%4)" : "+v"(d[0]), "+v"(d[1]), "+v"(d[2]), "+v"(d[3]) : "i"(N));
}
__device__ __forceinline__ void gate(unsigned due) {
  asm volatile("s_branch .Lg1_%=\n"
               ".Lg0_%=:\n\t"
               "s_sleep 1\n"
               ".Lg1_%=:\n\t"
               "s_memrealtime vcc\n\t"
               "s_waitcnt lgkmcnt(0)\n\t"
               "s_sub_i32 vcc_lo, vcc_lo, %0\n\t"
               "s_cmp_lt_i32 vcc_lo, 0\n\t"
               "s_cbranch_scc1 .Lg0_%="
               :: "s"(due) : "scc", "vcc", "memory");
}
__device__ __forceinline__ void gate_next(unsigned& due) {
  asm volatile("s_branch .Lg1_%=\n"
               ".Lg0_%=:\n\t"
               "s_sleep 1\n"
               ".Lg1_%=:\n\t"
               "s_memrealtime vcc\n\t"
               "s_waitcnt lgkmcnt(0)\n\t"
               "s_lshl_b32 vcc_lo, vcc_lo, 4\n\t"
               "s_sub_i32 vcc_lo, vcc_lo, %0\n\t"
               "s_cmp_lt_i32 vcc_lo, 0\n\t"
               "s_cbranch_scc1 .Lg0_%=\n\t"
               "s_setprio 0\n\t"
               "s_cmp_lt_i32 vcc_lo, %1\n\t"
               "s_cbranch_scc1 .Lg2_%=\n\t"
               "s_setprio 1\n"
               ".Lg2_%=:\n\t"
               "s_sub_i32 vcc_lo, vcc_lo, %2\n\t"
               "s_max_i32 vcc_lo, vcc_lo, 0\n\t"
               "s_add_i32 %0, %0, vcc_lo\n\t"
               "s_add_i32 %0, %0, %3"
               : "+s"(due) : "i"(PTH16), "i"(SLK16), "i"(DT16) : "scc", "vcc", "memory");
}
__device__ __forceinline__ void lds_barrier() {
  asm volatile("s_waitcnt lgkmcnt(0)" ::: "memory");
  __builtin_amdgcn_s_barrier();
  asm volatile("" ::: "memory");
}
__device__ __forceinline__ void lds_arrive(unsigned* c, int lane) {
  asm volatile("s_waitcnt lgkmcnt(0)" ::: "memory");
  if (lane == 0) __hip_atomic_fetch_add(c, 1u, RLX_WG);
}
__device__ __forceinline__ void lds_wait(unsigned* c, unsigned want) {
#pragma nounroll
  for (unsigned spins = 0; spins < (1u << 24); ++spins) {
    if (__builtin_amdgcn_readfirstlane(__hip_atomic_load(c, RLX_WG)) >= want) break;
    __builtin_amdgcn_s_sleep(1);
  }
  asm volatile("" ::: "memory");
}
#define STAMP(i) do {} while (0)
#define STAMP_OUT() do {} while (0)

constexpr int L_O = 0;
constexpr int L_ML = L_O + 4096;
constexpr int L_QP = L_ML + 64;
constexpr int L_Q = L_QP + 256;
constexpr int L_OB = L_Q + 256;
constexpr int L_CNT = L_OB + 32;
constexpr int L_END = L_CNT + 8;

struct St {
  v4u T[32][4];
  float Sc[8][4];
  v4u Q, mask;
  f32x4 O;
  float m2, l;
  v4u rs[4];
  int vk, vv, so;
  unsigned tb;
  float* lds;
  int lane, wave;
};

template <int I> __device__ __forceinline__ void request(St& z) {
  if constexpr (I > 0 && I < NCL) asm volatile("s_waitcnt vmcnt(%0)" :: "i"(LIM0) : "memory");
  else if constexpr (I < PRE && 4 * I > LIM) asm volatile("s_waitcnt vmcnt(%0)" :: "i"(LIM) : "memory");
  gate_next(z.tb);
  tile_load(z.T[I], (I & 8) ? z.vv : z.vk, z.rs[I >> 3], z.so + (I & 7) * 32768);
  SB();
}
template <int I, int W> __device__ __forceinline__ void consume(St& z) {
  const int g = z.lane >> 4, n = z.lane & 15;
  if constexpr ((I & 15) == 0) {
    z.Q = *(const v4u*)((const unsigned short*)(z.lds + L_Q) + (I >> 4) * 256 + (n >> 3) * 128 + 32 * g + 8 * (n & 3));
  }
  tile_wait<W>(z.T[I]);
  if constexpr ((I & 8) == 0) {
#pragma unroll
    for (int j = 0; j < 4; ++j) {
      const f32x4 d = mfma(z.T[I][j], z.Q, f32x4{0.f, 0.f, 0.f, 0.f});
      z.Sc[I & 7][j] = (dpp<0x00>(d.x) + dpp<0x55>(d.y)) + (dpp<0xAA>(d.z) + dpp<0xFF>(d.w));
    }
    if constexpr ((I & 7) == 7) {
      float m = z.Sc[0][0];
#pragma unroll
      for (int t = 0; t < 8; ++t)
#pragma unroll
        for (int j = 0; j < 4; ++j) m = fmax_(m, z.Sc[t][j]);
      z.m2 = xg_max(m) * C2;
      z.l = 0.f; z.O = f32x4{0.f, 0.f, 0.f, 0.f};
    }
    asm volatile("" : "+v"(z.Sc[I & 7][0]), "+v"(z.Sc[I & 7][1]), "+v"(z.Sc[I & 7][2]), "+v"(z.Sc[I & 7][3]));
  } else {
#pragma unroll
    for (int j = 0; j < 4; ++j) {
      const float p = __builtin_amdgcn_exp2f(fmaf(z.Sc[I & 7][j], C2, -z.m2));
      z.l += p;
      z.O = mfma(diag(dup_bf16(p), z.mask), z.T[I][j], z.O);
    }
    asm volatile("" : "+v"(z.O), "+v"(z.l));
    if constexpr ((I & 7) == 7) {
      constexpr int br = I >> 4;
      const float l = xg_sum(z.l);
      *(f32x4*)(z.lds + L_O + br * 2048 + z.wave * 256 + (g >> 1) * 128 + 8 * n + 4 * (g & 1)) = z.O;
      if (g == 0 && (n & 7) == 0) { z.lds[L_ML + br * 32 + (n >> 3) * 8 + z.wave] = z.m2; z.lds[L_ML + br * 32 + 16 + (n >> 3) * 8 + z.wave] = l; }
      lds_arrive((unsigned*)(z.lds + L_CNT) + 2 + br, z.lane);
    }
  }
  SB();
}
__device__ __forceinline__ void merge_publish(float* lds, unsigned char* ws, int br, int h, int row, int lane, unsigned tag) {
  lds_wait((unsigned*)(lds + L_CNT) + 2 + br, 8u);
  const float* ml = lds + L_ML + br * 32 + h * 8;
  const f32x4 m0 = *(const f32x4*)(ml), m1 = *(const f32x4*)(ml + 4), l0 = *(const f32x4*)(ml + 16), l1 = *(const f32x4*)(ml + 20);
  const float* op = lds + L_O + br * 2048 + h * 128 + lane;
  float ov[8][2];
#pragma unroll
  for (int w = 0; w < 8; ++w) { ov[w][0] = op[w * 256]; ov[w][1] = op[w * 256 + 64]; }
  const float M = fmax_(fmax_(fmax_(m0.x, m0.y), fmax_(m0.z, m0.w)), fmax_(fmax_(m1.x, m1.y), fmax_(m1.z, m1.w)));
  float o0 = 0.f, o1 = 0.f, L = 0.f;
#pragma unroll
  for (int w = 0; w < 8; ++w) {
    const float e = __builtin_amdgcn_exp2f((w < 4 ? m0[w & 3] : m1[w & 3]) - M);
    o0 = fmaf(e, ov[w][0], o0); o1 = fmaf(e, ov[w][1], o1);
    L = fmaf(e, w < 4 ? l0[w & 3] : l1[w & 3], L);
  }
  const int dst = WS_PG + (br * 256 + row) * PG_ROW;
  st_gran(ws, dst + (h * 128 + lane) * 8, tag, o0);
  st_gran(ws, dst + (h * 128 + 64 + lane) * 8, tag, o1);
  if (lane < 16) st_gran(ws, dst + (256 + h * 16 + lane) * 8, tag, (lane & 1) ? L : M);
}
template <int I, int N> struct Run {
  static __device__ __forceinline__ void go(St& z) { request<I>(z); consume<I - DEP + 1, 4 * (DEP - 1)>(z); Run<I + 1, N>::go(z); }
};
template <int N> struct Run<N, N> { static __device__ __forceinline__ void go(St&) {} };
template <int I, int N> struct Req { static __device__ __forceinline__ void go(St& z) { request<I>(z); Req<I + 1, N>::go(z); } };
template <int N> struct Req<N, N> { static __device__ __forceinline__ void go(St&) {} };
template <int I, int N, int LAST> struct Con {
  static __device__ __forceinline__ void go(St& z) { consume<I, 4 * (LAST - I)>(z); Con<I + 1, N, LAST>::go(z); }
};
template <int N, int LAST> struct Con<N, N, LAST> { static __device__ __forceinline__ void go(St&) {} };

__global__ void __launch_bounds__(NTHR, 3) gqa2_fused(Args a) {
  __shared__ __attribute__((aligned(16))) float lds[L_END];
  unsigned* const l_cnt = (unsigned*)(lds + L_CNT);
  const unsigned t0 = (unsigned)__builtin_amdgcn_s_memrealtime();

  const int tid = threadIdx.x, lane = tid & 63, wave = __builtin_amdgcn_readfirstlane(tid >> 6);
  const int g = lane >> 4, n = lane & 15;
  const int wg = blockIdx.x;
  const int ch = MAPV == 0 ? wg : ((wg & 7) << 5 | wg >> 3);
  const int kvh = ch >> 4, rb = (ch >> 3) & 1, s8 = ch & 7;
  unsigned char* const ws = (unsigned char*)a.ws;
  STAMP(0);
  const __amdgpu_buffer_rsrc_t rw = __builtin_amdgcn_make_buffer_rsrc(ws, 0, WS_ST, RSRC3);
  v4u mask;
  {
    const unsigned half = (n & 1) ? 0xffff0000u : 0x0000ffffu; const int r = (n & 7) >> 1;
    mask.x = r == 0 ? half : 0u; mask.y = r == 1 ? half : 0u; mask.z = r == 2 ? half : 0u; mask.w = r == 3 ? half : 0u;
  }
  const int pidx = kvh * 8 + s8;
  if (wave >= 8) {
    __builtin_amdgcn_s_setprio(3);
    const int hw = wave - 8, cg = s8 & 3, kh = s8 >> 2, sub = 2 * g + (n >> 3);
    const __amdgpu_buffer_rsrc_t rq = __builtin_amdgcn_make_buffer_rsrc((unsigned char*)a.wq[rb] + ((size_t)(kh * 288 + hw * 144) * NQ + kvh * 256 + cg * 64) * 2, 0, 143 * NQ * 2 + 128, RSRC3);
    const __amdgpu_buffer_rsrc_t rx = __builtin_amdgcn_make_buffer_rsrc((unsigned char*)a.x[rb] + (kh * 288 + hw * 144) * 2, 0, 144 * 2, RSRC3);
    const v4u x0 = __builtin_amdgcn_raw_buffer_load_b128(rx, sub * 36, 0, 0), x1 = __builtin_amdgcn_raw_buffer_load_b128(rx, sub * 36 + 16, 0, 0);
    const unsigned x2 = __builtin_amdgcn_raw_buffer_load_b32(rx, sub * 36 + 32, 0, 0);
    const unsigned ep = __builtin_amdgcn_raw_buffer_load_b32(rw, lane == 0 ? WS_EPOCH : OOB, 0, AUX_SC1);
    v4u wqv[18];
#pragma unroll
    for (int i = 0; i < 18; ++i) wqv[i] = __builtin_amdgcn_raw_buffer_load_b128(rq, sub * 18 * (NQ * 2) + (n & 7) * 16, i * NQ * 2, AUX_NT);
    SB();
    if (tid < 512 + 8) l_cnt[tid - 512] = 0u;
    lds_barrier();
    f32x4 a0 = {0.f, 0.f, 0.f, 0.f}, a1 = {0.f, 0.f, 0.f, 0.f};
#pragma unroll
    for (int i = 0; i < 9; ++i) {
      const unsigned xp = i < 4 ? x0[i & 3] : i < 8 ? x1[i & 3] : x2;
      a0 = mfma(diag((xp & 0xffffu) * 0x10001u, mask), wqv[2 * i], a0);
      a1 = mfma(diag((xp >> 16) * 0x10001u, mask), wqv[2 * i + 1], a1);
    }
    if ((g >> 1) == (n >> 3)) *(f32x4*)(lds + L_QP + (hw * 2 + (n >> 3)) * 64 + 8 * (n & 7) + 4 * (g & 1)) = a0 + a1;
    const unsigned tag = __builtin_amdgcn_readfirstlane(ep) + TAG_STEP;
    STAMP(1);
    lds_arrive(l_cnt + 0, lane);
    if (hw == 0) {
      lds_wait(l_cnt + 0, 2u);
      const float q = (lds[L_QP + lane] + lds[L_QP + 64 + lane]) + (lds[L_QP + 128 + lane] + lds[L_QP + 192 + lane]);
      st_gran(ws, WS_QG + ((rb * 16 + kvh) * 512 + kh * 256 + cg * 64 + lane) * 8, tag, q);
      if (lane == 0) l_cnt[5] = tag;
    }
    STAMP(2);
    gate((unsigned)__builtin_amdgcn_s_memrealtime() + (QG16 >> 4));
    {
      const int qo = WS_QG + ((hw * 16 + kvh) * 512 + lane) * 8;
      const int off[8] = {qo, qo + 512, qo + 1024, qo + 1536, qo + 2048, qo + 2560, qo + 3072, qo + 3584};
      v2u x[8];
      sweep<8, 8>(rw, off, tag, x);
#pragma unroll
      for (int k = 0; k < 4; ++k) ((unsigned short*)(lds + L_Q))[hw * 256 + k * 64 + lane] = f2bf(__uint_as_float(x[k].x) + __uint_as_float(x[k + 4].x));
      lds_arrive(l_cnt + 1, lane);
    }
    STAMP(3);
    {
      const int d1 = WS_PG + (256 + ch) * PG_ROW, d2 = WS_YG + (rb * 128 + pidx) * YG_ROW;
      unsigned u = __builtin_amdgcn_raw_buffer_load_b32(rw, hw == 0 ? (lane < 36 ? d1 + lane * 64 : OOB) : d2 + lane * 64, 0, AUX_SC1);
      u ^= __builtin_amdgcn_raw_buffer_load_b32(rw, (hw == 0 && lane < 16) ? d2 + 4096 + lane * 64 : OOB, 0, AUX_SC1);
      asm volatile("" :: "v"(u));
    }
    gate(t0 + (unsigned)((OFF16 + 16 * DT16) >> 4));
#pragma nounroll
    for (int br = 0; br < 2; ++br) {
      merge_publish(lds, ws, br, hw, ch, lane, tag);
      if (br == 0) gate(t0 + (unsigned)((OFF16 + 32 * DT16) >> 4));
    }
    STAMP(8);
  } else {
    St z;
    z.lds = lds; z.lane = lane; z.wave = wave; z.mask = mask;
    z.rs[0] = mkrs((unsigned char*)a.k[0] + (size_t)ch * 262144, 262144);
    z.rs[1] = mkrs((unsigned char*)a.v[0] + (size_t)ch * 262144, 262144);
    z.rs[2] = mkrs((unsigned char*)a.k[1] + (size_t)ch * 262144, 262144);
    z.rs[3] = mkrs((unsigned char*)a.v[1] + (size_t)ch * 262144, 262144);
    z.vk = ((lane >> 2) & 3) * 256 + g * 64 + (lane & 3) * 16; z.vv = lane * 16; z.so = wave * 4096;
    z.tb = (t0 << 4) + (unsigned)(OFF16 + ((wave * DT16) >> 3));
    const void* wop = a.wo[rb];
    GAS unsigned short* outp = (GAS unsigned short*)a.out;
    asm volatile("" : "+s"(wop), "+s"(outp));
    lds_barrier();
    Req<0, PRE>::go(z);
    STAMP(4);
    lds_wait(l_cnt + 1, 2u);
    const unsigned tag = __builtin_amdgcn_readfirstlane(l_cnt[5]);
    STAMP(5);
    Con<0, PRE - DEP + 1, PRE - 1>::go(z);
    Run<PRE, 17>::go(z);
    STAMP(14);
    Run<17, 25>::go(z);
    STAMP(15);
    Run<25, 32>::go(z);
    STAMP(6);
    Con<32 - DEP + 1, 32, 31>::go(z);
    STAMP(7);
    if (wave < 5) {
      const __amdgpu_buffer_rsrc_t ro = __builtin_amdgcn_make_buffer_rsrc((unsigned char*)wop + ((size_t)(kvh * 256 + s8 * 32) * DM) * 2, 0, 32 * DM * 2, RSRC3);
      const int wcol = wave * 128 + n * 8;
      const int vwo = (!NOWO && wcol < DM) ? g * (DM * 2) + wcol * 2 : OOB;
      v4u Wo[8];
      z.tb += (unsigned)(WO16 + wave * WOS16 - ((wave * DT16) >> 3));
      gate_next(z.tb);
#pragma unroll
      for (int i = 0; i < 8; ++i) Wo[i] = __builtin_amdgcn_raw_buffer_load_b128(ro, vwo, i * 4 * DM * 2, AUX_NT);
      SB();
      lds_wait(l_cnt + 4, 1u);
      STAMP(10);
      const v4u oa = *(const v4u*)(lds + L_OB + g * 8), ob = *(const v4u*)(lds + L_OB + g * 8 + 4);
      f32x4 Y0 = {0.f, 0.f, 0.f, 0.f}, Y1 = {0.f, 0.f, 0.f, 0.f};
#pragma unroll
      for (int i = 0; i < 4; ++i) { Y0 = mfma(diag(oa[i], mask), Wo[i], Y0); Y1 = mfma(diag(ob[i], mask), Wo[4 + i], Y1); }
      const f32x4 Y = Y0 + Y1;
      if (wcol < DM) {
        const int dst = WS_YG + (rb * 128 + pidx) * YG_ROW + (wave * 128 + lane) * 8;
        st_gran(ws, dst, tag, g < 2 ? Y.x : Y.z);
        st_gran(ws, dst + 512, tag, g < 2 ? Y.y : Y.w);
      }
      STAMP(11);
    } else if (wave == 5) {
      const int cb = WS_PG + (rb * 256 + kvh * 16) * PG_ROW;
      const int po = cb + (lane >> 5) * PG_ROW + (32 * s8 + (lane & 31)) * 8;
      const int off[9] = {po, po + 2 * PG_ROW, po + 4 * PG_ROW, po + 6 * PG_ROW, po + 8 * PG_ROW, po + 10 * PG_ROW, po + 12 * PG_ROW, po + 14 * PG_ROW,
                          cb + n * PG_ROW + (256 + 16 * (s8 >> 2) + (g & 1)) * 8};
      v2u x[9];
      lds_wait(l_cnt + 2 + rb, 8u);
      gate((unsigned)__builtin_amdgcn_s_memrealtime() + H1D);
      sweep<9, SL1>(rw, off, tag, x);
      STAMP(9);
      auto sw = __builtin_amdgcn_permlane16_swap(x[8].x, x[8].x, false, false);
      const float Ms = __uint_as_float(sw[0]), Ls = __uint_as_float(sw[1]);
      float Mx = fmax_(Ms, dpp<XOR1>(Ms)); Mx = fmax_(Mx, dpp<XOR2>(Mx)); Mx = fmax_(Mx, dpp<HMIR>(Mx)); Mx = fmax_(Mx, dpp<MIR>(Mx));
      const float w = __builtin_amdgcn_exp2f(Ms - Mx);
      float Lt = w * Ls; Lt += dpp<XOR1>(Lt); Lt += dpp<XOR2>(Lt); Lt += dpp<HMIR>(Lt); Lt += dpp<MIR>(Lt);
      const float ws_ = lane < 32 ? w : dpp<XOR1>(w);
      float o = dpp<0x150>(ws_) * __uint_as_float(x[0].x);
      o = fmaf(dpp<0x152>(ws_), __uint_as_float(x[1].x), o);
      o = fmaf(dpp<0x154>(ws_), __uint_as_float(x[2].x), o);
      o = fmaf(dpp<0x156>(ws_), __uint_as_float(x[3].x), o);
      o = fmaf(dpp<0x158>(ws_), __uint_as_float(x[4].x), o);
      o = fmaf(dpp<0x15a>(ws_), __uint_as_float(x[5].x), o);
      o = fmaf(dpp<0x15c>(ws_), __uint_as_float(x[6].x), o);
      o = fmaf(dpp<0x15e>(ws_), __uint_as_float(x[7].x), o);
      o = x32_sum(o) * __builtin_amdgcn_rcpf(Lt);
      if (lane < 32) ((unsigned*)(lds + L_OB))[(lane & 3) * 8 + (lane >> 2)] = dup_bf16(o);
      lds_arrive(l_cnt + 4, lane);
    } else if (wave == 6 || pidx < 16) {
      const int pos = wave == 6 ? pidx * 4 : 512 + (pidx >> 1) * 16 + (pidx & 1) * 4;
      const int so = WS_YG + (rb * 128 + (lane >> 2)) * YG_ROW + (pos + (lane & 3)) * 8;
      const int off[8] = {so, so + 16 * YG_ROW, so + 32 * YG_ROW, so + 48 * YG_ROW, so + 64 * YG_ROW, so + 80 * YG_ROW, so + 96 * YG_ROW, so + 112 * YG_ROW};
      const int p = pos + (lane & 3), pl = p & 63;
      const int col = (p & ~127) + 8 * (pl & 15) + 4 * ((pl >> 4) & 1) + 2 * (pl >> 5) + ((p >> 6) & 1);
      v2u x[8];
      lds_wait(l_cnt + 4, 1u);
      z.tb += (unsigned)(WO16 + 1600 - ((wave * DT16) >> 3));
      gate_next(z.tb);
      gate((unsigned)__builtin_amdgcn_s_memrealtime() + H2D);
      sweep<8, SL2>(rw, off, tag, x);
      STAMP(12);
      float y = ((__uint_as_float(x[0].x) + __uint_as_float(x[1].x)) + (__uint_as_float(x[2].x) + __uint_as_float(x[3].x)))
              + ((__uint_as_float(x[4].x) + __uint_as_float(x[5].x)) + (__uint_as_float(x[6].x) + __uint_as_float(x[7].x)));
      y += dpp<ROR4>(y); y += dpp<ROR8>(y);
      y = xg_sum(y);
      if (lane < 4) outp[rb * DM + col] = f2bf(y);
      if (rb == 0 && pos == 0 && lane == 0) __hip_atomic_store((gu32*)(ws + WS_EPOCH), tag, RLX_AGENT);
      STAMP(13);
    }
  }
  STAMP_OUT();
}

extern "C" void kernel_launch(void* const* d_in, const int* in_sizes, int n_in, void* d_out, int out_size, void* d_ws, size_t ws_size, hipStream_t stream) {
  Args a;
  a.x[0] = d_in[0]; a.k[0] = d_in[1]; a.v[0] = d_in[2]; a.wq[0] = d_in[3]; a.wo[0] = d_in[4];
  a.x[1] = d_in[5]; a.k[1] = d_in[6]; a.v[1] = d_in[7]; a.wq[1] = d_in[8]; a.wo[1] = d_in[9];
  a.out = d_out; a.ws = d_ws;
  gqa2_fused<<<NWG, NTHR, 0, stream>>>(a);
}
```

```cpp
#define OFF16 3600
#define T0LATE 1
#include <hip/hip_runtime.h>
#include <cstdint>

typedef unsigned v4u __attribute__((ext_vector_type(4)));
typedef unsigned v2u __attribute__((ext_vector_type(2)));
typedef float f32x4 __attribute__((ext_vector_type(4)));
typedef __bf16 bf16x8 __attribute__((ext_vector_type(8)));
typedef __bf16 bf16x2 __attribute__((ext_vector_type(2)));
typedef float f32x2 __attribute__((ext_vector_type(2)));
#define GAS __attribute__((address_space(1)))
typedef GAS unsigned gu32;
typedef GAS unsigned long long gu64;
#define RLX_AGENT __ATOMIC_RELAXED, __HIP_MEMORY_SCOPE_AGENT
#define RLX_WG __ATOMIC_RELAXED, __HIP_MEMORY_SCOPE_WORKGROUP

#define STAMPS 0
#define MAPV 2
#define DT16 1792
#define PRE 8
#define DEP 3
#define LIM 8
#define WO16 0
#define PTH16 400
#define SLK16 800
#define WOS16 600
#define NOWO 0
#define PREW 2
#define H1D 60
#define SL1 2
#define SL2 2
#define NCL 1
#define LIM0 0
#define H2D 80
#define QG16 1200

constexpr int DM = 576, NQ = 4096;
constexpr int NWG = 256, NTHR = 640;
constexpr int AUX_NT = 2, AUX_SC1 = 16;
constexpr int RSRC3 = 0x00020000;
constexpr int OOB = 0x7fff0000;
constexpr int WS_EPOCH = 0;
constexpr int WS_QG = 4096;
constexpr int PG_ROW = 288 * 8;
constexpr int WS_PG = WS_QG + 32 * 4096;
constexpr int YG_ROW = 640 * 8;
constexpr int WS_YG = WS_PG + 512 * PG_ROW;
constexpr int WS_ST = WS_YG + 256 * YG_ROW;
constexpr unsigned TAG_STEP = 0x9E3779B9u;
constexpr unsigned SPIN_CAP = 1u << 17;
constexpr float C2 = 0.088388347648318440f * 1.4426950408889634f;

struct Args {
  const void* x[2]; const void* k[2]; const void* v[2]; const void* wq[2]; const void* wo[2];
  void* out; void* ws;
};

#define SB() __builtin_amdgcn_sched_barrier(0)
__device__ __forceinline__ f32x4 mfma(v4u a, v4u b, f32x4 c) {
  return __builtin_amdgcn_mfma_f32_16x16x32_bf16(__builtin_bit_cast(bf16x8, a), __builtin_bit_cast(bf16x8, b), c, 0, 0, 0);
}
__device__ __forceinline__ unsigned dup_bf16(float f) {
  f32x2 v = {f, f};
  return __builtin_bit_cast(unsigned, __builtin_convertvector(v, bf16x2));
}
__device__ __forceinline__ unsigned short f2bf(float f) { return __builtin_bit_cast(unsigned short, (__bf16)f); }
__device__ __forceinline__ v4u diag(unsigned pp, v4u mask) { v4u r = {pp & mask.x, pp & mask.y, pp & mask.z, pp & mask.w}; return r; }
template <int CTRL> __device__ __forceinline__ float dpp(float x) {
  return __builtin_bit_cast(float, __builtin_amdgcn_mov_dpp(__builtin_bit_cast(int, x), CTRL, 0xf, 0xf, true));
}
constexpr int XOR1 = 0xB1, XOR2 = 0x4E, HMIR = 0x141, MIR = 0x140, ROR8 = 0x128, ROR4 = 0x124;
__device__ __forceinline__ float fmax_(float a, float b) { return __builtin_fmaxf(a, b); }
__device__ __forceinline__ float xg_max(float x) {
  auto s = __builtin_amdgcn_permlane16_swap(__float_as_uint(x), __float_as_uint(x), false, false);
  x = fmax_(__uint_as_float(s[0]), __uint_as_float(s[1]));
  auto t = __builtin_amdgcn_permlane32_swap(__float_as_uint(x), __float_as_uint(x), false, false);
  return fmax_(__uint_as_float(t[0]), __uint_as_float(t[1]));
}
__device__ __forceinline__ float x32_sum(float x) {
  auto t = __builtin_amdgcn_permlane32_swap(__float_as_uint(x), __float_as_uint(x), false, false);
  return __uint_as_float(t[0]) + __uint_as_float(t[1]);
}
__device__ __forceinline__ float xg_sum(float x) {
  auto s = __builtin_amdgcn_permlane16_swap(__float_as_uint(x), __float_as_uint(x), false, false);
  return x32_sum(__uint_as_float(s[0]) + __uint_as_float(s[1]));
}
__device__ __forceinline__ void st_gran(unsigned char* ws, int off, unsigned tag, float v) {
  __hip_atomic_store((gu64*)(ws + off), ((unsigned long long)tag << 32) | __float_as_uint(v), RLX_AGENT);
}
__device__ __forceinline__ v2u ldg(__amdgpu_buffer_rsrc_t rs, int off) { return __builtin_amdgcn_raw_buffer_load_b64(rs, off, 0, AUX_SC1); }
template <int N, int SLEEP> __device__ __forceinline__ void sweep(__amdgpu_buffer_rsrc_t rs, const int (&off)[N], unsigned tag, v2u (&x)[N]) {
#pragma unroll
  for (int k = 0; k < N; ++k) x[k] = ldg(rs, off[k]);
#pragma nounroll
  for (unsigned spins = 0; spins < SPIN_CAP; ++spins) {
    bool ok = true;
#pragma unroll
    for (int k = 0; k < N; ++k) ok &= x[k].y == tag;
    if (__all(ok)) break;
    __builtin_amdgcn_s_sleep(SLEEP);
    v2u y[N];
#pragma unroll
    for (int k = 0; k < N; ++k) y[k] = ldg(rs, x[k].y == tag ? OOB : off[k]);
    SB();
#pragma unroll
    for (int k = 0; k < N; ++k) x[k] = x[k].y == tag ? x[k] : y[k];
  }
}
__device__ __forceinline__ v4u mkrs(const void* p, unsigned bytes) {
  const unsigned long long u = (unsigned long long)p;
  v4u r = {(unsigned)u, (unsigned)(u >> 32) & 0xffffu, bytes, (unsigned)RSRC3};
  return r;
}
__device__ __forceinline__ void tile_load(v4u (&d)[4], int voff, v4u rs, int soff) {
  asm volatile("buffer_load_dwordx4 %0, %4, %5, %6 offen nt\n\t"
               "buffer_load_dwordx4 %1, %4, %5, %6 offen offset:1024 nt\n\t"
               "buffer_load_dwordx4 %2, %4, %5, %6 offen offset:2048 nt\n\t"
               "buffer_load_dwordx4 %3, %4, %5, %6 offen offset:3072 nt"
               : "=&v"(d[0]), "=&v"(d[1]), "=&v"(d[2]), "=&v"(d[3]) : "v"(voff), "s"(rs), "s"(soff));
}
template <int N> __device__ __forceinline__ void tile_wait(v4u (&d)[4]) {
  asm volatile("s_waitcnt vmcnt(%4)" : "+v"(d[0]), "+v"(d[1]), "+v"(d[2]), "+v"(d[3]) : "i"(N));
}
__device__ __forceinline__ void gate(unsigned due) {
  asm volatile("s_branch .Lg1_%=\n"
               ".Lg0_%=:\n\t"
               "s_sleep 1\n"
               ".Lg1_%=:\n\t"
               "s_memrealtime vcc\n\t"
               "s_waitcnt lgkmcnt(0)\n\t"
               "s_sub_i32 vcc_lo, vcc_lo, %0\n\t"
               "s_cmp_lt_i32 vcc_lo, 0\n\t"
               "s_cbranch_scc1 .Lg0_%="
               :: "s"(due) : "scc", "vcc", "memory");
}
__device__ __forceinline__ void gate_next(unsigned& due) {
  asm volatile("s_branch .Lg1_%=\n"
               ".Lg0_%=:\n\t"
               "s_sleep 1\n"
               ".Lg1_%=:\n\t"
               "s_memrealtime vcc\n\t"
               "s_waitcnt lgkmcnt(0)\n\t"
               "s_lshl_b32 vcc_lo, vcc_lo, 4\n\t"
               "s_sub_i32 vcc_lo, vcc_lo, %0\n\t"
               "s_cmp_lt_i32 vcc_lo, 0\n\t"
               "s_cbranch_scc1 .Lg0_%=\n\t"
               "s_setprio 0\n\t"
               "s_cmp_lt_i32 vcc_lo, %1\n\t"
               "s_cbranch_scc1 .Lg2_%=\n\t"
               "s_setprio 1\n"
               ".Lg2_%=:\n\t"
               "s_sub_i32 vcc_lo, vcc_lo, %2\n\t"
               "s_max_i32 vcc_lo, vcc_lo, 0\n\t"
               "s_add_i32 %0, %0, vcc_lo\n\t"
               "s_add_i32 %0, %0, %3"
               : "+s"(due) : "i"(PTH16), "i"(SLK16), "i"(DT16) : "scc", "vcc", "memory");
}
__device__ __forceinline__ void lds_barrier() {
  asm volatile("s_waitcnt lgkmcnt(0)" ::: "memory");
  __builtin_amdgcn_s_barrier();
  asm volatile("" ::: "memory");
}
__device__ __forceinline__ void lds_arrive(unsigned* c, int lane) {
  asm volatile("s_waitcnt lgkmcnt(0)" ::: "memory");
  if (lane == 0) __hip_atomic_fetch_add(c, 1u, RLX_WG);
}
__device__ __forceinline__ void lds_wait(unsigned* c, unsigned want) {
#pragma nounroll
  for (unsigned spins = 0; spins < (1u << 24); ++spins) {
    if (__builtin_amdgcn_readfirstlane(__hip_atomic_load(c, RLX_WG)) >= want) break;
    __builtin_amdgcn_s_sleep(1);
  }
  asm volatile("" ::: "memory");
}
#define STAMP(i) do {} while (0)
#define STAMP_OUT() do {} while (0)

constexpr int L_O = 0;
constexpr int L_ML = L_O + 4096;
constexpr int L_QP = L_ML + 64;
constexpr int L_Q = L_QP + 256;
constexpr int L_OB = L_Q + 256;
constexpr int L_CNT = L_OB + 32;
constexpr int L_END = L_CNT + 8;

struct St {
  v4u T[32][4];
  float Sc[8][4];
  v4u Q, mask;
  f32x4 O;
  float m2, l;
  v4u rs[4];
  int vk, vv, so;
  unsigned tb;
  float* lds;
  int lane, wave;
};

template <int I> __device__ __forceinline__ void request(St& z) {
  if constexpr (I > 0 && I < NCL) asm volatile("s_waitcnt vmcnt(%0)" :: "i"(LIM0) : "memory");
  else if constexpr (I < PRE && 4 * I > LIM) asm volatile("s_waitcnt vmcnt(%0)" :: "i"(LIM) : "memory");
  gate_next(z.tb);
  tile_load(z.T[I], (I & 8) ? z.vv : z.vk, z.rs[I >> 3], z.so + (I & 7) * 32768);
  SB();
}
template <int I, int W> __device__ __forceinline__ void consume(St& z) {
  const int g = z.lane >> 4, n = z.lane & 15;
  if constexpr ((I & 15) == 0) {
    z.Q = *(const v4u*)((const unsigned short*)(z.lds + L_Q) + (I >> 4) * 256 + (n >> 3) * 128 + 32 * g + 8 * (n & 3));
  }
  tile_wait<W>(z.T[I]);
  if constexpr ((I & 8) == 0) {
#pragma unroll
    for (int j = 0; j < 4; ++j) {
      const f32x4 d = mfma(z.T[I][j], z.Q, f32x4{0.f, 0.f, 0.f, 0.f});
      z.Sc[I & 7][j] = (dpp<0x00>(d.x) + dpp<0x55>(d.y)) + (dpp<0xAA>(d.z) + dpp<0xFF>(d.w));
    }
    if constexpr ((I & 7) == 7) {
      float m = z.Sc[0][0];
#pragma unroll
      for (int t = 0; t < 8; ++t)
#pragma unroll
        for (int j = 0; j < 4; ++j) m = fmax_(m, z.Sc[t][j]);
      z.m2 = xg_max(m) * C2;
      z.l = 0.f; z.O = f32x4{0.f, 0.f, 0.f, 0.f};
    }
    asm volatile("" : "+v"(z.Sc[I & 7][0]), "+v"(z.Sc[I & 7][1]), "+v"(z.Sc[I & 7][2]), "+v"(z.Sc[I & 7][3]));
  } else {
#pragma unroll
    for (int j = 0; j < 4; ++j) {
      const float p = __builtin_amdgcn_exp2f(fmaf(z.Sc[I & 7][j], C2, -z.m2));
      z.l += p;
      z.O = mfma(diag(dup_bf16(p), z.mask), z.T[I][j], z.O);
    }
    asm volatile("" : "+v"(z.O), "+v"(z.l));
    if constexpr ((I & 7) == 7) {
      constexpr int br = I >> 4;
      const float l = xg_sum(z.l);
      *(f32x4*)(z.lds + L_O + br * 2048 + z.wave * 256 + (g >> 1) * 128 + 8 * n + 4 * (g & 1)) = z.O;
      if (g == 0 && (n & 7) == 0) { z.lds[L_ML + br * 32 + (n >> 3) * 8 + z.wave] = z.m2; z.lds[L_ML + br * 32 + 16 + (n >> 3) * 8 + z.wave] = l; }
      lds_arrive((unsigned*)(z.lds + L_CNT) + 2 + br, z.lane);
    }
  }
  SB();
}
__device__ __forceinline__ void merge_publish(float* lds, unsigned char* ws, int br, int h, int row, int lane, unsigned tag) {
  lds_wait((unsigned*)(lds + L_CNT) + 2 + br, 8u);
  const float* ml = lds + L_ML + br * 32 + h * 8;
  const f32x4 m0 = *(const f32x4*)(ml), m1 = *(const f32x4*)(ml + 4), l0 = *(const f32x4*)(ml + 16), l1 = *(const f32x4*)(ml + 20);
  const float* op = lds + L_O + br * 2048 + h * 128 + lane;
  float ov[8][2];
#pragma unroll
  for (int w = 0; w < 8; ++w) { ov[w][0] = op[w * 256]; ov[w][1] = op[w * 256 + 64]; }
  const float M = fmax_(fmax_(fmax_(m0.x, m0.y), fmax_(m0.z, m0.w)), fmax_(fmax_(m1.x, m1.y), fmax_(m1.z, m1.w)));
  float o0 = 0.f, o1 = 0.f, L = 0.f;
#pragma unroll
  for (int w = 0; w < 8; ++w) {
    const float e = __builtin_amdgcn_exp2f((w < 4 ? m0[w & 3] : m1[w & 3]) - M);
    o0 = fmaf(e, ov[w][0], o0); o1 = fmaf(e, ov[w][1], o1);
    L = fmaf(e, w < 4 ? l0[w & 3] : l1[w & 3], L);
  }
  const int dst = WS_PG + (br * 256 + row) * PG_ROW;
  st_gran(ws, dst + (h * 128 + lane) * 8, tag, o0);
  st_gran(ws, dst + (h * 128 + 64 + lane) * 8, tag, o1);
  if (lane < 16) st_gran(ws, dst + (256 + h * 16 + lane) * 8, tag, (lane & 1) ? L : M);
}
template <int I, int N> struct Run {
  static __device__ __forceinline__ void go(St& z) { request<I>(z); consume<I - DEP + 1, 4 * (DEP - 1)>(z); Run<I + 1, N>::go(z); }
};
template <int N> struct Run<N, N> { static __device__ __forceinline__ void go(St&) {} };
template <int I, int N> struct Req { static __device__ __forceinline__ void go(St& z) { request<I>(z); Req<I + 1, N>::go(z); } };
template <int N> struct Req<N, N> { static __device__ __forceinline__ void go(St&) {} };
template <int I, int N, int LAST> struct Con {
  static __device__ __forceinline__ void go(St& z) { consume<I, 4 * (LAST - I)>(z); Con<I + 1, N, LAST>::go(z); }
};
template <int N, int LAST> struct Con<N, N, LAST> { static __device__ __forceinline__ void go(St&) {} };

__global__ void __launch_bounds__(NTHR, 3) gqa2_fused(Args a) {
  __shared__ __attribute__((aligned(16))) float lds[L_END];
  unsigned* const l_cnt = (unsigned*)(lds + L_CNT);
  unsigned long long t0l;
  asm volatile("s_memrealtime %0\n\ts_waitcnt lgkmcnt(0)" : "=s"(t0l) : "s"(a.ws), "s"(a.k[0]) : "memory");
  const unsigned t0 = (unsigned)t0l;

  const int tid = threadIdx.x, lane = tid & 63, wave = __builtin_amdgcn_readfirstlane(tid >> 6);
  const int g = lane >> 4, n = lane & 15;
  const int wg = blockIdx.x;
  const int ch = MAPV == 0 ? wg : ((wg & 7) << 5 | wg >> 3);
  const int kvh = ch >> 4, rb = (ch >> 3) & 1, s8 = ch & 7;
  unsigned char* const ws = (unsigned char*)a.ws;
  STAMP(0);
  const __amdgpu_buffer_rsrc_t rw = __builtin_amdgcn_make_buffer_rsrc(ws, 0, WS_ST, RSRC3);
  v4u mask;
  {
    const unsigned half = (n & 1) ? 0xffff0000u : 0x0000ffffu; const int r = (n & 7) >> 1;
    mask.x = r == 0 ? half : 0u; mask.y = r == 1 ? half : 0u; mask.z = r == 2 ? half : 0u; mask.w = r == 3 ? half : 0u;
  }
  const int pidx = kvh * 8 + s8;
  if (wave >= 8) {
    __builtin_amdgcn_s_setprio(3);
    const int hw = wave - 8, cg = s8 & 3, kh = s8 >> 2, sub = 2 * g + (n >> 3);
    const __amdgpu_buffer_rsrc_t rq = __builtin_amdgcn_make_buffer_rsrc((unsigned char*)a.wq[rb] + ((size_t)(kh * 288 + hw * 144) * NQ + kvh * 256 + cg * 64) * 2, 0, 143 * NQ * 2 + 128, RSRC3);
    const __amdgpu_buffer_rsrc_t rx = __builtin_amdgcn_make_buffer_rsrc((unsigned char*)a.x[rb] + (kh * 288 + hw * 144) * 2, 0, 144 * 2, RSRC3);
    const v4u x0 = __builtin_amdgcn_raw_buffer_load_b128(rx, sub * 36, 0, 0), x1 = __builtin_amdgcn_raw_buffer_load_b128(rx, sub * 36 + 16, 0, 0);
    const unsigned x2 = __builtin_amdgcn_raw_buffer_load_b32(rx, sub * 36 + 32, 0, 0);
    const unsigned ep = __builtin_amdgcn_raw_buffer_load_b32(rw, lane == 0 ? WS_EPOCH : OOB, 0, AUX_SC1);
    v4u wqv[18];
#pragma unroll
    for (int i = 0; i < 18; ++i) wqv[i] = __builtin_amdgcn_raw_buffer_load_b128(rq, sub * 18 * (NQ * 2) + (n & 7) * 16, i * NQ * 2, AUX_NT);
    SB();
    if (tid < 512 + 8) l_cnt[tid - 512] = 0u;
    lds_barrier();
    f32x4 a0 = {0.f, 0.f, 0.f, 0.f}, a1 = {0.f, 0.f, 0.f, 0.f};
#pragma unroll
    for (int i = 0; i < 9; ++i) {
      const unsigned xp = i < 4 ? x0[i & 3] : i < 8 ? x1[i & 3] : x2;
      a0 = mfma(diag((xp & 0xffffu) * 0x10001u, mask), wqv[2 * i], a0);
      a1 = mfma(diag((xp >> 16) * 0x10001u, mask), wqv[2 * i + 1], a1);
    }
    if ((g >> 1) == (n >> 3)) *(f32x4*)(lds + L_QP + (hw * 2 + (n >> 3)) * 64 + 8 * (n & 7) + 4 * (g & 1)) = a0 + a1;
    const unsigned tag = __builtin_amdgcn_readfirstlane(ep) + TAG_STEP;
    STAMP(1);
    lds_arrive(l_cnt + 0, lane);
    if (hw == 0) {
      lds_wait(l_cnt + 0, 2u);
      const float q = (lds[L_QP + lane] + lds[L_QP + 64 + lane]) + (lds[L_QP + 128 + lane] + lds[L_QP + 192 + lane]);
      st_gran(ws, WS_QG + ((rb * 16 + kvh) * 512 + kh * 256 + cg * 64 + lane) * 8, tag, q);
      if (lane == 0) l_cnt[5] = tag;
    }
    STAMP(2);
    gate((unsigned)__builtin_amdgcn_s_memrealtime() + (QG16 >> 4));
    {
      const int qo = WS_QG + ((hw * 16 + kvh) * 512 + lane) * 8;
      const int off[8] = {qo, qo + 512, qo + 1024, qo + 1536, qo + 2048, qo + 2560, qo + 3072, qo + 3584};
      v2u x[8];
      sweep<8, 8>(rw, off, tag, x);
#pragma unroll
      for (int k = 0; k < 4; ++k) ((unsigned short*)(lds + L_Q))[hw * 256 + k * 64 + lane] = f2bf(__uint_as_float(x[k].x) + __uint_as_float(x[k + 4].x));
      lds_arrive(l_cnt + 1, lane);
    }
    STAMP(3);
    {
      const int d1 = WS_PG + (256 + ch) * PG_ROW, d2 = WS_YG + (rb * 128 + pidx) * YG_ROW;
      unsigned u = __builtin_amdgcn_raw_buffer_load_b32(rw, hw == 0 ? (lane < 36 ? d1 + lane * 64 : OOB) : d2 + lane * 64, 0, AUX_SC1);
      u ^= __builtin_amdgcn_raw_buffer_load_b32(rw, (hw == 0 && lane < 16) ? d2 + 4096 + lane * 64 : OOB, 0, AUX_SC1);
      asm volatile("" :: "v"(u));
    }
    gate(t0 + (unsigned)((OFF16 + 16 * DT16) >> 4));
#pragma nounroll
    for (int br = 0; br < 2; ++br) {
      merge_publish(lds, ws, br, hw, ch, lane, tag);
      if (br == 0) gate(t0 + (unsigned)((OFF16 + 32 * DT16) >> 4));
    }
    STAMP(8);
  } else {
    St z;
    z.lds = lds; z.lane = lane; z.wave = wave; z.mask = mask;
    z.rs[0] = mkrs((unsigned char*)a.k[0] + (size_t)ch * 262144, 262144);
    z.rs[1] = mkrs((unsigned char*)a.v[0] + (size_t)ch * 262144, 262144);
    z.rs[2] = mkrs((unsigned char*)a.k[1] + (size_t)ch * 262144, 262144);
    z.rs[3] = mkrs((unsigned char*)a.v[1] + (size_t)ch * 262144, 262144);
    z.vk = ((lane >> 2) & 3) * 256 + g * 64 + (lane & 3) * 16; z.vv = lane * 16; z.so = wave * 4096;
    z.tb = (t0 << 4) + (unsigned)(OFF16 + ((wave * DT16) >> 3));
    const void* wop = a.wo[rb];
    GAS unsigned short* outp = (GAS unsigned short*)a.out;
    asm volatile("" : "+s"(wop), "+s"(outp));
    lds_barrier();
    Req<0, PRE>::go(z);
    STAMP(4);
    lds_wait(l_cnt + 1, 2u);
    const unsigned tag = __builtin_amdgcn_readfirstlane(l_cnt[5]);
    STAMP(5);
    Con<0, PRE - DEP + 1, PRE - 1>::go(z);
    Run<PRE, 17>::go(z);
    STAMP(14);
    Run<17, 25>::go(z);
    STAMP(15);
    Run<25, 32>::go(z);
    STAMP(6);
    Con<32 - DEP + 1, 32, 31>::go(z);
    STAMP(7);
    if (wave < 5) {
      const __amdgpu_buffer_rsrc_t ro = __builtin_amdgcn_make_buffer_rsrc((unsigned char*)wop + ((size_t)(kvh * 256 + s8 * 32) * DM) * 2, 0, 32 * DM * 2, RSRC3);
      const int wcol = wave * 128 + n * 8;
      const int vwo = (!NOWO && wcol < DM) ? g * (DM * 2) + wcol * 2 : OOB;
      v4u Wo[8];
      z.tb += (unsigned)(WO16 + wave * WOS16 - ((wave * DT16) >> 3));
      gate_next(z.tb);
#pragma unroll
      for (int i = 0; i < 8; ++i) Wo[i] = __builtin_amdgcn_raw_buffer_load_b128(ro, vwo, i * 4 * DM * 2, AUX_NT);
      SB();
      lds_wait(l_cnt + 4, 1u);
      STAMP(10);
      const v4u oa = *(const v4u*)(lds + L_OB + g * 8), ob = *(const v4u*)(lds + L_OB + g * 8 + 4);
      f32x4 Y0 = {0.f, 0.f, 0.f, 0.f}, Y1 = {0.f, 0.f, 0.f, 0.f};
#pragma unroll
      for (int i = 0; i < 4; ++i) { Y0 = mfma(diag(oa[i], mask), Wo[i], Y0); Y1 = mfma(diag(ob[i], mask), Wo[4 + i], Y1); }
      const f32x4 Y = Y0 + Y1;
      if (wcol < DM) {
        const int dst = WS_YG + (rb * 128 + pidx) * YG_ROW + (wave * 128 + lane) * 8;
        st_gran(ws, dst, tag, g < 2 ? Y.x : Y.z);
        st_gran(ws, dst + 512, tag, g < 2 ? Y.y : Y.w);
      }
      STAMP(11);
    } else if (wave == 5) {
      const int cb = WS_PG + (rb * 256 + kvh * 16) * PG_ROW;
      const int po = cb + (lane >> 5) * PG_ROW + (32 * s8 + (lane & 31)) * 8;
      const int off[9] = {po, po + 2 * PG_ROW, po + 4 * PG_ROW, po + 6 * PG_ROW, po + 8 * PG_ROW, po + 10 * PG_ROW, po + 12 * PG_ROW, po + 14 * PG_ROW,
                          cb + n * PG_ROW + (256 + 16 * (s8 >> 2) + (g & 1)) * 8};
      v2u x[9];
      lds_wait(l_cnt + 2 + rb, 8u);
      gate((unsigned)__builtin_amdgcn_s_memrealtime() + H1D);
      sweep<9, SL1>(rw, off, tag, x);
      STAMP(9);
      auto sw = __builtin_amdgcn_permlane16_swap(x[8].x, x[8].x, false, false);
      const float Ms = __uint_as_float(sw[0]), Ls = __uint_as_float(sw[1]);
      float Mx = fmax_(Ms, dpp<XOR1>(Ms)); Mx = fmax_(Mx, dpp<XOR2>(Mx)); Mx = fmax_(Mx, dpp<HMIR>(Mx)); Mx = fmax_(Mx, dpp<MIR>(Mx));
      const float w = __builtin_amdgcn_exp2f(Ms - Mx);
      float Lt = w * Ls; Lt += dpp<XOR1>(Lt); Lt += dpp<XOR2>(Lt); Lt += dpp<HMIR>(Lt); Lt += dpp<MIR>(Lt);
      const float ws_ = lane < 32 ? w : dpp<XOR1>(w);
      float o = dpp<0x150>(ws_) * __uint_as_float(x[0].x);
      o = fmaf(dpp<0x152>(ws_), __uint_as_float(x[1].x), o);
      o = fmaf(dpp<0x154>(ws_), __uint_as_float(x[2].x), o);
      o = fmaf(dpp<0x156>(ws_), __uint_as_float(x[3].x), o);
      o = fmaf(dpp<0x158>(ws_), __uint_as_float(x[4].x), o);
      o = fmaf(dpp<0x15a>(ws_), __uint_as_float(x[5].x), o);
      o = fmaf(dpp<0x15c>(ws_), __uint_as_float(x[6].x), o);
      o = fmaf(dpp<0x15e>(ws_), __uint_as_float(x[7].x), o);
      o = x32_sum(o) * __builtin_amdgcn_rcpf(Lt);
      if (lane < 32) ((unsigned*)(lds + L_OB))[(lane & 3) * 8 + (lane >> 2)] = dup_bf16(o);
      lds_arrive(l_cnt + 4, lane);
    } else if (wave == 6 || pidx < 16) {
      const int pos = wave == 6 ? pidx * 4 : 512 + (pidx >> 1) * 16 + (pidx & 1) * 4;
      const int so = WS_YG + (rb * 128 + (lane >> 2)) * YG_ROW + (pos + (lane & 3)) * 8;
      const int off[8] = {so, so + 16 * YG_ROW, so + 32 * YG_ROW, so + 48 * YG_ROW, so + 64 * YG_ROW, so + 80 * YG_ROW, so + 96 * YG_ROW, so + 112 * YG_ROW};
      const int p = pos + (lane & 3), pl = p & 63;
      const int col = (p & ~127) + 8 * (pl & 15) + 4 * ((pl >> 4) & 1) + 2 * (pl >> 5) + ((p >> 6) & 1);
      v2u x[8];
      lds_wait(l_cnt + 4, 1u);
      z.tb += (unsigned)(WO16 + 1600 - ((wave * DT16) >> 3));
      gate_next(z.tb);
      gate((unsigned)__builtin_amdgcn_s_memrealtime() + H2D);
      sweep<8, SL2>(rw, off, tag, x);
      STAMP(12);
      float y = ((__uint_as_float(x[0].x) + __uint_as_float(x[1].x)) + (__uint_as_float(x[2].x) + __uint_as_float(x[3].x)))
              + ((__uint_as_float(x[4].x) + __uint_as_float(x[5].x)) + (__uint_as_float(x[6].x) + __uint_as_float(x[7].x)));
      y += dpp<ROR4>(y); y += dpp<ROR8>(y);
      y = xg_sum(y);
      if (lane < 4) outp[rb * DM + col] = f2bf(y);
      if (rb == 0 && pos == 0 && lane == 0) __hip_atomic_store((gu32*)(ws + WS_EPOCH), tag, RLX_AGENT);
      STAMP(13);
    }
  }
  STAMP_OUT();
}

extern "C" void kernel_launch(void* const* d_in, const int* in_sizes, int n_in, void* d_out, int out_size, void* d_ws, size_t ws_size, hipStream_t stream) {
  Args a;
  a.x[0] = d_in[0]; a.k[0] = d_in[1]; a.v[0] = d_in[2]; a.wq[0] = d_in[3]; a.wo[0] = d_in[4];
  a.x[1] = d_in[5]; a.k[1] = d_in[6]; a.v[1] = d_in[7]; a.wq[1] = d_in[8]; a.wo[1] = d_in[9];
  a.out = d_out; a.ws = d_ws;
  gqa2_fused<<<NWG, NTHR, 0, stream>>>(a);
}
```

```cpp
#define PADT 7000
#define PADW 0
#include <hip/hip_runtime.h>
#include <cstdint>

typedef unsigned v4u __attribute__((ext_vector_type(4)));
typedef unsigned v2u __attribute__((ext_vector_type(2)));
typedef float f32x4 __attribute__((ext_vector_type(4)));
typedef __bf16 bf16x8 __attribute__((ext_vector_type(8)));
typedef __bf16 bf16x2 __attribute__((ext_vector_type(2)));
typedef float f32x2 __attribute__((ext_vector_type(2)));
#define GAS __attribute__((address_space(1)))
typedef GAS unsigned gu32;
typedef GAS unsigned long long gu64;
#define RLX_AGENT __ATOMIC_RELAXED, __HIP_MEMORY_SCOPE_AGENT
#define RLX_WG __ATOMIC_RELAXED, __HIP_MEMORY_SCOPE_WORKGROUP

#define STAMPS 0
#define MAPV 2
#define DT16 1792
#define OFF16 3600
#define PRE 8
#define DEP 3
#define LIM 8
#define WO16 0
#define PTH16 400
#define SLK16 800
#define WOS16 600
#define NOWO 0
#define PREW 2
#define H1D 60
#define SL1 2
#define SL2 2
#define T0LATE 0
#define NCL 1
#define LIM0 0
#define H2D 80
#define QG16 1200

constexpr int DM = 576, NQ = 4096;
constexpr int NWG = 256, NTHR = 640;
constexpr int AUX_NT = 2, AUX_SC1 = 16;
constexpr int RSRC3 = 0x00020000;
constexpr int OOB = 0x7fff0000;
constexpr int WS_EPOCH = 0;
constexpr int WS_QG = 4096;
constexpr int PG_ROW = 288 * 8;
constexpr int WS_PG = WS_QG + 32 * 4096;
constexpr int YG_ROW = 640 * 8;
constexpr int WS_YG = WS_PG + 512 * PG_ROW;
constexpr int WS_ST = WS_YG + 256 * YG_ROW;
constexpr unsigned TAG_STEP = 0x9E3779B9u;
constexpr unsigned SPIN_CAP = 1u << 17;
constexpr float C2 = 0.088388347648318440f * 1.4426950408889634f;

struct Args {
  const void* x[2]; const void* k[2]; const void* v[2]; const void* wq[2]; const void* wo[2];
  void* out; void* ws;
};

#define SB() __builtin_amdgcn_sched_barrier(0)
__device__ __forceinline__ f32x4 mfma(v4u a, v4u b, f32x4 c) {
  return __builtin_amdgcn_mfma_f32_16x16x32_bf16(__builtin_bit_cast(bf16x8, a), __builtin_bit_cast(bf16x8, b), c, 0, 0, 0);
}
__device__ __forceinline__ unsigned dup_bf16(float f) {
  f32x2 v = {f, f};
  return __builtin_bit_cast(unsigned, __builtin_convertvector(v, bf16x2));
}
__device__ __forceinline__ unsigned short f2bf(float f) { return __builtin_bit_cast(unsigned short, (__bf16)f); }
__device__ __forceinline__ v4u diag(unsigned pp, v4u mask) { v4u r = {pp & mask.x, pp & mask.y, pp & mask.z, pp & mask.w}; return r; }
template <int CTRL> __device__ __forceinline__ float dpp(float x) {
  return __builtin_bit_cast(float, __builtin_amdgcn_mov_dpp(__builtin_bit_cast(int, x), CTRL, 0xf, 0xf, true));
}
constexpr int XOR1 = 0xB1, XOR2 = 0x4E, HMIR = 0x141, MIR = 0x140, ROR8 = 0x128, ROR4 = 0x124;
__device__ __forceinline__ float fmax_(float a, float b) { return __builtin_fmaxf(a, b); }
__device__ __forceinline__ float xg_max(float x) {
  auto s = __builtin_amdgcn_permlane16_swap(__float_as_uint(x), __float_as_uint(x), false, false);
  x = fmax_(__uint_as_float(s[0]), __uint_as_float(s[1]));
  auto t = __builtin_amdgcn_permlane32_swap(__float_as_uint(x), __float_as_uint(x), false, false);
  return fmax_(__uint_as_float(t[0]), __uint_as_float(t[1]));
}
__device__ __forceinline__ float x32_sum(float x) {
  auto t = __builtin_amdgcn_permlane32_swap(__float_as_uint(x), __float_as_uint(x), false, false);
  return __uint_as_float(t[0]) + __uint_as_float(t[1]);
}
__device__ __forceinline__ float xg_sum(float x) {
  auto s = __builtin_amdgcn_permlane16_swap(__float_as_uint(x), __float_as_uint(x), false, false);
  return x32_sum(__uint_as_float(s[0]) + __uint_as_float(s[1]));
}
__device__ __forceinline__ void st_gran(unsigned char* ws, int off, unsigned tag, float v) {
  __hip_atomic_store((gu64*)(ws + off), ((unsigned long long)tag << 32) | __float_as_uint(v), RLX_AGENT);
}
__device__ __forceinline__ v2u ldg(__amdgpu_buffer_rsrc_t rs, int off) { return __builtin_amdgcn_raw_buffer_load_b64(rs, off, 0, AUX_SC1); }
template <int N, int SLEEP> __device__ __forceinline__ void sweep(__amdgpu_buffer_rsrc_t rs, const int (&off)[N], unsigned tag, v2u (&x)[N]) {
#pragma unroll
  for (int k = 0; k < N; ++k) x[k] = ldg(rs, off[k]);
#pragma nounroll
  for (unsigned spins = 0; spins < SPIN_CAP; ++spins) {
    bool ok = true;
#pragma unroll
    for (int k = 0; k < N; ++k) ok &= x[k].y == tag;
    if (__all(ok)) break;
    __builtin_amdgcn_s_sleep(SLEEP);
    v2u y[N];
#pragma unroll
    for (int k = 0; k < N; ++k) y[k] = ldg(rs, x[k].y == tag ? OOB : off[k]);
    SB();
#pragma unroll
    for (int k = 0; k < N; ++k) x[k] = x[k].y == tag ? x[k] : y[k];
  }
}
__device__ __forceinline__ v4u mkrs(const void* p, unsigned bytes) {
  const unsigned long long u = (unsigned long long)p;
  v4u r = {(unsigned)u, (unsigned)(u >> 32) & 0xffffu, bytes, (unsigned)RSRC3};
  return r;
}
__device__ __forceinline__ void tile_load(v4u (&d)[4], int voff, v4u rs, int soff) {
  asm volatile("buffer_load_dwordx4 %0, %4, %5, %6 offen nt\n\t"
               "buffer_load_dwordx4 %1, %4, %5, %6 offen offset:1024 nt\n\t"
               "buffer_load_dwordx4 %2, %4, %5, %6 offen offset:2048 nt\n\t"
               "buffer_load_dwordx4 %3, %4, %5, %6 offen offset:3072 nt"
               : "=&v"(d[0]), "=&v"(d[1]), "=&v"(d[2]), "=&v"(d[3]) : "v"(voff), "s"(rs), "s"(soff));
}
template <int N> __device__ __forceinline__ void tile_wait(v4u (&d)[4]) {
  asm volatile("s_waitcnt vmcnt(%4)" : "+v"(d[0]), "+v"(d[1]), "+v"(d[2]), "+v"(d[3]) : "i"(N));
}
__device__ __forceinline__ void gate(unsigned due) {
  asm volatile("s_branch .Lg1_%=\n"
               ".Lg0_%=:\n\t"
               "s_sleep 1\n"
               ".Lg1_%=:\n\t"
               "s_memrealtime vcc\n\t"
               "s_waitcnt lgkmcnt(0)\n\t"
               "s_sub_i32 vcc_lo, vcc_lo, %0\n\t"
               "s_cmp_lt_i32 vcc_lo, 0\n\t"
               "s_cbranch_scc1 .Lg0_%="
               :: "s"(due) : "scc", "vcc", "memory");
}
__device__ __forceinline__ void gate_next(unsigned& due) {
  asm volatile("s_branch .Lg1_%=\n"
               ".Lg0_%=:\n\t"
               "s_sleep 1\n"
               ".Lg1_%=:\n\t"
               "s_memrealtime vcc\n\t"
               "s_waitcnt lgkmcnt(0)\n\t"
               "s_lshl_b32 vcc_lo, vcc_lo, 4\n\t"
               "s_sub_i32 vcc_lo, vcc_lo, %0\n\t"
               "s_cmp_lt_i32 vcc_lo, 0\n\t"
               "s_cbranch_scc1 .Lg0_%=\n\t"
               "s_setprio 0\n\t"
               "s_cmp_lt_i32 vcc_lo, %1\n\t"
               "s_cbranch_scc1 .Lg2_%=\n\t"
               "s_setprio 1\n"
               ".Lg2_%=:\n\t"
               "s_sub_i32 vcc_lo, vcc_lo, %2\n\t"
               "s_max_i32 vcc_lo, vcc_lo, 0\n\t"
               "s_add_i32 %0, %0, vcc_lo\n\t"
               "s_add_i32 %0, %0, %3"
               : "+s"(due) : "i"(PTH16), "i"(SLK16), "i"(DT16) : "scc", "vcc", "memory");
}
__device__ __forceinline__ void lds_barrier() {
  asm volatile("s_waitcnt lgkmcnt(0)" ::: "memory");
  __builtin_amdgcn_s_barrier();
  asm volatile("" ::: "memory");
}
__device__ __forceinline__ void lds_arrive(unsigned* c, int lane) {
  asm volatile("s_waitcnt lgkmcnt(0)" ::: "memory");
  if (lane == 0) __hip_atomic_fetch_add(c, 1u, RLX_WG);
}
__device__ __forceinline__ void lds_wait(unsigned* c, unsigned want) {
#pragma nounroll
  for (unsigned spins = 0; spins < (1u << 24); ++spins) {
    if (__builtin_amdgcn_readfirstlane(__hip_atomic_load(c, RLX_WG)) >= want) break;
    __builtin_amdgcn_s_sleep(1);
  }
  asm volatile("" ::: "memory");
}
#define STAMP(i) do {} while (0)
#define STAMP_OUT() do {} while (0)

constexpr int L_O = 0;
constexpr int L_ML = L_O + 4096;
constexpr int L_QP = L_ML + 64;
constexpr int L_Q = L_QP + 256;
constexpr int L_OB = L_Q + 256;
constexpr int L_CNT = L_OB + 32;
constexpr int L_END = L_CNT + 8;

struct St {
  v4u T[32][4];
  float Sc[8][4];
  v4u Q, mask;
  f32x4 O;
  float m2, l;
  v4u rs[4];
  int vk, vv, so;
  unsigned tb;
  float* lds;
  int lane, wave;
};

template <int I> __device__ __forceinline__ void request(St& z) {
  if constexpr (I > 0 && I < NCL) asm volatile("s_waitcnt vmcnt(%0)" :: "i"(LIM0) : "memory");
  else if constexpr (I < PRE && 4 * I > LIM) asm volatile("s_waitcnt vmcnt(%0)" :: "i"(LIM) : "memory");
  gate_next(z.tb);
  tile_load(z.T[I], (I & 8) ? z.vv : z.vk, z.rs[I >> 3], z.so + (I & 7) * 32768);
  SB();
}
template <int I, int W> __device__ __forceinline__ void consume(St& z) {
  const int g = z.lane >> 4, n = z.lane & 15;
  if constexpr ((I & 15) == 0) {
    z.Q = *(const v4u*)((const unsigned short*)(z.lds + L_Q) + (I >> 4) * 256 + (n >> 3) * 128 + 32 * g + 8 * (n & 3));
  }
  tile_wait<W>(z.T[I]);
  if constexpr ((I & 8) == 0) {
#pragma unroll
    for (int j = 0; j < 4; ++j) {
      const f32x4 d = mfma(z.T[I][j], z.Q, f32x4{0.f, 0.f, 0.f, 0.f});
      z.Sc[I & 7][j] = (dpp<0x00>(d.x) + dpp<0x55>(d.y)) + (dpp<0xAA>(d.z) + dpp<0xFF>(d.w));
    }
    if constexpr ((I & 7) == 7) {
      float m = z.Sc[0][0];
#pragma unroll
      for (int t = 0; t < 8; ++t)
#pragma unroll
        for (int j = 0; j < 4; ++j) m = fmax_(m, z.Sc[t][j]);
      z.m2 = xg_max(m) * C2;
      z.l = 0.f; z.O = f32x4{0.f, 0.f, 0.f, 0.f};
    }
    asm volatile("" : "+v"(z.Sc[I & 7][0]), "+v"(z.Sc[I & 7][1]), "+v"(z.Sc[I & 7][2]), "+v"(z.Sc[I & 7][3]));
  } else {
#pragma unroll
    for (int j = 0; j < 4; ++j) {
      const float p = __builtin_amdgcn_exp2f(fmaf(z.Sc[I & 7][j], C2, -z.m2));
      z.l += p;
      z.O = mfma(diag(dup_bf16(p), z.mask), z.T[I][j], z.O);
    }
    asm volatile("" : "+v"(z.O), "+v"(z.l));
    if constexpr ((I & 7) == 7) {
      constexpr int br = I >> 4;
      const float l = xg_sum(z.l);
      *(f32x4*)(z.lds + L_O + br * 2048 + z.wave * 256 + (g >> 1) * 128 + 8 * n + 4 * (g & 1)) = z.O;
      if (g == 0 && (n & 7) == 0) { z.lds[L_ML + br * 32 + (n >> 3) * 8 + z.wave] = z.m2; z.lds[L_ML + br * 32 + 16 + (n >> 3) * 8 + z.wave] = l; }
      lds_arrive((unsigned*)(z.lds + L_CNT) + 2 + br, z.lane);
    }
  }
  SB();
}
__device__ __forceinline__ void merge_publish(float* lds, unsigned char* ws, int br, int h, int row, int lane, unsigned tag) {
  lds_wait((unsigned*)(lds + L_CNT) + 2 + br, 8u);
  const float* ml = lds + L_ML + br * 32 + h * 8;
  const f32x4 m0 = *(const f32x4*)(ml), m1 = *(const f32x4*)(ml + 4), l0 = *(const f32x4*)(ml + 16), l1 = *(const f32x4*)(ml + 20);
  const float* op = lds + L_O + br * 2048 + h * 128 + lane;
  float ov[8][2];
#pragma unroll
  for (int w = 0; w < 8; ++w) { ov[w][0] = op[w * 256]; ov[w][1] = op[w * 256 + 64]; }
  const float M = fmax_(fmax_(fmax_(m0.x, m0.y), fmax_(m0.z, m0.w)), fmax_(fmax_(m1.x, m1.y), fmax_(m1.z, m1.w)));
  float o0 = 0.f, o1 = 0.f, L = 0.f;
#pragma unroll
  for (int w = 0; w < 8; ++w) {
    const float e = __builtin_amdgcn_exp2f((w < 4 ? m0[w & 3] : m1[w & 3]) - M);
    o0 = fmaf(e, ov[w][0], o0); o1 = fmaf(e, ov[w][1], o1);
    L = fmaf(e, w < 4 ? l0[w & 3] : l1[w & 3], L);
  }
  const int dst = WS_PG + (br * 256 + row) * PG_ROW;
  st_gran(ws, dst + (h * 128 + lane) * 8, tag, o0);
  st_gran(ws, dst + (h * 128 + 64 + lane) * 8, tag, o1);
  if (lane < 16) st_gran(ws, dst + (256 + h * 16 + lane) * 8, tag, (lane & 1) ? L : M);
}
template <int I, int N> struct Run {
  static __device__ __forceinline__ void go(St& z) { request<I>(z); consume<I - DEP + 1, 4 * (DEP - 1)>(z); Run<I + 1, N>::go(z); }
};
template <int N> struct Run<N, N> { static __device__ __forceinline__ void go(St&) {} };
template <int I, int N> struct Req { static __device__ __forceinline__ void go(St& z) { request<I>(z); Req<I + 1, N>::go(z); } };
template <int N> struct Req<N, N> { static __device__ __forceinline__ void go(St&) {} };
template <int I, int N, int LAST> struct Con {
  static __device__ __forceinline__ void go(St& z) { consume<I, 4 * (LAST - I)>(z); Con<I + 1, N, LAST>::go(z); }
};
template <int N, int LAST> struct Con<N, N, LAST> { static __device__ __forceinline__ void go(St&) {} };

__global__ void __launch_bounds__(NTHR, 3) gqa2_fused(Args a) {
  __shared__ __attribute__((aligned(16))) float lds[L_END];
  unsigned* const l_cnt = (unsigned*)(lds + L_CNT);
  const unsigned t0 = (unsigned)__builtin_amdgcn_s_memrealtime();

  const int tid = threadIdx.x, lane = tid & 63, wave = __builtin_amdgcn_readfirstlane(tid >> 6);
  const int g = lane >> 4, n = lane & 15;
  const int wg = blockIdx.x;
  const int ch = MAPV == 0 ? wg : ((wg & 7) << 5 | wg >> 3);
  const int kvh = ch >> 4, rb = (ch >> 3) & 1, s8 = ch & 7;
  unsigned char* const ws = (unsigned char*)a.ws;
  STAMP(0);
  const __amdgpu_buffer_rsrc_t rw = __builtin_amdgcn_make_buffer_rsrc(ws, 0, WS_ST, RSRC3);
  v4u mask;
  {
    const unsigned half = (n & 1) ? 0xffff0000u : 0x0000ffffu; const int r = (n & 7) >> 1;
    mask.x = r == 0 ? half : 0u; mask.y = r == 1 ? half : 0u; mask.z = r == 2 ? half : 0u; mask.w = r == 3 ? half : 0u;
  }
  const int pidx = kvh * 8 + s8;
  if (wave >= 8) {
    __builtin_amdgcn_s_setprio(3);
    const int hw = wave - 8, cg = s8 & 3, kh = s8 >> 2, sub = 2 * g + (n >> 3);
    const __amdgpu_buffer_rsrc_t rq = __builtin_amdgcn_make_buffer_rsrc((unsigned char*)a.wq[rb] + ((size_t)(kh * 288 + hw * 144) * NQ + kvh * 256 + cg * 64) * 2, 0, 143 * NQ * 2 + 128, RSRC3);
    const __amdgpu_buffer_rsrc_t rx = __builtin_amdgcn_make_buffer_rsrc((unsigned char*)a.x[rb] + (kh * 288 + hw * 144) * 2, 0, 144 * 2, RSRC3);
    const v4u x0 = __builtin_amdgcn_raw_buffer_load_b128(rx, sub * 36, 0, 0), x1 = __builtin_amdgcn_raw_buffer_load_b128(rx, sub * 36 + 16, 0, 0);
    const unsigned x2 = __builtin_amdgcn_raw_buffer_load_b32(rx, sub * 36 + 32, 0, 0);
    const unsigned ep = __builtin_amdgcn_raw_buffer_load_b32(rw, lane == 0 ? WS_EPOCH : OOB, 0, AUX_SC1);
    v4u wqv[18];
#pragma unroll
    for (int i = 0; i < 18; ++i) wqv[i] = __builtin_amdgcn_raw_buffer_load_b128(rq, sub * 18 * (NQ * 2) + (n & 7) * 16, i * NQ * 2, AUX_NT);
    SB();
    if (tid < 512 + 8) l_cnt[tid - 512] = 0u;
    lds_barrier();
    f32x4 a0 = {0.f, 0.f, 0.f, 0.f}, a1 = {0.f, 0.f, 0.f, 0.f};
#pragma unroll
    for (int i = 0; i < 9; ++i) {
      const unsigned xp = i < 4 ? x0[i & 3] : i < 8 ? x1[i & 3] : x2;
      a0 = mfma(diag((xp & 0xffffu) * 0x10001u, mask), wqv[2 * i], a0);
      a1 = mfma(diag((xp >> 16) * 0x10001u, mask), wqv[2 * i + 1], a1);
    }
    if ((g >> 1) == (n >> 3)) *(f32x4*)(lds + L_QP + (hw * 2 + (n >> 3)) * 64 + 8 * (n & 7) + 4 * (g & 1)) = a0 + a1;
    const unsigned tag = __builtin_amdgcn_readfirstlane(ep) + TAG_STEP;
    STAMP(1);
    lds_arrive(l_cnt + 0, lane);
    if (hw == 0) {
      lds_wait(l_cnt + 0, 2u);
      const float q = (lds[L_QP + lane] + lds[L_QP + 64 + lane]) + (lds[L_QP + 128 + lane] + lds[L_QP + 192 + lane]);
      st_gran(ws, WS_QG + ((rb * 16 + kvh) * 512 + kh * 256 + cg * 64 + lane) * 8, tag, q);
      if (lane == 0) l_cnt[5] = tag;
    }
    STAMP(2);
    gate((unsigned)__builtin_amdgcn_s_memrealtime() + (QG16 >> 4));
    {
      const int qo = WS_QG + ((hw * 16 + kvh) * 512 + lane) * 8;
      const int off[8] = {qo, qo + 512, qo + 1024, qo + 1536, qo + 2048, qo + 2560, qo + 3072, qo + 3584};
      v2u x[8];
      sweep<8, 8>(rw, off, tag, x);
#pragma unroll
      for (int k = 0; k < 4; ++k) ((unsigned short*)(lds + L_Q))[hw * 256 + k * 64 + lane] = f2bf(__uint_as_float(x[k].x) + __uint_as_float(x[k + 4].x));
      lds_arrive(l_cnt + 1, lane);
    }
    STAMP(3);
    {
      const int d1 = WS_PG + (256 + ch) * PG_ROW, d2 = WS_YG + (rb * 128 + pidx) * YG_ROW;
      unsigned u = __builtin_amdgcn_raw_buffer_load_b32(rw, hw == 0 ? (lane < 36 ? d1 + lane * 64 : OOB) : d2 + lane * 64, 0, AUX_SC1);
      u ^= __builtin_amdgcn_raw_buffer_load_b32(rw, (hw == 0 && lane < 16) ? d2 + 4096 + lane * 64 : OOB, 0, AUX_SC1);
      asm volatile("" :: "v"(u));
    }
    gate(t0 + (unsigned)((OFF16 + 16 * DT16) >> 4));
#pragma nounroll
    for (int br = 0; br < 2; ++br) {
      merge_publish(lds, ws, br, hw, ch, lane, tag);
      if (br == 0) gate(t0 + (unsigned)((OFF16 + 32 * DT16) >> 4));
    }
    STAMP(8);
    if (PADW < 0 || wg == PADW) gate(t0 + PADT);
  } else {
    St z;
    z.lds = lds; z.lane = lane; z.wave = wave; z.mask = mask;
    z.rs[0] = mkrs((unsigned char*)a.k[0] + (size_t)ch * 262144, 262144);
    z.rs[1] = mkrs((unsigned char*)a.v[0] + (size_t)ch * 262144, 262144);
    z.rs[2] = mkrs((unsigned char*)a.k[1] + (size_t)ch * 262144, 262144);
    z.rs[3] = mkrs((unsigned char*)a.v[1] + (size_t)ch * 262144, 262144);
    z.vk = ((lane >> 2) & 3) * 256 + g * 64 + (lane & 3) * 16; z.vv = lane * 16; z.so = wave * 4096;
    z.tb = (t0 << 4) + (unsigned)(OFF16 + ((wave * DT16) >> 3));
    const void* wop = a.wo[rb];
    GAS unsigned short* outp = (GAS unsigned short*)a.out;
    asm volatile("" : "+s"(wop), "+s"(outp));
    lds_barrier();
    Req<0, PRE>::go(z);
    STAMP(4);
    lds_wait(l_cnt + 1, 2u);
    const unsigned tag = __builtin_amdgcn_readfirstlane(l_cnt[5]);
    STAMP(5);
    Con<0, PRE - DEP + 1, PRE - 1>::go(z);
    Run<PRE, 17>::go(z);
    STAMP(14);
    Run<17, 25>::go(z);
    STAMP(15);
    Run<25, 32>::go(z);
    STAMP(6);
    Con<32 - DEP + 1, 32, 31>::go(z);
    STAMP(7);
    if (wave < 5) {
      const __amdgpu_buffer_rsrc_t ro = __builtin_amdgcn_make_buffer_rsrc((unsigned char*)wop + ((size_t)(kvh * 256 + s8 * 32) * DM) * 2, 0, 32 * DM * 2, RSRC3);
      const int wcol = wave * 128 + n * 8;
      const int vwo = (!NOWO && wcol < DM) ? g * (DM * 2) + wcol * 2 : OOB;
      v4u Wo[8];
      z.tb += (unsigned)(WO16 + wave * WOS16 - ((wave * DT16) >> 3));
      gate_next(z.tb);
#pragma unroll
      for (int i = 0; i < 8; ++i) Wo[i] = __builtin_amdgcn_raw_buffer_load_b128(ro, vwo, i * 4 * DM * 2, AUX_NT);
      SB();
      lds_wait(l_cnt + 4, 1u);
      STAMP(10);
      const v4u oa = *(const v4u*)(lds + L_OB + g * 8), ob = *(const v4u*)(lds + L_OB + g * 8 + 4);
      f32x4 Y0 = {0.f, 0.f, 0.f, 0.f}, Y1 = {0.f, 0.f, 0.f, 0.f};
#pragma unroll
      for (int i = 0; i < 4; ++i) { Y0 = mfma(diag(oa[i], mask), Wo[i], Y0); Y1 = mfma(diag(ob[i], mask), Wo[4 + i], Y1); }
      const f32x4 Y = Y0 + Y1;
      if (wcol < DM) {
        const int dst = WS_YG + (rb * 128 + pidx) * YG_ROW + (wave * 128 + lane) * 8;
        st_gran(ws, dst, tag, g < 2 ? Y.x : Y.z);
        st_gran(ws, dst + 512, tag, g < 2 ? Y.y : Y.w);
      }
      STAMP(11);
    } else if (wave == 5) {
      const int cb = WS_PG + (rb * 256 + kvh * 16) * PG_ROW;
      const int po = cb + (lane >> 5) * PG_ROW + (32 * s8 + (lane & 31)) * 8;
      const int off[9] = {po, po + 2 * PG_ROW, po + 4 * PG_ROW, po + 6 * PG_ROW, po + 8 * PG_ROW, po + 10 * PG_ROW, po + 12 * PG_ROW, po + 14 * PG_ROW,
                          cb + n * PG_ROW + (256 + 16 * (s8 >> 2) + (g & 1)) * 8};
      v2u x[9];
      lds_wait(l_cnt + 2 + rb, 8u);
      gate((unsigned)__builtin_amdgcn_s_memrealtime() + H1D);
      sweep<9, SL1>(rw, off, tag, x);
      STAMP(9);
      auto sw = __builtin_amdgcn_permlane16_swap(x[8].x, x[8].x, false, false);
      const float Ms = __uint_as_float(sw[0]), Ls = __uint_as_float(sw[1]);
      float Mx = fmax_(Ms, dpp<XOR1>(Ms)); Mx = fmax_(Mx, dpp<XOR2>(Mx)); Mx = fmax_(Mx, dpp<HMIR>(Mx)); Mx = fmax_(Mx, dpp<MIR>(Mx));
      const float w = __builtin_amdgcn_exp2f(Ms - Mx);
      float Lt = w * Ls; Lt += dpp<XOR1>(Lt); Lt += dpp<XOR2>(Lt); Lt += dpp<HMIR>(Lt); Lt += dpp<MIR>(Lt);
      const float ws_ = lane < 32 ? w : dpp<XOR1>(w);
      float o = dpp<0x150>(ws_) * __uint_as_float(x[0].x);
      o = fmaf(dpp<0x152>(ws_), __uint_as_float(x[1].x), o);
      o = fmaf(dpp<0x154>(ws_), __uint_as_float(x[2].x), o);
      o = fmaf(dpp<0x156>(ws_), __uint_as_float(x[3].x), o);
      o = fmaf(dpp<0x158>(ws_), __uint_as_float(x[4].x), o);
      o = fmaf(dpp<0x15a>(ws_), __uint_as_float(x[5].x), o);
      o = fmaf(dpp<0x15c>(ws_), __uint_as_float(x[6].x), o);
      o = fmaf(dpp<0x15e>(ws_), __uint_as_float(x[7].x), o);
      o = x32_sum(o) * __builtin_amdgcn_rcpf(Lt);
      if (lane < 32) ((unsigned*)(lds + L_OB))[(lane & 3) * 8 + (lane >> 2)] = dup_bf16(o);
      lds_arrive(l_cnt + 4, lane);
    } else if (wave == 6 || pidx < 16) {
      const int pos = wave == 6 ? pidx * 4 : 512 + (pidx >> 1) * 16 + (pidx & 1) * 4;
      const int so = WS_YG + (rb * 128 + (lane >> 2)) * YG_ROW + (pos + (lane & 3)) * 8;
      const int off[8] = {so, so + 16 * YG_ROW, so + 32 * YG_ROW, so + 48 * YG_ROW, so + 64 * YG_ROW, so + 80 * YG_ROW, so + 96 * YG_ROW, so + 112 * YG_ROW};
      const int p = pos + (lane & 3), pl = p & 63;
      const int col = (p & ~127) + 8 * (pl & 15) + 4 * ((pl >> 4) & 1) + 2 * (pl >> 5) + ((p >> 6) & 1);
      v2u x[8];
      lds_wait(l_cnt + 4, 1u);
      z.tb += (unsigned)(WO16 + 1600 - ((wave * DT16) >> 3));
      gate_next(z.tb);
      gate((unsigned)__builtin_amdgcn_s_memrealtime() + H2D);
      sweep<8, SL2>(rw, off, tag, x);
      STAMP(12);
      float y = ((__uint_as_float(x[0].x) + __uint_as_float(x[1].x)) + (__uint_as_float(x[2].x) + __uint_as_float(x[3].x)))
              + ((__uint_as_float(x[4].x) + __uint_as_float(x[5].x)) + (__uint_as_float(x[6].x) + __uint_as_float(x[7].x)));
      y += dpp<ROR4>(y); y += dpp<ROR8>(y);
      y = xg_sum(y);
      if (lane < 4) outp[rb * DM + col] = f2bf(y);
      if (rb == 0 && pos == 0 && lane == 0) __hip_atomic_store((gu32*)(ws + WS_EPOCH), tag, RLX_AGENT);
      STAMP(13);
    }
  }
  STAMP_OUT();
}

extern "C" void kernel_launch(void* const* d_in, const int* in_sizes, int n_in, void* d_out, int out_size, void* d_ws, size_t ws_size, hipStream_t stream) {
  Args a;
  a.x[0] = d_in[0]; a.k[0] = d_in[1]; a.v[0] = d_in[2]; a.wq[0] = d_in[3]; a.wo[0] = d_in[4];
  a.x[1] = d_in[5]; a.k[1] = d_in[6]; a.v[1] = d_in[7]; a.wq[1] = d_in[8]; a.wo[1] = d_in[9];
  a.out = d_out; a.ws = d_ws;
  gqa2_fused<<<NWG, NTHR, 0, stream>>>(a);
}
```

```cpp
#define PAT 1
#define DT16 1760
#define SLK16 100000
#define LIM 2
#define OFF16 1600
#include <hip/hip_runtime.h>
#include <cstdint>

typedef unsigned v4u __attribute__((ext_vector_type(4)));
typedef unsigned v2u __attribute__((ext_vector_type(2)));
typedef float f32x4 __attribute__((ext_vector_type(4)));
typedef __bf16 bf16x8 __attribute__((ext_vector_type(8)));
typedef __bf16 bf16x2 __attribute__((ext_vector_type(2)));
typedef float f32x2 __attribute__((ext_vector_type(2)));
#define GAS __attribute__((address_space(1)))
#define LAS __attribute__((address_space(3)))
typedef GAS unsigned gu32;
typedef GAS unsigned long long gu64;
#define RLX_AGENT __ATOMIC_RELAXED, __HIP_MEMORY_SCOPE_AGENT
#define RLX_WG __ATOMIC_RELAXED, __HIP_MEMORY_SCOPE_WORKGROUP

#define STAMPS 0
#define ANC -1
#define PRE 8
#define DEP 3
#define WO16 0
#define WOS16 600
#define QGD 75
#define PTH16 400
#define H1D 40
#define H2D 70
#define PREW 1
#define HBAR 1
#define JST 8192
#define WST 1024
#define SOFT16 0
#define NSOFT 6
#define NOWO 0

constexpr int DM = 576, NQ = 4096;
constexpr int NWG = 256, NTHR = 640;
constexpr int AUX_NT = 2, AUX_SC1 = 16;
constexpr int RSRC3 = 0x00020000;
constexpr int OOB = 0x7fff0000;
constexpr int WS_EPOCH = 0;
constexpr int WS_QG = 4096;
constexpr int WS_MG = WS_QG + 32 * 4096;
constexpr int WS_PG = WS_MG + 64 * 1024;
constexpr int PG_ROW = 256 * 8;
constexpr int WS_YG = WS_PG + 512 * PG_ROW;
constexpr int YG_ROW = 640 * 8;
constexpr int WS_ST = WS_YG + 256 * YG_ROW;
constexpr unsigned TAG_STEP = 0x9E3779B9u;
constexpr unsigned SPIN_CAP = 1u << 17, LSPIN_CAP = 1u << 22;
constexpr float C2 = 0.088388347648318440f * 1.4426950408889634f;
constexpr unsigned EMPTY_O = 0xffffffffu, EMPTY_B = 0x0000ffffu;

struct Args {
  const void* x[2]; const void* k[2]; const void* v[2]; const void* wq[2]; const void* wo[2];
  void* out; void* ws;
};

#define SB() __builtin_amdgcn_sched_barrier(0)
__device__ __forceinline__ f32x4 mfma(v4u a, v4u b, f32x4 c) {
  return __builtin_amdgcn_mfma_f32_16x16x32_bf16(__builtin_bit_cast(bf16x8, a), __builtin_bit_cast(bf16x8, b), c, 0, 0, 0);
}
__device__ __forceinline__ unsigned dup_bf16(float f) {
  f32x2 v = {f, f};
  return __builtin_bit_cast(unsigned, __builtin_convertvector(v, bf16x2));
}
__device__ __forceinline__ unsigned short f2bf(float f) { return __builtin_bit_cast(unsigned short, (__bf16)f); }
__device__ __forceinline__ v4u diag(unsigned pp, v4u mask) { v4u r = {pp & mask.x, pp & mask.y, pp & mask.z, pp & mask.w}; return r; }
template <int CTRL> __device__ __forceinline__ float dpp(float x) {
  return __builtin_bit_cast(float, __builtin_amdgcn_mov_dpp(__builtin_bit_cast(int, x), CTRL, 0xf, 0xf, true));
}
constexpr int XOR1 = 0xB1, XOR2 = 0x4E, HMIR = 0x141, MIR = 0x140, ROR8 = 0x128, ROR4 = 0x124;
__device__ __forceinline__ float fmax_(float a, float b) { return __builtin_fmaxf(a, b); }
__device__ __forceinline__ float xg_max(float x) {
  auto s = __builtin_amdgcn_permlane16_swap(__float_as_uint(x), __float_as_uint(x), false, false);
  x = fmax_(__uint_as_float(s[0]), __uint_as_float(s[1]));
  auto t = __builtin_amdgcn_permlane32_swap(__float_as_uint(x), __float_as_uint(x), false, false);
  return fmax_(__uint_as_float(t[0]), __uint_as_float(t[1]));
}
__device__ __forceinline__ float x32_sum(float x) {
  auto t = __builtin_amdgcn_permlane32_swap(__float_as_uint(x), __float_as_uint(x), false, false);
  return __uint_as_float(t[0]) + __uint_as_float(t[1]);
}
__device__ __forceinline__ float xg_sum(float x) {
  auto s = __builtin_amdgcn_permlane16_swap(__float_as_uint(x), __float_as_uint(x), false, false);
  return x32_sum(__uint_as_float(s[0]) + __uint_as_float(s[1]));
}
__device__ __forceinline__ void st_gran(unsigned char* ws, int off, unsigned tag, float v) {
  __hip_atomic_store((gu64*)(ws + off), ((unsigned long long)tag << 32) | __float_as_uint(v), RLX_AGENT);
}
__device__ __forceinline__ v2u ldg(__amdgpu_buffer_rsrc_t rs, int off) { return __builtin_amdgcn_raw_buffer_load_b64(rs, off, 0, AUX_SC1); }
template <int N, int STEP, int SLEEP> __device__ __forceinline__ void sweep(__amdgpu_buffer_rsrc_t rs, int off, unsigned tag, v2u (&x)[N]) {
#pragma unroll
  for (int k = 0; k < N; ++k) x[k] = ldg(rs, off + k * STEP);
  SB();
  bool ok = true;
#pragma unroll
  for (int k = 0; k < N; ++k) ok &= x[k].y == tag;
  if (__builtin_expect(__all(ok), 1)) return;
#pragma nounroll
  for (unsigned spins = 0; spins < SPIN_CAP; ++spins) {
    __builtin_amdgcn_s_sleep(SLEEP);
    v2u y[N];
#pragma unroll
    for (int k = 0; k < N; ++k) y[k] = ldg(rs, x[k].y == tag ? OOB : off + k * STEP);
    SB();
    ok = true;
#pragma unroll
    for (int k = 0; k < N; ++k) { x[k] = x[k].y == tag ? x[k] : y[k]; ok &= x[k].y == tag; }
    if (__all(ok)) break;
  }
}
__device__ __forceinline__ v4u mkrs(const void* p, unsigned bytes) {
  const unsigned long long u = (unsigned long long)p;
  v4u r = {(unsigned)u, (unsigned)(u >> 32) & 0xffffu, bytes, (unsigned)RSRC3};
  return r;
}
__device__ __forceinline__ void tile_load(v4u (&d)[4], int voff, v4u rs, int soff) {
  asm volatile("buffer_load_dwordx4 %0, %4, %5, %6 offen nt\n\t"
               "buffer_load_dwordx4 %1, %4, %5, %7 offen nt\n\t"
               "buffer_load_dwordx4 %2, %4, %5, %8 offen nt\n\t"
               "buffer_load_dwordx4 %3, %4, %5, %9 offen nt"
               : "=&v"(d[0]), "=&v"(d[1]), "=&v"(d[2]), "=&v"(d[3]) : "v"(voff), "s"(rs), "s"(soff), "s"(soff + JST), "s"(soff + 2 * JST), "s"(soff + 3 * JST));
}
template <int N> __device__ __forceinline__ void tile_wait(v4u (&d)[4]) {
  asm volatile("s_waitcnt vmcnt(%4)" : "+v"(d[0]), "+v"(d[1]), "+v"(d[2]), "+v"(d[3]) : "i"(N));
}
template <int N> __device__ __forceinline__ void piece_wait(v4u& d) { asm volatile("s_waitcnt vmcnt(%1)" : "+v"(d) : "i"(N)); }
__device__ __forceinline__ void pause(int ticks) {
  unsigned due;
  asm volatile("s_memrealtime vcc\n\t"
               "s_waitcnt lgkmcnt(0)\n\t"
               "s_add_i32 %0, vcc_lo, %1\n"
               ".Lg0_%=:\n\t"
               "s_sleep 1\n\t"
               "s_memrealtime vcc\n\t"
               "s_waitcnt lgkmcnt(0)\n\t"
               "s_sub_i32 vcc_lo, vcc_lo, %0\n\t"
               "s_cmp_lt_i32 vcc_lo, 0\n\t"
               "s_cbranch_scc1 .Lg0_%="
               : "=&s"(due) : "i"(ticks) : "scc", "vcc", "memory");
}
__device__ __forceinline__ void gate_next(unsigned& due) {
  asm volatile("s_branch .Lg1_%=\n"
               ".Lg0_%=:\n\t"
               "s_sleep 1\n"
               ".Lg1_%=:\n\t"
               "s_memrealtime vcc\n\t"
               "s_waitcnt lgkmcnt(0)\n\t"
               "s_lshl_b32 vcc_lo, vcc_lo, 4\n\t"
               "s_sub_i32 vcc_lo, vcc_lo, %0\n\t"
               "s_cmp_lt_i32 vcc_lo, 0\n\t"
               "s_cbranch_scc1 .Lg0_%=\n\t"
               "s_setprio 0\n\t"
               "s_cmp_lt_i32 vcc_lo, %1\n\t"
               "s_cbranch_scc1 .Lg2_%=\n\t"
               "s_setprio 1\n"
               ".Lg2_%=:\n\t"
               "s_sub_i32 vcc_lo, vcc_lo, %2\n\t"
               "s_max_i32 vcc_lo, vcc_lo, 0\n\t"
               "s_add_i32 %0, %0, vcc_lo\n\t"
               "s_add_i32 %0, %0, %3"
               : "+s"(due) : "i"(PTH16), "i"(SLK16), "i"(DT16) : "scc", "vcc", "memory");
}
__device__ __forceinline__ void lds_barrier() {
  asm volatile("s_waitcnt lgkmcnt(0)" ::: "memory");
  __builtin_amdgcn_s_barrier();
  asm volatile("" ::: "memory");
}
__device__ __forceinline__ void lds_arrive(unsigned* c, int lane) {
  asm volatile("s_waitcnt lgkmcnt(0)" ::: "memory");
  if (lane == 0) __hip_atomic_fetch_add(c, 1u, RLX_WG);
}
template <int SLEEP> __device__ __forceinline__ unsigned lds_wait(unsigned* c, unsigned want) {
  unsigned v = 0;
#pragma nounroll
  for (unsigned spins = 0; spins < LSPIN_CAP; ++spins) {
    v = __builtin_amdgcn_readfirstlane(__hip_atomic_load(c, RLX_WG));
    if (v >= want) break;
    __builtin_amdgcn_s_sleep(SLEEP);
  }
  asm volatile("" ::: "memory");
  return v;
}
__device__ __forceinline__ unsigned lds_addr(const void* p) { return (unsigned)(size_t)(const LAS void*)p; }
template <int SLEEP> __device__ __forceinline__ v2u lds_take2(unsigned addr) {
  v2u v;
#pragma nounroll
  for (unsigned spins = 0; spins < LSPIN_CAP; ++spins) {
    asm volatile("ds_read2st64_b32 %0, %1 offset1:1\n\ts_waitcnt lgkmcnt(0)" : "=&v"(v) : "v"(addr) : "memory");
    if (__all(v.x != EMPTY_O && v.y != EMPTY_O)) break;
    if (SLEEP) __builtin_amdgcn_s_sleep(SLEEP);
  }
  return v;
}
#define STAMP(i) do {} while (0)
#define STAMP_OUT() do {} while (0)

constexpr int L_O = 0;
constexpr int L_ML = L_O + 4096;
constexpr int L_QP = L_ML + 64;
constexpr int L_Q = L_QP + 256;
constexpr int L_OB = L_Q + 256;
constexpr int L_CNT = L_OB + 32;
constexpr int L_END = L_CNT + 8;

struct St {
  v4u T[32][4];
  float Sc[8][4];
  unsigned P[8][4];
  v4u Q, mask;
  f32x4 O;
  v4u rs[4];
  int vk, vv, so;
  unsigned tb;
  float* lds;
  int lane, wave;
};

template <int I> __device__ __forceinline__ void request(St& z) {
  if constexpr (4 * I > LIM) asm volatile("s_waitcnt vmcnt(%0)" :: "i"(LIM) : "memory");
  gate_next(z.tb);
  if constexpr (SOFT16 && I < NSOFT) z.tb += SOFT16;
  tile_load(z.T[I], (I & 8) ? z.vv : z.vk, z.rs[I >> 3], z.so + (I & 7) * 32768);
  SB();
}
template <int I, int W> __device__ __forceinline__ void consume(St& z) {
  const int g = z.lane >> 4, n = z.lane & 15;
  constexpr int br = I >> 4;
  if constexpr ((I & 15) == 0) {
    z.Q = *(const v4u*)((const unsigned short*)(z.lds + L_Q) + br * 256 + (n >> 3) * 128 + 32 * g + 8 * (n & 3));
  }
  if constexpr ((I & 8) == 0) {
    tile_wait<W>(z.T[I]);
#pragma unroll
    for (int j = 0; j < 4; ++j) {
      const f32x4 d = mfma(z.T[I][j], z.Q, f32x4{0.f, 0.f, 0.f, 0.f});
      z.Sc[I & 7][j] = (dpp<0x00>(d.x) + dpp<0x55>(d.y)) + (dpp<0xAA>(d.z) + dpp<0xFF>(d.w));
    }
    if constexpr ((I & 7) == 7) {
      float m = z.Sc[0][0];
#pragma unroll
      for (int t = 0; t < 8; ++t)
#pragma unroll
        for (int j = 0; j < 4; ++j) m = fmax_(m, z.Sc[t][j]);
      const float m2 = xg_max(m) * C2;
      float l = 0.f;
#pragma unroll
      for (int t = 0; t < 8; ++t) {
        float p[4];
#pragma unroll
        for (int j = 0; j < 4; ++j) { p[j] = __builtin_amdgcn_exp2f(fmaf(z.Sc[t][j], C2, -m2)); z.P[t][j] = dup_bf16(p[j]); }
        l += (p[0] + p[1]) + (p[2] + p[3]);
        asm volatile("" : "+v"(z.P[t][0]), "+v"(z.P[t][1]), "+v"(z.P[t][2]), "+v"(z.P[t][3]));
      }
      l = xg_sum(l);
      if (g == 0 && (n & 7) == 0) { z.lds[L_ML + br * 32 + (n >> 3) * 8 + z.wave] = m2; z.lds[L_ML + br * 32 + 16 + (n >> 3) * 8 + z.wave] = l; }
      lds_arrive((unsigned*)(z.lds + L_CNT) + 2 + br, z.lane);
      z.O = f32x4{0.f, 0.f, 0.f, 0.f};
    } else {
      asm volatile("" : "+v"(z.Sc[I & 7][0]), "+v"(z.Sc[I & 7][1]), "+v"(z.Sc[I & 7][2]), "+v"(z.Sc[I & 7][3]));
    }
  } else if constexpr (W == 0) {
    v4u A[4];
#pragma unroll
    for (int j = 0; j < 4; ++j) A[j] = diag(z.P[I & 7][j], z.mask);
    float* dst = z.lds + L_O + br * 2048 + z.wave * 256 + (g >> 1) * 128 + 8 * n + 4 * (g & 1);
    unsigned da = lds_addr(dst);
    asm volatile("" : "+v"(A[0]), "+v"(A[1]), "+v"(A[2]), "+v"(A[3]), "+v"(da));
    piece_wait<3>(z.T[I][0]); z.O = mfma(A[0], z.T[I][0], z.O); SB();
    piece_wait<2>(z.T[I][1]); z.O = mfma(A[1], z.T[I][1], z.O); SB();
    piece_wait<1>(z.T[I][2]); z.O = mfma(A[2], z.T[I][2], z.O); SB();
    piece_wait<0>(z.T[I][3]); z.O = mfma(A[3], z.T[I][3], z.O);
    *(LAS f32x4*)(size_t)da = z.O;
  } else {
    tile_wait<W>(z.T[I]);
#pragma unroll
    for (int j = 0; j < 4; ++j) z.O = mfma(diag(z.P[I & 7][j], z.mask), z.T[I][j], z.O);
    asm volatile("" : "+v"(z.O));
    if constexpr ((I & 7) == 7) *(f32x4*)(z.lds + L_O + br * 2048 + z.wave * 256 + (g >> 1) * 128 + 8 * n + 4 * (g & 1)) = z.O;
  }
  SB();
}
template <int I, int N> struct Run {
  static __device__ __forceinline__ void go(St& z) { request<I>(z); consume<I - DEP + 1, 4 * (DEP - 1)>(z); Run<I + 1, N>::go(z); }
};
template <int N> struct Run<N, N> { static __device__ __forceinline__ void go(St&) {} };
template <int I, int N> struct Req { static __device__ __forceinline__ void go(St& z) { request<I>(z); Req<I + 1, N>::go(z); } };
template <int N> struct Req<N, N> { static __device__ __forceinline__ void go(St&) {} };
template <int I, int N, int LAST> struct Con {
  static __device__ __forceinline__ void go(St& z) { consume<I, 4 * (LAST - I)>(z); Con<I + 1, N, LAST>::go(z); }
};
template <int N, int LAST> struct Con<N, N, LAST> { static __device__ __forceinline__ void go(St&) {} };

__global__ void __launch_bounds__(NTHR, 3) gqa2_fused(Args a) {
  __shared__ __attribute__((aligned(16))) float lds[L_END];
  unsigned* const l_cnt = (unsigned*)(lds + L_CNT);
  STAMP(0);
  const int tid = threadIdx.x, lane = tid & 63, wave = __builtin_amdgcn_readfirstlane(tid >> 6);
  const int g = lane >> 4, n = lane & 15;
  const int wg = blockIdx.x;
  const int ch = (wg & 7) << 5 | wg >> 3;
  const int kvh = ch >> 4, rb = (ch >> 3) & 1, s8 = ch & 7;
  if (tid < 512) {
    const v4u e = {EMPTY_O, EMPTY_O, EMPTY_O, EMPTY_O};
    *(v4u*)(lds + L_O + tid * 4) = e; *(v4u*)(lds + L_O + 2048 + tid * 4) = e;
  } else if (tid < 512 + 40) {
    ((unsigned*)(lds + L_OB))[tid - 512] = tid < 512 + 32 ? EMPTY_B : 0u;
  }
  asm volatile("" : "+s"(a.x[0]), "+s"(a.x[1]), "+s"(a.k[0]), "+s"(a.k[1]), "+s"(a.v[0]), "+s"(a.v[1]), "+s"(a.wq[0]), "+s"(a.wq[1]), "+s"(a.wo[0]), "+s"(a.wo[1]), "+s"(a.out), "+s"(a.ws));
  unsigned char* const ws = (unsigned char*)a.ws;
  const void* const wop = rb ? a.wo[1] : a.wo[0];
  const v4u rk0 = mkrs((unsigned char*)a.k[0] + (size_t)ch * 262144, 262144);
  unsigned probe;
  asm volatile("s_cmp_lg_u32 %2, 0\n\ts_cbranch_scc1 .Lp%=\n\ts_mov_b64 exec, 1\n\tbuffer_load_dword %0, off, %1, 0 nt\n\ts_mov_b64 exec, -1\n.Lp%=:"
               : "=v"(probe) : "s"(rk0), "s"(wave) : "scc");
  lds_barrier();
  const __amdgpu_buffer_rsrc_t rw = __builtin_amdgcn_make_buffer_rsrc(ws, 0, WS_ST, RSRC3);
  v4u mask;
  {
    const unsigned half = (n & 1) ? 0xffff0000u : 0x0000ffffu; const int r = (n & 7) >> 1;
    mask.x = r == 0 ? half : 0u; mask.y = r == 1 ? half : 0u; mask.z = r == 2 ? half : 0u; mask.w = r == 3 ? half : 0u;
  }
  const int pidx = kvh * 8 + s8;
  if (__builtin_expect(wave >= 8, 1)) {
    __builtin_amdgcn_s_setprio(3);
    const int hw = wave - 8, cg = s8 & 3, kh = s8 >> 2, sub = 2 * g + (n >> 3);
    const __amdgpu_buffer_rsrc_t rq = __builtin_amdgcn_make_buffer_rsrc((unsigned char*)(rb ? a.wq[1] : a.wq[0]) + ((size_t)(kh * 288 + hw * 144) * NQ + kvh * 256 + cg * 64) * 2, 0, 143 * NQ * 2 + 128, RSRC3);
    const __amdgpu_buffer_rsrc_t rx = __builtin_amdgcn_make_buffer_rsrc((unsigned char*)(rb ? a.x[1] : a.x[0]) + (kh * 288 + hw * 144) * 2, 0, 144 * 2, RSRC3);
    v4u wqv[18];
#pragma unroll
    for (int i = 0; i < 18; ++i) wqv[i] = __builtin_amdgcn_raw_buffer_load_b128(rq, sub * (NQ * 2) + (n & 7) * 16, i * 8 * NQ * 2, AUX_NT);
    const unsigned xa = __builtin_amdgcn_raw_buffer_load_b32(rx, n * 16 + g * 4, 0, 0), xb = __builtin_amdgcn_raw_buffer_load_b32(rx, n * 16 + g * 4 + 256, 0, 0);
    const unsigned ep = __builtin_amdgcn_raw_buffer_load_b32(rw, lane == 0 ? WS_EPOCH : OOB, 0, AUX_SC1);
    SB();
    f32x4 a0 = {0.f, 0.f, 0.f, 0.f}, a1 = {0.f, 0.f, 0.f, 0.f};
    const unsigned sel = n < 8 ? 0x01000100u : 0x03020302u;
#define QSTEP(acc, i, src, k) acc = mfma(diag(__builtin_amdgcn_perm(0u, (unsigned)__builtin_amdgcn_mov_dpp((int)(src), 0x150 + (k), 0xf, 0xf, true), sel), mask), wqv[i], acc)
    QSTEP(a0, 0, xa, 0); QSTEP(a1, 1, xa, 1); QSTEP(a0, 2, xa, 2); QSTEP(a1, 3, xa, 3); QSTEP(a0, 4, xa, 4); QSTEP(a1, 5, xa, 5);
    QSTEP(a0, 6, xa, 6); QSTEP(a1, 7, xa, 7); QSTEP(a0, 8, xa, 8); QSTEP(a1, 9, xa, 9); QSTEP(a0, 10, xa, 10); QSTEP(a1, 11, xa, 11);
    QSTEP(a0, 12, xa, 12); QSTEP(a1, 13, xa, 13); QSTEP(a0, 14, xa, 14); QSTEP(a1, 15, xa, 15); QSTEP(a0, 16, xb, 0); QSTEP(a1, 17, xb, 1);
    if ((g >> 1) == (n >> 3)) *(f32x4*)(lds + L_QP + (hw * 2 + (n >> 3)) * 64 + 8 * (n & 7) + 4 * (g & 1)) = a0 + a1;
    const unsigned tag = __builtin_amdgcn_readfirstlane(ep) + TAG_STEP;
    STAMP(1);
    lds_arrive(l_cnt + 0, lane);
    if (hw == 0) {
      lds_wait<1>(l_cnt + 0, 2u);
      const float q = (lds[L_QP + lane] + lds[L_QP + 64 + lane]) + (lds[L_QP + 128 + lane] + lds[L_QP + 192 + lane]);
      st_gran(ws, WS_QG + ((rb * 16 + kvh) * 512 + kh * 256 + cg * 64 + lane) * 8, tag, q);
      if (lane == 0) l_cnt[5] = tag;
    }
    STAMP(2);
    pause(2 * QGD);
    {
      v2u x[8];
      sweep<8, 512, 8>(rw, WS_QG + ((hw * 16 + kvh) * 512 + lane) * 8, tag, x);
#pragma unroll
      for (int k = 0; k < 4; ++k) ((unsigned short*)(lds + L_Q))[hw * 256 + k * 64 + lane] = f2bf(__uint_as_float(x[k].x) + __uint_as_float(x[k + 4].x));
      lds_arrive(l_cnt + 1, lane);
    }
    STAMP(3);
#pragma nounroll
    for (int br = 0; br < 2; ++br) {
      lds_wait<8>(l_cnt + 2 + br, 8u);
      const float* ml = lds + L_ML + br * 32 + hw * 8;
      const f32x4 m0 = *(const f32x4*)(ml), m1 = *(const f32x4*)(ml + 4), l0 = *(const f32x4*)(ml + 16), l1 = *(const f32x4*)(ml + 20);
      const float M = fmax_(fmax_(fmax_(m0.x, m0.y), fmax_(m0.z, m0.w)), fmax_(fmax_(m1.x, m1.y), fmax_(m1.z, m1.w)));
      float L = 0.f;
#pragma unroll
      for (int w = 0; w < 8; ++w) L = fmaf(__builtin_amdgcn_exp2f((w < 4 ? m0[w & 3] : m1[w & 3]) - M), w < 4 ? l0[w & 3] : l1[w & 3], L);
      const int mg = WS_MG + ((br * 16 + kvh) * 2 + hw) * 1024;
      if (lane < 8) st_gran(ws, mg + (ch & 15) * 64 + lane * 8, tag, (lane & 1) ? L : M);
      if (br == 1) {
        const int d1 = WS_PG + (256 + ch) * PG_ROW, d2 = WS_YG + (rb * 128 + pidx) * YG_ROW;
        unsigned u = __builtin_amdgcn_raw_buffer_load_b32(rw, hw == 0 ? (lane < 32 ? d1 + lane * 64 : lane < 48 ? d2 + 2048 + lane * 64 : OOB) : d2 + lane * 64, 0, AUX_SC1);
        if (lane == 0) u ^= *(const GAS unsigned*)((const unsigned char*)wop + (size_t)(kvh * 256 + s8 * 32 + hw * 31) * DM * 2);
        asm volatile("" :: "v"(u));
      }
      pause(2 * QGD);
      v2u x[1];
      sweep<1, 0, 8>(rw, mg + n * 64 + (g & 1) * 8, tag, x);
      auto sw = __builtin_amdgcn_permlane16_swap(x[0].x, x[0].x, false, false);
      const float Ms = __uint_as_float(sw[0]), Ls = __uint_as_float(sw[1]);
      float Mx = fmax_(Ms, dpp<XOR1>(Ms)); Mx = fmax_(Mx, dpp<XOR2>(Mx)); Mx = fmax_(Mx, dpp<HMIR>(Mx)); Mx = fmax_(Mx, dpp<MIR>(Mx));
      float Lt = __builtin_amdgcn_exp2f(Ms - Mx) * Ls; Lt += dpp<XOR1>(Lt); Lt += dpp<XOR2>(Lt); Lt += dpp<HMIR>(Lt); Lt += dpp<MIR>(Lt);
      const float inv = 1.0f / Lt;
      float c[8];
#pragma unroll
      for (int w = 0; w < 8; ++w) c[w] = __builtin_amdgcn_exp2f((w < 4 ? m0[w & 3] : m1[w & 3]) - Mx) * inv;
      const unsigned oa = lds_addr(lds + L_O + br * 2048 + hw * 128 + lane);
      GAS unsigned char* dst = (GAS unsigned char*)ws + WS_PG + (br * 256 + ch) * PG_ROW + (hw * 128 + lane) * 8;
      asm volatile("" : "+v"(dst));
      float o0 = 0.f, o1 = 0.f;
      { const v2u v = lds_take2<8>(oa); o0 = c[0] * __uint_as_float(v.x); o1 = c[0] * __uint_as_float(v.y); }
#pragma unroll
      for (int w = 1; w < 8; ++w) { const v2u v = lds_take2<0>(oa + w * 1024); o0 = fmaf(c[w], __uint_as_float(v.x), o0); o1 = fmaf(c[w], __uint_as_float(v.y), o1); }
      __hip_atomic_store((gu64*)dst, ((unsigned long long)tag << 32) | __float_as_uint(o0), RLX_AGENT);
      __hip_atomic_store((gu64*)(dst + 512), ((unsigned long long)tag << 32) | __float_as_uint(o1), RLX_AGENT);
      if (hw == 0 && lane == 0) __hip_atomic_store(l_cnt + 4, (unsigned)br + 1u, RLX_WG);
    }
    STAMP(8);
  } else {
    St z;
    const int phs = PAT == 2 ? (wg >> 3) & 7 : wave;
    z.lds = lds; z.lane = lane; z.wave = wave; z.mask = mask;
    z.rs[0] = rk0;
    z.rs[1] = mkrs((unsigned char*)a.v[0] + (size_t)ch * 262144, 262144);
    z.rs[2] = mkrs((unsigned char*)a.k[1] + (size_t)ch * 262144, 262144);
    z.rs[3] = mkrs((unsigned char*)a.v[1] + (size_t)ch * 262144, 262144);
    z.vk = ((lane >> 2) & 3) * 256 + g * 64 + (lane & 3) * 16; z.vv = lane * 16; z.so = wave * WST;
    GAS unsigned short* outp = (GAS unsigned short*)a.out;
    if (wave == 0) {
      unsigned long long ta;
      asm volatile("s_waitcnt vmcnt(0)\n\ts_memrealtime %0\n\ts_waitcnt lgkmcnt(0)" : "=s"(ta) : "v"(probe) : "memory");
      if (lane == 0) __hip_atomic_store(l_cnt + 6, ((unsigned)ta << 4) | 1u, RLX_WG);
    }
    z.tb = lds_wait<1>(l_cnt + 6, 1u) + (unsigned)(OFF16 + ((phs * DT16) >> 3));
    STAMP(1);
    Req<0, PRE>::go(z);
    STAMP(4);
    lds_wait<1>(l_cnt + 1, 2u);
    const unsigned tag = __builtin_amdgcn_readfirstlane(l_cnt[5]);
    STAMP(5);
    Con<0, PRE - DEP + 1, PRE - 1>::go(z);
    Run<PRE, 17>::go(z);
    STAMP(14);
    Run<17, 25>::go(z);
    STAMP(15);
    Run<25, 32>::go(z);
    STAMP(6);
    Con<32 - DEP + 1, 32, 31>::go(z);
    STAMP(7);
    const unsigned oba = lds_addr(lds + L_OB + g * 8);
    if (wave < 5) {
      const __amdgpu_buffer_rsrc_t ro = __builtin_amdgcn_make_buffer_rsrc((unsigned char*)wop + ((size_t)(kvh * 256 + s8 * 32) * DM) * 2, 0, 32 * DM * 2, RSRC3);
      const int wcol = wave * 128 + n * 8;
      const int vwo = (!NOWO && wcol < DM) ? g * (DM * 2) + wcol * 2 : OOB;
      v4u Wo[8];
      z.tb += (unsigned)(WO16 + wave * WOS16 - ((phs * DT16) >> 3));
      gate_next(z.tb);
#pragma unroll
      for (int i = 0; i < 8; ++i) Wo[i] = __builtin_amdgcn_raw_buffer_load_b128(ro, vwo, i * 4 * DM * 2, AUX_NT);
      GAS unsigned char* dst = (GAS unsigned char*)ws + WS_YG + (rb * 128 + pidx) * YG_ROW + (wave * 128 + lane) * 8;
      asm volatile("" : "+v"(dst), "+v"(Wo[0]), "+v"(Wo[1]), "+v"(Wo[2]), "+v"(Wo[3]), "+v"(Wo[4]), "+v"(Wo[5]), "+v"(Wo[6]), "+v"(Wo[7]));
      STAMP(2);
      v4u oa, ob;
#pragma nounroll
      for (unsigned spins = 0; spins < LSPIN_CAP; ++spins) {
        asm volatile("ds_read_b128 %0, %2\n\tds_read_b128 %1, %2 offset:16\n\ts_waitcnt lgkmcnt(0)" : "=&v"(oa), "=&v"(ob) : "v"(oba) : "memory");
        if (__all(oa.x != EMPTY_B && oa.y != EMPTY_B && oa.z != EMPTY_B && oa.w != EMPTY_B && ob.x != EMPTY_B && ob.y != EMPTY_B && ob.z != EMPTY_B && ob.w != EMPTY_B)) break;
      }
      STAMP(10);
      f32x4 Y0 = {0.f, 0.f, 0.f, 0.f}, Y1 = {0.f, 0.f, 0.f, 0.f};
#pragma unroll
      for (int i = 0; i < 4; ++i) { Y0 = mfma(diag(oa[i], mask), Wo[i], Y0); Y1 = mfma(diag(ob[i], mask), Wo[4 + i], Y1); }
      const f32x4 Y = Y0 + Y1;
      if (wcol < DM) {
        __hip_atomic_store((gu64*)dst, ((unsigned long long)tag << 32) | __float_as_uint(g < 2 ? Y.x : Y.z), RLX_AGENT);
        __hip_atomic_store((gu64*)(dst + 512), ((unsigned long long)tag << 32) | __float_as_uint(g < 2 ? Y.y : Y.w), RLX_AGENT);
      }
      STAMP(11);
    } else if (wave == 5) {
      const int po = WS_PG + (rb * 256 + kvh * 16 + (lane >> 5)) * PG_ROW + (32 * s8 + (lane & 31)) * 8;
      v2u x[8];
      lds_wait<0>(l_cnt + 4, (unsigned)rb + 1u);
      pause(H1D);
      sweep<8, 2 * PG_ROW, 1>(rw, po, tag, x);
      STAMP(9);
      const float o = x32_sum(((__uint_as_float(x[0].x) + __uint_as_float(x[1].x)) + (__uint_as_float(x[2].x) + __uint_as_float(x[3].x)))
                            + ((__uint_as_float(x[4].x) + __uint_as_float(x[5].x)) + (__uint_as_float(x[6].x) + __uint_as_float(x[7].x))));
      if (lane < 32) ((unsigned*)(lds + L_OB))[(lane & 3) * 8 + (lane >> 2)] = dup_bf16(o);
    } else if (wave == 6 || pidx < 16) {
      const int pos = wave == 6 ? pidx * 4 : 512 + (pidx >> 1) * 16 + (pidx & 1) * 4;
      const int so = WS_YG + (rb * 128 + (lane >> 2)) * YG_ROW + (pos + (lane & 3)) * 8;
      const int p = pos + (lane & 3), pl = p & 63;
      const int col = (p & ~127) + 8 * (pl & 15) + 4 * ((pl >> 4) & 1) + 2 * (pl >> 5) + ((p >> 6) & 1);
      GAS unsigned short* op = outp + rb * DM + col;
      asm volatile("" : "+v"(op));
      v2u x[8];
      z.tb += (unsigned)(WO16 + 4 * WOS16 + 1200 - ((phs * DT16) >> 3));
      gate_next(z.tb);
#pragma nounroll
      for (unsigned spins = 0; spins < LSPIN_CAP; ++spins) {
        if (__builtin_amdgcn_readfirstlane(__hip_atomic_load((unsigned*)(lds + L_OB), RLX_WG)) != EMPTY_B) break;
        __builtin_amdgcn_s_sleep(1);
      }
      pause(H2D);
      sweep<8, 16 * YG_ROW, 1>(rw, so, tag, x);
      STAMP(12);
      float y = ((__uint_as_float(x[0].x) + __uint_as_float(x[1].x)) + (__uint_as_float(x[2].x) + __uint_as_float(x[3].x)))
              + ((__uint_as_float(x[4].x) + __uint_as_float(x[5].x)) + (__uint_as_float(x[6].x) + __uint_as_float(x[7].x)));
      y += dpp<ROR4>(y); y += dpp<ROR8>(y);
      y = xg_sum(y);
      if (lane < 4) *op = f2bf(y);
      if (rb == 0 && pos == 0 && lane == 0) __hip_atomic_store((gu32*)(ws + WS_EPOCH), tag, RLX_AGENT);
      STAMP(13);
    }
  }
  STAMP_OUT();
}

extern "C" void kernel_launch(void* const* d_in, const int* in_sizes, int n_in, void* d_out, int out_size, void* d_ws, size_t ws_size, hipStream_t stream) {
  Args a;
  a.x[0] = d_in[0]; a.k[0] = d_in[1]; a.v[0] = d_in[2]; a.wq[0] = d_in[3]; a.wo[0] = d_in[4];
  a.x[1] = d_in[5]; a.k[1] = d_in[6]; a.v[1] = d_in[7]; a.wq[1] = d_in[8]; a.wo[1] = d_in[9];
  a.out = d_out; a.ws = d_ws;
  gqa2_fused<<<NWG, NTHR, 0, stream>>>(a);
}
```

```cpp
#define DT16 1840
#define OFF16 1750
#include <hip/hip_runtime.h>
#include <cstdint>

typedef unsigned v4u __attribute__((ext_vector_type(4)));
typedef unsigned v2u __attribute__((ext_vector_type(2)));
typedef float f32x4 __attribute__((ext_vector_type(4)));
typedef __bf16 bf16x8 __attribute__((ext_vector_type(8)));
typedef __bf16 bf16x2 __attribute__((ext_vector_type(2)));
typedef float f32x2 __attribute__((ext_vector_type(2)));
#define GAS __attribute__((address_space(1)))
#define LAS __attribute__((address_space(3)))
typedef GAS unsigned gu32;
typedef GAS unsigned long long gu64;
#define RLX_AGENT __ATOMIC_RELAXED, __HIP_MEMORY_SCOPE_AGENT
#define RLX_WG __ATOMIC_RELAXED, __HIP_MEMORY_SCOPE_WORKGROUP

#define STAMPS 0
#define ANC -2
#define PRE 8
#define DEP 3
#define LIM 2
#define WO16 0
#define WOS16 600
#define QGD 75
#define PTH16 400
#define SLK16 100000
#define H1D 40
#define H2D 70
#define PREW 1
#define HBAR 1
#define PAT 1
#define JST 8192
#define WST 1024
#define SOFT16 0
#define NSOFT 6
#define H2G16 2000
#define WOA16 0
#define WOSA16 600
#define NOWO 0

constexpr int DM = 576, NQ = 4096;
constexpr int NWG = 256, NTHR = 640;
constexpr int AUX_NT = 2, AUX_SC1 = 16;
constexpr int RSRC3 = 0x00020000;
constexpr int OOB = 0x7fff0000;
constexpr int WS_EPOCH = 0;
constexpr int WS_QG = 4096;
constexpr int WS_MG = WS_QG + 32 * 4096;
constexpr int WS_PG = WS_MG + 64 * 1024;
constexpr int PG_ROW = 256 * 8;
constexpr int WS_YG = WS_PG + 512 * PG_ROW;
constexpr int YG_ROW = 640 * 8;
constexpr int WS_ST = WS_YG + 256 * YG_ROW;
constexpr unsigned TAG_STEP = 0x9E3779B9u;
constexpr unsigned SPIN_CAP = 1u << 17, LSPIN_CAP = 1u << 22;
constexpr float C2 = 0.088388347648318440f * 1.4426950408889634f;
constexpr unsigned EMPTY_O = 0xffffffffu, EMPTY_B = 0x0000ffffu;

struct Args {
  const void* x[2]; const void* k[2]; const void* v[2]; const void* wq[2]; const void* wo[2];
  void* out; void* ws;
};

#define SB() __builtin_amdgcn_sched_barrier(0)
__device__ __forceinline__ f32x4 mfma(v4u a, v4u b, f32x4 c) {
  return __builtin_amdgcn_mfma_f32_16x16x32_bf16(__builtin_bit_cast(bf16x8, a), __builtin_bit_cast(bf16x8, b), c, 0, 0, 0);
}
__device__ __forceinline__ unsigned dup_bf16(float f) {
  f32x2 v = {f, f};
  return __builtin_bit_cast(unsigned, __builtin_convertvector(v, bf16x2));
}
__device__ __forceinline__ unsigned short f2bf(float f) { return __builtin_bit_cast(unsigned short, (__bf16)f); }
__device__ __forceinline__ v4u diag(unsigned pp, v4u mask) { v4u r = {pp & mask.x, pp & mask.y, pp & mask.z, pp & mask.w}; return r; }
template <int CTRL> __device__ __forceinline__ float dpp(float x) {
  return __builtin_bit_cast(float, __builtin_amdgcn_mov_dpp(__builtin_bit_cast(int, x), CTRL, 0xf, 0xf, true));
}
constexpr int XOR1 = 0xB1, XOR2 = 0x4E, HMIR = 0x141, MIR = 0x140, ROR8 = 0x128, ROR4 = 0x124;
__device__ __forceinline__ float fmax_(float a, float b) { return __builtin_fmaxf(a, b); }
__device__ __forceinline__ float xg_max(float x) {
  auto s = __builtin_amdgcn_permlane16_swap(__float_as_uint(x), __float_as_uint(x), false, false);
  x = fmax_(__uint_as_float(s[0]), __uint_as_float(s[1]));
  auto t = __builtin_amdgcn_permlane32_swap(__float_as_uint(x), __float_as_uint(x), false, false);
  return fmax_(__uint_as_float(t[0]), __uint_as_float(t[1]));
}
__device__ __forceinline__ float x32_sum(float x) {
  auto t = __builtin_amdgcn_permlane32_swap(__float_as_uint(x), __float_as_uint(x), false, false);
  return __uint_as_float(t[0]) + __uint_as_float(t[1]);
}
__device__ __forceinline__ float xg_sum(float x) {
  auto s = __builtin_amdgcn_permlane16_swap(__float_as_uint(x), __float_as_uint(x), false, false);
  return x32_sum(__uint_as_float(s[0]) + __uint_as_float(s[1]));
}
__device__ __forceinline__ void st_gran(unsigned char* ws, int off, unsigned tag, float v) {
  __hip_atomic_store((gu64*)(ws + off), ((unsigned long long)tag << 32) | __float_as_uint(v), RLX_AGENT);
}
__device__ __forceinline__ v2u ldg(__amdgpu_buffer_rsrc_t rs, int off) { return __builtin_amdgcn_raw_buffer_load_b64(rs, off, 0, AUX_SC1); }
template <int N, int STEP, int SLEEP> __device__ __forceinline__ void sweep(__amdgpu_buffer_rsrc_t rs, int off, unsigned tag, v2u (&x)[N]) {
#pragma unroll
  for (int k = 0; k < N; ++k) x[k] = ldg(rs, off + k * STEP);
  SB();
  bool ok = true;
#pragma unroll
  for (int k = 0; k < N; ++k) ok &= x[k].y == tag;
  if (__builtin_expect(__all(ok), 1)) return;
#pragma nounroll
  for (unsigned spins = 0; spins < SPIN_CAP; ++spins) {
    __builtin_amdgcn_s_sleep(SLEEP);
    v2u y[N];
#pragma unroll
    for (int k = 0; k < N; ++k) y[k] = ldg(rs, x[k].y == tag ? OOB : off + k * STEP);
    SB();
    ok = true;
#pragma unroll
    for (int k = 0; k < N; ++k) { x[k] = x[k].y == tag ? x[k] : y[k]; ok &= x[k].y == tag; }
    if (__all(ok)) break;
  }
}
__device__ __forceinline__ v4u mkrs(const void* p, unsigned bytes) {
  const unsigned long long u = (unsigned long long)p;
  v4u r = {(unsigned)u, (unsigned)(u >> 32) & 0xffffu, bytes, (unsigned)RSRC3};
  return r;
}
__device__ __forceinline__ void tile_load(v4u (&d)[4], int voff, v4u rs, int soff) {
  asm volatile("buffer_load_dwordx4 %0, %4, %5, %6 offen nt\n\t"
               "buffer_load_dwordx4 %1, %4, %5, %7 offen nt\n\t"
               "buffer_load_dwordx4 %2, %4, %5, %8 offen nt\n\t"
               "buffer_load_dwordx4 %3, %4, %5, %9 offen nt"
               : "=&v"(d[0]), "=&v"(d[1]), "=&v"(d[2]), "=&v"(d[3]) : "v"(voff), "s"(rs), "s"(soff), "s"(soff + JST), "s"(soff + 2 * JST), "s"(soff + 3 * JST));
}
template <int N> __device__ __forceinline__ void tile_wait(v4u (&d)[4]) {
  asm volatile("s_waitcnt vmcnt(%4)" : "+v"(d[0]), "+v"(d[1]), "+v"(d[2]), "+v"(d[3]) : "i"(N));
}
template <int N> __device__ __forceinline__ void piece_wait(v4u& d) { asm volatile("s_waitcnt vmcnt(%1)" : "+v"(d) : "i"(N)); }
__device__ __forceinline__ void pause(int ticks) {
  unsigned due;
  asm volatile("s_memrealtime vcc\n\t"
               "s_waitcnt lgkmcnt(0)\n\t"
               "s_add_i32 %0, vcc_lo, %1\n"
               ".Lg0_%=:\n\t"
               "s_sleep 1\n\t"
               "s_memrealtime vcc\n\t"
               "s_waitcnt lgkmcnt(0)\n\t"
               "s_sub_i32 vcc_lo, vcc_lo, %0\n\t"
               "s_cmp_lt_i32 vcc_lo, 0\n\t"
               "s_cbranch_scc1 .Lg0_%="
               : "=&s"(due) : "i"(ticks) : "scc", "vcc", "memory");
}
__device__ __forceinline__ void gate_next(unsigned& due) {
  asm volatile("s_branch .Lg1_%=\n"
               ".Lg0_%=:\n\t"
               "s_sleep 1\n"
               ".Lg1_%=:\n\t"
               "s_memrealtime vcc\n\t"
               "s_waitcnt lgkmcnt(0)\n\t"
               "s_lshl_b32 vcc_lo, vcc_lo, 4\n\t"
               "s_sub_i32 vcc_lo, vcc_lo, %0\n\t"
               "s_cmp_lt_i32 vcc_lo, 0\n\t"
               "s_cbranch_scc1 .Lg0_%=\n\t"
               "s_setprio 0\n\t"
               "s_cmp_lt_i32 vcc_lo, %1\n\t"
               "s_cbranch_scc1 .Lg2_%=\n\t"
               "s_setprio 1\n"
               ".Lg2_%=:\n\t"
               "s_sub_i32 vcc_lo, vcc_lo, %2\n\t"
               "s_max_i32 vcc_lo, vcc_lo, 0\n\t"
               "s_add_i32 %0, %0, vcc_lo\n\t"
               "s_add_i32 %0, %0, %3"
               : "+s"(due) : "i"(PTH16), "i"(SLK16), "i"(DT16) : "scc", "vcc", "memory");
}
__device__ __forceinline__ void lds_barrier() {
  asm volatile("s_waitcnt lgkmcnt(0)" ::: "memory");
  __builtin_amdgcn_s_barrier();
  asm volatile("" ::: "memory");
}
__device__ __forceinline__ void lds_arrive(unsigned* c, int lane) {
  asm volatile("s_waitcnt lgkmcnt(0)" ::: "memory");
  if (lane == 0) __hip_atomic_fetch_add(c, 1u, RLX_WG);
}
template <int SLEEP> __device__ __forceinline__ unsigned lds_wait(unsigned* c, unsigned want) {
  unsigned v = 0;
#pragma nounroll
  for (unsigned spins = 0; spins < LSPIN_CAP; ++spins) {
    v = __builtin_amdgcn_readfirstlane(__hip_atomic_load(c, RLX_WG));
    if (v >= want) break;
    __builtin_amdgcn_s_sleep(SLEEP);
  }
  asm volatile("" ::: "memory");
  return v;
}
__device__ __forceinline__ unsigned lds_addr(const void* p) { return (unsigned)(size_t)(const LAS void*)p; }
template <int SLEEP> __device__ __forceinline__ v2u lds_take2(unsigned addr) {
  v2u v;
#pragma nounroll
  for (unsigned spins = 0; spins < LSPIN_CAP; ++spins) {
    asm volatile("ds_read2st64_b32 %0, %1 offset1:1\n\ts_waitcnt lgkmcnt(0)" : "=&v"(v) : "v"(addr) : "memory");
    if (__all(v.x != EMPTY_O && v.y != EMPTY_O)) break;
    if (SLEEP) __builtin_amdgcn_s_sleep(SLEEP);
  }
  return v;
}
#define STAMP(i) do {} while (0)
#define STAMP_OUT() do {} while (0)

constexpr int L_O = 0;
constexpr int L_ML = L_O + 4096;
constexpr int L_QP = L_ML + 64;
constexpr int L_Q = L_QP + 256;
constexpr int L_OB = L_Q + 256;
constexpr int L_CNT = L_OB + 32;
constexpr int L_END = L_CNT + 8;

struct St {
  v4u T[32][4];
  float Sc[8][4];
  unsigned P[8][4];
  v4u Q, mask;
  f32x4 O;
  v4u rs[4];
  int vk, vv, so;
  unsigned tb;
  float* lds;
  int lane, wave;
};

template <int I> __device__ __forceinline__ void request(St& z) {
  if constexpr (4 * I > LIM) asm volatile("s_waitcnt vmcnt(%0)" :: "i"(LIM) : "memory");
  gate_next(z.tb);
  if constexpr (SOFT16 && I < NSOFT) z.tb += SOFT16;
  tile_load(z.T[I], (I & 8) ? z.vv : z.vk, z.rs[I >> 3], z.so + (I & 7) * 32768);
  SB();
}
template <int I, int W> __device__ __forceinline__ void consume(St& z) {
  const int g = z.lane >> 4, n = z.lane & 15;
  constexpr int br = I >> 4;
  if constexpr ((I & 15) == 0) {
    z.Q = *(const v4u*)((const unsigned short*)(z.lds + L_Q) + br * 256 + (n >> 3) * 128 + 32 * g + 8 * (n & 3));
  }
  if constexpr ((I & 8) == 0) {
    tile_wait<W>(z.T[I]);
#pragma unroll
    for (int j = 0; j < 4; ++j) {
      const f32x4 d = mfma(z.T[I][j], z.Q, f32x4{0.f, 0.f, 0.f, 0.f});
      z.Sc[I & 7][j] = (dpp<0x00>(d.x) + dpp<0x55>(d.y)) + (dpp<0xAA>(d.z) + dpp<0xFF>(d.w));
    }
    if constexpr ((I & 7) == 7) {
      float m = z.Sc[0][0];
#pragma unroll
      for (int t = 0; t < 8; ++t)
#pragma unroll
        for (int j = 0; j < 4; ++j) m = fmax_(m, z.Sc[t][j]);
      const float m2 = xg_max(m) * C2;
      float l = 0.f;
#pragma unroll
      for (int t = 0; t < 8; ++t) {
        float p[4];
#pragma unroll
        for (int j = 0; j < 4; ++j) { p[j] = __builtin_amdgcn_exp2f(fmaf(z.Sc[t][j], C2, -m2)); z.P[t][j] = dup_bf16(p[j]); }
        l += (p[0] + p[1]) + (p[2] + p[3]);
        asm volatile("" : "+v"(z.P[t][0]), "+v"(z.P[t][1]), "+v"(z.P[t][2]), "+v"(z.P[t][3]));
      }
      l = xg_sum(l);
      if (g == 0 && (n & 7) == 0) { z.lds[L_ML + br * 32 + (n >> 3) * 8 + z.wave] = m2; z.lds[L_ML + br * 32 + 16 + (n >> 3) * 8 + z.wave] = l; }
      lds_arrive((unsigned*)(z.lds + L_CNT) + 2 + br, z.lane);
      z.O = f32x4{0.f, 0.f, 0.f, 0.f};
    } else {
      asm volatile("" : "+v"(z.Sc[I & 7][0]), "+v"(z.Sc[I & 7][1]), "+v"(z.Sc[I & 7][2]), "+v"(z.Sc[I & 7][3]));
    }
  } else if constexpr (W == 0) {
    v4u A[4];
#pragma unroll
    for (int j = 0; j < 4; ++j) A[j] = diag(z.P[I & 7][j], z.mask);
    float* dst = z.lds + L_O + br * 2048 + z.wave * 256 + (g >> 1) * 128 + 8 * n + 4 * (g & 1);
    unsigned da = lds_addr(dst);
    asm volatile("" : "+v"(A[0]), "+v"(A[1]), "+v"(A[2]), "+v"(A[3]), "+v"(da));
    piece_wait<3>(z.T[I][0]); z.O = mfma(A[0], z.T[I][0], z.O); SB();
    piece_wait<2>(z.T[I][1]); z.O = mfma(A[1], z.T[I][1], z.O); SB();
    piece_wait<1>(z.T[I][2]); z.O = mfma(A[2], z.T[I][2], z.O); SB();
    piece_wait<0>(z.T[I][3]); z.O = mfma(A[3], z.T[I][3], z.O);
    *(LAS f32x4*)(size_t)da = z.O;
  } else {
    tile_wait<W>(z.T[I]);
#pragma unroll
    for (int j = 0; j < 4; ++j) z.O = mfma(diag(z.P[I & 7][j], z.mask), z.T[I][j], z.O);
    asm volatile("" : "+v"(z.O));
    if constexpr ((I & 7) == 7) *(f32x4*)(z.lds + L_O + br * 2048 + z.wave * 256 + (g >> 1) * 128 + 8 * n + 4 * (g & 1)) = z.O;
  }
  SB();
}
template <int I, int N> struct Run {
  static __device__ __forceinline__ void go(St& z) { request<I>(z); consume<I - DEP + 1, 4 * (DEP - 1)>(z); Run<I + 1, N>::go(z); }
};
template <int N> struct Run<N, N> { static __device__ __forceinline__ void go(St&) {} };
template <int I, int N> struct Req { static __device__ __forceinline__ void go(St& z) { request<I>(z); Req<I + 1, N>::go(z); } };
template <int N> struct Req<N, N> { static __device__ __forceinline__ void go(St&) {} };
template <int I, int N, int LAST> struct Con {
  static __device__ __forceinline__ void go(St& z) { consume<I, 4 * (LAST - I)>(z); Con<I + 1, N, LAST>::go(z); }
};
template <int N, int LAST> struct Con<N, N, LAST> { static __device__ __forceinline__ void go(St&) {} };

__global__ void __launch_bounds__(NTHR, 3) gqa2_fused(Args a) {
  __shared__ __attribute__((aligned(16))) float lds[L_END];
  unsigned* const l_cnt = (unsigned*)(lds + L_CNT);
  STAMP(0);
  const unsigned t0 = (unsigned)__builtin_amdgcn_s_memrealtime();
  const int tid = threadIdx.x, lane = tid & 63, wave = __builtin_amdgcn_readfirstlane(tid >> 6);
  const int g = lane >> 4, n = lane & 15;
  const int wg = blockIdx.x;
  const int ch = (wg & 7) << 5 | wg >> 3;
  const int kvh = ch >> 4, rb = (ch >> 3) & 1, s8 = ch & 7;
  if (tid < 512) {
    const v4u e = {EMPTY_O, EMPTY_O, EMPTY_O, EMPTY_O};
    *(v4u*)(lds + L_O + tid * 4) = e; *(v4u*)(lds + L_O + 2048 + tid * 4) = e;
  } else if (tid < 512 + 40) {
    ((unsigned*)(lds + L_OB))[tid - 512] = tid < 512 + 32 ? EMPTY_B : 0u;
  }
  asm volatile("" : "+s"(a.x[0]), "+s"(a.x[1]), "+s"(a.k[0]), "+s"(a.k[1]), "+s"(a.v[0]), "+s"(a.v[1]), "+s"(a.wq[0]), "+s"(a.wq[1]), "+s"(a.wo[0]), "+s"(a.wo[1]), "+s"(a.out), "+s"(a.ws));
  unsigned char* const ws = (unsigned char*)a.ws;
  const void* const wop = rb ? a.wo[1] : a.wo[0];
  const v4u rk0 = mkrs((unsigned char*)a.k[0] + (size_t)ch * 262144, 262144);
  lds_barrier();
  const __amdgpu_buffer_rsrc_t rw = __builtin_amdgcn_make_buffer_rsrc(ws, 0, WS_ST, RSRC3);
  v4u mask;
  {
    const unsigned half = (n & 1) ? 0xffff0000u : 0x0000ffffu; const int r = (n & 7) >> 1;
    mask.x = r == 0 ? half : 0u; mask.y = r == 1 ? half : 0u; mask.z = r == 2 ? half : 0u; mask.w = r == 3 ? half : 0u;
  }
  const int pidx = kvh * 8 + s8;
  if (__builtin_expect(wave >= 8, 1)) {
    __builtin_amdgcn_s_setprio(3);
    const int hw = wave - 8, cg = s8 & 3, kh = s8 >> 2, sub = 2 * g + (n >> 3);
    const __amdgpu_buffer_rsrc_t rq = __builtin_amdgcn_make_buffer_rsrc((unsigned char*)(rb ? a.wq[1] : a.wq[0]) + ((size_t)(kh * 288 + hw * 144) * NQ + kvh * 256 + cg * 64) * 2, 0, 143 * NQ * 2 + 128, RSRC3);
    const __amdgpu_buffer_rsrc_t rx = __builtin_amdgcn_make_buffer_rsrc((unsigned char*)(rb ? a.x[1] : a.x[0]) + (kh * 288 + hw * 144) * 2, 0, 144 * 2, RSRC3);
    v4u wqv[18];
#pragma unroll
    for (int i = 0; i < 18; ++i) wqv[i] = __builtin_amdgcn_raw_buffer_load_b128(rq, sub * (NQ * 2) + (n & 7) * 16, i * 8 * NQ * 2, AUX_NT);
    const unsigned xa = __builtin_amdgcn_raw_buffer_load_b32(rx, n * 16 + g * 4, 0, 0), xb = __builtin_amdgcn_raw_buffer_load_b32(rx, n * 16 + g * 4 + 256, 0, 0);
    const unsigned ep = __builtin_amdgcn_raw_buffer_load_b32(rw, lane == 0 ? WS_EPOCH : OOB, 0, AUX_SC1);
    SB();
    f32x4 a0 = {0.f, 0.f, 0.f, 0.f}, a1 = {0.f, 0.f, 0.f, 0.f};
    const unsigned sel = n < 8 ? 0x01000100u : 0x03020302u;
#define QSTEP(acc, i, src, k) acc = mfma(diag(__builtin_amdgcn_perm(0u, (unsigned)__builtin_amdgcn_mov_dpp((int)(src), 0x150 + (k), 0xf, 0xf, true), sel), mask), wqv[i], acc)
    QSTEP(a0, 0, xa, 0); QSTEP(a1, 1, xa, 1); QSTEP(a0, 2, xa, 2); QSTEP(a1, 3, xa, 3); QSTEP(a0, 4, xa, 4); QSTEP(a1, 5, xa, 5);
    QSTEP(a0, 6, xa, 6); QSTEP(a1, 7, xa, 7); QSTEP(a0, 8, xa, 8); QSTEP(a1, 9, xa, 9); QSTEP(a0, 10, xa, 10); QSTEP(a1, 11, xa, 11);
    QSTEP(a0, 12, xa, 12); QSTEP(a1, 13, xa, 13); QSTEP(a0, 14, xa, 14); QSTEP(a1, 15, xa, 15); QSTEP(a0, 16, xb, 0); QSTEP(a1, 17, xb, 1);
    if ((g >> 1) == (n >> 3)) *(f32x4*)(lds + L_QP + (hw * 2 + (n >> 3)) * 64 + 8 * (n & 7) + 4 * (g & 1)) = a0 + a1;
    const unsigned tag = __builtin_amdgcn_readfirstlane(ep) + TAG_STEP;
    STAMP(1);
    lds_arrive(l_cnt + 0, lane);
    if (hw == 0) {
      lds_wait<1>(l_cnt + 0, 2u);
      const float q = (lds[L_QP + lane] + lds[L_QP + 64 + lane]) + (lds[L_QP + 128 + lane] + lds[L_QP + 192 + lane]);
      st_gran(ws, WS_QG + ((rb * 16 + kvh) * 512 + kh * 256 + cg * 64 + lane) * 8, tag, q);
      if (lane == 0) l_cnt[5] = tag;
    }
    STAMP(2);
    pause(2 * QGD);
    {
      v2u x[8];
      sweep<8, 512, 8>(rw, WS_QG + ((hw * 16 + kvh) * 512 + lane) * 8, tag, x);
#pragma unroll
      for (int k = 0; k < 4; ++k) ((unsigned short*)(lds + L_Q))[hw * 256 + k * 64 + lane] = f2bf(__uint_as_float(x[k].x) + __uint_as_float(x[k + 4].x));
      lds_arrive(l_cnt + 1, lane);
    }
    STAMP(3);
#pragma nounroll
    for (int br = 0; br < 2; ++br) {
      lds_wait<8>(l_cnt + 2 + br, 8u);
      const float* ml = lds + L_ML + br * 32 + hw * 8;
      const f32x4 m0 = *(const f32x4*)(ml), m1 = *(const f32x4*)(ml + 4), l0 = *(const f32x4*)(ml + 16), l1 = *(const f32x4*)(ml + 20);
      const float M = fmax_(fmax_(fmax_(m0.x, m0.y), fmax_(m0.z, m0.w)), fmax_(fmax_(m1.x, m1.y), fmax_(m1.z, m1.w)));
      float L = 0.f;
#pragma unroll
      for (int w = 0; w < 8; ++w) L = fmaf(__builtin_amdgcn_exp2f((w < 4 ? m0[w & 3] : m1[w & 3]) - M), w < 4 ? l0[w & 3] : l1[w & 3], L);
      const int mg = WS_MG + ((br * 16 + kvh) * 2 + hw) * 1024;
      if (lane < 8) st_gran(ws, mg + (ch & 15) * 64 + lane * 8, tag, (lane & 1) ? L : M);
      if (br == 1) {
        const int d1 = WS_PG + (256 + ch) * PG_ROW, d2 = WS_YG + (rb * 128 + pidx) * YG_ROW;
        unsigned u = __builtin_amdgcn_raw_buffer_load_b32(rw, hw == 0 ? (lane < 32 ? d1 + lane * 64 : lane < 48 ? d2 + 2048 + lane * 64 : OOB) : d2 + lane * 64, 0, AUX_SC1);
        if (lane == 0) u ^= *(const GAS unsigned*)((const unsigned char*)wop + (size_t)(kvh * 256 + s8 * 32 + hw * 31) * DM * 2);
        asm volatile("" :: "v"(u));
      }
      pause(2 * QGD);
      v2u x[1];
      sweep<1, 0, 8>(rw, mg + n * 64 + (g & 1) * 8, tag, x);
      auto sw = __builtin_amdgcn_permlane16_swap(x[0].x, x[0].x, false, false);
      const float Ms = __uint_as_float(sw[0]), Ls = __uint_as_float(sw[1]);
      float Mx = fmax_(Ms, dpp<XOR1>(Ms)); Mx = fmax_(Mx, dpp<XOR2>(Mx)); Mx = fmax_(Mx, dpp<HMIR>(Mx)); Mx = fmax_(Mx, dpp<MIR>(Mx));
      float Lt = __builtin_amdgcn_exp2f(Ms - Mx) * Ls; Lt += dpp<XOR1>(Lt); Lt += dpp<XOR2>(Lt); Lt += dpp<HMIR>(Lt); Lt += dpp<MIR>(Lt);
      const float inv = 1.0f / Lt;
      float c[8];
#pragma unroll
      for (int w = 0; w < 8; ++w) c[w] = __builtin_amdgcn_exp2f((w < 4 ? m0[w & 3] : m1[w & 3]) - Mx) * inv;
      const unsigned oa = lds_addr(lds + L_O + br * 2048 + hw * 128 + lane);
      GAS unsigned char* dst = (GAS unsigned char*)ws + WS_PG + (br * 256 + ch) * PG_ROW + (hw * 128 + lane) * 8;
      asm volatile("" : "+v"(dst));
      float o0 = 0.f, o1 = 0.f;
      { const v2u v = lds_take2<8>(oa); o0 = c[0] * __uint_as_float(v.x); o1 = c[0] * __uint_as_float(v.y); }
#pragma unroll
      for (int w = 1; w < 8; ++w) { const v2u v = lds_take2<0>(oa + w * 1024); o0 = fmaf(c[w], __uint_as_float(v.x), o0); o1 = fmaf(c[w], __uint_as_float(v.y), o1); }
      __hip_atomic_store((gu64*)dst, ((unsigned long long)tag << 32) | __float_as_uint(o0), RLX_AGENT);
      __hip_atomic_store((gu64*)(dst + 512), ((unsigned long long)tag << 32) | __float_as_uint(o1), RLX_AGENT);
      if (hw == 0 && lane == 0) __hip_atomic_store(l_cnt + 4, (unsigned)br + 1u, RLX_WG);
    }
    STAMP(8);
  } else {
    St z;
    const int phs = PAT == 2 ? (wg >> 3) & 7 : wave;
    z.lds = lds; z.lane = lane; z.wave = wave; z.mask = mask;
    z.rs[0] = rk0;
    z.rs[1] = mkrs((unsigned char*)a.v[0] + (size_t)ch * 262144, 262144);
    z.rs[2] = mkrs((unsigned char*)a.k[1] + (size_t)ch * 262144, 262144);
    z.rs[3] = mkrs((unsigned char*)a.v[1] + (size_t)ch * 262144, 262144);
    z.vk = ((lane >> 2) & 3) * 256 + g * 64 + (lane & 3) * 16; z.vv = lane * 16; z.so = wave * WST;
    GAS unsigned short* outp = (GAS unsigned short*)a.out;
    z.tb = (t0 << 4) + (unsigned)(OFF16 + ((phs * DT16) >> 3));
    STAMP(1);
    Req<0, PRE>::go(z);
    STAMP(4);
    lds_wait<1>(l_cnt + 1, 2u);
    const unsigned tag = __builtin_amdgcn_readfirstlane(l_cnt[5]);
    STAMP(5);
    Con<0, PRE - DEP + 1, PRE - 1>::go(z);
    Run<PRE, 17>::go(z);
    STAMP(14);
    Run<17, 25>::go(z);
    STAMP(15);
    Run<25, 32>::go(z);
    STAMP(6);
    Con<32 - DEP + 1, 32, 31>::go(z);
    STAMP(7);
    const unsigned oba = lds_addr(lds + L_OB + g * 8);
    if (wave < 5) {
      const __amdgpu_buffer_rsrc_t ro = __builtin_amdgcn_make_buffer_rsrc((unsigned char*)wop + ((size_t)(kvh * 256 + s8 * 32) * DM) * 2, 0, 32 * DM * 2, RSRC3);
      const int wcol = wave * 128 + n * 8;
      const int vwo = (!NOWO && wcol < DM) ? g * (DM * 2) + wcol * 2 : OOB;
      v4u Wo[8];
      z.tb += (unsigned)((rb ? WO16 + wave * WOS16 : WOA16 + wave * WOSA16) - ((phs * DT16) >> 3));
      gate_next(z.tb);
#pragma unroll
      for (int i = 0; i < 8; ++i) Wo[i] = __builtin_amdgcn_raw_buffer_load_b128(ro, vwo, i * 4 * DM * 2, AUX_NT);
      GAS unsigned char* dst = (GAS unsigned char*)ws + WS_YG + (rb * 128 + pidx) * YG_ROW + (wave * 128 + lane) * 8;
      asm volatile("" : "+v"(dst), "+v"(Wo[0]), "+v"(Wo[1]), "+v"(Wo[2]), "+v"(Wo[3]), "+v"(Wo[4]), "+v"(Wo[5]), "+v"(Wo[6]), "+v"(Wo[7]));
      STAMP(2);
      v4u oa, ob;
#pragma nounroll
      for (unsigned spins = 0; spins < LSPIN_CAP; ++spins) {
        asm volatile("ds_read_b128 %0, %2\n\tds_read_b128 %1, %2 offset:16\n\ts_waitcnt lgkmcnt(0)" : "=&v"(oa), "=&v"(ob) : "v"(oba) : "memory");
        if (__all(oa.x != EMPTY_B && oa.y != EMPTY_B && oa.z != EMPTY_B && oa.w != EMPTY_B && ob.x != EMPTY_B && ob.y != EMPTY_B && ob.z != EMPTY_B && ob.w != EMPTY_B)) break;
      }
      STAMP(10);
      f32x4 Y0 = {0.f, 0.f, 0.f, 0.f}, Y1 = {0.f, 0.f, 0.f, 0.f};
#pragma unroll
      for (int i = 0; i < 4; ++i) { Y0 = mfma(diag(oa[i], mask), Wo[i], Y0); Y1 = mfma(diag(ob[i], mask), Wo[4 + i], Y1); }
      const f32x4 Y = Y0 + Y1;
      if (wcol < DM) {
        __hip_atomic_store((gu64*)dst, ((unsigned long long)tag << 32) | __float_as_uint(g < 2 ? Y.x : Y.z), RLX_AGENT);
        __hip_atomic_store((gu64*)(dst + 512), ((unsigned long long)tag << 32) | __float_as_uint(g < 2 ? Y.y : Y.w), RLX_AGENT);
      }
      STAMP(11);
    } else if (wave == 5) {
      const int po = WS_PG + (rb * 256 + kvh * 16 + (lane >> 5)) * PG_ROW + (32 * s8 + (lane & 31)) * 8;
      v2u x[8];
      lds_wait<0>(l_cnt + 4, (unsigned)rb + 1u);
      if (rb) pause(H1D);
      sweep<8, 2 * PG_ROW, 1>(rw, po, tag, x);
      STAMP(9);
      const float o = x32_sum(((__uint_as_float(x[0].x) + __uint_as_float(x[1].x)) + (__uint_as_float(x[2].x) + __uint_as_float(x[3].x)))
                            + ((__uint_as_float(x[4].x) + __uint_as_float(x[5].x)) + (__uint_as_float(x[6].x) + __uint_as_float(x[7].x))));
      if (lane < 32) ((unsigned*)(lds + L_OB))[(lane & 3) * 8 + (lane >> 2)] = dup_bf16(o);
    } else if (wave == 6 || pidx < 16) {
      const int pos = wave == 6 ? pidx * 4 : 512 + (pidx >> 1) * 16 + (pidx & 1) * 4;
      const int so = WS_YG + (rb * 128 + (lane >> 2)) * YG_ROW + (pos + (lane & 3)) * 8;
      const int p = pos + (lane & 3), pl = p & 63;
      const int col = (p & ~127) + 8 * (pl & 15) + 4 * ((pl >> 4) & 1) + 2 * (pl >> 5) + ((p >> 6) & 1);
      GAS unsigned short* op = outp + rb * DM + col;
      asm volatile("" : "+v"(op));
      v2u x[8];
      z.tb += (unsigned)((rb ? WO16 + 4 * WOS16 : WOA16 + 4 * WOSA16) + H2G16 - ((phs * DT16) >> 3));
#pragma nounroll
      for (unsigned spins = 0; spins < LSPIN_CAP; ++spins) {
        if (__builtin_amdgcn_readfirstlane(__hip_atomic_load((unsigned*)(lds + L_OB), RLX_WG)) != EMPTY_B) break;
        __builtin_amdgcn_s_sleep(1);
      }
      pause(H2D);
      gate_next(z.tb);
      sweep<8, 16 * YG_ROW, 1>(rw, so, tag, x);
      STAMP(12);
      float y = ((__uint_as_float(x[0].x) + __uint_as_float(x[1].x)) + (__uint_as_float(x[2].x) + __uint_as_float(x[3].x)))
              + ((__uint_as_float(x[4].x) + __uint_as_float(x[5].x)) + (__uint_as_float(x[6].x) + __uint_as_float(x[7].x)));
      y += dpp<ROR4>(y); y += dpp<ROR8>(y);
      y = xg_sum(y);
      if (lane < 4) *op = f2bf(y);
      if (rb == 0 && pos == 0 && lane == 0) __hip_atomic_store((gu32*)(ws + WS_EPOCH), tag, RLX_AGENT);
      STAMP(13);
    }
  }
  STAMP_OUT();
}

extern "C" void kernel_launch(void* const* d_in, const int* in_sizes, int n_in, void* d_out, int out_size, void* d_ws, size_t ws_size, hipStream_t stream) {
  Args a;
  a.x[0] = d_in[0]; a.k[0] = d_in[1]; a.v[0] = d_in[2]; a.wq[0] = d_in[3]; a.wo[0] = d_in[4];
  a.x[1] = d_in[5]; a.k[1] = d_in[6]; a.v[1] = d_in[7]; a.wq[1] = d_in[8]; a.wo[1] = d_in[9];
  a.out = d_out; a.ws = d_ws;
  gqa2_fused<<<NWG, NTHR, 0, stream>>>(a);
}
```

```cpp
#define PADT 7000
#define FENCE 1
#include <hip/hip_runtime.h>
#include <cstdint>

typedef unsigned v4u __attribute__((ext_vector_type(4)));
typedef unsigned v2u __attribute__((ext_vector_type(2)));
typedef float f32x4 __attribute__((ext_vector_type(4)));
typedef __bf16 bf16x8 __attribute__((ext_vector_type(8)));
typedef __bf16 bf16x2 __attribute__((ext_vector_type(2)));
typedef float f32x2 __attribute__((ext_vector_type(2)));
#define GAS __attribute__((address_space(1)))
#define LAS __attribute__((address_space(3)))
typedef GAS unsigned gu32;
typedef GAS unsigned long long gu64;
#define RLX_AGENT __ATOMIC_RELAXED, __HIP_MEMORY_SCOPE_AGENT
#define RLX_WG __ATOMIC_RELAXED, __HIP_MEMORY_SCOPE_WORKGROUP

#define STAMPS 0
#define DT16 1840
#define ANC -2
#define OFF16 1750
#define PRE 8
#define DEP 3
#define LIM 2
#define WO16 0
#define WOS16 600
#define QGD 75
#define PTH16 400
#define SLK16 100000
#define H1D 40
#define H2D 70
#define PREW 1
#define HBAR 1
#define PAT 1
#define JST 8192
#define WST 1024
#define SOFT16 0
#define NSOFT 6
#define H2G16 2000
#define WOA16 0
#define WOSA16 600
#define EXTRA 0
#define NOWO 0

constexpr int DM = 576, NQ = 4096;
constexpr int NWG = 256, NTHR = 640;
constexpr int AUX_NT = 2, AUX_SC1 = 16;
constexpr int RSRC3 = 0x00020000;
constexpr int OOB = 0x7fff0000;
constexpr int WS_EPOCH = 0;
constexpr int WS_QG = 4096;
constexpr int WS_MG = WS_QG + 32 * 4096;
constexpr int WS_PG = WS_MG + 64 * 1024;
constexpr int PG_ROW = 256 * 8;
constexpr int WS_YG = WS_PG + 512 * PG_ROW;
constexpr int YG_ROW = 640 * 8;
constexpr int WS_ST = WS_YG + 256 * YG_ROW;
constexpr unsigned TAG_STEP = 0x9E3779B9u;
constexpr unsigned SPIN_CAP = 1u << 17, LSPIN_CAP = 1u << 22;
constexpr float C2 = 0.088388347648318440f * 1.4426950408889634f;
constexpr unsigned EMPTY_O = 0xffffffffu, EMPTY_B = 0x0000ffffu;

struct Args {
  const void* x[2]; const void* k[2]; const void* v[2]; const void* wq[2]; const void* wo[2];
  void* out; void* ws;
};

#define SB() __builtin_amdgcn_sched_barrier(0)
__device__ __forceinline__ f32x4 mfma(v4u a, v4u b, f32x4 c) {
  return __builtin_amdgcn_mfma_f32_16x16x32_bf16(__builtin_bit_cast(bf16x8, a), __builtin_bit_cast(bf16x8, b), c, 0, 0, 0);
}
__device__ __forceinline__ unsigned dup_bf16(float f) {
  f32x2 v = {f, f};
  return __builtin_bit_cast(unsigned, __builtin_convertvector(v, bf16x2));
}
__device__ __forceinline__ unsigned short f2bf(float f) { return __builtin_bit_cast(unsigned short, (__bf16)f); }
__device__ __forceinline__ v4u diag(unsigned pp, v4u mask) { v4u r = {pp & mask.x, pp & mask.y, pp & mask.z, pp & mask.w}; return r; }
template <int CTRL> __device__ __forceinline__ float dpp(float x) {
  return __builtin_bit_cast(float, __builtin_amdgcn_mov_dpp(__builtin_bit_cast(int, x), CTRL, 0xf, 0xf, true));
}
constexpr int XOR1 = 0xB1, XOR2 = 0x4E, HMIR = 0x141, MIR = 0x140, ROR8 = 0x128, ROR4 = 0x124;
__device__ __forceinline__ float fmax_(float a, float b) { return __builtin_fmaxf(a, b); }
__device__ __forceinline__ float xg_max(float x) {
  auto s = __builtin_amdgcn_permlane16_swap(__float_as_uint(x), __float_as_uint(x), false, false);
  x = fmax_(__uint_as_float(s[0]), __uint_as_float(s[1]));
  auto t = __builtin_amdgcn_permlane32_swap(__float_as_uint(x), __float_as_uint(x), false, false);
  return fmax_(__uint_as_float(t[0]), __uint_as_float(t[1]));
}
__device__ __forceinline__ float x32_sum(float x) {
  auto t = __builtin_amdgcn_permlane32_swap(__float_as_uint(x), __float_as_uint(x), false, false);
  return __uint_as_float(t[0]) + __uint_as_float(t[1]);
}
__device__ __forceinline__ float xg_sum(float x) {
  auto s = __builtin_amdgcn_permlane16_swap(__float_as_uint(x), __float_as_uint(x), false, false);
  return x32_sum(__uint_as_float(s[0]) + __uint_as_float(s[1]));
}
__device__ __forceinline__ void st_gran(unsigned char* ws, int off, unsigned tag, float v) {
  __hip_atomic_store((gu64*)(ws + off), ((unsigned long long)tag << 32) | __float_as_uint(v), RLX_AGENT);
}
__device__ __forceinline__ v2u ldg(__amdgpu_buffer_rsrc_t rs, int off) { return __builtin_amdgcn_raw_buffer_load_b64(rs, off, 0, AUX_SC1); }
template <int N, int STEP, int SLEEP> __device__ __forceinline__ void sweep(__amdgpu_buffer_rsrc_t rs, int off, unsigned tag, v2u (&x)[N]) {
#pragma unroll
  for (int k = 0; k < N; ++k) x[k] = ldg(rs, off + k * STEP);
  SB();
  bool ok = true;
#pragma unroll
  for (int k = 0; k < N; ++k) ok &= x[k].y == tag;
  if (__builtin_expect(__all(ok), 1)) return;
#pragma nounroll
  for (unsigned spins = 0; spins < SPIN_CAP; ++spins) {
    __builtin_amdgcn_s_sleep(SLEEP);
    v2u y[N];
#pragma unroll
    for (int k = 0; k < N; ++k) y[k] = ldg(rs, x[k].y == tag ? OOB : off + k * STEP);
    SB();
    ok = true;
#pragma unroll
    for (int k = 0; k < N; ++k) { x[k] = x[k].y == tag ? x[k] : y[k]; ok &= x[k].y == tag; }
    if (__all(ok)) break;
  }
}
__device__ __forceinline__ v4u mkrs(const void* p, unsigned bytes) {
  const unsigned long long u = (unsigned long long)p;
  v4u r = {(unsigned)u, (unsigned)(u >> 32) & 0xffffu, bytes, (unsigned)RSRC3};
  return r;
}
__device__ __forceinline__ void tile_load(v4u (&d)[4], int voff, v4u rs, int soff) {
  asm volatile("buffer_load_dwordx4 %0, %4, %5, %6 offen nt\n\t"
               "buffer_load_dwordx4 %1, %4, %5, %7 offen nt\n\t"
               "buffer_load_dwordx4 %2, %4, %5, %8 offen nt\n\t"
               "buffer_load_dwordx4 %3, %4, %5, %9 offen nt"
               : "=&v"(d[0]), "=&v"(d[1]), "=&v"(d[2]), "=&v"(d[3]) : "v"(voff), "s"(rs), "s"(soff), "s"(soff + JST), "s"(soff + 2 * JST), "s"(soff + 3 * JST));
}
template <int N> __device__ __forceinline__ void tile_wait(v4u (&d)[4]) {
  asm volatile("s_waitcnt vmcnt(%4)" : "+v"(d[0]), "+v"(d[1]), "+v"(d[2]), "+v"(d[3]) : "i"(N));
}
template <int N> __device__ __forceinline__ void piece_wait(v4u& d) { asm volatile("s_waitcnt vmcnt(%1)" : "+v"(d) : "i"(N)); }
__device__ __forceinline__ void pause(int ticks) {
  unsigned due;
  asm volatile("s_memrealtime vcc\n\t"
               "s_waitcnt lgkmcnt(0)\n\t"
               "s_add_i32 %0, vcc_lo, %1\n"
               ".Lg0_%=:\n\t"
               "s_sleep 1\n\t"
               "s_memrealtime vcc\n\t"
               "s_waitcnt lgkmcnt(0)\n\t"
               "s_sub_i32 vcc_lo, vcc_lo, %0\n\t"
               "s_cmp_lt_i32 vcc_lo, 0\n\t"
               "s_cbranch_scc1 .Lg0_%="
               : "=&s"(due) : "i"(ticks) : "scc", "vcc", "memory");
}
__device__ __forceinline__ void gate_next(unsigned& due) {
  asm volatile("s_branch .Lg1_%=\n"
               ".Lg0_%=:\n\t"
               "s_sleep 1\n"
               ".Lg1_%=:\n\t"
               "s_memrealtime vcc\n\t"
               "s_waitcnt lgkmcnt(0)\n\t"
               "s_lshl_b32 vcc_lo, vcc_lo, 4\n\t"
               "s_sub_i32 vcc_lo, vcc_lo, %0\n\t"
               "s_cmp_lt_i32 vcc_lo, 0\n\t"
               "s_cbranch_scc1 .Lg0_%=\n\t"
               "s_setprio 0\n\t"
               "s_cmp_lt_i32 vcc_lo, %1\n\t"
               "s_cbranch_scc1 .Lg2_%=\n\t"
               "s_setprio 1\n"
               ".Lg2_%=:\n\t"
               "s_sub_i32 vcc_lo, vcc_lo, %2\n\t"
               "s_max_i32 vcc_lo, vcc_lo, 0\n\t"
               "s_add_i32 %0, %0, vcc_lo\n\t"
               "s_add_i32 %0, %0, %3"
               : "+s"(due) : "i"(PTH16), "i"(SLK16), "i"(DT16) : "scc", "vcc", "memory");
}
__device__ __forceinline__ void lds_barrier() {
  asm volatile("s_waitcnt lgkmcnt(0)" ::: "memory");
  __builtin_amdgcn_s_barrier();
  asm volatile("" ::: "memory");
}
__device__ __forceinline__ void lds_arrive(unsigned* c, int lane) {
  asm volatile("s_waitcnt lgkmcnt(0)" ::: "memory");
  if (lane == 0) __hip_atomic_fetch_add(c, 1u, RLX_WG);
}
template <int SLEEP> __device__ __forceinline__ unsigned lds_wait(unsigned* c, unsigned want) {
  unsigned v = 0;
#pragma nounroll
  for (unsigned spins = 0; spins < LSPIN_CAP; ++spins) {
    v = __builtin_amdgcn_readfirstlane(__hip_atomic_load(c, RLX_WG));
    if (v >= want) break;
    __builtin_amdgcn_s_sleep(SLEEP);
  }
  asm volatile("" ::: "memory");
  return v;
}
__device__ __forceinline__ unsigned lds_addr(const void* p) { return (unsigned)(size_t)(const LAS void*)p; }
template <int SLEEP> __device__ __forceinline__ v2u lds_take2(unsigned addr) {
  v2u v;
#pragma nounroll
  for (unsigned spins = 0; spins < LSPIN_CAP; ++spins) {
    asm volatile("ds_read2st64_b32 %0, %1 offset1:1\n\ts_waitcnt lgkmcnt(0)" : "=&v"(v) : "v"(addr) : "memory");
    if (__all(v.x != EMPTY_O && v.y != EMPTY_O)) break;
    if (SLEEP) __builtin_amdgcn_s_sleep(SLEEP);
  }
  return v;
}
#define STAMP(i) do {} while (0)
#define STAMP_OUT() do {} while (0)

constexpr int L_O = 0;
constexpr int L_ML = L_O + 4096;
constexpr int L_QP = L_ML + 64;
constexpr int L_Q = L_QP + 256;
constexpr int L_OB = L_Q + 256;
constexpr int L_CNT = L_OB + 32;
constexpr int L_END = L_CNT + 8;

struct St {
  v4u T[32][4];
  float Sc[8][4];
  unsigned P[8][4];
  v4u Q, mask;
  f32x4 O;
  v4u rs[4];
  int vk, vv, so;
  unsigned tb;
  float* lds;
  int lane, wave;
};

template <int I> __device__ __forceinline__ void request(St& z) {
  if constexpr (4 * I > LIM) asm volatile("s_waitcnt vmcnt(%0)" :: "i"(LIM) : "memory");
  gate_next(z.tb);
  if constexpr (SOFT16 && I < NSOFT) z.tb += SOFT16;
  tile_load(z.T[I], (I & 8) ? z.vv : z.vk, z.rs[I >> 3], z.so + (I & 7) * 32768);
  SB();
}
template <int I, int W> __device__ __forceinline__ void consume(St& z) {
  const int g = z.lane >> 4, n = z.lane & 15;
  constexpr int br = I >> 4;
  if constexpr ((I & 15) == 0) {
    z.Q = *(const v4u*)((const unsigned short*)(z.lds + L_Q) + br * 256 + (n >> 3) * 128 + 32 * g + 8 * (n & 3));
  }
  if constexpr ((I & 8) == 0) {
    tile_wait<W>(z.T[I]);
#pragma unroll
    for (int j = 0; j < 4; ++j) {
      const f32x4 d = mfma(z.T[I][j], z.Q, f32x4{0.f, 0.f, 0.f, 0.f});
      z.Sc[I & 7][j] = (dpp<0x00>(d.x) + dpp<0x55>(d.y)) + (dpp<0xAA>(d.z) + dpp<0xFF>(d.w));
    }
    if constexpr ((I & 7) == 7) {
      float m = z.Sc[0][0];
#pragma unroll
      for (int t = 0; t < 8; ++t)
#pragma unroll
        for (int j = 0; j < 4; ++j) m = fmax_(m, z.Sc[t][j]);
      const float m2 = xg_max(m) * C2;
      float l = 0.f;
#pragma unroll
      for (int t = 0; t < 8; ++t) {
        float p[4];
#pragma unroll
        for (int j = 0; j < 4; ++j) { p[j] = __builtin_amdgcn_exp2f(fmaf(z.Sc[t][j], C2, -m2)); z.P[t][j] = dup_bf16(p[j]); }
        l += (p[0] + p[1]) + (p[2] + p[3]);
        asm volatile("" : "+v"(z.P[t][0]), "+v"(z.P[t][1]), "+v"(z.P[t][2]), "+v"(z.P[t][3]));
      }
      l = xg_sum(l);
      if (g == 0 && (n & 7) == 0) { z.lds[L_ML + br * 32 + (n >> 3) * 8 + z.wave] = m2; z.lds[L_ML + br * 32 + 16 + (n >> 3) * 8 + z.wave] = l; }
      lds_arrive((unsigned*)(z.lds + L_CNT) + 2 + br, z.lane);
      z.O = f32x4{0.f, 0.f, 0.f, 0.f};
    } else {
      asm volatile("" : "+v"(z.Sc[I & 7][0]), "+v"(z.Sc[I & 7][1]), "+v"(z.Sc[I & 7][2]), "+v"(z.Sc[I & 7][3]));
    }
  } else if constexpr (W == 0) {
    v4u A[4];
#pragma unroll
    for (int j = 0; j < 4; ++j) A[j] = diag(z.P[I & 7][j], z.mask);
    float* dst = z.lds + L_O + br * 2048 + z.wave * 256 + (g >> 1) * 128 + 8 * n + 4 * (g & 1);
    unsigned da = lds_addr(dst);
    asm volatile("" : "+v"(A[0]), "+v"(A[1]), "+v"(A[2]), "+v"(A[3]), "+v"(da));
    piece_wait<3>(z.T[I][0]); z.O = mfma(A[0], z.T[I][0], z.O); SB();
    piece_wait<2>(z.T[I][1]); z.O = mfma(A[1], z.T[I][1], z.O); SB();
    piece_wait<1>(z.T[I][2]); z.O = mfma(A[2], z.T[I][2], z.O); SB();
    piece_wait<0>(z.T[I][3]); z.O = mfma(A[3], z.T[I][3], z.O);
    *(LAS f32x4*)(size_t)da = z.O;
  } else {
    tile_wait<W>(z.T[I]);
#pragma unroll
    for (int j = 0; j < 4; ++j) z.O = mfma(diag(z.P[I & 7][j], z.mask), z.T[I][j], z.O);
    asm volatile("" : "+v"(z.O));
    if constexpr ((I & 7) == 7) *(f32x4*)(z.lds + L_O + br * 2048 + z.wave * 256 + (g >> 1) * 128 + 8 * n + 4 * (g & 1)) = z.O;
  }
  SB();
}
template <int I, int N> struct Run {
  static __device__ __forceinline__ void go(St& z) { request<I>(z); consume<I - DEP + 1, 4 * (DEP - 1)>(z); Run<I + 1, N>::go(z); }
};
template <int N> struct Run<N, N> { static __device__ __forceinline__ void go(St&) {} };
template <int I, int N> struct Req { static __device__ __forceinline__ void go(St& z) { request<I>(z); Req<I + 1, N>::go(z); } };
template <int N> struct Req<N, N> { static __device__ __forceinline__ void go(St&) {} };
template <int I, int N, int LAST> struct Con {
  static __device__ __forceinline__ void go(St& z) { consume<I, 4 * (LAST - I)>(z); Con<I + 1, N, LAST>::go(z); }
};
template <int N, int LAST> struct Con<N, N, LAST> { static __device__ __forceinline__ void go(St&) {} };

__global__ void __launch_bounds__(NTHR, 3) gqa2_fused(Args a) {
  __shared__ __attribute__((aligned(16))) float lds[L_END];
  unsigned* const l_cnt = (unsigned*)(lds + L_CNT);
  STAMP(0);
  const unsigned t0 = (unsigned)__builtin_amdgcn_s_memrealtime();
  const int tid = threadIdx.x, lane = tid & 63, wave = __builtin_amdgcn_readfirstlane(tid >> 6);
  const int g = lane >> 4, n = lane & 15;
  const int wg = blockIdx.x;
  const int ch = (wg & 7) << 5 | wg >> 3;
  const int kvh = ch >> 4, rb = (ch >> 3) & 1, s8 = ch & 7;
  if (tid < 512) {
    const v4u e = {EMPTY_O, EMPTY_O, EMPTY_O, EMPTY_O};
    *(v4u*)(lds + L_O + tid * 4) = e; *(v4u*)(lds + L_O + 2048 + tid * 4) = e;
  } else if (tid < 512 + 40) {
    ((unsigned*)(lds + L_OB))[tid - 512] = tid < 512 + 32 ? EMPTY_B : 0u;
  }
  asm volatile("" : "+s"(a.x[0]), "+s"(a.x[1]), "+s"(a.k[0]), "+s"(a.k[1]), "+s"(a.v[0]), "+s"(a.v[1]), "+s"(a.wq[0]), "+s"(a.wq[1]), "+s"(a.wo[0]), "+s"(a.wo[1]), "+s"(a.out), "+s"(a.ws));
  unsigned char* const ws = (unsigned char*)a.ws;
  const void* const wop = rb ? a.wo[1] : a.wo[0];
  const v4u rk0 = mkrs((unsigned char*)a.k[0] + (size_t)ch * 262144, 262144);
  lds_barrier();
  const __amdgpu_buffer_rsrc_t rw = __builtin_amdgcn_make_buffer_rsrc(ws, 0, WS_ST, RSRC3);
  v4u mask;
  {
    const unsigned half = (n & 1) ? 0xffff0000u : 0x0000ffffu; const int r = (n & 7) >> 1;
    mask.x = r == 0 ? half : 0u; mask.y = r == 1 ? half : 0u; mask.z = r == 2 ? half : 0u; mask.w = r == 3 ? half : 0u;
  }
  const int pidx = kvh * 8 + s8;
  if (__builtin_expect(wave >= 8, 1)) {
    __builtin_amdgcn_s_setprio(3);
    unsigned z_due; (void)z_due;
    const int hw = wave - 8, cg = s8 & 3, kh = s8 >> 2, sub = 2 * g + (n >> 3);
    const __amdgpu_buffer_rsrc_t rq = __builtin_amdgcn_make_buffer_rsrc((unsigned char*)(rb ? a.wq[1] : a.wq[0]) + ((size_t)(kh * 288 + hw * 144) * NQ + kvh * 256 + cg * 64) * 2, 0, 143 * NQ * 2 + 128, RSRC3);
    const __amdgpu_buffer_rsrc_t rx = __builtin_amdgcn_make_buffer_rsrc((unsigned char*)(rb ? a.x[1] : a.x[0]) + (kh * 288 + hw * 144) * 2, 0, 144 * 2, RSRC3);
    v4u wqv[18];
#pragma unroll
    for (int i = 0; i < 18; ++i) wqv[i] = __builtin_amdgcn_raw_buffer_load_b128(rq, sub * (NQ * 2) + (n & 7) * 16, i * 8 * NQ * 2, AUX_NT);
    const unsigned xa = __builtin_amdgcn_raw_buffer_load_b32(rx, n * 16 + g * 4, 0, 0), xb = __builtin_amdgcn_raw_buffer_load_b32(rx, n * 16 + g * 4 + 256, 0, 0);
    const unsigned ep = __builtin_amdgcn_raw_buffer_load_b32(rw, lane == 0 ? WS_EPOCH : OOB, 0, AUX_SC1);
    SB();
    f32x4 a0 = {0.f, 0.f, 0.f, 0.f}, a1 = {0.f, 0.f, 0.f, 0.f};
    const unsigned sel = n < 8 ? 0x01000100u : 0x03020302u;
#define QSTEP(acc, i, src, k) acc = mfma(diag(__builtin_amdgcn_perm(0u, (unsigned)__builtin_amdgcn_mov_dpp((int)(src), 0x150 + (k), 0xf, 0xf, true), sel), mask), wqv[i], acc)
    QSTEP(a0, 0, xa, 0); QSTEP(a1, 1, xa, 1); QSTEP(a0, 2, xa, 2); QSTEP(a1, 3, xa, 3); QSTEP(a0, 4, xa, 4); QSTEP(a1, 5, xa, 5);
    QSTEP(a0, 6, xa, 6); QSTEP(a1, 7, xa, 7); QSTEP(a0, 8, xa, 8); QSTEP(a1, 9, xa, 9); QSTEP(a0, 10, xa, 10); QSTEP(a1, 11, xa, 11);
    QSTEP(a0, 12, xa, 12); QSTEP(a1, 13, xa, 13); QSTEP(a0, 14, xa, 14); QSTEP(a1, 15, xa, 15); QSTEP(a0, 16, xb, 0); QSTEP(a1, 17, xb, 1);
    if ((g >> 1) == (n >> 3)) *(f32x4*)(lds + L_QP + (hw * 2 + (n >> 3)) * 64 + 8 * (n & 7) + 4 * (g & 1)) = a0 + a1;
    const unsigned tag = __builtin_amdgcn_readfirstlane(ep) + TAG_STEP;
    STAMP(1);
    lds_arrive(l_cnt + 0, lane);
    if (hw == 0) {
      lds_wait<1>(l_cnt + 0, 2u);
      const float q = (lds[L_QP + lane] + lds[L_QP + 64 + lane]) + (lds[L_QP + 128 + lane] + lds[L_QP + 192 + lane]);
      st_gran(ws, WS_QG + ((rb * 16 + kvh) * 512 + kh * 256 + cg * 64 + lane) * 8, tag, q);
      if (lane == 0) l_cnt[5] = tag;
    }
    STAMP(2);
    pause(2 * QGD);
    {
      v2u x[8];
      sweep<8, 512, 8>(rw, WS_QG + ((hw * 16 + kvh) * 512 + lane) * 8, tag, x);
#pragma unroll
      for (int k = 0; k < 4; ++k) ((unsigned short*)(lds + L_Q))[hw * 256 + k * 64 + lane] = f2bf(__uint_as_float(x[k].x) + __uint_as_float(x[k + 4].x));
      lds_arrive(l_cnt + 1, lane);
    }
    STAMP(3);
#pragma nounroll
    for (int br = 0; br < 2; ++br) {
      lds_wait<8>(l_cnt + 2 + br, 8u);
      const float* ml = lds + L_ML + br * 32 + hw * 8;
      const f32x4 m0 = *(const f32x4*)(ml), m1 = *(const f32x4*)(ml + 4), l0 = *(const f32x4*)(ml + 16), l1 = *(const f32x4*)(ml + 20);
      const float M = fmax_(fmax_(fmax_(m0.x, m0.y), fmax_(m0.z, m0.w)), fmax_(fmax_(m1.x, m1.y), fmax_(m1.z, m1.w)));
      float L = 0.f;
#pragma unroll
      for (int w = 0; w < 8; ++w) L = fmaf(__builtin_amdgcn_exp2f((w < 4 ? m0[w & 3] : m1[w & 3]) - M), w < 4 ? l0[w & 3] : l1[w & 3], L);
      const int mg = WS_MG + ((br * 16 + kvh) * 2 + hw) * 1024;
      if (lane < 8) st_gran(ws, mg + (ch & 15) * 64 + lane * 8, tag, (lane & 1) ? L : M);
      if (br == 1) {
        const int d1 = WS_PG + (256 + ch) * PG_ROW, d2 = WS_YG + (rb * 128 + pidx) * YG_ROW;
        unsigned u = __builtin_amdgcn_raw_buffer_load_b32(rw, hw == 0 ? (lane < 32 ? d1 + lane * 64 : lane < 48 ? d2 + 2048 + lane * 64 : OOB) : d2 + lane * 64, 0, AUX_SC1);
        if (lane == 0) u ^= *(const GAS unsigned*)((const unsigned char*)wop + (size_t)(kvh * 256 + s8 * 32 + hw * 31) * DM * 2);
        asm volatile("" :: "v"(u));
      }
      pause(2 * QGD);
      v2u x[1];
      sweep<1, 0, 8>(rw, mg + n * 64 + (g & 1) * 8, tag, x);
      auto sw = __builtin_amdgcn_permlane16_swap(x[0].x, x[0].x, false, false);
      const float Ms = __uint_as_float(sw[0]), Ls = __uint_as_float(sw[1]);
      float Mx = fmax_(Ms, dpp<XOR1>(Ms)); Mx = fmax_(Mx, dpp<XOR2>(Mx)); Mx = fmax_(Mx, dpp<HMIR>(Mx)); Mx = fmax_(Mx, dpp<MIR>(Mx));
      float Lt = __builtin_amdgcn_exp2f(Ms - Mx) * Ls; Lt += dpp<XOR1>(Lt); Lt += dpp<XOR2>(Lt); Lt += dpp<HMIR>(Lt); Lt += dpp<MIR>(Lt);
      const float inv = 1.0f / Lt;
      float c[8];
#pragma unroll
      for (int w = 0; w < 8; ++w) c[w] = __builtin_amdgcn_exp2f((w < 4 ? m0[w & 3] : m1[w & 3]) - Mx) * inv;
      const unsigned oa = lds_addr(lds + L_O + br * 2048 + hw * 128 + lane);
      GAS unsigned char* dst = (GAS unsigned char*)ws + WS_PG + (br * 256 + ch) * PG_ROW + (hw * 128 + lane) * 8;
      asm volatile("" : "+v"(dst));
      float o0 = 0.f, o1 = 0.f;
      { const v2u v = lds_take2<8>(oa); o0 = c[0] * __uint_as_float(v.x); o1 = c[0] * __uint_as_float(v.y); }
#pragma unroll
      for (int w = 1; w < 8; ++w) { const v2u v = lds_take2<0>(oa + w * 1024); o0 = fmaf(c[w], __uint_as_float(v.x), o0); o1 = fmaf(c[w], __uint_as_float(v.y), o1); }
      __hip_atomic_store((gu64*)dst, ((unsigned long long)tag << 32) | __float_as_uint(o0), RLX_AGENT);
      __hip_atomic_store((gu64*)(dst + 512), ((unsigned long long)tag << 32) | __float_as_uint(o1), RLX_AGENT);
      if (hw == 0 && lane == 0) __hip_atomic_store(l_cnt + 4, (unsigned)br + 1u, RLX_WG);
    }
    STAMP(8);
    z_due = (t0 + 5000u) << 4; gate_next(z_due);
    if (hw == 0) __builtin_amdgcn_fence(__ATOMIC_ACQ_REL, "");
    z_due = (t0 + PADT) << 4; gate_next(z_due);
  } else {
    St z;
    const int phs = PAT == 2 ? (wg >> 3) & 7 : wave;
    z.lds = lds; z.lane = lane; z.wave = wave; z.mask = mask;
    z.rs[0] = rk0;
    z.rs[1] = mkrs((unsigned char*)a.v[0] + (size_t)ch * 262144, 262144);
    z.rs[2] = mkrs((unsigned char*)a.k[1] + (size_t)ch * 262144, 262144);
    z.rs[3] = mkrs((unsigned char*)a.v[1] + (size_t)ch * 262144, 262144);
    z.vk = ((lane >> 2) & 3) * 256 + g * 64 + (lane & 3) * 16; z.vv = lane * 16; z.so = wave * WST;
    GAS unsigned short* outp = (GAS unsigned short*)a.out;
    z.tb = (t0 << 4) + (unsigned)(OFF16 + ((phs * DT16) >> 3));
    STAMP(1);
    Req<0, PRE>::go(z);
    STAMP(4);
    lds_wait<1>(l_cnt + 1, 2u);
    const unsigned tag = __builtin_amdgcn_readfirstlane(l_cnt[5]);
    STAMP(5);
    Con<0, PRE - DEP + 1, PRE - 1>::go(z);
    Run<PRE, 17>::go(z);
    STAMP(14);
    Run<17, 25>::go(z);
    STAMP(15);
    Run<25, 32>::go(z);
    STAMP(6);
    Con<32 - DEP + 1, 32, 31>::go(z);
    STAMP(7);
    const unsigned oba = lds_addr(lds + L_OB + g * 8);
    if (wave < 5) {
      const __amdgpu_buffer_rsrc_t ro = __builtin_amdgcn_make_buffer_rsrc((unsigned char*)wop + ((size_t)(kvh * 256 + s8 * 32) * DM) * 2, 0, 32 * DM * 2, RSRC3);
      const int wcol = wave * 128 + n * 8;
      const int vwo = (!NOWO && wcol < DM) ? g * (DM * 2) + wcol * 2 : OOB;
      v4u Wo[8];
      z.tb += (unsigned)((rb ? WO16 + wave * WOS16 : WOA16 + wave * WOSA16) - ((phs * DT16) >> 3));
      gate_next(z.tb);
#pragma unroll
      for (int i = 0; i < 8; ++i) Wo[i] = __builtin_amdgcn_raw_buffer_load_b128(ro, vwo, i * 4 * DM * 2, AUX_NT);
      GAS unsigned char* dst = (GAS unsigned char*)ws + WS_YG + (rb * 128 + pidx) * YG_ROW + (wave * 128 + lane) * 8;
      asm volatile("" : "+v"(dst), "+v"(Wo[0]), "+v"(Wo[1]), "+v"(Wo[2]), "+v"(Wo[3]), "+v"(Wo[4]), "+v"(Wo[5]), "+v"(Wo[6]), "+v"(Wo[7]));
      STAMP(2);
      v4u oa, ob;
#pragma nounroll
      for (unsigned spins = 0; spins < LSPIN_CAP; ++spins) {
        asm volatile("ds_read_b128 %0, %2\n\tds_read_b128 %1, %2 offset:16\n\ts_waitcnt lgkmcnt(0)" : "=&v"(oa), "=&v"(ob) : "v"(oba) : "memory");
        if (__all(oa.x != EMPTY_B && oa.y != EMPTY_B && oa.z != EMPTY_B && oa.w != EMPTY_B && ob.x != EMPTY_B && ob.y != EMPTY_B && ob.z != EMPTY_B && ob.w != EMPTY_B)) break;
      }
      STAMP(10);
      f32x4 Y0 = {0.f, 0.f, 0.f, 0.f}, Y1 = {0.f, 0.f, 0.f, 0.f};
#pragma unroll
      for (int i = 0; i < 4; ++i) { Y0 = mfma(diag(oa[i], mask), Wo[i], Y0); Y1 = mfma(diag(ob[i], mask), Wo[4 + i], Y1); }
      const f32x4 Y = Y0 + Y1;
      if (wcol < DM) {
        __hip_atomic_store((gu64*)dst, ((unsigned long long)tag << 32) | __float_as_uint(g < 2 ? Y.x : Y.z), RLX_AGENT);
        __hip_atomic_store((gu64*)(dst + 512), ((unsigned long long)tag << 32) | __float_as_uint(g < 2 ? Y.y : Y.w), RLX_AGENT);
      }
      STAMP(11);
    } else if (wave == 5) {
      const int po = WS_PG + (rb * 256 + kvh * 16 + (lane >> 5)) * PG_ROW + (32 * s8 + (lane & 31)) * 8;
      v2u x[8];
      lds_wait<0>(l_cnt + 4, (unsigned)rb + 1u);
      if (rb) pause(H1D);
      sweep<8, 2 * PG_ROW, 1>(rw, po, tag, x);
      STAMP(9);
      const float o = x32_sum(((__uint_as_float(x[0].x) + __uint_as_float(x[1].x)) + (__uint_as_float(x[2].x) + __uint_as_float(x[3].x)))
                            + ((__uint_as_float(x[4].x) + __uint_as_float(x[5].x)) + (__uint_as_float(x[6].x) + __uint_as_float(x[7].x))));
      if (lane < 32) ((unsigned*)(lds + L_OB))[(lane & 3) * 8 + (lane >> 2)] = dup_bf16(o);
    } else if (wave == 6 || pidx < 16) {
      const int pos = wave == 6 ? pidx * 4 : 512 + (pidx >> 1) * 16 + (pidx & 1) * 4;
      const int so = WS_YG + (rb * 128 + (lane >> 2)) * YG_ROW + (pos + (lane & 3)) * 8;
      const int p = pos + (lane & 3), pl = p & 63;
      const int col = (p & ~127) + 8 * (pl & 15) + 4 * ((pl >> 4) & 1) + 2 * (pl >> 5) + ((p >> 6) & 1);
      GAS unsigned short* op = outp + rb * DM + col;
      asm volatile("" : "+v"(op));
      v2u x[8];
      z.tb += (unsigned)((rb ? WO16 + 4 * WOS16 : WOA16 + 4 * WOSA16) + H2G16 - ((phs * DT16) >> 3));
#pragma nounroll
      for (unsigned spins = 0; spins < LSPIN_CAP; ++spins) {
        if (__builtin_amdgcn_readfirstlane(__hip_atomic_load((unsigned*)(lds + L_OB), RLX_WG)) != EMPTY_B) break;
        __builtin_amdgcn_s_sleep(1);
      }
      pause(H2D);
      gate_next(z.tb);
      sweep<8, 16 * YG_ROW, 1>(rw, so, tag, x);
      STAMP(12);
      float y = ((__uint_as_float(x[0].x) + __uint_as_float(x[1].x)) + (__uint_as_float(x[2].x) + __uint_as_float(x[3].x)))
              + ((__uint_as_float(x[4].x) + __uint_as_float(x[5].x)) + (__uint_as_float(x[6].x) + __uint_as_float(x[7].x)));
      y += dpp<ROR4>(y); y += dpp<ROR8>(y);
      y = xg_sum(y);
      if (lane < 4) *op = f2bf(y);
      if (rb == 0 && pos == 0 && lane == 0) __hip_atomic_store((gu32*)(ws + WS_EPOCH), tag, RLX_AGENT);
      STAMP(13);
    }
  }
  STAMP_OUT();
}

extern "C" void kernel_launch(void* const* d_in, const int* in_sizes, int n_in, void* d_out, int out_size, void* d_ws, size_t ws_size, hipStream_t stream) {
  Args a;
  a.x[0] = d_in[0]; a.k[0] = d_in[1]; a.v[0] = d_in[2]; a.wq[0] = d_in[3]; a.wo[0] = d_in[4];
  a.x[1] = d_in[5]; a.k[1] = d_in[6]; a.v[1] = d_in[7]; a.wq[1] = d_in[8]; a.wo[1] = d_in[9];
  a.out = d_out; a.ws = d_ws;
  gqa2_fused<<<NWG, NTHR, 0, stream>>>(a);
}
```

```cpp
#define OUTSC 0
#include <hip/hip_runtime.h>
#include <cstdint>

typedef unsigned v4u __attribute__((ext_vector_type(4)));
typedef unsigned v2u __attribute__((ext_vector_type(2)));
typedef float f32x4 __attribute__((ext_vector_type(4)));
typedef __bf16 bf16x8 __attribute__((ext_vector_type(8)));
typedef __bf16 bf16x2 __attribute__((ext_vector_type(2)));
typedef float f32x2 __attribute__((ext_vector_type(2)));
#define GAS __attribute__((address_space(1)))
#define LAS __attribute__((address_space(3)))
typedef GAS unsigned gu32;
typedef GAS unsigned long long gu64;
#define RLX_AGENT __ATOMIC_RELAXED, __HIP_MEMORY_SCOPE_AGENT
#define RLX_WG __ATOMIC_RELAXED, __HIP_MEMORY_SCOPE_WORKGROUP

#define STAMPS 0
#define DT16 1840
#define ANC -2
#define OFF16 1750
#define PRE 8
#define DEP 3
#define LIM 2
#define WO16 0
#define WOS16 600
#define QGD 75
#define PTH16 400
#define SLK16 100000
#define H1D 40
#define H2D 70
#define PREW 1
#define HBAR 1
#define PAT 1
#define JST 8192
#define WST 1024
#define SOFT16 0
#define NSOFT 6
#define H2G16 2000
#define WOA16 0
#define WOSA16 600
#define NOWO 0

constexpr int DM = 576, NQ = 4096;
constexpr int NWG = 256, NTHR = 640;
constexpr int AUX_NT = 2, AUX_SC1 = 16;
constexpr int RSRC3 = 0x00020000;
constexpr int OOB = 0x7fff0000;
constexpr int WS_EPOCH = 0;
constexpr int WS_QG = 4096;
constexpr int WS_MG = WS_QG + 32 * 4096;
constexpr int WS_PG = WS_MG + 64 * 1024;
constexpr int PG_ROW = 256 * 8;
constexpr int WS_YG = WS_PG + 512 * PG_ROW;
constexpr int YG_ROW = 640 * 8;
constexpr int WS_ST = WS_YG + 256 * YG_ROW;
constexpr unsigned TAG_STEP = 0x9E3779B9u;
constexpr unsigned SPIN_CAP = 1u << 17, LSPIN_CAP = 1u << 22;
constexpr float C2 = 0.088388347648318440f * 1.4426950408889634f;
constexpr unsigned EMPTY_O = 0xffffffffu, EMPTY_B = 0x0000ffffu;

struct Args {
  const void* x[2]; const void* k[2]; const void* v[2]; const void* wq[2]; const void* wo[2];
  void* out; void* ws;
};

#define SB() __builtin_amdgcn_sched_barrier(0)
__device__ __forceinline__ f32x4 mfma(v4u a, v4u b, f32x4 c) {
  return __builtin_amdgcn_mfma_f32_16x16x32_bf16(__builtin_bit_cast(bf16x8, a), __builtin_bit_cast(bf16x8, b), c, 0, 0, 0);
}
__device__ __forceinline__ unsigned dup_bf16(float f) {
  f32x2 v = {f, f};
  return __builtin_bit_cast(unsigned, __builtin_convertvector(v, bf16x2));
}
__device__ __forceinline__ unsigned short f2bf(float f) { return __builtin_bit_cast(unsigned short, (__bf16)f); }
__device__ __forceinline__ v4u diag(unsigned pp, v4u mask) { v4u r = {pp & mask.x, pp & mask.y, pp & mask.z, pp & mask.w}; return r; }
template <int CTRL> __device__ __forceinline__ float dpp(float x) {
  return __builtin_bit_cast(float, __builtin_amdgcn_mov_dpp(__builtin_bit_cast(int, x), CTRL, 0xf, 0xf, true));
}
constexpr int XOR1 = 0xB1, XOR2 = 0x4E, HMIR = 0x141, MIR = 0x140, ROR8 = 0x128, ROR4 = 0x124;
__device__ __forceinline__ float fmax_(float a, float b) { return __builtin_fmaxf(a, b); }
__device__ __forceinline__ float xg_max(float x) {
  auto s = __builtin_amdgcn_permlane16_swap(__float_as_uint(x), __float_as_uint(x), false, false);
  x = fmax_(__uint_as_float(s[0]), __uint_as_float(s[1]));
  auto t = __builtin_amdgcn_permlane32_swap(__float_as_uint(x), __float_as_uint(x), false, false);
  return fmax_(__uint_as_float(t[0]), __uint_as_float(t[1]));
}
__device__ __forceinline__ float x32_sum(float x) {
  auto t = __builtin_amdgcn_permlane32_swap(__float_as_uint(x), __float_as_uint(x), false, false);
  return __uint_as_float(t[0]) + __uint_as_float(t[1]);
}
__device__ __forceinline__ float xg_sum(float x) {
  auto s = __builtin_amdgcn_permlane16_swap(__float_as_uint(x), __float_as_uint(x), false, false);
  return x32_sum(__uint_as_float(s[0]) + __uint_as_float(s[1]));
}
__device__ __forceinline__ void st_gran(unsigned char* ws, int off, unsigned tag, float v) {
  __hip_atomic_store((gu64*)(ws + off), ((unsigned long long)tag << 32) | __float_as_uint(v), RLX_AGENT);
}
__device__ __forceinline__ v2u ldg(__amdgpu_buffer_rsrc_t rs, int off) { return __builtin_amdgcn_raw_buffer_load_b64(rs, off, 0, AUX_SC1); }
template <int N, int STEP, int SLEEP> __device__ __forceinline__ void sweep(__amdgpu_buffer_rsrc_t rs, int off, unsigned tag, v2u (&x)[N]) {
#pragma unroll
  for (int k = 0; k < N; ++k) x[k] = ldg(rs, off + k * STEP);
  SB();
  bool ok = true;
#pragma unroll
  for (int k = 0; k < N; ++k) ok &= x[k].y == tag;
  if (__builtin_expect(__all(ok), 1)) return;
#pragma nounroll
  for (unsigned spins = 0; spins < SPIN_CAP; ++spins) {
    __builtin_amdgcn_s_sleep(SLEEP);
    v2u y[N];
#pragma unroll
    for (int k = 0; k < N; ++k) y[k] = ldg(rs, x[k].y == tag ? OOB : off + k * STEP);
    SB();
    ok = true;
#pragma unroll
    for (int k = 0; k < N; ++k) { x[k] = x[k].y == tag ? x[k] : y[k]; ok &= x[k].y == tag; }
    if (__all(ok)) break;
  }
}
__device__ __forceinline__ v4u mkrs(const void* p, unsigned bytes) {
  const unsigned long long u = (unsigned long long)p;
  v4u r = {(unsigned)u, (unsigned)(u >> 32) & 0xffffu, bytes, (unsigned)RSRC3};
  return r;
}
__device__ __forceinline__ void tile_load(v4u (&d)[4], int voff, v4u rs, int soff) {
  asm volatile("buffer_load_dwordx4 %0, %4, %5, %6 offen nt\n\t"
               "buffer_load_dwordx4 %1, %4, %5, %7 offen nt\n\t"
               "buffer_load_dwordx4 %2, %4, %5, %8 offen nt\n\t"
               "buffer_load_dwordx4 %3, %4, %5, %9 offen nt"
               : "=&v"(d[0]), "=&v"(d[1]), "=&v"(d[2]), "=&v"(d[3]) : "v"(voff), "s"(rs), "s"(soff), "s"(soff + JST), "s"(soff + 2 * JST), "s"(soff + 3 * JST));
}
template <int N> __device__ __forceinline__ void tile_wait(v4u (&d)[4]) {
  asm volatile("s_waitcnt vmcnt(%4)" : "+v"(d[0]), "+v"(d[1]), "+v"(d[2]), "+v"(d[3]) : "i"(N));
}
template <int N> __device__ __forceinline__ void piece_wait(v4u& d) { asm volatile("s_waitcnt vmcnt(%1)" : "+v"(d) : "i"(N)); }
__device__ __forceinline__ void pause(int ticks) {
  unsigned due;
  asm volatile("s_memrealtime vcc\n\t"
               "s_waitcnt lgkmcnt(0)\n\t"
               "s_add_i32 %0, vcc_lo, %1\n"
               ".Lg0_%=:\n\t"
               "s_sleep 1\n\t"
               "s_memrealtime vcc\n\t"
               "s_waitcnt lgkmcnt(0)\n\t"
               "s_sub_i32 vcc_lo, vcc_lo, %0\n\t"
               "s_cmp_lt_i32 vcc_lo, 0\n\t"
               "s_cbranch_scc1 .Lg0_%="
               : "=&s"(due) : "i"(ticks) : "scc", "vcc", "memory");
}
__device__ __forceinline__ void gate_next(unsigned& due) {
  asm volatile("s_branch .Lg1_%=\n"
               ".Lg0_%=:\n\t"
               "s_sleep 1\n"
               ".Lg1_%=:\n\t"
               "s_memrealtime vcc\n\t"
               "s_waitcnt lgkmcnt(0)\n\t"
               "s_lshl_b32 vcc_lo, vcc_lo, 4\n\t"
               "s_sub_i32 vcc_lo, vcc_lo, %0\n\t"
               "s_cmp_lt_i32 vcc_lo, 0\n\t"
               "s_cbranch_scc1 .Lg0_%=\n\t"
               "s_setprio 0\n\t"
               "s_cmp_lt_i32 vcc_lo, %1\n\t"
               "s_cbranch_scc1 .Lg2_%=\n\t"
               "s_setprio 1\n"
               ".Lg2_%=:\n\t"
               "s_sub_i32 vcc_lo, vcc_lo, %2\n\t"
               "s_max_i32 vcc_lo, vcc_lo, 0\n\t"
               "s_add_i32 %0, %0, vcc_lo\n\t"
               "s_add_i32 %0, %0, %3"
               : "+s"(due) : "i"(PTH16), "i"(SLK16), "i"(DT16) : "scc", "vcc", "memory");
}
__device__ __forceinline__ void lds_barrier() {
  asm volatile("s_waitcnt lgkmcnt(0)" ::: "memory");
  __builtin_amdgcn_s_barrier();
  asm volatile("" ::: "memory");
}
__device__ __forceinline__ void lds_arrive(unsigned* c, int lane) {
  asm volatile("s_waitcnt lgkmcnt(0)" ::: "memory");
  if (lane == 0) __hip_atomic_fetch_add(c, 1u, RLX_WG);
}
template <int SLEEP> __device__ __forceinline__ unsigned lds_wait(unsigned* c, unsigned want) {
  unsigned v = 0;
#pragma nounroll
  for (unsigned spins = 0; spins < LSPIN_CAP; ++spins) {
    v = __builtin_amdgcn_readfirstlane(__hip_atomic_load(c, RLX_WG));
    if (v >= want) break;
    __builtin_amdgcn_s_sleep(SLEEP);
  }
  asm volatile("" ::: "memory");
  return v;
}
__device__ __forceinline__ unsigned lds_addr(const void* p) { return (unsigned)(size_t)(const LAS void*)p; }
template <int SLEEP> __device__ __forceinline__ v2u lds_take2(unsigned addr) {
  v2u v;
#pragma nounroll
  for (unsigned spins = 0; spins < LSPIN_CAP; ++spins) {
    asm volatile("ds_read2st64_b32 %0, %1 offset1:1\n\ts_waitcnt lgkmcnt(0)" : "=&v"(v) : "v"(addr) : "memory");
    if (__all(v.x != EMPTY_O && v.y != EMPTY_O)) break;
    if (SLEEP) __builtin_amdgcn_s_sleep(SLEEP);
  }
  return v;
}
#define STAMP(i) do {} while (0)
#define STAMP_OUT() do {} while (0)

constexpr int L_O = 0;
constexpr int L_ML = L_O + 4096;
constexpr int L_QP = L_ML + 64;
constexpr int L_Q = L_QP + 256;
constexpr int L_OB = L_Q + 256;
constexpr int L_CNT = L_OB + 32;
constexpr int L_END = L_CNT + 8;

struct St {
  v4u T[32][4];
  float Sc[8][4];
  unsigned P[8][4];
  v4u Q, mask;
  f32x4 O;
  v4u rs[4];
  int vk, vv, so;
  unsigned tb;
  float* lds;
  int lane, wave;
};

template <int I> __device__ __forceinline__ void request(St& z) {
  if constexpr (4 * I > LIM) asm volatile("s_waitcnt vmcnt(%0)" :: "i"(LIM) : "memory");
  gate_next(z.tb);
  if constexpr (SOFT16 && I < NSOFT) z.tb += SOFT16;
  tile_load(z.T[I], (I & 8) ? z.vv : z.vk, z.rs[I >> 3], z.so + (I & 7) * 32768);
  SB();
}
template <int I, int W> __device__ __forceinline__ void consume(St& z) {
  const int g = z.lane >> 4, n = z.lane & 15;
  constexpr int br = I >> 4;
  if constexpr ((I & 15) == 0) {
    z.Q = *(const v4u*)((const unsigned short*)(z.lds + L_Q) + br * 256 + (n >> 3) * 128 + 32 * g + 8 * (n & 3));
  }
  if constexpr ((I & 8) == 0) {
    tile_wait<W>(z.T[I]);
#pragma unroll
    for (int j = 0; j < 4; ++j) {
      const f32x4 d = mfma(z.T[I][j], z.Q, f32x4{0.f, 0.f, 0.f, 0.f});
      z.Sc[I & 7][j] = (dpp<0x00>(d.x) + dpp<0x55>(d.y)) + (dpp<0xAA>(d.z) + dpp<0xFF>(d.w));
    }
    if constexpr ((I & 7) == 7) {
      float m = z.Sc[0][0];
#pragma unroll
      for (int t = 0; t < 8; ++t)
#pragma unroll
        for (int j = 0; j < 4; ++j) m = fmax_(m, z.Sc[t][j]);
      const float m2 = xg_max(m) * C2;
      float l = 0.f;
#pragma unroll
      for (int t = 0; t < 8; ++t) {
        float p[4];
#pragma unroll
        for (int j = 0; j < 4; ++j) { p[j] = __builtin_amdgcn_exp2f(fmaf(z.Sc[t][j], C2, -m2)); z.P[t][j] = dup_bf16(p[j]); }
        l += (p[0] + p[1]) + (p[2] + p[3]);
        asm volatile("" : "+v"(z.P[t][0]), "+v"(z.P[t][1]), "+v"(z.P[t][2]), "+v"(z.P[t][3]));
      }
      l = xg_sum(l);
      if (g == 0 && (n & 7) == 0) { z.lds[L_ML + br * 32 + (n >> 3) * 8 + z.wave] = m2; z.lds[L_ML + br * 32 + 16 + (n >> 3) * 8 + z.wave] = l; }
      lds_arrive((unsigned*)(z.lds + L_CNT) + 2 + br, z.lane);
      z.O = f32x4{0.f, 0.f, 0.f, 0.f};
    } else {
      asm volatile("" : "+v"(z.Sc[I & 7][0]), "+v"(z.Sc[I & 7][1]), "+v"(z.Sc[I & 7][2]), "+v"(z.Sc[I & 7][3]));
    }
  } else if constexpr (W == 0) {
    v4u A[4];
#pragma unroll
    for (int j = 0; j < 4; ++j) A[j] = diag(z.P[I & 7][j], z.mask);
    float* dst = z.lds + L_O + br * 2048 + z.wave * 256 + (g >> 1) * 128 + 8 * n + 4 * (g & 1);
    unsigned da = lds_addr(dst);
    asm volatile("" : "+v"(A[0]), "+v"(A[1]), "+v"(A[2]), "+v"(A[3]), "+v"(da));
    piece_wait<3>(z.T[I][0]); z.O = mfma(A[0], z.T[I][0], z.O); SB();
    piece_wait<2>(z.T[I][1]); z.O = mfma(A[1], z.T[I][1], z.O); SB();
    piece_wait<1>(z.T[I][2]); z.O = mfma(A[2], z.T[I][2], z.O); SB();
    piece_wait<0>(z.T[I][3]); z.O = mfma(A[3], z.T[I][3], z.O);
    *(LAS f32x4*)(size_t)da = z.O;
  } else {
    tile_wait<W>(z.T[I]);
#pragma unroll
    for (int j = 0; j < 4; ++j) z.O = mfma(diag(z.P[I & 7][j], z.mask), z.T[I][j], z.O);
    asm volatile("" : "+v"(z.O));
    if constexpr ((I & 7) == 7) *(f32x4*)(z.lds + L_O + br * 2048 + z.wave * 256 + (g >> 1) * 128 + 8 * n + 4 * (g & 1)) = z.O;
  }
  SB();
}
template <int I, int N> struct Run {
  static __device__ __forceinline__ void go(St& z) { request<I>(z); consume<I - DEP + 1, 4 * (DEP - 1)>(z); Run<I + 1, N>::go(z); }
};
template <int N> struct Run<N, N> { static __device__ __forceinline__ void go(St&) {} };
template <int I, int N> struct Req { static __device__ __forceinline__ void go(St& z) { request<I>(z); Req<I + 1, N>::go(z); } };
template <int N> struct Req<N, N> { static __device__ __forceinline__ void go(St&) {} };
template <int I, int N, int LAST> struct Con {
  static __device__ __forceinline__ void go(St& z) { consume<I, 4 * (LAST - I)>(z); Con<I + 1, N, LAST>::go(z); }
};
template <int N, int LAST> struct Con<N, N, LAST> { static __device__ __forceinline__ void go(St&) {} };

__global__ void __launch_bounds__(NTHR, 3) gqa2_fused(Args a) {
  __shared__ __attribute__((aligned(16))) float lds[L_END];
  unsigned* const l_cnt = (unsigned*)(lds + L_CNT);
  STAMP(0);
  const unsigned t0 = (unsigned)__builtin_amdgcn_s_memrealtime();
  const int tid = threadIdx.x, lane = tid & 63, wave = __builtin_amdgcn_readfirstlane(tid >> 6);
  const int g = lane >> 4, n = lane & 15;
  const int wg = blockIdx.x;
  const int ch = (wg & 7) << 5 | wg >> 3;
  const int kvh = ch >> 4, rb = (ch >> 3) & 1, s8 = ch & 7;
  if (tid < 512) {
    const v4u e = {EMPTY_O, EMPTY_O, EMPTY_O, EMPTY_O};
    *(v4u*)(lds + L_O + tid * 4) = e; *(v4u*)(lds + L_O + 2048 + tid * 4) = e;
  } else if (tid < 512 + 40) {
    ((unsigned*)(lds + L_OB))[tid - 512] = tid < 512 + 32 ? EMPTY_B : 0u;
  }
  asm volatile("" : "+s"(a.x[0]), "+s"(a.x[1]), "+s"(a.k[0]), "+s"(a.k[1]), "+s"(a.v[0]), "+s"(a.v[1]), "+s"(a.wq[0]), "+s"(a.wq[1]), "+s"(a.wo[0]), "+s"(a.wo[1]), "+s"(a.out), "+s"(a.ws));
  unsigned char* const ws = (unsigned char*)a.ws;
  const void* const wop = rb ? a.wo[1] : a.wo[0];
  const v4u rk0 = mkrs((unsigned char*)a.k[0] + (size_t)ch * 262144, 262144);
  lds_barrier();
  const __amdgpu_buffer_rsrc_t rw = __builtin_amdgcn_make_buffer_rsrc(ws, 0, WS_ST, RSRC3);
  v4u mask;
  {
    const unsigned half = (n & 1) ? 0xffff0000u : 0x0000ffffu; const int r = (n & 7) >> 1;
    mask.x = r == 0 ? half : 0u; mask.y = r == 1 ? half : 0u; mask.z = r == 2 ? half : 0u; mask.w = r == 3 ? half : 0u;
  }
  const int pidx = kvh * 8 + s8;
  if (__builtin_expect(wave >= 8, 1)) {
    __builtin_amdgcn_s_setprio(3);
    const int hw = wave - 8, cg = s8 & 3, kh = s8 >> 2, sub = 2 * g + (n >> 3);
    const __amdgpu_buffer_rsrc_t rq = __builtin_amdgcn_make_buffer_rsrc((unsigned char*)(rb ? a.wq[1] : a.wq[0]) + ((size_t)(kh * 288 + hw * 144) * NQ + kvh * 256 + cg * 64) * 2, 0, 143 * NQ * 2 + 128, RSRC3);
    const __amdgpu_buffer_rsrc_t rx = __builtin_amdgcn_make_buffer_rsrc((unsigned char*)(rb ? a.x[1] : a.x[0]) + (kh * 288 + hw * 144) * 2, 0, 144 * 2, RSRC3);
    v4u wqv[18];
#pragma unroll
    for (int i = 0; i < 18; ++i) wqv[i] = __builtin_amdgcn_raw_buffer_load_b128(rq, sub * (NQ * 2) + (n & 7) * 16, i * 8 * NQ * 2, AUX_NT);
    const unsigned xa = __builtin_amdgcn_raw_buffer_load_b32(rx, n * 16 + g * 4, 0, 0), xb = __builtin_amdgcn_raw_buffer_load_b32(rx, n * 16 + g * 4 + 256, 0, 0);
    const unsigned ep = __builtin_amdgcn_raw_buffer_load_b32(rw, lane == 0 ? WS_EPOCH : OOB, 0, AUX_SC1);
    SB();
    f32x4 a0 = {0.f, 0.f, 0.f, 0.f}, a1 = {0.f, 0.f, 0.f, 0.f};
    const unsigned sel = n < 8 ? 0x01000100u : 0x03020302u;
#define QSTEP(acc, i, src, k) acc = mfma(diag(__builtin_amdgcn_perm(0u, (unsigned)__builtin_amdgcn_mov_dpp((int)(src), 0x150 + (k), 0xf, 0xf, true), sel), mask), wqv[i], acc)
    QSTEP(a0, 0, xa, 0); QSTEP(a1, 1, xa, 1); QSTEP(a0, 2, xa, 2); QSTEP(a1, 3, xa, 3); QSTEP(a0, 4, xa, 4); QSTEP(a1, 5, xa, 5);
    QSTEP(a0, 6, xa, 6); QSTEP(a1, 7, xa, 7); QSTEP(a0, 8, xa, 8); QSTEP(a1, 9, xa, 9); QSTEP(a0, 10, xa, 10); QSTEP(a1, 11, xa, 11);
    QSTEP(a0, 12, xa, 12); QSTEP(a1, 13, xa, 13); QSTEP(a0, 14, xa, 14); QSTEP(a1, 15, xa, 15); QSTEP(a0, 16, xb, 0); QSTEP(a1, 17, xb, 1);
    if ((g >> 1) == (n >> 3)) *(f32x4*)(lds + L_QP + (hw * 2 + (n >> 3)) * 64 + 8 * (n & 7) + 4 * (g & 1)) = a0 + a1;
    const unsigned tag = __builtin_amdgcn_readfirstlane(ep) + TAG_STEP;
    STAMP(1);
    lds_arrive(l_cnt + 0, lane);
    if (hw == 0) {
      lds_wait<1>(l_cnt + 0, 2u);
      const float q = (lds[L_QP + lane] + lds[L_QP + 64 + lane]) + (lds[L_QP + 128 + lane] + lds[L_QP + 192 + lane]);
      st_gran(ws, WS_QG + ((rb * 16 + kvh) * 512 + kh * 256 + cg * 64 + lane) * 8, tag, q);
      if (lane == 0) l_cnt[5] = tag;
    }
    STAMP(2);
    pause(2 * QGD);
    {
      v2u x[8];
      sweep<8, 512, 8>(rw, WS_QG + ((hw * 16 + kvh) * 512 + lane) * 8, tag, x);
#pragma unroll
      for (int k = 0; k < 4; ++k) ((unsigned short*)(lds + L_Q))[hw * 256 + k * 64 + lane] = f2bf(__uint_as_float(x[k].x) + __uint_as_float(x[k + 4].x));
      lds_arrive(l_cnt + 1, lane);
    }
    STAMP(3);
#pragma nounroll
    for (int br = 0; br < 2; ++br) {
      lds_wait<8>(l_cnt + 2 + br, 8u);
      const float* ml = lds + L_ML + br * 32 + hw * 8;
      const f32x4 m0 = *(const f32x4*)(ml), m1 = *(const f32x4*)(ml + 4), l0 = *(const f32x4*)(ml + 16), l1 = *(const f32x4*)(ml + 20);
      const float M = fmax_(fmax_(fmax_(m0.x, m0.y), fmax_(m0.z, m0.w)), fmax_(fmax_(m1.x, m1.y), fmax_(m1.z, m1.w)));
      float L = 0.f;
#pragma unroll
      for (int w = 0; w < 8; ++w) L = fmaf(__builtin_amdgcn_exp2f((w < 4 ? m0[w & 3] : m1[w & 3]) - M), w < 4 ? l0[w & 3] : l1[w & 3], L);
      const int mg = WS_MG + ((br * 16 + kvh) * 2 + hw) * 1024;
      if (lane < 8) st_gran(ws, mg + (ch & 15) * 64 + lane * 8, tag, (lane & 1) ? L : M);
      if (br == 1) {
        const int d1 = WS_PG + (256 + ch) * PG_ROW, d2 = WS_YG + (rb * 128 + pidx) * YG_ROW;
        unsigned u = __builtin_amdgcn_raw_buffer_load_b32(rw, hw == 0 ? (lane < 32 ? d1 + lane * 64 : lane < 48 ? d2 + 2048 + lane * 64 : OOB) : d2 + lane * 64, 0, AUX_SC1);
        if (lane == 0) u ^= *(const GAS unsigned*)((const unsigned char*)wop + (size_t)(kvh * 256 + s8 * 32 + hw * 31) * DM * 2);
        asm volatile("" :: "v"(u));
      }
      pause(2 * QGD);
      v2u x[1];
      sweep<1, 0, 8>(rw, mg + n * 64 + (g & 1) * 8, tag, x);
      auto sw = __builtin_amdgcn_permlane16_swap(x[0].x, x[0].x, false, false);
      const float Ms = __uint_as_float(sw[0]), Ls = __uint_as_float(sw[1]);
      float Mx = fmax_(Ms, dpp<XOR1>(Ms)); Mx = fmax_(Mx, dpp<XOR2>(Mx)); Mx = fmax_(Mx, dpp<HMIR>(Mx)); Mx = fmax_(Mx, dpp<MIR>(Mx));
      float Lt = __builtin_amdgcn_exp2f(Ms - Mx) * Ls; Lt += dpp<XOR1>(Lt); Lt += dpp<XOR2>(Lt); Lt += dpp<HMIR>(Lt); Lt += dpp<MIR>(Lt);
      const float inv = 1.0f / Lt;
      float c[8];
#pragma unroll
      for (int w = 0; w < 8; ++w) c[w] = __builtin_amdgcn_exp2f((w < 4 ? m0[w & 3] : m1[w & 3]) - Mx) * inv;
      const unsigned oa = lds_addr(lds + L_O + br * 2048 + hw * 128 + lane);
      GAS unsigned char* dst = (GAS unsigned char*)ws + WS_PG + (br * 256 + ch) * PG_ROW + (hw * 128 + lane) * 8;
      asm volatile("" : "+v"(dst));
      float o0 = 0.f, o1 = 0.f;
      { const v2u v = lds_take2<8>(oa); o0 = c[0] * __uint_as_float(v.x); o1 = c[0] * __uint_as_float(v.y); }
#pragma unroll
      for (int w = 1; w < 8; ++w) { const v2u v = lds_take2<0>(oa + w * 1024); o0 = fmaf(c[w], __uint_as_float(v.x), o0); o1 = fmaf(c[w], __uint_as_float(v.y), o1); }
      __hip_atomic_store((gu64*)dst, ((unsigned long long)tag << 32) | __float_as_uint(o0), RLX_AGENT);
      __hip_atomic_store((gu64*)(dst + 512), ((unsigned long long)tag << 32) | __float_as_uint(o1), RLX_AGENT);
      if (hw == 0 && lane == 0) __hip_atomic_store(l_cnt + 4, (unsigned)br + 1u, RLX_WG);
    }
    STAMP(8);
  } else {
    St z;
    const int phs = PAT == 2 ? (wg >> 3) & 7 : wave;
    z.lds = lds; z.lane = lane; z.wave = wave; z.mask = mask;
    z.rs[0] = rk0;
    z.rs[1] = mkrs((unsigned char*)a.v[0] + (size_t)ch * 262144, 262144);
    z.rs[2] = mkrs((unsigned char*)a.k[1] + (size_t)ch * 262144, 262144);
    z.rs[3] = mkrs((unsigned char*)a.v[1] + (size_t)ch * 262144, 262144);
    z.vk = ((lane >> 2) & 3) * 256 + g * 64 + (lane & 3) * 16; z.vv = lane * 16; z.so = wave * WST;
    GAS unsigned short* outp = (GAS unsigned short*)a.out;
    z.tb = (t0 << 4) + (unsigned)(OFF16 + ((phs * DT16) >> 3));
    STAMP(1);
    Req<0, PRE>::go(z);
    STAMP(4);
    lds_wait<1>(l_cnt + 1, 2u);
    const unsigned tag = __builtin_amdgcn_readfirstlane(l_cnt[5]);
    STAMP(5);
    Con<0, PRE - DEP + 1, PRE - 1>::go(z);
    Run<PRE, 17>::go(z);
    STAMP(14);
    Run<17, 25>::go(z);
    STAMP(15);
    Run<25, 32>::go(z);
    STAMP(6);
    Con<32 - DEP + 1, 32, 31>::go(z);
    STAMP(7);
    const unsigned oba = lds_addr(lds + L_OB + g * 8);
    if (wave < 5) {
      const __amdgpu_buffer_rsrc_t ro = __builtin_amdgcn_make_buffer_rsrc((unsigned char*)wop + ((size_t)(kvh * 256 + s8 * 32) * DM) * 2, 0, 32 * DM * 2, RSRC3);
      const int wcol = wave * 128 + n * 8;
      const int vwo = (!NOWO && wcol < DM) ? g * (DM * 2) + wcol * 2 : OOB;
      v4u Wo[8];
      z.tb += (unsigned)((rb ? WO16 + wave * WOS16 : WOA16 + wave * WOSA16) - ((phs * DT16) >> 3));
      gate_next(z.tb);
#pragma unroll
      for (int i = 0; i < 8; ++i) Wo[i] = __builtin_amdgcn_raw_buffer_load_b128(ro, vwo, i * 4 * DM * 2, AUX_NT);
      GAS unsigned char* dst = (GAS unsigned char*)ws + WS_YG + (rb * 128 + pidx) * YG_ROW + (wave * 128 + lane) * 8;
      asm volatile("" : "+v"(dst), "+v"(Wo[0]), "+v"(Wo[1]), "+v"(Wo[2]), "+v"(Wo[3]), "+v"(Wo[4]), "+v"(Wo[5]), "+v"(Wo[6]), "+v"(Wo[7]));
      STAMP(2);
      v4u oa, ob;
#pragma nounroll
      for (unsigned spins = 0; spins < LSPIN_CAP; ++spins) {
        asm volatile("ds_read_b128 %0, %2\n\tds_read_b128 %1, %2 offset:16\n\ts_waitcnt lgkmcnt(0)" : "=&v"(oa), "=&v"(ob) : "v"(oba) : "memory");
        if (__all(oa.x != EMPTY_B && oa.y != EMPTY_B && oa.z != EMPTY_B && oa.w != EMPTY_B && ob.x != EMPTY_B && ob.y != EMPTY_B && ob.z != EMPTY_B && ob.w != EMPTY_B)) break;
      }
      STAMP(10);
      f32x4 Y0 = {0.f, 0.f, 0.f, 0.f}, Y1 = {0.f, 0.f, 0.f, 0.f};
#pragma unroll
      for (int i = 0; i < 4; ++i) { Y0 = mfma(diag(oa[i], mask), Wo[i], Y0); Y1 = mfma(diag(ob[i], mask), Wo[4 + i], Y1); }
      const f32x4 Y = Y0 + Y1;
      if (wcol < DM) {
        __hip_atomic_store((gu64*)dst, ((unsigned long long)tag << 32) | __float_as_uint(g < 2 ? Y.x : Y.z), RLX_AGENT);
        __hip_atomic_store((gu64*)(dst + 512), ((unsigned long long)tag << 32) | __float_as_uint(g < 2 ? Y.y : Y.w), RLX_AGENT);
      }
      STAMP(11);
    } else if (wave == 5) {
      const int po = WS_PG + (rb * 256 + kvh * 16 + (lane >> 5)) * PG_ROW + (32 * s8 + (lane & 31)) * 8;
      v2u x[8];
      lds_wait<0>(l_cnt + 4, (unsigned)rb + 1u);
      if (rb) pause(H1D);
      sweep<8, 2 * PG_ROW, 1>(rw, po, tag, x);
      STAMP(9);
      const float o = x32_sum(((__uint_as_float(x[0].x) + __uint_as_float(x[1].x)) + (__uint_as_float(x[2].x) + __uint_as_float(x[3].x)))
                            + ((__uint_as_float(x[4].x) + __uint_as_float(x[5].x)) + (__uint_as_float(x[6].x) + __uint_as_float(x[7].x))));
      if (lane < 32) ((unsigned*)(lds + L_OB))[(lane & 3) * 8 + (lane >> 2)] = dup_bf16(o);
    } else if (wave == 6 || pidx < 16) {
      const int pos = wave == 6 ? pidx * 4 : 512 + (pidx >> 1) * 16 + (pidx & 1) * 4;
      const int so = WS_YG + (rb * 128 + (lane >> 2)) * YG_ROW + (pos + (lane & 3)) * 8;
      const int p = pos + (lane & 3), pl = p & 63;
      const int col = (p & ~127) + 8 * (pl & 15) + 4 * ((pl >> 4) & 1) + 2 * (pl >> 5) + ((p >> 6) & 1);
      GAS unsigned short* op = outp + rb * DM + col;
      asm volatile("" : "+v"(op));
      v2u x[8];
      z.tb += (unsigned)((rb ? WO16 + 4 * WOS16 : WOA16 + 4 * WOSA16) + H2G16 - ((phs * DT16) >> 3));
#pragma nounroll
      for (unsigned spins = 0; spins < LSPIN_CAP; ++spins) {
        if (__builtin_amdgcn_readfirstlane(__hip_atomic_load((unsigned*)(lds + L_OB), RLX_WG)) != EMPTY_B) break;
        __builtin_amdgcn_s_sleep(1);
      }
      pause(H2D);
      gate_next(z.tb);
      sweep<8, 16 * YG_ROW, 1>(rw, so, tag, x);
      STAMP(12);
      float y = ((__uint_as_float(x[0].x) + __uint_as_float(x[1].x)) + (__uint_as_float(x[2].x) + __uint_as_float(x[3].x)))
              + ((__uint_as_float(x[4].x) + __uint_as_float(x[5].x)) + (__uint_as_float(x[6].x) + __uint_as_float(x[7].x)));
      y += dpp<ROR4>(y); y += dpp<ROR8>(y);
      y = xg_sum(y);
      if (lane < 4) *op = f2bf(y);
      if (rb == 0 && pos == 0 && lane == 0) __hip_atomic_store((gu32*)(ws + WS_EPOCH), tag, RLX_AGENT);
      STAMP(13);
    }
  }
  STAMP_OUT();
}

extern "C" void kernel_launch(void* const* d_in, const int* in_sizes, int n_in, void* d_out, int out_size, void* d_ws, size_t ws_size, hipStream_t stream) {
  Args a;
  a.x[0] = d_in[0]; a.k[0] = d_in[1]; a.v[0] = d_in[2]; a.wq[0] = d_in[3]; a.wo[0] = d_in[4];
  a.x[1] = d_in[5]; a.k[1] = d_in[6]; a.v[1] = d_in[7]; a.wq[1] = d_in[8]; a.wo[1] = d_in[9];
  a.out = d_out; a.ws = d_ws;
  gqa2_fused<<<NWG, NTHR, 0, stream>>>(a);
}
```

```cpp
#define ANC -1
#define OFF16 300
#define DT16 1840
#include <hip/hip_runtime.h>
#include <cstdint>

typedef unsigned v4u __attribute__((ext_vector_type(4)));
typedef unsigned v2u __attribute__((ext_vector_type(2)));
typedef float f32x4 __attribute__((ext_vector_type(4)));
typedef __bf16 bf16x8 __attribute__((ext_vector_type(8)));
typedef __bf16 bf16x2 __attribute__((ext_vector_type(2)));
typedef float f32x2 __attribute__((ext_vector_type(2)));
#define GAS __attribute__((address_space(1)))
#define LAS __attribute__((address_space(3)))
typedef GAS unsigned gu32;
typedef GAS unsigned long long gu64;
#define RLX_AGENT __ATOMIC_RELAXED, __HIP_MEMORY_SCOPE_AGENT
#define RLX_WG __ATOMIC_RELAXED, __HIP_MEMORY_SCOPE_WORKGROUP

#define STAMPS 0
#define PRE 8
#define DEP 3
#define LIM 2
#define WO16 0
#define WOS16 600
#define QGD 75
#define PTH16 400
#define SLK16 100000
#define H1D 40
#define H2D 70
#define PREW 1
#define HBAR 1
#define PAT 1
#define JST 8192
#define WST 1024
#define SOFT16 0
#define NSOFT 6
#define H2G16 2000
#define WOA16 0
#define WOSA16 600
#define OUTSC 0
#define SLOWSTART 0
#define NOWO 0

constexpr int DM = 576, NQ = 4096;
constexpr int NWG = 256, NTHR = 640;
constexpr int AUX_NT = 2, AUX_SC1 = 16;
constexpr int RSRC3 = 0x00020000;
constexpr int OOB = 0x7fff0000;
constexpr int WS_EPOCH = 0;
constexpr int WS_QG = 4096;
constexpr int WS_MG = WS_QG + 32 * 4096;
constexpr int WS_PG = WS_MG + 64 * 1024;
constexpr int PG_ROW = 256 * 8;
constexpr int WS_YG = WS_PG + 512 * PG_ROW;
constexpr int YG_ROW = 640 * 8;
constexpr int WS_ST = WS_YG + 256 * YG_ROW;
constexpr unsigned TAG_STEP = 0x9E3779B9u;
constexpr unsigned SPIN_CAP = 1u << 17, LSPIN_CAP = 1u << 22;
constexpr float C2 = 0.088388347648318440f * 1.4426950408889634f;
constexpr unsigned EMPTY_O = 0xffffffffu, EMPTY_B = 0x0000ffffu;

struct Args {
  const void* x[2]; const void* k[2]; const void* v[2]; const void* wq[2]; const void* wo[2];
  void* out; void* ws;
};

#define SB() __builtin_amdgcn_sched_barrier(0)
__device__ __forceinline__ f32x4 mfma(v4u a, v4u b, f32x4 c) {
  return __builtin_amdgcn_mfma_f32_16x16x32_bf16(__builtin_bit_cast(bf16x8, a), __builtin_bit_cast(bf16x8, b), c, 0, 0, 0);
}
__device__ __forceinline__ unsigned dup_bf16(float f) {
  f32x2 v = {f, f};
  return __builtin_bit_cast(unsigned, __builtin_convertvector(v, bf16x2));
}
__device__ __forceinline__ unsigned short f2bf(float f) { return __builtin_bit_cast(unsigned short, (__bf16)f); }
__device__ __forceinline__ v4u diag(unsigned pp, v4u mask) { v4u r = {pp & mask.x, pp & mask.y, pp & mask.z, pp & mask.w}; return r; }
template <int CTRL> __device__ __forceinline__ float dpp(float x) {
  return __builtin_bit_cast(float, __builtin_amdgcn_mov_dpp(__builtin_bit_cast(int, x), CTRL, 0xf, 0xf, true));
}
constexpr int XOR1 = 0xB1, XOR2 = 0x4E, HMIR = 0x141, MIR = 0x140, ROR8 = 0x128, ROR4 = 0x124;
__device__ __forceinline__ float fmax_(float a, float b) { return __builtin_fmaxf(a, b); }
__device__ __forceinline__ float xg_max(float x) {
  auto s = __builtin_amdgcn_permlane16_swap(__float_as_uint(x), __float_as_uint(x), false, false);
  x = fmax_(__uint_as_float(s[0]), __uint_as_float(s[1]));
  auto t = __builtin_amdgcn_permlane32_swap(__float_as_uint(x), __float_as_uint(x), false, false);
  return fmax_(__uint_as_float(t[0]), __uint_as_float(t[1]));
}
__device__ __forceinline__ float x32_sum(float x) {
  auto t = __builtin_amdgcn_permlane32_swap(__float_as_uint(x), __float_as_uint(x), false, false);
  return __uint_as_float(t[0]) + __uint_as_float(t[1]);
}
__device__ __forceinline__ float xg_sum(float x) {
  auto s = __builtin_amdgcn_permlane16_swap(__float_as_uint(x), __float_as_uint(x), false, false);
  return x32_sum(__uint_as_float(s[0]) + __uint_as_float(s[1]));
}
__device__ __forceinline__ void st_gran(unsigned char* ws, int off, unsigned tag, float v) {
  __hip_atomic_store((gu64*)(ws + off), ((unsigned long long)tag << 32) | __float_as_uint(v), RLX_AGENT);
}
__device__ __forceinline__ v2u ldg(__amdgpu_buffer_rsrc_t rs, int off) { return __builtin_amdgcn_raw_buffer_load_b64(rs, off, 0, AUX_SC1); }
template <int N, int STEP, int SLEEP> __device__ __forceinline__ void sweep(__amdgpu_buffer_rsrc_t rs, int off, unsigned tag, v2u (&x)[N]) {
#pragma unroll
  for (int k = 0; k < N; ++k) x[k] = ldg(rs, off + k * STEP);
  SB();
  bool ok = true;
#pragma unroll
  for (int k = 0; k < N; ++k) ok &= x[k].y == tag;
  if (__builtin_expect(__all(ok), 1)) return;
#pragma nounroll
  for (unsigned spins = 0; spins < SPIN_CAP; ++spins) {
    __builtin_amdgcn_s_sleep(SLEEP);
    v2u y[N];
#pragma unroll
    for (int k = 0; k < N; ++k) y[k] = ldg(rs, x[k].y == tag ? OOB : off + k * STEP);
    SB();
    ok = true;
#pragma unroll
    for (int k = 0; k < N; ++k) { x[k] = x[k].y == tag ? x[k] : y[k]; ok &= x[k].y == tag; }
    if (__all(ok)) break;
  }
}
__device__ __forceinline__ v4u mkrs(const void* p, unsigned bytes) {
  const unsigned long long u = (unsigned long long)p;
  v4u r = {(unsigned)u, (unsigned)(u >> 32) & 0xffffu, bytes, (unsigned)RSRC3};
  return r;
}
__device__ __forceinline__ void tile_load(v4u (&d)[4], int voff, v4u rs, int soff) {
  asm volatile("buffer_load_dwordx4 %0, %4, %5, %6 offen nt\n\t"
               "buffer_load_dwordx4 %1, %4, %5, %7 offen nt\n\t"
               "buffer_load_dwordx4 %2, %4, %5, %8 offen nt\n\t"
               "buffer_load_dwordx4 %3, %4, %5, %9 offen nt"
               : "=&v"(d[0]), "=&v"(d[1]), "=&v"(d[2]), "=&v"(d[3]) : "v"(voff), "s"(rs), "s"(soff), "s"(soff + JST), "s"(soff + 2 * JST), "s"(soff + 3 * JST));
}
template <int N> __device__ __forceinline__ void tile_wait(v4u (&d)[4]) {
  asm volatile("s_waitcnt vmcnt(%4)" : "+v"(d[0]), "+v"(d[1]), "+v"(d[2]), "+v"(d[3]) : "i"(N));
}
template <int N> __device__ __forceinline__ void piece_wait(v4u& d) { asm volatile("s_waitcnt vmcnt(%1)" : "+v"(d) : "i"(N)); }
__device__ __forceinline__ void pause(int ticks) {
  unsigned due;
  asm volatile("s_memrealtime vcc\n\t"
               "s_waitcnt lgkmcnt(0)\n\t"
               "s_add_i32 %0, vcc_lo, %1\n"
               ".Lg0_%=:\n\t"
               "s_sleep 1\n\t"
               "s_memrealtime vcc\n\t"
               "s_waitcnt lgkmcnt(0)\n\t"
               "s_sub_i32 vcc_lo, vcc_lo, %0\n\t"
               "s_cmp_lt_i32 vcc_lo, 0\n\t"
               "s_cbranch_scc1 .Lg0_%="
               : "=&s"(due) : "i"(ticks) : "scc", "vcc", "memory");
}
__device__ __forceinline__ void gate_next(unsigned& due) {
  asm volatile("s_branch .Lg1_%=\n"
               ".Lg0_%=:\n\t"
               "s_sleep 1\n"
               ".Lg1_%=:\n\t"
               "s_memrealtime vcc\n\t"
               "s_waitcnt lgkmcnt(0)\n\t"
               "s_lshl_b32 vcc_lo, vcc_lo, 4\n\t"
               "s_sub_i32 vcc_lo, vcc_lo, %0\n\t"
               "s_cmp_lt_i32 vcc_lo, 0\n\t"
               "s_cbranch_scc1 .Lg0_%=\n\t"
               "s_setprio 0\n\t"
               "s_cmp_lt_i32 vcc_lo, %1\n\t"
               "s_cbranch_scc1 .Lg2_%=\n\t"
               "s_setprio 1\n"
               ".Lg2_%=:\n\t"
               "s_sub_i32 vcc_lo, vcc_lo, %2\n\t"
               "s_max_i32 vcc_lo, vcc_lo, 0\n\t"
               "s_add_i32 %0, %0, vcc_lo\n\t"
               "s_add_i32 %0, %0, %3"
               : "+s"(due) : "i"(PTH16), "i"(SLK16), "i"(DT16) : "scc", "vcc", "memory");
}
__device__ __forceinline__ void lds_barrier() {
  asm volatile("s_waitcnt lgkmcnt(0)" ::: "memory");
  __builtin_amdgcn_s_barrier();
  asm volatile("" ::: "memory");
}
__device__ __forceinline__ void lds_arrive(unsigned* c, int lane) {
  asm volatile("s_waitcnt lgkmcnt(0)" ::: "memory");
  if (lane == 0) __hip_atomic_fetch_add(c, 1u, RLX_WG);
}
template <int SLEEP> __device__ __forceinline__ unsigned lds_wait(unsigned* c, unsigned want) {
  unsigned v = 0;
#pragma nounroll
  for (unsigned spins = 0; spins < LSPIN_CAP; ++spins) {
    v = __builtin_amdgcn_readfirstlane(__hip_atomic_load(c, RLX_WG));
    if (v >= want) break;
    __builtin_amdgcn_s_sleep(SLEEP);
  }
  asm volatile("" ::: "memory");
  return v;
}
__device__ __forceinline__ unsigned lds_addr(const void* p) { return (unsigned)(size_t)(const LAS void*)p; }
template <int SLEEP> __device__ __forceinline__ v2u lds_take2(unsigned addr) {
  v2u v;
#pragma nounroll
  for (unsigned spins = 0; spins < LSPIN_CAP; ++spins) {
    asm volatile("ds_read2st64_b32 %0, %1 offset1:1\n\ts_waitcnt lgkmcnt(0)" : "=&v"(v) : "v"(addr) : "memory");
    if (__all(v.x != EMPTY_O && v.y != EMPTY_O)) break;
    if (SLEEP) __builtin_amdgcn_s_sleep(SLEEP);
  }
  return v;
}
#define STAMP(i) do {} while (0)
#define STAMP_OUT() do {} while (0)

constexpr int L_O = 0;
constexpr int L_ML = L_O + 4096;
constexpr int L_QP = L_ML + 64;
constexpr int L_Q = L_QP + 256;
constexpr int L_OB = L_Q + 256;
constexpr int L_CNT = L_OB + 32;
constexpr int L_END = L_CNT + 8;

struct St {
  v4u T[32][4];
  float Sc[8][4];
  unsigned P[8][4];
  v4u Q, mask;
  f32x4 O;
  v4u rs[4];
  int vk, vv, so;
  unsigned tb;
  float* lds;
  int lane, wave;
};

template <int I> __device__ __forceinline__ void request(St& z) {
  if constexpr (4 * I > LIM) asm volatile("s_waitcnt vmcnt(%0)" :: "i"(LIM) : "memory");
  gate_next(z.tb);
  if constexpr (SOFT16 && I < NSOFT) z.tb += SOFT16;
  tile_load(z.T[I], (I & 8) ? z.vv : z.vk, z.rs[I >> 3], z.so + (I & 7) * 32768);
  SB();
}
template <int I, int W> __device__ __forceinline__ void consume(St& z) {
  const int g = z.lane >> 4, n = z.lane & 15;
  constexpr int br = I >> 4;
  if constexpr ((I & 15) == 0) {
    z.Q = *(const v4u*)((const unsigned short*)(z.lds + L_Q) + br * 256 + (n >> 3) * 128 + 32 * g + 8 * (n & 3));
  }
  if constexpr ((I & 8) == 0) {
    tile_wait<W>(z.T[I]);
#pragma unroll
    for (int j = 0; j < 4; ++j) {
      const f32x4 d = mfma(z.T[I][j], z.Q, f32x4{0.f, 0.f, 0.f, 0.f});
      z.Sc[I & 7][j] = (dpp<0x00>(d.x) + dpp<0x55>(d.y)) + (dpp<0xAA>(d.z) + dpp<0xFF>(d.w));
    }
    if constexpr ((I & 7) == 7) {
      float m = z.Sc[0][0];
#pragma unroll
      for (int t = 0; t < 8; ++t)
#pragma unroll
        for (int j = 0; j < 4; ++j) m = fmax_(m, z.Sc[t][j]);
      const float m2 = xg_max(m) * C2;
      float l = 0.f;
#pragma unroll
      for (int t = 0; t < 8; ++t) {
        float p[4];
#pragma unroll
        for (int j = 0; j < 4; ++j) { p[j] = __builtin_amdgcn_exp2f(fmaf(z.Sc[t][j], C2, -m2)); z.P[t][j] = dup_bf16(p[j]); }
        l += (p[0] + p[1]) + (p[2] + p[3]);
        asm volatile("" : "+v"(z.P[t][0]), "+v"(z.P[t][1]), "+v"(z.P[t][2]), "+v"(z.P[t][3]));
      }
      l = xg_sum(l);
      if (g == 0 && (n & 7) == 0) { z.lds[L_ML + br * 32 + (n >> 3) * 8 + z.wave] = m2; z.lds[L_ML + br * 32 + 16 + (n >> 3) * 8 + z.wave] = l; }
      lds_arrive((unsigned*)(z.lds + L_CNT) + 2 + br, z.lane);
      z.O = f32x4{0.f, 0.f, 0.f, 0.f};
    } else {
      asm volatile("" : "+v"(z.Sc[I & 7][0]), "+v"(z.Sc[I & 7][1]), "+v"(z.Sc[I & 7][2]), "+v"(z.Sc[I & 7][3]));
    }
  } else if constexpr (W == 0) {
    v4u A[4];
#pragma unroll
    for (int j = 0; j < 4; ++j) A[j] = diag(z.P[I & 7][j], z.mask);
    float* dst = z.lds + L_O + br * 2048 + z.wave * 256 + (g >> 1) * 128 + 8 * n + 4 * (g & 1);
    unsigned da = lds_addr(dst);
    asm volatile("" : "+v"(A[0]), "+v"(A[1]), "+v"(A[2]), "+v"(A[3]), "+v"(da));
    piece_wait<3>(z.T[I][0]); z.O = mfma(A[0], z.T[I][0], z.O); SB();
    piece_wait<2>(z.T[I][1]); z.O = mfma(A[1], z.T[I][1], z.O); SB();
    piece_wait<1>(z.T[I][2]); z.O = mfma(A[2], z.T[I][2], z.O); SB();
    piece_wait<0>(z.T[I][3]); z.O = mfma(A[3], z.T[I][3], z.O);
    *(LAS f32x4*)(size_t)da = z.O;
  } else {
    tile_wait<W>(z.T[I]);
#pragma unroll
    for (int j = 0; j < 4; ++j) z.O = mfma(diag(z.P[I & 7][j], z.mask), z.T[I][j], z.O);
    asm volatile("" : "+v"(z.O));
    if constexpr ((I & 7) == 7) *(f32x4*)(z.lds + L_O + br * 2048 + z.wave * 256 + (g >> 1) * 128 + 8 * n + 4 * (g & 1)) = z.O;
  }
  SB();
}
template <int I, int N> struct Run {
  static __device__ __forceinline__ void go(St& z) { request<I>(z); consume<I - DEP + 1, 4 * (DEP - 1)>(z); Run<I + 1, N>::go(z); }
};
template <int N> struct Run<N, N> { static __device__ __forceinline__ void go(St&) {} };
template <int I, int N> struct Req { static __device__ __forceinline__ void go(St& z) { request<I>(z); Req<I + 1, N>::go(z); } };
template <int N> struct Req<N, N> { static __device__ __forceinline__ void go(St&) {} };
template <int I, int N, int LAST> struct Con {
  static __device__ __forceinline__ void go(St& z) { consume<I, 4 * (LAST - I)>(z); Con<I + 1, N, LAST>::go(z); }
};
template <int N, int LAST> struct Con<N, N, LAST> { static __device__ __forceinline__ void go(St&) {} };

__global__ void __launch_bounds__(NTHR, 3) gqa2_fused(Args a) {
  __shared__ __attribute__((aligned(16))) float lds[L_END];
  unsigned* const l_cnt = (unsigned*)(lds + L_CNT);
  STAMP(0);
  const int tid = threadIdx.x, lane = tid & 63, wave = __builtin_amdgcn_readfirstlane(tid >> 6);
  const int g = lane >> 4, n = lane & 15;
  const int wg = blockIdx.x;
  const int ch = (wg & 7) << 5 | wg >> 3;
  const int kvh = ch >> 4, rb = (ch >> 3) & 1, s8 = ch & 7;
  if (tid < 512) {
    const v4u e = {EMPTY_O, EMPTY_O, EMPTY_O, EMPTY_O};
    *(v4u*)(lds + L_O + tid * 4) = e; *(v4u*)(lds + L_O + 2048 + tid * 4) = e;
  } else if (tid < 512 + 40) {
    ((unsigned*)(lds + L_OB))[tid - 512] = tid < 512 + 32 ? EMPTY_B : 0u;
  }
  asm volatile("" : "+s"(a.x[0]), "+s"(a.x[1]), "+s"(a.k[0]), "+s"(a.k[1]), "+s"(a.v[0]), "+s"(a.v[1]), "+s"(a.wq[0]), "+s"(a.wq[1]), "+s"(a.wo[0]), "+s"(a.wo[1]), "+s"(a.out), "+s"(a.ws));
  unsigned char* const ws = (unsigned char*)a.ws;
  const void* const wop = rb ? a.wo[1] : a.wo[0];
  const v4u rk0 = mkrs((unsigned char*)a.k[0] + (size_t)ch * 262144, 262144);
  unsigned probe;
  asm volatile("s_cmp_lg_u32 %2, 0\n\ts_cbranch_scc1 .Lp%=\n\ts_mov_b64 exec, 1\n\tbuffer_load_dword %0, off, %1, 0 nt\n\ts_mov_b64 exec, -1\n.Lp%=:"
               : "=v"(probe) : "s"(rk0), "s"(wave) : "scc");
  lds_barrier();
  const __amdgpu_buffer_rsrc_t rw = __builtin_amdgcn_make_buffer_rsrc(ws, 0, WS_ST, RSRC3);
  v4u mask;
  {
    const unsigned half = (n & 1) ? 0xffff0000u : 0x0000ffffu; const int r = (n & 7) >> 1;
    mask.x = r == 0 ? half : 0u; mask.y = r == 1 ? half : 0u; mask.z = r == 2 ? half : 0u; mask.w = r == 3 ? half : 0u;
  }
  const int pidx = kvh * 8 + s8;
  if (__builtin_expect(wave >= 8, 1)) {
    __builtin_amdgcn_s_setprio(3);
    const int hw = wave - 8, cg = s8 & 3, kh = s8 >> 2, sub = 2 * g + (n >> 3);
    const __amdgpu_buffer_rsrc_t rq = __builtin_amdgcn_make_buffer_rsrc((unsigned char*)(rb ? a.wq[1] : a.wq[0]) + ((size_t)(kh * 288 + hw * 144) * NQ + kvh * 256 + cg * 64) * 2, 0, 143 * NQ * 2 + 128, RSRC3);
    const __amdgpu_buffer_rsrc_t rx = __builtin_amdgcn_make_buffer_rsrc((unsigned char*)(rb ? a.x[1] : a.x[0]) + (kh * 288 + hw * 144) * 2, 0, 144 * 2, RSRC3);
    v4u wqv[18];
#pragma unroll
    for (int i = 0; i < 18; ++i) wqv[i] = __builtin_amdgcn_raw_buffer_load_b128(rq, sub * (NQ * 2) + (n & 7) * 16, i * 8 * NQ * 2, AUX_NT);
    const unsigned xa = __builtin_amdgcn_raw_buffer_load_b32(rx, n * 16 + g * 4, 0, 0), xb = __builtin_amdgcn_raw_buffer_load_b32(rx, n * 16 + g * 4 + 256, 0, 0);
    const unsigned ep = __builtin_amdgcn_raw_buffer_load_b32(rw, lane == 0 ? WS_EPOCH : OOB, 0, AUX_SC1);
    SB();
    f32x4 a0 = {0.f, 0.f, 0.f, 0.f}, a1 = {0.f, 0.f, 0.f, 0.f};
    const unsigned sel = n < 8 ? 0x01000100u : 0x03020302u;
#define QSTEP(acc, i, src, k) acc = mfma(diag(__builtin_amdgcn_perm(0u, (unsigned)__builtin_amdgcn_mov_dpp((int)(src), 0x150 + (k), 0xf, 0xf, true), sel), mask), wqv[i], acc)
    QSTEP(a0, 0, xa, 0); QSTEP(a1, 1, xa, 1); QSTEP(a0, 2, xa, 2); QSTEP(a1, 3, xa, 3); QSTEP(a0, 4, xa, 4); QSTEP(a1, 5, xa, 5);
    QSTEP(a0, 6, xa, 6); QSTEP(a1, 7, xa, 7); QSTEP(a0, 8, xa, 8); QSTEP(a1, 9, xa, 9); QSTEP(a0, 10, xa, 10); QSTEP(a1, 11, xa, 11);
    QSTEP(a0, 12, xa, 12); QSTEP(a1, 13, xa, 13); QSTEP(a0, 14, xa, 14); QSTEP(a1, 15, xa, 15); QSTEP(a0, 16, xb, 0); QSTEP(a1, 17, xb, 1);
    if ((g >> 1) == (n >> 3)) *(f32x4*)(lds + L_QP + (hw * 2 + (n >> 3)) * 64 + 8 * (n & 7) + 4 * (g & 1)) = a0 + a1;
    const unsigned tag = __builtin_amdgcn_readfirstlane(ep) + TAG_STEP;
    STAMP(1);
    lds_arrive(l_cnt + 0, lane);
    if (hw == 0) {
      lds_wait<1>(l_cnt + 0, 2u);
      const float q = (lds[L_QP + lane] + lds[L_QP + 64 + lane]) + (lds[L_QP + 128 + lane] + lds[L_QP + 192 + lane]);
      st_gran(ws, WS_QG + ((rb * 16 + kvh) * 512 + kh * 256 + cg * 64 + lane) * 8, tag, q);
      if (lane == 0) l_cnt[5] = tag;
    }
    STAMP(2);
    pause(2 * QGD);
    {
      v2u x[8];
      sweep<8, 512, 8>(rw, WS_QG + ((hw * 16 + kvh) * 512 + lane) * 8, tag, x);
#pragma unroll
      for (int k = 0; k < 4; ++k) ((unsigned short*)(lds + L_Q))[hw * 256 + k * 64 + lane] = f2bf(__uint_as_float(x[k].x) + __uint_as_float(x[k + 4].x));
      lds_arrive(l_cnt + 1, lane);
    }
    STAMP(3);
#pragma nounroll
    for (int br = 0; br < 2; ++br) {
      lds_wait<8>(l_cnt + 2 + br, 8u);
      const float* ml = lds + L_ML + br * 32 + hw * 8;
      const f32x4 m0 = *(const f32x4*)(ml), m1 = *(const f32x4*)(ml + 4), l0 = *(const f32x4*)(ml + 16), l1 = *(const f32x4*)(ml + 20);
      const float M = fmax_(fmax_(fmax_(m0.x, m0.y), fmax_(m0.z, m0.w)), fmax_(fmax_(m1.x, m1.y), fmax_(m1.z, m1.w)));
      float L = 0.f;
#pragma unroll
      for (int w = 0; w < 8; ++w) L = fmaf(__builtin_amdgcn_exp2f((w < 4 ? m0[w & 3] : m1[w & 3]) - M), w < 4 ? l0[w & 3] : l1[w & 3], L);
      const int mg = WS_MG + ((br * 16 + kvh) * 2 + hw) * 1024;
      if (lane < 8) st_gran(ws, mg + (ch & 15) * 64 + lane * 8, tag, (lane & 1) ? L : M);
      if (br == 1) {
        const int d1 = WS_PG + (256 + ch) * PG_ROW, d2 = WS_YG + (rb * 128 + pidx) * YG_ROW;
        unsigned u = __builtin_amdgcn_raw_buffer_load_b32(rw, hw == 0 ? (lane < 32 ? d1 + lane * 64 : lane < 48 ? d2 + 2048 + lane * 64 : OOB) : d2 + lane * 64, 0, AUX_SC1);
        if (lane == 0) u ^= *(const GAS unsigned*)((const unsigned char*)wop + (size_t)(kvh * 256 + s8 * 32 + hw * 31) * DM * 2);
        asm volatile("" :: "v"(u));
      }
      pause(2 * QGD);
      v2u x[1];
      sweep<1, 0, 8>(rw, mg + n * 64 + (g & 1) * 8, tag, x);
      auto sw = __builtin_amdgcn_permlane16_swap(x[0].x, x[0].x, false, false);
      const float Ms = __uint_as_float(sw[0]), Ls = __uint_as_float(sw[1]);
      float Mx = fmax_(Ms, dpp<XOR1>(Ms)); Mx = fmax_(Mx, dpp<XOR2>(Mx)); Mx = fmax_(Mx, dpp<HMIR>(Mx)); Mx = fmax_(Mx, dpp<MIR>(Mx));
      float Lt = __builtin_amdgcn_exp2f(Ms - Mx) * Ls; Lt += dpp<XOR1>(Lt); Lt += dpp<XOR2>(Lt); Lt += dpp<HMIR>(Lt); Lt += dpp<MIR>(Lt);
      const float inv = 1.0f / Lt;
      float c[8];
#pragma unroll
      for (int w = 0; w < 8; ++w) c[w] = __builtin_amdgcn_exp2f((w < 4 ? m0[w & 3] : m1[w & 3]) - Mx) * inv;
      const unsigned oa = lds_addr(lds + L_O + br * 2048 + hw * 128 + lane);
      GAS unsigned char* dst = (GAS unsigned char*)ws + WS_PG + (br * 256 + ch) * PG_ROW + (hw * 128 + lane) * 8;
      asm volatile("" : "+v"(dst));
      float o0 = 0.f, o1 = 0.f;
      { const v2u v = lds_take2<8>(oa); o0 = c[0] * __uint_as_float(v.x); o1 = c[0] * __uint_as_float(v.y); }
#pragma unroll
      for (int w = 1; w < 8; ++w) { const v2u v = lds_take2<0>(oa + w * 1024); o0 = fmaf(c[w], __uint_as_float(v.x), o0); o1 = fmaf(c[w], __uint_as_float(v.y), o1); }
      __hip_atomic_store((gu64*)dst, ((unsigned long long)tag << 32) | __float_as_uint(o0), RLX_AGENT);
      __hip_atomic_store((gu64*)(dst + 512), ((unsigned long long)tag << 32) | __float_as_uint(o1), RLX_AGENT);
      if (hw == 0 && lane == 0) __hip_atomic_store(l_cnt + 4, (unsigned)br + 1u, RLX_WG);
    }
    STAMP(8);
  } else {
    St z;
    const int phs = PAT == 2 ? (wg >> 3) & 7 : wave;
    z.lds = lds; z.lane = lane; z.wave = wave; z.mask = mask;
    z.rs[0] = rk0;
    z.rs[1] = mkrs((unsigned char*)a.v[0] + (size_t)ch * 262144, 262144);
    z.rs[2] = mkrs((unsigned char*)a.k[1] + (size_t)ch * 262144, 262144);
    z.rs[3] = mkrs((unsigned char*)a.v[1] + (size_t)ch * 262144, 262144);
    z.vk = ((lane >> 2) & 3) * 256 + g * 64 + (lane & 3) * 16; z.vv = lane * 16; z.so = wave * WST;
    GAS unsigned short* outp = (GAS unsigned short*)a.out;
    if (wave == 0) {
      unsigned long long ta;
      asm volatile("s_waitcnt vmcnt(0)\n\ts_memrealtime %0\n\ts_waitcnt lgkmcnt(0)" : "=s"(ta) : "v"(probe) : "memory");
      if (lane == 0) __hip_atomic_store(l_cnt + 6, ((unsigned)ta << 4) | 1u, RLX_WG);
    }
    z.tb = lds_wait<1>(l_cnt + 6, 1u) + (unsigned)(OFF16 + ((phs * DT16) >> 3));
    STAMP(1);
    Req<0, PRE>::go(z);
    STAMP(4);
    lds_wait<1>(l_cnt + 1, 2u);
    const unsigned tag = __builtin_amdgcn_readfirstlane(l_cnt[5]);
    STAMP(5);
    Con<0, PRE - DEP + 1, PRE - 1>::go(z);
    Run<PRE, 17>::go(z);
    STAMP(14);
    Run<17, 25>::go(z);
    STAMP(15);
    Run<25, 32>::go(z);
    STAMP(6);
    Con<32 - DEP + 1, 32, 31>::go(z);
    STAMP(7);
    const unsigned oba = lds_addr(lds + L_OB + g * 8);
    if (wave < 5) {
      const __amdgpu_buffer_rsrc_t ro = __builtin_amdgcn_make_buffer_rsrc((unsigned char*)wop + ((size_t)(kvh * 256 + s8 * 32) * DM) * 2, 0, 32 * DM * 2, RSRC3);
      const int wcol = wave * 128 + n * 8;
      const int vwo = (!NOWO && wcol < DM) ? g * (DM * 2) + wcol * 2 : OOB;
      v4u Wo[8];
      z.tb += (unsigned)((rb ? WO16 + wave * WOS16 : WOA16 + wave * WOSA16) - ((phs * DT16) >> 3));
      gate_next(z.tb);
#pragma unroll
      for (int i = 0; i < 8; ++i) Wo[i] = __builtin_amdgcn_raw_buffer_load_b128(ro, vwo, i * 4 * DM * 2, AUX_NT);
      GAS unsigned char* dst = (GAS unsigned char*)ws + WS_YG + (rb * 128 + pidx) * YG_ROW + (wave * 128 + lane) * 8;
      asm volatile("" : "+v"(dst), "+v"(Wo[0]), "+v"(Wo[1]), "+v"(Wo[2]), "+v"(Wo[3]), "+v"(Wo[4]), "+v"(Wo[5]), "+v"(Wo[6]), "+v"(Wo[7]));
      STAMP(2);
      v4u oa, ob;
#pragma nounroll
      for (unsigned spins = 0; spins < LSPIN_CAP; ++spins) {
        asm volatile("ds_read_b128 %0, %2\n\tds_read_b128 %1, %2 offset:16\n\ts_waitcnt lgkmcnt(0)" : "=&v"(oa), "=&v"(ob) : "v"(oba) : "memory");
        if (__all(oa.x != EMPTY_B && oa.y != EMPTY_B && oa.z != EMPTY_B && oa.w != EMPTY_B && ob.x != EMPTY_B && ob.y != EMPTY_B && ob.z != EMPTY_B && ob.w != EMPTY_B)) break;
      }
      STAMP(10);
      f32x4 Y0 = {0.f, 0.f, 0.f, 0.f}, Y1 = {0.f, 0.f, 0.f, 0.f};
#pragma unroll
      for (int i = 0; i < 4; ++i) { Y0 = mfma(diag(oa[i], mask), Wo[i], Y0); Y1 = mfma(diag(ob[i], mask), Wo[4 + i], Y1); }
      const f32x4 Y = Y0 + Y1;
      if (wcol < DM) {
        __hip_atomic_store((gu64*)dst, ((unsigned long long)tag << 32) | __float_as_uint(g < 2 ? Y.x : Y.z), RLX_AGENT);
        __hip_atomic_store((gu64*)(dst + 512), ((unsigned long long)tag << 32) | __float_as_uint(g < 2 ? Y.y : Y.w), RLX_AGENT);
      }
      STAMP(11);
    } else if (wave == 5) {
      const int po = WS_PG + (rb * 256 + kvh * 16 + (lane >> 5)) * PG_ROW + (32 * s8 + (lane & 31)) * 8;
      v2u x[8];
      lds_wait<0>(l_cnt + 4, (unsigned)rb + 1u);
      if (rb) pause(H1D);
      sweep<8, 2 * PG_ROW, 1>(rw, po, tag, x);
      STAMP(9);
      const float o = x32_sum(((__uint_as_float(x[0].x) + __uint_as_float(x[1].x)) + (__uint_as_float(x[2].x) + __uint_as_float(x[3].x)))
                            + ((__uint_as_float(x[4].x) + __uint_as_float(x[5].x)) + (__uint_as_float(x[6].x) + __uint_as_float(x[7].x))));
      if (lane < 32) ((unsigned*)(lds + L_OB))[(lane & 3) * 8 + (lane >> 2)] = dup_bf16(o);
    } else if (wave == 6 || pidx < 16) {
      const int pos = wave == 6 ? pidx * 4 : 512 + (pidx >> 1) * 16 + (pidx & 1) * 4;
      const int so = WS_YG + (rb * 128 + (lane >> 2)) * YG_ROW + (pos + (lane & 3)) * 8;
      const int p = pos + (lane & 3), pl = p & 63;
      const int col = (p & ~127) + 8 * (pl & 15) + 4 * ((pl >> 4) & 1) + 2 * (pl >> 5) + ((p >> 6) & 1);
      GAS unsigned short* op = outp + rb * DM + col;
      asm volatile("" : "+v"(op));
      v2u x[8];
      z.tb += (unsigned)((rb ? WO16 + 4 * WOS16 : WOA16 + 4 * WOSA16) + H2G16 - ((phs * DT16) >> 3));
#pragma nounroll
      for (unsigned spins = 0; spins < LSPIN_CAP; ++spins) {
        if (__builtin_amdgcn_readfirstlane(__hip_atomic_load((unsigned*)(lds + L_OB), RLX_WG)) != EMPTY_B) break;
        __builtin_amdgcn_s_sleep(1);
      }
      pause(H2D);
      gate_next(z.tb);
      sweep<8, 16 * YG_ROW, 1>(rw, so, tag, x);
      STAMP(12);
      float y = ((__uint_as_float(x[0].x) + __uint_as_float(x[1].x)) + (__uint_as_float(x[2].x) + __uint_as_float(x[3].x)))
              + ((__uint_as_float(x[4].x) + __uint_as_float(x[5].x)) + (__uint_as_float(x[6].x) + __uint_as_float(x[7].x)));
      y += dpp<ROR4>(y); y += dpp<ROR8>(y);
      y = xg_sum(y);
      if (lane < 4) *op = f2bf(y);
      if (rb == 0 && pos == 0 && lane == 0) __hip_atomic_store((gu32*)(ws + WS_EPOCH), tag, RLX_AGENT);
      STAMP(13);
    }
  }
  STAMP_OUT();
}

extern "C" void kernel_launch(void* const* d_in, const int* in_sizes, int n_in, void* d_out, int out_size, void* d_ws, size_t ws_size, hipStream_t stream) {
  Args a;
  a.x[0] = d_in[0]; a.k[0] = d_in[1]; a.v[0] = d_in[2]; a.wq[0] = d_in[3]; a.wo[0] = d_in[4];
  a.x[1] = d_in[5]; a.k[1] = d_in[6]; a.v[1] = d_in[7]; a.wq[1] = d_in[8]; a.wo[1] = d_in[9];
  a.out = d_out; a.ws = d_ws;
  gqa2_fused<<<NWG, NTHR, 0, stream>>>(a);
}
```

```cpp
#define ANC -1
#define QGA 280
#define OFF16 600
#define DT16 1792
#include <hip/hip_runtime.h>
#include <cstdint>

typedef unsigned v4u __attribute__((ext_vector_type(4)));
typedef unsigned v2u __attribute__((ext_vector_type(2)));
typedef float f32x4 __attribute__((ext_vector_type(4)));
typedef __bf16 bf16x8 __attribute__((ext_vector_type(8)));
typedef __bf16 bf16x2 __attribute__((ext_vector_type(2)));
typedef float f32x2 __attribute__((ext_vector_type(2)));
#define GAS __attribute__((address_space(1)))
#define LAS __attribute__((address_space(3)))
typedef GAS unsigned gu32;
typedef GAS unsigned long long gu64;
#define RLX_AGENT __ATOMIC_RELAXED, __HIP_MEMORY_SCOPE_AGENT
#define RLX_WG __ATOMIC_RELAXED, __HIP_MEMORY_SCOPE_WORKGROUP

#define STAMPS 0
#define PRE 8
#define DEP 3
#define LIM 2
#define WO16 0
#define WOS16 600
#define QGD 75
#define PTH16 400
#define SLK16 100000
#define H1D 40
#define H2D 70
#define PREW 0
#define HBAR 1
#define PAT 1
#define JST 8192
#define WST 1024
#define SOFT16 0
#define NSOFT 6
#define H2G16 2000
#define WOA16 0
#define WOSA16 600
#define OUTSC 0
#define SLOWSTART 0
#define P016 2400
#define NLIM 32
#define LIMB 8
#define DF16 0
#define JIT16 160
#define ABL 0
#define QGB 600
#define MLD 350
#define NOWO 0

constexpr int DM = 576, NQ = 4096;
constexpr int NWG = 256, NTHR = 640;
constexpr int AUX_NT = 2, AUX_SC1 = 16;
constexpr int RSRC3 = 0x00020000;
constexpr int OOB = 0x7fff0000;
constexpr int WS_EPOCH = 0;
constexpr int WS_QG = 4096;
constexpr int WS_MG = WS_QG + 32 * 4096;
constexpr int WS_PG = WS_MG + 64 * 1024;
constexpr int PG_ROW = 256 * 8;
constexpr int WS_YG = WS_PG + 512 * PG_ROW;
constexpr int YG_ROW = 640 * 8;
constexpr int WS_ST = WS_YG + 256 * YG_ROW;
constexpr unsigned TAG_STEP = 0x9E3779B9u;
constexpr unsigned SPIN_CAP = 1u << 17, LSPIN_CAP = 1u << 22;
constexpr float C2 = 0.088388347648318440f * 1.4426950408889634f;
constexpr unsigned EMPTY_O = 0xffffffffu, EMPTY_B = 0x0000ffffu;

struct Args {
  const void* x[2]; const void* k[2]; const void* v[2]; const void* wq[2]; const void* wo[2];
  void* out; void* ws;
};

#define SB() __builtin_amdgcn_sched_barrier(0)
__device__ __forceinline__ f32x4 mfma(v4u a, v4u b, f32x4 c) {
  return __builtin_amdgcn_mfma_f32_16x16x32_bf16(__builtin_bit_cast(bf16x8, a), __builtin_bit_cast(bf16x8, b), c, 0, 0, 0);
}
__device__ __forceinline__ unsigned dup_bf16(float f) {
  f32x2 v = {f, f};
  return __builtin_bit_cast(unsigned, __builtin_convertvector(v, bf16x2));
}
__device__ __forceinline__ unsigned short f2bf(float f) { return __builtin_bit_cast(unsigned short, (__bf16)f); }
__device__ __forceinline__ v4u diag(unsigned pp, v4u mask) { v4u r = {pp & mask.x, pp & mask.y, pp & mask.z, pp & mask.w}; return r; }
template <int CTRL> __device__ __forceinline__ float dpp(float x) {
  return __builtin_bit_cast(float, __builtin_amdgcn_mov_dpp(__builtin_bit_cast(int, x), CTRL, 0xf, 0xf, true));
}
constexpr int XOR1 = 0xB1, XOR2 = 0x4E, HMIR = 0x141, MIR = 0x140, ROR8 = 0x128, ROR4 = 0x124;
__device__ __forceinline__ float fmax_(float a, float b) { return __builtin_fmaxf(a, b); }
__device__ __forceinline__ float xg_max(float x) {
  auto s = __builtin_amdgcn_permlane16_swap(__float_as_uint(x), __float_as_uint(x), false, false);
  x = fmax_(__uint_as_float(s[0]), __uint_as_float(s[1]));
  auto t = __builtin_amdgcn_permlane32_swap(__float_as_uint(x), __float_as_uint(x), false, false);
  return fmax_(__uint_as_float(t[0]), __uint_as_float(t[1]));
}
__device__ __forceinline__ float x32_sum(float x) {
  auto t = __builtin_amdgcn_permlane32_swap(__float_as_uint(x), __float_as_uint(x), false, false);
  return __uint_as_float(t[0]) + __uint_as_float(t[1]);
}
__device__ __forceinline__ float xg_sum(float x) {
  auto s = __builtin_amdgcn_permlane16_swap(__float_as_uint(x), __float_as_uint(x), false, false);
  return x32_sum(__uint_as_float(s[0]) + __uint_as_float(s[1]));
}
__device__ __forceinline__ void st_gran(unsigned char* ws, int off, unsigned tag, float v) {
  __hip_atomic_store((gu64*)(ws + off), ((unsigned long long)tag << 32) | __float_as_uint(v), RLX_AGENT);
}
__device__ __forceinline__ v2u ldg(__amdgpu_buffer_rsrc_t rs, int off) { return __builtin_amdgcn_raw_buffer_load_b64(rs, off, 0, AUX_SC1); }
template <int N, int STEP, int SLEEP> __device__ __forceinline__ void sweep(__amdgpu_buffer_rsrc_t rs, int off, unsigned tag, v2u (&x)[N]) {
#pragma unroll
  for (int k = 0; k < N; ++k) x[k] = ldg(rs, off + k * STEP);
  SB();
  bool ok = true;
#pragma unroll
  for (int k = 0; k < N; ++k) ok &= x[k].y == tag;
  if (__builtin_expect(__all(ok), 1)) return;
#pragma nounroll
  for (unsigned spins = 0; spins < SPIN_CAP; ++spins) {
    __builtin_amdgcn_s_sleep(SLEEP);
    v2u y[N];
#pragma unroll
    for (int k = 0; k < N; ++k) y[k] = ldg(rs, x[k].y == tag ? OOB : off + k * STEP);
    SB();
    ok = true;
#pragma unroll
    for (int k = 0; k < N; ++k) { x[k] = x[k].y == tag ? x[k] : y[k]; ok &= x[k].y == tag; }
    if (__all(ok)) break;
  }
}
__device__ __forceinline__ v4u mkrs(const void* p, unsigned bytes) {
  const unsigned long long u = (unsigned long long)p;
  v4u r = {(unsigned)u, (unsigned)(u >> 32) & 0xffffu, bytes, (unsigned)RSRC3};
  return r;
}
__device__ __forceinline__ void tile_load(v4u (&d)[4], int voff, v4u rs, int soff) {
  asm volatile("buffer_load_dwordx4 %0, %4, %5, %6 offen nt\n\t"
               "buffer_load_dwordx4 %1, %4, %5, %7 offen nt\n\t"
               "buffer_load_dwordx4 %2, %4, %5, %8 offen nt\n\t"
               "buffer_load_dwordx4 %3, %4, %5, %9 offen nt"
               : "=&v"(d[0]), "=&v"(d[1]), "=&v"(d[2]), "=&v"(d[3]) : "v"(voff), "s"(rs), "s"(soff), "s"(soff + JST), "s"(soff + 2 * JST), "s"(soff + 3 * JST));
}
template <int N> __device__ __forceinline__ void tile_wait(v4u (&d)[4]) {
  asm volatile("s_waitcnt vmcnt(%4)" : "+v"(d[0]), "+v"(d[1]), "+v"(d[2]), "+v"(d[3]) : "i"(N));
}
template <int N> __device__ __forceinline__ void piece_wait(v4u& d) { asm volatile("s_waitcnt vmcnt(%1)" : "+v"(d) : "i"(N)); }
__device__ __forceinline__ void pause(int ticks) {
  unsigned due;
  asm volatile("s_memrealtime vcc\n\t"
               "s_waitcnt lgkmcnt(0)\n\t"
               "s_add_i32 %0, vcc_lo, %1\n"
               ".Lg0_%=:\n\t"
               "s_sleep 1\n\t"
               "s_memrealtime vcc\n\t"
               "s_waitcnt lgkmcnt(0)\n\t"
               "s_sub_i32 vcc_lo, vcc_lo, %0\n\t"
               "s_cmp_lt_i32 vcc_lo, 0\n\t"
               "s_cbranch_scc1 .Lg0_%="
               : "=&s"(due) : "i"(ticks) : "scc", "vcc", "memory");
}
__device__ __forceinline__ void gate_next(unsigned& due) {
  asm volatile("s_branch .Lg1_%=\n"
               ".Lg0_%=:\n\t"
               "s_sleep 1\n"
               ".Lg1_%=:\n\t"
               "s_memrealtime vcc\n\t"
               "s_waitcnt lgkmcnt(0)\n\t"
               "s_lshl_b32 vcc_lo, vcc_lo, 4\n\t"
               "s_sub_i32 vcc_lo, vcc_lo, %0\n\t"
               "s_cmp_lt_i32 vcc_lo, 0\n\t"
               "s_cbranch_scc1 .Lg0_%=\n\t"
               "s_setprio 0\n\t"
               "s_cmp_lt_i32 vcc_lo, %1\n\t"
               "s_cbranch_scc1 .Lg2_%=\n\t"
               "s_setprio 1\n"
               ".Lg2_%=:\n\t"
               "s_sub_i32 vcc_lo, vcc_lo, %2\n\t"
               "s_max_i32 vcc_lo, vcc_lo, 0\n\t"
               "s_add_i32 %0, %0, vcc_lo\n\t"
               "s_add_i32 %0, %0, %3"
               : "+s"(due) : "i"(PTH16), "i"(SLK16), "i"(DT16) : "scc", "vcc", "memory");
}
__device__ __forceinline__ void gate2(unsigned& line, unsigned& nxt) {
  asm volatile("s_sub_i32 vcc_lo, %1, %0\n\t"
               "s_max_i32 vcc_lo, vcc_lo, 0\n\t"
               "s_add_i32 vcc_hi, %0, vcc_lo\n\t"
               "s_mov_b32 %1, vcc_hi\n\t"
               "s_branch .Lg1_%=\n"
               ".Lg0_%=:\n\t"
               "s_sleep 1\n"
               ".Lg1_%=:\n\t"
               "s_memrealtime vcc\n\t"
               "s_waitcnt lgkmcnt(0)\n\t"
               "s_lshl_b32 vcc_lo, vcc_lo, 4\n\t"
               "s_sub_i32 vcc_hi, vcc_lo, %1\n\t"
               "s_cmp_lt_i32 vcc_hi, 0\n\t"
               "s_cbranch_scc1 .Lg0_%=\n\t"
               "s_sub_i32 vcc_hi, vcc_hi, %4\n\t"
               "s_max_i32 vcc_hi, vcc_hi, 0\n\t"
               "s_add_i32 %1, %1, vcc_hi\n\t"
               "s_add_i32 %1, %1, %3\n\t"
               "s_add_i32 %0, %0, %2"
               : "+s"(line), "+s"(nxt) : "i"(DT16), "i"(DF16), "i"(JIT16) : "scc", "vcc", "memory");
}
__device__ __forceinline__ void lds_barrier() {
  asm volatile("s_waitcnt lgkmcnt(0)" ::: "memory");
  __builtin_amdgcn_s_barrier();
  asm volatile("" ::: "memory");
}
__device__ __forceinline__ void lds_arrive(unsigned* c, int lane) {
  asm volatile("s_waitcnt lgkmcnt(0)" ::: "memory");
  if (lane == 0) __hip_atomic_fetch_add(c, 1u, RLX_WG);
}
template <int SLEEP> __device__ __forceinline__ unsigned lds_wait(unsigned* c, unsigned want) {
  unsigned v = 0;
#pragma nounroll
  for (unsigned spins = 0; spins < LSPIN_CAP; ++spins) {
    v = __builtin_amdgcn_readfirstlane(__hip_atomic_load(c, RLX_WG));
    if (v >= want) break;
    __builtin_amdgcn_s_sleep(SLEEP);
  }
  asm volatile("" ::: "memory");
  return v;
}
__device__ __forceinline__ unsigned lds_addr(const void* p) { return (unsigned)(size_t)(const LAS void*)p; }
template <int SLEEP> __device__ __forceinline__ v2u lds_take2(unsigned addr) {
  v2u v;
#pragma nounroll
  for (unsigned spins = 0; spins < LSPIN_CAP; ++spins) {
    asm volatile("ds_read2st64_b32 %0, %1 offset1:1\n\ts_waitcnt lgkmcnt(0)" : "=&v"(v) : "v"(addr) : "memory");
    if (__all(v.x != EMPTY_O && v.y != EMPTY_O)) break;
    if (SLEEP) __builtin_amdgcn_s_sleep(SLEEP);
  }
  return v;
}
#define STAMP(i) do {} while (0)
#define STAMP_OUT() do {} while (0)

constexpr int L_O = 0;
constexpr int L_ML = L_O + 4096;
constexpr int L_QP = L_ML + 64;
constexpr int L_Q = L_QP + 256;
constexpr int L_OB = L_Q + 256;
constexpr int L_CNT = L_OB + 32;
constexpr int L_END = L_CNT + 8;

struct St {
  v4u T[32][4];
  float Sc[8][4];
  unsigned P[8][4];
  v4u Q, mask;
  f32x4 O;
  v4u rs[4];
  int vk, vv, so;
  unsigned tb, nx;
  float* lds;
  int lane, wave;
};

template <int I> __device__ __forceinline__ void request(St& z) {
  constexpr int lim = I < NLIM ? LIM : LIMB;
  if constexpr (4 * I > lim) asm volatile("s_waitcnt vmcnt(%0)" :: "i"(lim) : "memory");
  gate_next(z.tb);
  if constexpr (SOFT16 && I < NSOFT) z.tb += SOFT16;
  tile_load(z.T[I], (I & 8) ? z.vv : z.vk, z.rs[I >> 3], z.so + (I & 7) * 32768);
  SB();
}
template <int I, int W> __device__ __forceinline__ void consume(St& z) {
  const int g = z.lane >> 4, n = z.lane & 15;
  constexpr int br = I >> 4;
  if constexpr ((I & 15) == 0) {
    if constexpr (br) lds_wait<1>((unsigned*)(z.lds + L_CNT) + 7, 1u);
    z.Q = *(const v4u*)((const unsigned short*)(z.lds + L_Q) + br * 256 + (n >> 3) * 128 + 32 * g + 8 * (n & 3));
  }
  if constexpr ((I & 8) == 0) {
    tile_wait<W>(z.T[I]);
#pragma unroll
    for (int j = 0; j < 4; ++j) {
      const f32x4 d = mfma(z.T[I][j], z.Q, f32x4{0.f, 0.f, 0.f, 0.f});
      z.Sc[I & 7][j] = (dpp<0x00>(d.x) + dpp<0x55>(d.y)) + (dpp<0xAA>(d.z) + dpp<0xFF>(d.w));
    }
    if constexpr ((I & 7) == 7) {
      float m = z.Sc[0][0];
#pragma unroll
      for (int t = 0; t < 8; ++t)
#pragma unroll
        for (int j = 0; j < 4; ++j) m = fmax_(m, z.Sc[t][j]);
      const float m2 = xg_max(m) * C2;
      float l = 0.f;
#pragma unroll
      for (int t = 0; t < 8; ++t) {
        float p[4];
#pragma unroll
        for (int j = 0; j < 4; ++j) { p[j] = __builtin_amdgcn_exp2f(fmaf(z.Sc[t][j], C2, -m2)); z.P[t][j] = dup_bf16(p[j]); }
        l += (p[0] + p[1]) + (p[2] + p[3]);
        asm volatile("" : "+v"(z.P[t][0]), "+v"(z.P[t][1]), "+v"(z.P[t][2]), "+v"(z.P[t][3]));
      }
      l = xg_sum(l);
      if (g == 0 && (n & 7) == 0) { z.lds[L_ML + br * 32 + (n >> 3) * 8 + z.wave] = m2; z.lds[L_ML + br * 32 + 16 + (n >> 3) * 8 + z.wave] = l; }
      lds_arrive((unsigned*)(z.lds + L_CNT) + 2 + br, z.lane);
      z.O = f32x4{0.f, 0.f, 0.f, 0.f};
    } else {
      asm volatile("" : "+v"(z.Sc[I & 7][0]), "+v"(z.Sc[I & 7][1]), "+v"(z.Sc[I & 7][2]), "+v"(z.Sc[I & 7][3]));
    }
  } else if constexpr (W == 0) {
    v4u A[4];
#pragma unroll
    for (int j = 0; j < 4; ++j) A[j] = diag(z.P[I & 7][j], z.mask);
    float* dst = z.lds + L_O + br * 2048 + z.wave * 256 + (g >> 1) * 128 + 8 * n + 4 * (g & 1);
    unsigned da = lds_addr(dst);
    asm volatile("" : "+v"(A[0]), "+v"(A[1]), "+v"(A[2]), "+v"(A[3]), "+v"(da));
    piece_wait<3>(z.T[I][0]); z.O = mfma(A[0], z.T[I][0], z.O); SB();
    piece_wait<2>(z.T[I][1]); z.O = mfma(A[1], z.T[I][1], z.O); SB();
    piece_wait<1>(z.T[I][2]); z.O = mfma(A[2], z.T[I][2], z.O); SB();
    piece_wait<0>(z.T[I][3]); z.O = mfma(A[3], z.T[I][3], z.O);
    *(LAS f32x4*)(size_t)da = z.O;
  } else {
    tile_wait<W>(z.T[I]);
#pragma unroll
    for (int j = 0; j < 4; ++j) z.O = mfma(diag(z.P[I & 7][j], z.mask), z.T[I][j], z.O);
    asm volatile("" : "+v"(z.O));
    if constexpr ((I & 7) == 7) *(f32x4*)(z.lds + L_O + br * 2048 + z.wave * 256 + (g >> 1) * 128 + 8 * n + 4 * (g & 1)) = z.O;
  }
  SB();
}
template <int I, int N> struct Run {
  static __device__ __forceinline__ void go(St& z) { request<I>(z); consume<I - DEP + 1, 4 * (DEP - 1)>(z); Run<I + 1, N>::go(z); }
};
template <int N> struct Run<N, N> { static __device__ __forceinline__ void go(St&) {} };
template <int I, int N> struct Req { static __device__ __forceinline__ void go(St& z) { request<I>(z); Req<I + 1, N>::go(z); } };
template <int N> struct Req<N, N> { static __device__ __forceinline__ void go(St&) {} };
template <int I, int N, int LAST> struct Con {
  static __device__ __forceinline__ void go(St& z) { consume<I, 4 * (LAST - I)>(z); Con<I + 1, N, LAST>::go(z); }
};
template <int N, int LAST> struct Con<N, N, LAST> { static __device__ __forceinline__ void go(St&) {} };

__global__ void __launch_bounds__(NTHR, 3) gqa2_fused(Args a) {
  __shared__ __attribute__((aligned(16))) float lds[L_END];
  unsigned* const l_cnt = (unsigned*)(lds + L_CNT);
  STAMP(0);
  const int tid = threadIdx.x, lane = tid & 63, wave = __builtin_amdgcn_readfirstlane(tid >> 6);
  const int g = lane >> 4, n = lane & 15;
  const int wg = blockIdx.x;
  const int ch = (wg & 7) << 5 | wg >> 3;
  const int kvh = ch >> 4, rb = (ch >> 3) & 1, s8 = ch & 7;
  if (tid < 512) {
    const v4u e = {EMPTY_O, EMPTY_O, EMPTY_O, EMPTY_O};
    *(v4u*)(lds + L_O + tid * 4) = e; *(v4u*)(lds + L_O + 2048 + tid * 4) = e;
  } else if (tid < 512 + 40) {
    ((unsigned*)(lds + L_OB))[tid - 512] = tid < 512 + 32 ? EMPTY_B : 0u;
  }
  asm volatile("" : "+s"(a.x[0]), "+s"(a.x[1]), "+s"(a.k[0]), "+s"(a.k[1]), "+s"(a.v[0]), "+s"(a.v[1]), "+s"(a.wq[0]), "+s"(a.wq[1]), "+s"(a.wo[0]), "+s"(a.wo[1]), "+s"(a.out), "+s"(a.ws));
  unsigned char* const ws = (unsigned char*)a.ws;
  const void* const wop = rb ? a.wo[1] : a.wo[0];
  const v4u rk0 = mkrs((unsigned char*)a.k[0] + (size_t)ch * 262144, 262144);
  unsigned probe;
  asm volatile("s_cmp_lg_u32 %2, 0\n\ts_cbranch_scc1 .Lp%=\n\ts_mov_b64 exec, 1\n\tbuffer_load_dword %0, off, %1, 0 nt\n\ts_mov_b64 exec, -1\n.Lp%=:"
               : "=v"(probe) : "s"(rk0), "s"(wave - (ANC == -3 ? 7 : 0)) : "scc");
  lds_barrier();
  const __amdgpu_buffer_rsrc_t rw = __builtin_amdgcn_make_buffer_rsrc(ws, 0, WS_ST, RSRC3);
  v4u mask;
  {
    const unsigned half = (n & 1) ? 0xffff0000u : 0x0000ffffu; const int r = (n & 7) >> 1;
    mask.x = r == 0 ? half : 0u; mask.y = r == 1 ? half : 0u; mask.z = r == 2 ? half : 0u; mask.w = r == 3 ? half : 0u;
  }
  const int pidx = kvh * 8 + s8;
  if (__builtin_expect(wave >= 8, 1)) {
    __builtin_amdgcn_s_setprio(3);
    const int hw = wave - 8, cg = s8 & 3, kh = s8 >> 2, sub = 2 * g + (n >> 3);
    const __amdgpu_buffer_rsrc_t rq = __builtin_amdgcn_make_buffer_rsrc((unsigned char*)(rb ? a.wq[1] : a.wq[0]) + ((size_t)(kh * 288 + hw * 144) * NQ + kvh * 256 + cg * 64) * 2, 0, 143 * NQ * 2 + 128, RSRC3);
    const __amdgpu_buffer_rsrc_t rx = __builtin_amdgcn_make_buffer_rsrc((unsigned char*)(rb ? a.x[1] : a.x[0]) + (kh * 288 + hw * 144) * 2, 0, 144 * 2, RSRC3);
    v4u wqv[18];
#pragma unroll
    for (int i = 0; i < 18; ++i) wqv[i] = __builtin_amdgcn_raw_buffer_load_b128(rq, (ABL & 4) ? OOB : sub * (NQ * 2) + (n & 7) * 16, i * 8 * NQ * 2, AUX_NT);
    const unsigned xa = __builtin_amdgcn_raw_buffer_load_b32(rx, n * 16 + g * 4, 0, 0), xb = __builtin_amdgcn_raw_buffer_load_b32(rx, n * 16 + g * 4 + 256, 0, 0);
    const unsigned ep = __builtin_amdgcn_raw_buffer_load_b32(rw, lane == 0 ? WS_EPOCH : OOB, 0, AUX_SC1);
    SB();
    f32x4 a0 = {0.f, 0.f, 0.f, 0.f}, a1 = {0.f, 0.f, 0.f, 0.f};
    const unsigned sel = n < 8 ? 0x01000100u : 0x03020302u;
#define QSTEP(acc, i, src, k) acc = mfma(diag(__builtin_amdgcn_perm(0u, (unsigned)__builtin_amdgcn_mov_dpp((int)(src), 0x150 + (k), 0xf, 0xf, true), sel), mask), wqv[i], acc)
    QSTEP(a0, 0, xa, 0); QSTEP(a1, 1, xa, 1); QSTEP(a0, 2, xa, 2); QSTEP(a1, 3, xa, 3); QSTEP(a0, 4, xa, 4); QSTEP(a1, 5, xa, 5);
    QSTEP(a0, 6, xa, 6); QSTEP(a1, 7, xa, 7); QSTEP(a0, 8, xa, 8); QSTEP(a1, 9, xa, 9); QSTEP(a0, 10, xa, 10); QSTEP(a1, 11, xa, 11);
    QSTEP(a0, 12, xa, 12); QSTEP(a1, 13, xa, 13); QSTEP(a0, 14, xa, 14); QSTEP(a1, 15, xa, 15); QSTEP(a0, 16, xb, 0); QSTEP(a1, 17, xb, 1);
    if ((g >> 1) == (n >> 3)) *(f32x4*)(lds + L_QP + (hw * 2 + (n >> 3)) * 64 + 8 * (n & 7) + 4 * (g & 1)) = a0 + a1;
    const unsigned tag = __builtin_amdgcn_readfirstlane(ep) + TAG_STEP;
    STAMP(1);
    lds_arrive(l_cnt + 0, lane);
    if (hw == 0) {
      lds_wait<1>(l_cnt + 0, 2u);
      const float q = (lds[L_QP + lane] + lds[L_QP + 64 + lane]) + (lds[L_QP + 128 + lane] + lds[L_QP + 192 + lane]);
      st_gran(ws, WS_QG + ((rb * 16 + kvh) * 512 + kh * 256 + cg * 64 + lane) * 8, tag, q);
      if (lane == 0) l_cnt[5] = tag;
    }
    STAMP(2);
    pause(QGA);
    if (hw) pause(QGB - QGA);
    {
      v2u x[8];
      sweep<8, 512, 8>(rw, WS_QG + ((hw * 16 + kvh) * 512 + lane) * 8, tag, x);
#pragma unroll
      for (int k = 0; k < 4; ++k) ((unsigned short*)(lds + L_Q))[hw * 256 + k * 64 + lane] = f2bf(__uint_as_float(x[k].x) + __uint_as_float(x[k + 4].x));
      lds_arrive(hw ? l_cnt + 7 : l_cnt + 1, lane);
    }
    STAMP(3);
#pragma nounroll
    for (int br = 0; br < 2; ++br) {
      lds_wait<8>(l_cnt + 2 + br, 8u);
      const float* ml = lds + L_ML + br * 32 + hw * 8;
      const f32x4 m0 = *(const f32x4*)(ml), m1 = *(const f32x4*)(ml + 4), l0 = *(const f32x4*)(ml + 16), l1 = *(const f32x4*)(ml + 20);
      const float M = fmax_(fmax_(fmax_(m0.x, m0.y), fmax_(m0.z, m0.w)), fmax_(fmax_(m1.x, m1.y), fmax_(m1.z, m1.w)));
      float L = 0.f;
#pragma unroll
      for (int w = 0; w < 8; ++w) L = fmaf(__builtin_amdgcn_exp2f((w < 4 ? m0[w & 3] : m1[w & 3]) - M), w < 4 ? l0[w & 3] : l1[w & 3], L);
      const int mg = WS_MG + ((br * 16 + kvh) * 2 + hw) * 1024;
      if (lane < 8) st_gran(ws, mg + (ch & 15) * 64 + lane * 8, tag, (lane & 1) ? L : M);
#if PREW == 1 && !(ABL & 2)
      if (br == 1) {
        const int d1 = WS_PG + (256 + ch) * PG_ROW, d2 = WS_YG + (rb * 128 + pidx) * YG_ROW;
        unsigned u = __builtin_amdgcn_raw_buffer_load_b32(rw, hw == 0 ? (lane < 32 ? d1 + lane * 64 : lane < 48 ? d2 + 2048 + lane * 64 : OOB) : d2 + lane * 64, 0, AUX_SC1);
        if (lane == 0) u ^= *(const GAS unsigned*)((const unsigned char*)wop + (size_t)(kvh * 256 + s8 * 32 + hw * 31) * DM * 2);
        asm volatile("" :: "v"(u));
      }
#endif
      pause(MLD);
      v2u x[1];
      sweep<1, 0, 8>(rw, mg + n * 64 + (g & 1) * 8, tag, x);
      auto sw = __builtin_amdgcn_permlane16_swap(x[0].x, x[0].x, false, false);
      const float Ms = __uint_as_float(sw[0]), Ls = __uint_as_float(sw[1]);
      float Mx = fmax_(Ms, dpp<XOR1>(Ms)); Mx = fmax_(Mx, dpp<XOR2>(Mx)); Mx = fmax_(Mx, dpp<HMIR>(Mx)); Mx = fmax_(Mx, dpp<MIR>(Mx));
      float Lt = __builtin_amdgcn_exp2f(Ms - Mx) * Ls; Lt += dpp<XOR1>(Lt); Lt += dpp<XOR2>(Lt); Lt += dpp<HMIR>(Lt); Lt += dpp<MIR>(Lt);
      const float inv = 1.0f / Lt;
      float c[8];
#pragma unroll
      for (int w = 0; w < 8; ++w) c[w] = __builtin_amdgcn_exp2f((w < 4 ? m0[w & 3] : m1[w & 3]) - Mx) * inv;
      const unsigned oa = lds_addr(lds + L_O + br * 2048 + hw * 128 + lane);
      GAS unsigned char* dst = (GAS unsigned char*)ws + WS_PG + (br * 256 + ch) * PG_ROW + (hw * 128 + lane) * 8;
      asm volatile("" : "+v"(dst));
      float o0 = 0.f, o1 = 0.f;
      { const v2u v = lds_take2<8>(oa); o0 = c[0] * __uint_as_float(v.x); o1 = c[0] * __uint_as_float(v.y); }
#pragma unroll
      for (int w = 1; w < 8; ++w) { const v2u v = lds_take2<0>(oa + w * 1024); o0 = fmaf(c[w], __uint_as_float(v.x), o0); o1 = fmaf(c[w], __uint_as_float(v.y), o1); }
      __hip_atomic_store((gu64*)dst, ((unsigned long long)tag << 32) | __float_as_uint(o0), RLX_AGENT);
      __hip_atomic_store((gu64*)(dst + 512), ((unsigned long long)tag << 32) | __float_as_uint(o1), RLX_AGENT);
      if (hw == 0 && lane == 0) __hip_atomic_store(l_cnt + 4, (unsigned)br + 1u, RLX_WG);
    }
    STAMP(8);
  } else {
    St z;
    const int phs = PAT == 2 ? (wg >> 3) & 7 : wave;
    z.lds = lds; z.lane = lane; z.wave = wave; z.mask = mask;
    z.rs[0] = rk0;
    z.rs[1] = mkrs((unsigned char*)a.v[0] + (size_t)ch * 262144, 262144);
    z.rs[2] = mkrs((unsigned char*)a.k[1] + (size_t)ch * 262144, 262144);
    z.rs[3] = mkrs((unsigned char*)a.v[1] + (size_t)ch * 262144, 262144);
    z.vk = (ABL & 8) ? lane * 16 : ((lane >> 2) & 3) * 256 + g * 64 + (lane & 3) * 16; z.vv = lane * 16; z.so = wave * WST;
    GAS unsigned short* outp = (GAS unsigned short*)a.out;
    if (wave == 0) {
      unsigned long long ta;
      asm volatile("s_waitcnt vmcnt(0)\n\ts_memrealtime %0\n\ts_waitcnt lgkmcnt(0)" : "=s"(ta) : "v"(probe) : "memory");
      if (lane == 0) __hip_atomic_store(l_cnt + 6, ((unsigned)ta << 4) | 1u, RLX_WG);
    }
    z.tb = lds_wait<1>(l_cnt + 6, 1u) + (unsigned)(OFF16 + ((phs * DT16) >> 3));
    z.nx = z.tb;
    STAMP(1);
    Req<0, PRE>::go(z);
    STAMP(4);
    lds_wait<1>(l_cnt + 1, 1u);
    const unsigned tag = __builtin_amdgcn_readfirstlane(l_cnt[5]);
    STAMP(5);
    Con<0, PRE - DEP + 1, PRE - 1>::go(z);
    Run<PRE, 17>::go(z);
    STAMP(14);
    Run<17, 25>::go(z);
    STAMP(15);
    Run<25, 32>::go(z);
    STAMP(6);
    Con<32 - DEP + 1, 32, 31>::go(z);
    STAMP(7);
    const unsigned oba = lds_addr(lds + L_OB + g * 8);
    if (wave < 5) {
      const __amdgpu_buffer_rsrc_t ro = __builtin_amdgcn_make_buffer_rsrc((unsigned char*)wop + ((size_t)(kvh * 256 + s8 * 32) * DM) * 2, 0, 32 * DM * 2, RSRC3);
      const int wcol = wave * 128 + n * 8;
      const int vwo = (!NOWO && wcol < DM) ? g * (DM * 2) + wcol * 2 : OOB;
      v4u Wo[8];
      z.tb += (unsigned)((rb ? WO16 + wave * WOS16 : WOA16 + wave * WOSA16) - ((phs * DT16) >> 3));
      gate_next(z.tb);
#pragma unroll
      for (int i = 0; i < 8; ++i) Wo[i] = __builtin_amdgcn_raw_buffer_load_b128(ro, vwo, i * 4 * DM * 2, AUX_NT);
      GAS unsigned char* dst = (GAS unsigned char*)ws + WS_YG + (rb * 128 + pidx) * YG_ROW + (wave * 128 + lane) * 8;
      asm volatile("" : "+v"(dst), "+v"(Wo[0]), "+v"(Wo[1]), "+v"(Wo[2]), "+v"(Wo[3]), "+v"(Wo[4]), "+v"(Wo[5]), "+v"(Wo[6]), "+v"(Wo[7]));
      STAMP(2);
      v4u oa, ob;
#pragma nounroll
      for (unsigned spins = 0; spins < LSPIN_CAP; ++spins) {
        asm volatile("ds_read_b128 %0, %2\n\tds_read_b128 %1, %2 offset:16\n\ts_waitcnt lgkmcnt(0)" : "=&v"(oa), "=&v"(ob) : "v"(oba) : "memory");
        if (__all(oa.x != EMPTY_B && oa.y != EMPTY_B && oa.z != EMPTY_B && oa.w != EMPTY_B && ob.x != EMPTY_B && ob.y != EMPTY_B && ob.z != EMPTY_B && ob.w != EMPTY_B)) break;
      }
      STAMP(10);
      f32x4 Y0 = {0.f, 0.f, 0.f, 0.f}, Y1 = {0.f, 0.f, 0.f, 0.f};
#pragma unroll
      for (int i = 0; i < 4; ++i) { Y0 = mfma(diag(oa[i], mask), Wo[i], Y0); Y1 = mfma(diag(ob[i], mask), Wo[4 + i], Y1); }
      const f32x4 Y = Y0 + Y1;
      if (wcol < DM) {
        __hip_atomic_store((gu64*)dst, ((unsigned long long)tag << 32) | __float_as_uint(g < 2 ? Y.x : Y.z), RLX_AGENT);
        __hip_atomic_store((gu64*)(dst + 512), ((unsigned long long)tag << 32) | __float_as_uint(g < 2 ? Y.y : Y.w), RLX_AGENT);
      }
      STAMP(11);
    } else if (wave == 5) {
      const int po = WS_PG + (rb * 256 + kvh * 16 + (lane >> 5)) * PG_ROW + (32 * s8 + (lane & 31)) * 8;
      v2u x[8];
      lds_wait<0>(l_cnt + 4, (unsigned)rb + 1u);
      if (rb) pause(H1D);
      sweep<8, 2 * PG_ROW, 1>(rw, po, tag, x);
      STAMP(9);
      const float o = x32_sum(((__uint_as_float(x[0].x) + __uint_as_float(x[1].x)) + (__uint_as_float(x[2].x) + __uint_as_float(x[3].x)))
                            + ((__uint_as_float(x[4].x) + __uint_as_float(x[5].x)) + (__uint_as_float(x[6].x) + __uint_as_float(x[7].x))));
      if (lane < 32) ((unsigned*)(lds + L_OB))[(lane & 3) * 8 + (lane >> 2)] = dup_bf16(o);
    } else if (wave == 6 || pidx < 16) {
      const int pos = wave == 6 ? pidx * 4 : 512 + (pidx >> 1) * 16 + (pidx & 1) * 4;
      const int so = WS_YG + (rb * 128 + (lane >> 2)) * YG_ROW + (pos + (lane & 3)) * 8;
      const int p = pos + (lane & 3), pl = p & 63;
      const int col = (p & ~127) + 8 * (pl & 15) + 4 * ((pl >> 4) & 1) + 2 * (pl >> 5) + ((p >> 6) & 1);
      GAS unsigned short* op = outp + rb * DM + col;
      asm volatile("" : "+v"(op));
      v2u x[8];
      z.tb += (unsigned)((rb ? WO16 + 4 * WOS16 : WOA16 + 4 * WOSA16) + H2G16 - ((phs * DT16) >> 3));
#pragma nounroll
      for (unsigned spins = 0; spins < LSPIN_CAP; ++spins) {
        if (__builtin_amdgcn_readfirstlane(__hip_atomic_load((unsigned*)(lds + L_OB), RLX_WG)) != EMPTY_B) break;
        __builtin_amdgcn_s_sleep(1);
      }
      pause(H2D);
      gate_next(z.tb);
      sweep<8, 16 * YG_ROW, 1>(rw, so, tag, x);
      STAMP(12);
      float y = ((__uint_as_float(x[0].x) + __uint_as_float(x[1].x)) + (__uint_as_float(x[2].x) + __uint_as_float(x[3].x)))
              + ((__uint_as_float(x[4].x) + __uint_as_float(x[5].x)) + (__uint_as_float(x[6].x) + __uint_as_float(x[7].x)));
      y += dpp<ROR4>(y); y += dpp<ROR8>(y);
      y = xg_sum(y);
      if (lane < 4) *op = f2bf(y);
      if (rb == 0 && pos == 0 && lane == 0) __hip_atomic_store((gu32*)(ws + WS_EPOCH), tag, RLX_AGENT);
      STAMP(13);
    }
  }
  STAMP_OUT();
}

extern "C" void kernel_launch(void* const* d_in, const int* in_sizes, int n_in, void* d_out, int out_size, void* d_ws, size_t ws_size, hipStream_t stream) {
  Args a;
  a.x[0] = d_in[0]; a.k[0] = d_in[1]; a.v[0] = d_in[2]; a.wq[0] = d_in[3]; a.wo[0] = d_in[4];
  a.x[1] = d_in[5]; a.k[1] = d_in[6]; a.v[1] = d_in[7]; a.wq[1] = d_in[8]; a.wo[1] = d_in[9];
  a.out = d_out; a.ws = d_ws;
  gqa2_fused<<<NWG, NTHR, 0, stream>>>(a);
}
```

```cpp
#define OFF16 600
#define DT16 1792
#include <hip/hip_runtime.h>
#include <cstdint>

typedef unsigned v4u __attribute__((ext_vector_type(4)));
typedef unsigned v2u __attribute__((ext_vector_type(2)));
typedef float f32x4 __attribute__((ext_vector_type(4)));
typedef __bf16 bf16x8 __attribute__((ext_vector_type(8)));
typedef __bf16 bf16x2 __attribute__((ext_vector_type(2)));
typedef float f32x2 __attribute__((ext_vector_type(2)));
#define GAS __attribute__((address_space(1)))
#define LAS __attribute__((address_space(3)))
typedef GAS unsigned gu32;
typedef GAS unsigned long long gu64;
#define RLX_AGENT __ATOMIC_RELAXED, __HIP_MEMORY_SCOPE_AGENT
#define RLX_WG __ATOMIC_RELAXED, __HIP_MEMORY_SCOPE_WORKGROUP

#define STAMPS 0
#define ANC -2
#define PRE 8
#define DEP 3
#define LIM 2
#define WO16 0
#define WOS16 600
#define QGD 75
#define PTH16 400
#define SLK16 100000
#define H1D 40
#define H2D 70
#define PREW 0
#define HBAR 1
#define PAT 1
#define JST 8192
#define WST 1024
#define SOFT16 0
#define NSOFT 6
#define H2G16 2000
#define WOA16 0
#define WOSA16 600
#define OUTSC 0
#define SLOWSTART 0
#define P016 2400
#define NLIM 32
#define LIMB 8
#define DF16 0
#define JIT16 160
#define ABL 0
#define QGA 280
#define QGB 600
#define MLD 350
#define PIPE 0
#define GAP1 8
#define GAP2 8
#define NOWO 0

constexpr int DM = 576, NQ = 4096;
constexpr int NWG = 256, NTHR = 640;
constexpr int AUX_NT = 2, AUX_SC1 = 16;
constexpr int RSRC3 = 0x00020000;
constexpr int OOB = 0x7fff0000;
constexpr int WS_EPOCH = 0;
constexpr int WS_QG = 4096;
constexpr int WS_MG = WS_QG + 32 * 4096;
constexpr int WS_PG = WS_MG + 64 * 1024;
constexpr int PG_ROW = 256 * 8;
constexpr int WS_YG = WS_PG + 512 * PG_ROW;
constexpr int YG_ROW = 576 * 8;
constexpr int WS_ST = WS_YG + 256 * YG_ROW;
constexpr unsigned TAG_STEP = 0x9E3779B9u;
constexpr unsigned SPIN_CAP = 1u << 17, LSPIN_CAP = 1u << 22;
constexpr float C2 = 0.088388347648318440f * 1.4426950408889634f;
constexpr unsigned EMPTY_O = 0xffffffffu, EMPTY_B = 0x0000ffffu;

struct Args {
  const void* x[2]; const void* k[2]; const void* v[2]; const void* wq[2]; const void* wo[2];
  void* out; void* ws;
};

#define SB() __builtin_amdgcn_sched_barrier(0)
__device__ __forceinline__ f32x4 mfma(v4u a, v4u b, f32x4 c) {
  return __builtin_amdgcn_mfma_f32_16x16x32_bf16(__builtin_bit_cast(bf16x8, a), __builtin_bit_cast(bf16x8, b), c, 0, 0, 0);
}
__device__ __forceinline__ unsigned dup_bf16(float f) {
  f32x2 v = {f, f};
  return __builtin_bit_cast(unsigned, __builtin_convertvector(v, bf16x2));
}
__device__ __forceinline__ unsigned short f2bf(float f) { return __builtin_bit_cast(unsigned short, (__bf16)f); }
__device__ __forceinline__ v4u diag(unsigned pp, v4u mask) { v4u r = {pp & mask.x, pp & mask.y, pp & mask.z, pp & mask.w}; return r; }
template <int CTRL> __device__ __forceinline__ float dpp(float x) {
  return __builtin_bit_cast(float, __builtin_amdgcn_mov_dpp(__builtin_bit_cast(int, x), CTRL, 0xf, 0xf, true));
}
constexpr int XOR1 = 0xB1, XOR2 = 0x4E, HMIR = 0x141, MIR = 0x140, ROR8 = 0x128, ROR4 = 0x124;
__device__ __forceinline__ float fmax_(float a, float b) { return __builtin_fmaxf(a, b); }
__device__ __forceinline__ float xg_max(float x) {
  auto s = __builtin_amdgcn_permlane16_swap(__float_as_uint(x), __float_as_uint(x), false, false);
  x = fmax_(__uint_as_float(s[0]), __uint_as_float(s[1]));
  auto t = __builtin_amdgcn_permlane32_swap(__float_as_uint(x), __float_as_uint(x), false, false);
  return fmax_(__uint_as_float(t[0]), __uint_as_float(t[1]));
}
__device__ __forceinline__ float x32_sum(float x) {
  auto t = __builtin_amdgcn_permlane32_swap(__float_as_uint(x), __float_as_uint(x), false, false);
  return __uint_as_float(t[0]) + __uint_as_float(t[1]);
}
__device__ __forceinline__ float xg_sum(float x) {
  auto s = __builtin_amdgcn_permlane16_swap(__float_as_uint(x), __float_as_uint(x), false, false);
  return x32_sum(__uint_as_float(s[0]) + __uint_as_float(s[1]));
}
__device__ __forceinline__ void st_gran(unsigned char* ws, int off, unsigned tag, float v) {
  __hip_atomic_store((gu64*)(ws + off), ((unsigned long long)tag << 32) | __float_as_uint(v), RLX_AGENT);
}
__device__ __forceinline__ void st_gran2(__amdgpu_buffer_rsrc_t rs, int off, unsigned tag, float a, float b) {
  __builtin_amdgcn_raw_buffer_store_b128(v4u{__float_as_uint(a), tag, __float_as_uint(b), tag}, rs, off, 0, AUX_SC1);
}
__device__ __forceinline__ v2u ldg(__amdgpu_buffer_rsrc_t rs, int off) { return __builtin_amdgcn_raw_buffer_load_b64(rs, off, 0, AUX_SC1); }
template <int N, int STEP, int SLEEP> __device__ __forceinline__ void sweep(__amdgpu_buffer_rsrc_t rs, int off, unsigned tag, v2u (&x)[N]) {
#pragma unroll
  for (int k = 0; k < N; ++k) x[k] = ldg(rs, off + k * STEP);
  SB();
  bool ok = true;
#pragma unroll
  for (int k = 0; k < N; ++k) ok &= x[k].y == tag;
  if (__builtin_expect(__all(ok), 1)) return;
#pragma nounroll
  for (unsigned spins = 0; spins < SPIN_CAP; ++spins) {
    __builtin_amdgcn_s_sleep(SLEEP);
    v2u y[N];
#pragma unroll
    for (int k = 0; k < N; ++k) y[k] = ldg(rs, x[k].y == tag ? OOB : off + k * STEP);
    SB();
    ok = true;
#pragma unroll
    for (int k = 0; k < N; ++k) { x[k] = x[k].y == tag ? x[k] : y[k]; ok &= x[k].y == tag; }
    if (__all(ok)) break;
  }
}
template <int STEP> __device__ __forceinline__ void pass4(v4u (&y)[4], const v4u (&x)[4], int off, v4u rs, unsigned tag) {
  int o[4];
#pragma unroll
  for (int k = 0; k < 4; ++k) o[k] = (x[k].y == tag && x[k].w == tag) ? OOB : off + k * STEP;
  asm volatile("buffer_load_dwordx4 %0, %4, %8, 0 offen sc1\n\t"
               "buffer_load_dwordx4 %1, %5, %8, 0 offen sc1\n\t"
               "buffer_load_dwordx4 %2, %6, %8, 0 offen sc1\n\t"
               "buffer_load_dwordx4 %3, %7, %8, 0 offen sc1"
               : "=&v"(y[0]), "=&v"(y[1]), "=&v"(y[2]), "=&v"(y[3]) : "v"(o[0]), "v"(o[1]), "v"(o[2]), "v"(o[3]), "s"(rs) : "memory");
}
__device__ __forceinline__ bool take4(v4u (&x)[4], v4u (&y)[4], unsigned tag) {
  asm volatile("s_waitcnt vmcnt(4)" : "+v"(y[0]), "+v"(y[1]), "+v"(y[2]), "+v"(y[3]));
  bool ok = true;
#pragma unroll
  for (int k = 0; k < 4; ++k) {
    const bool have = x[k].y == tag && x[k].w == tag;
    x[k] = have ? x[k] : y[k];
    ok &= x[k].y == tag && x[k].w == tag;
  }
  return __all(ok);
}
__device__ __forceinline__ void pause(int ticks);
template <int STEP, int GAP> __device__ __forceinline__ void sweep2(v4u rs, int off, unsigned tag, v4u (&x)[4], v4u (&ya)[4], v4u (&yb)[4]) {
#pragma unroll
  for (int k = 0; k < 4; ++k) x[k] = v4u{0u, ~tag, 0u, ~tag};
  pass4<STEP>(ya, x, off, rs, tag);
  __builtin_amdgcn_s_sleep(GAP);
#pragma nounroll
  for (unsigned spins = 0; spins < SPIN_CAP; ++spins) {
    pass4<STEP>(yb, x, off, rs, tag);
    if (take4(x, ya, tag)) break;
    pass4<STEP>(ya, x, off, rs, tag);
    if (take4(x, yb, tag)) break;
  }
}
#define KEEP8(A, B) asm volatile("s_waitcnt vmcnt(0)" :: "v"(A[0]), "v"(A[1]), "v"(A[2]), "v"(A[3]), "v"(B[0]), "v"(B[1]), "v"(B[2]), "v"(B[3]) : "memory")
__device__ __forceinline__ v4u mkrs(const void* p, unsigned bytes) {
  const unsigned long long u = (unsigned long long)p;
  v4u r = {(unsigned)u, (unsigned)(u >> 32) & 0xffffu, bytes, (unsigned)RSRC3};
  return r;
}
__device__ __forceinline__ void tile_load(v4u (&d)[4], int voff, v4u rs, int soff) {
  asm volatile("buffer_load_dwordx4 %0, %4, %5, %6 offen nt\n\t"
               "buffer_load_dwordx4 %1, %4, %5, %7 offen nt\n\t"
               "buffer_load_dwordx4 %2, %4, %5, %8 offen nt\n\t"
               "buffer_load_dwordx4 %3, %4, %5, %9 offen nt"
               : "=&v"(d[0]), "=&v"(d[1]), "=&v"(d[2]), "=&v"(d[3]) : "v"(voff), "s"(rs), "s"(soff), "s"(soff + JST), "s"(soff + 2 * JST), "s"(soff + 3 * JST));
}
template <int N> __device__ __forceinline__ void tile_wait(v4u (&d)[4]) {
  asm volatile("s_waitcnt vmcnt(%4)" : "+v"(d[0]), "+v"(d[1]), "+v"(d[2]), "+v"(d[3]) : "i"(N));
}
template <int N> __device__ __forceinline__ void piece_wait(v4u& d) { asm volatile("s_waitcnt vmcnt(%1)" : "+v"(d) : "i"(N)); }
__device__ __forceinline__ void pause(int ticks) {
  unsigned due;
  asm volatile("s_memrealtime vcc\n\t"
               "s_waitcnt lgkmcnt(0)\n\t"
               "s_add_i32 %0, vcc_lo, %1\n"
               ".Lg0_%=:\n\t"
               "s_sleep 1\n\t"
               "s_memrealtime vcc\n\t"
               "s_waitcnt lgkmcnt(0)\n\t"
               "s_sub_i32 vcc_lo, vcc_lo, %0\n\t"
               "s_cmp_lt_i32 vcc_lo, 0\n\t"
               "s_cbranch_scc1 .Lg0_%="
               : "=&s"(due) : "i"(ticks) : "scc", "vcc", "memory");
}
__device__ __forceinline__ void gate_next(unsigned& due) {
  asm volatile("s_branch .Lg1_%=\n"
               ".Lg0_%=:\n\t"
               "s_sleep 1\n"
               ".Lg1_%=:\n\t"
               "s_memrealtime vcc\n\t"
               "s_waitcnt lgkmcnt(0)\n\t"
               "s_lshl_b32 vcc_lo, vcc_lo, 4\n\t"
               "s_sub_i32 vcc_lo, vcc_lo, %0\n\t"
               "s_cmp_lt_i32 vcc_lo, 0\n\t"
               "s_cbranch_scc1 .Lg0_%=\n\t"
               "s_setprio 0\n\t"
               "s_cmp_lt_i32 vcc_lo, %1\n\t"
               "s_cbranch_scc1 .Lg2_%=\n\t"
               "s_setprio 1\n"
               ".Lg2_%=:\n\t"
               "s_sub_i32 vcc_lo, vcc_lo, %2\n\t"
               "s_max_i32 vcc_lo, vcc_lo, 0\n\t"
               "s_add_i32 %0, %0, vcc_lo\n\t"
               "s_add_i32 %0, %0, %3"
               : "+s"(due) : "i"(PTH16), "i"(SLK16), "i"(DT16) : "scc", "vcc", "memory");
}
__device__ __forceinline__ void gate2(unsigned& line, unsigned& nxt) {
  asm volatile("s_sub_i32 vcc_lo, %1, %0\n\t"
               "s_max_i32 vcc_lo, vcc_lo, 0\n\t"
               "s_add_i32 vcc_hi, %0, vcc_lo\n\t"
               "s_mov_b32 %1, vcc_hi\n\t"
               "s_branch .Lg1_%=\n"
               ".Lg0_%=:\n\t"
               "s_sleep 1\n"
               ".Lg1_%=:\n\t"
               "s_memrealtime vcc\n\t"
               "s_waitcnt lgkmcnt(0)\n\t"
               "s_lshl_b32 vcc_lo, vcc_lo, 4\n\t"
               "s_sub_i32 vcc_hi, vcc_lo, %1\n\t"
               "s_cmp_lt_i32 vcc_hi, 0\n\t"
               "s_cbranch_scc1 .Lg0_%=\n\t"
               "s_sub_i32 vcc_hi, vcc_hi, %4\n\t"
               "s_max_i32 vcc_hi, vcc_hi, 0\n\t"
               "s_add_i32 %1, %1, vcc_hi\n\t"
               "s_add_i32 %1, %1, %3\n\t"
               "s_add_i32 %0, %0, %2"
               : "+s"(line), "+s"(nxt) : "i"(DT16), "i"(DF16), "i"(JIT16) : "scc", "vcc", "memory");
}
__device__ __forceinline__ void lds_barrier() {
  asm volatile("s_waitcnt lgkmcnt(0)" ::: "memory");
  __builtin_amdgcn_s_barrier();
  asm volatile("" ::: "memory");
}
__device__ __forceinline__ void lds_arrive(unsigned* c, int lane) {
  asm volatile("s_waitcnt lgkmcnt(0)" ::: "memory");
  if (lane == 0) __hip_atomic_fetch_add(c, 1u, RLX_WG);
}
template <int SLEEP> __device__ __forceinline__ unsigned lds_wait(unsigned* c, unsigned want) {
  unsigned v = 0;
#pragma nounroll
  for (unsigned spins = 0; spins < LSPIN_CAP; ++spins) {
    v = __builtin_amdgcn_readfirstlane(__hip_atomic_load(c, RLX_WG));
    if (v >= want) break;
    __builtin_amdgcn_s_sleep(SLEEP);
  }
  asm volatile("" ::: "memory");
  return v;
}
__device__ __forceinline__ unsigned lds_addr(const void* p) { return (unsigned)(size_t)(const LAS void*)p; }
template <int SLEEP> __device__ __forceinline__ v2u lds_take2(unsigned addr) {
  v2u v;
#pragma nounroll
  for (unsigned spins = 0; spins < LSPIN_CAP; ++spins) {
    asm volatile("ds_read_b64 %0, %1\n\ts_waitcnt lgkmcnt(0)" : "=&v"(v) : "v"(addr) : "memory");
    if (__all(v.x != EMPTY_O && v.y != EMPTY_O)) break;
    if (SLEEP) __builtin_amdgcn_s_sleep(SLEEP);
  }
  return v;
}
#define STAMP(i) do {} while (0)
#define STAMP_OUT() do {} while (0)

constexpr int L_O = 0;
constexpr int L_ML = L_O + 4096;
constexpr int L_QP = L_ML + 64;
constexpr int L_Q = L_QP + 256;
constexpr int L_OB = L_Q + 256;
constexpr int L_CNT = L_OB + 32;
constexpr int L_END = L_CNT + 8;

struct St {
  v4u T[32][4];
  float Sc[8][4];
  unsigned P[8][4];
  v4u Q, mask;
  f32x4 O;
  v4u rs[4];
  int vk, vv, so;
  unsigned tb, nx;
  float* lds;
  int lane, wave;
};

template <int I> __device__ __forceinline__ void request(St& z) {
  constexpr int lim = I < NLIM ? LIM : LIMB;
  if constexpr (4 * I > lim) asm volatile("s_waitcnt vmcnt(%0)" :: "i"(lim) : "memory");
  gate_next(z.tb);
  if constexpr (SOFT16 && I < NSOFT) z.tb += SOFT16;
  tile_load(z.T[I], (I & 8) ? z.vv : z.vk, z.rs[I >> 3], z.so + (I & 7) * 32768);
  SB();
}
template <int I, int W> __device__ __forceinline__ void consume(St& z) {
  const int g = z.lane >> 4, n = z.lane & 15;
  constexpr int br = I >> 4;
  if constexpr ((I & 15) == 0) {
    if constexpr (br) lds_wait<1>((unsigned*)(z.lds + L_CNT) + 7, 1u);
    z.Q = *(const v4u*)((const unsigned short*)(z.lds + L_Q) + br * 256 + (n >> 3) * 128 + 32 * g + 8 * (n & 3));
  }
  if constexpr ((I & 8) == 0) {
    tile_wait<W>(z.T[I]);
#pragma unroll
    for (int j = 0; j < 4; ++j) {
      const f32x4 d = mfma(z.T[I][j], z.Q, f32x4{0.f, 0.f, 0.f, 0.f});
      z.Sc[I & 7][j] = (dpp<0x00>(d.x) + dpp<0x55>(d.y)) + (dpp<0xAA>(d.z) + dpp<0xFF>(d.w));
    }
    if constexpr ((I & 7) == 7) {
      float m = z.Sc[0][0];
#pragma unroll
      for (int t = 0; t < 8; ++t)
#pragma unroll
        for (int j = 0; j < 4; ++j) m = fmax_(m, z.Sc[t][j]);
      const float m2 = xg_max(m) * C2;
      float l = 0.f;
#pragma unroll
      for (int t = 0; t < 8; ++t) {
        float p[4];
#pragma unroll
        for (int j = 0; j < 4; ++j) { p[j] = __builtin_amdgcn_exp2f(fmaf(z.Sc[t][j], C2, -m2)); z.P[t][j] = dup_bf16(p[j]); }
        l += (p[0] + p[1]) + (p[2] + p[3]);
        asm volatile("" : "+v"(z.P[t][0]), "+v"(z.P[t][1]), "+v"(z.P[t][2]), "+v"(z.P[t][3]));
      }
      l = xg_sum(l);
      if (g == 0 && (n & 7) == 0) { z.lds[L_ML + br * 32 + (n >> 3) * 8 + z.wave] = m2; z.lds[L_ML + br * 32 + 16 + (n >> 3) * 8 + z.wave] = l; }
      lds_arrive((unsigned*)(z.lds + L_CNT) + 2 + br, z.lane);
      z.O = f32x4{0.f, 0.f, 0.f, 0.f};
    } else {
      asm volatile("" : "+v"(z.Sc[I & 7][0]), "+v"(z.Sc[I & 7][1]), "+v"(z.Sc[I & 7][2]), "+v"(z.Sc[I & 7][3]));
    }
  } else if constexpr (W == 0) {
    v4u A[4];
#pragma unroll
    for (int j = 0; j < 4; ++j) A[j] = diag(z.P[I & 7][j], z.mask);
    float* dst = z.lds + L_O + br * 2048 + z.wave * 256 + (g >> 1) * 128 + 8 * n + 4 * (g & 1);
    unsigned da = lds_addr(dst);
    asm volatile("" : "+v"(A[0]), "+v"(A[1]), "+v"(A[2]), "+v"(A[3]), "+v"(da));
    piece_wait<3>(z.T[I][0]); z.O = mfma(A[0], z.T[I][0], z.O); SB();
    piece_wait<2>(z.T[I][1]); z.O = mfma(A[1], z.T[I][1], z.O); SB();
    piece_wait<1>(z.T[I][2]); z.O = mfma(A[2], z.T[I][2], z.O); SB();
    piece_wait<0>(z.T[I][3]); z.O = mfma(A[3], z.T[I][3], z.O);
    *(LAS f32x4*)(size_t)da = z.O;
  } else {
    tile_wait<W>(z.T[I]);
#pragma unroll
    for (int j = 0; j < 4; ++j) z.O = mfma(diag(z.P[I & 7][j], z.mask), z.T[I][j], z.O);
    asm volatile("" : "+v"(z.O));
    if constexpr ((I & 7) == 7) *(f32x4*)(z.lds + L_O + br * 2048 + z.wave * 256 + (g >> 1) * 128 + 8 * n + 4 * (g & 1)) = z.O;
  }
  SB();
}
template <int I, int N> struct Run {
  static __device__ __forceinline__ void go(St& z) { request<I>(z); consume<I - DEP + 1, 4 * (DEP - 1)>(z); Run<I + 1, N>::go(z); }
};
template <int N> struct Run<N, N> { static __device__ __forceinline__ void go(St&) {} };
template <int I, int N> struct Req { static __device__ __forceinline__ void go(St& z) { request<I>(z); Req<I + 1, N>::go(z); } };
template <int N> struct Req<N, N> { static __device__ __forceinline__ void go(St&) {} };
template <int I, int N, int LAST> struct Con {
  static __device__ __forceinline__ void go(St& z) { consume<I, 4 * (LAST - I)>(z); Con<I + 1, N, LAST>::go(z); }
};
template <int N, int LAST> struct Con<N, N, LAST> { static __device__ __forceinline__ void go(St&) {} };

typedef unsigned v16u __attribute__((ext_vector_type(16)));
typedef unsigned v8u __attribute__((ext_vector_type(8)));
__device__ __forceinline__ unsigned char* ptr64(unsigned lo, unsigned hi) { return (unsigned char*)(((unsigned long long)hi << 32) | lo); }

__global__ void __launch_bounds__(NTHR, 3) gqa2_fused(Args) {
  __shared__ __attribute__((aligned(16))) float lds[L_END];
  unsigned* const l_cnt = (unsigned*)(lds + L_CNT);
  v16u ka; v8u kb;
  asm volatile("s_load_dwordx16 %0, %2, 0x0\n\ts_load_dwordx8 %1, %2, 0x40" : "=&s"(ka), "=&s"(kb) : "s"(__builtin_amdgcn_kernarg_segment_ptr()));
  const int tid = threadIdx.x, lane = tid & 63, wave = __builtin_amdgcn_readfirstlane(tid >> 6);
  const int g = lane >> 4, n = lane & 15;
  const int wg = blockIdx.x;
  const int ch = (wg & 7) << 5 | wg >> 3;
  const int kvh = ch >> 4, rb = (ch >> 3) & 1, s8 = ch & 7;
  {
    const v4u e = {EMPTY_O, EMPTY_O, EMPTY_O, EMPTY_O};
    const int i = tid < 512 ? tid : 0;
    *(v4u*)(lds + L_O + i * 4) = e; *(v4u*)(lds + L_O + 2048 + i * 4) = e;
    const int j = tid < 40 ? tid : 0;
    ((unsigned*)(lds + L_OB))[j] = j < 32 ? EMPTY_B : 0u;
  }
  asm volatile("s_waitcnt lgkmcnt(0)" : "+s"(ka), "+s"(kb) :: "memory");
  const v4u rk0 = mkrs(ptr64(ka[4], ka[5]) + (size_t)ch * 262144, 262144);
  unsigned probe;
  asm volatile("s_cmp_lg_u32 %2, 0\n\ts_cbranch_scc1 .Lp%=\n\ts_mov_b64 exec, 1\n\tbuffer_load_dword %0, off, %1, 0 nt\n\ts_mov_b64 exec, -1\n.Lp%=:"
               : "=v"(probe) : "s"(rk0), "s"(wave) : "scc");
  __builtin_amdgcn_s_barrier();
  asm volatile("" ::: "memory");
  unsigned char* const ws = ptr64(kb[6], kb[7]);
  const unsigned char* const wop = rb ? ptr64(kb[2], kb[3]) : ptr64(kb[0], kb[1]);
  const __amdgpu_buffer_rsrc_t rw = __builtin_amdgcn_make_buffer_rsrc(ws, 0, WS_ST, RSRC3);
  v4u mask;
  {
    const unsigned half = (n & 1) ? 0xffff0000u : 0x0000ffffu; const int r = (n & 7) >> 1;
    mask.x = r == 0 ? half : 0u; mask.y = r == 1 ? half : 0u; mask.z = r == 2 ? half : 0u; mask.w = r == 3 ? half : 0u;
  }
  const int pidx = kvh * 8 + s8;
  if (__builtin_expect(wave >= 8, 1)) {
    asm volatile("" ::: "memory");
    __builtin_amdgcn_s_setprio(3);
    const int hw = wave - 8, cg = s8 & 3, kh = s8 >> 2, sub = 2 * g + (n >> 3);
    const __amdgpu_buffer_rsrc_t rq = __builtin_amdgcn_make_buffer_rsrc((rb ? ptr64(ka[14], ka[15]) : ptr64(ka[12], ka[13])) + ((size_t)(kh * 288 + hw * 144) * NQ + kvh * 256 + cg * 64) * 2, 0, 143 * NQ * 2 + 128, RSRC3);
    const __amdgpu_buffer_rsrc_t rx = __builtin_amdgcn_make_buffer_rsrc((rb ? ptr64(ka[2], ka[3]) : ptr64(ka[0], ka[1])) + (kh * 288 + hw * 144) * 2, 0, 144 * 2, RSRC3);
    v4u wqv[18];
#pragma unroll
    for (int i = 0; i < 18; ++i) wqv[i] = __builtin_amdgcn_raw_buffer_load_b128(rq, (ABL & 4) ? OOB : sub * (NQ * 2) + (n & 7) * 16, i * 8 * NQ * 2, AUX_NT);
    const unsigned xa = __builtin_amdgcn_raw_buffer_load_b32(rx, n * 16 + g * 4, 0, 0), xb = __builtin_amdgcn_raw_buffer_load_b32(rx, n * 16 + g * 4 + 256, 0, 0);
    const unsigned ep = __builtin_amdgcn_raw_buffer_load_b32(rw, lane == 0 ? WS_EPOCH : OOB, 0, AUX_SC1);
    SB();
    f32x4 a0 = {0.f, 0.f, 0.f, 0.f}, a1 = {0.f, 0.f, 0.f, 0.f};
    const unsigned sel = n < 8 ? 0x01000100u : 0x03020302u;
#define QSTEP(acc, i, src, k) acc = mfma(diag(__builtin_amdgcn_perm(0u, (unsigned)__builtin_amdgcn_mov_dpp((int)(src), 0x150 + (k), 0xf, 0xf, true), sel), mask), wqv[i], acc)
    QSTEP(a0, 0, xa, 0); QSTEP(a1, 1, xa, 1); QSTEP(a0, 2, xa, 2); QSTEP(a1, 3, xa, 3); QSTEP(a0, 4, xa, 4); QSTEP(a1, 5, xa, 5);
    QSTEP(a0, 6, xa, 6); QSTEP(a1, 7, xa, 7); QSTEP(a0, 8, xa, 8); QSTEP(a1, 9, xa, 9); QSTEP(a0, 10, xa, 10); QSTEP(a1, 11, xa, 11);
    QSTEP(a0, 12, xa, 12); QSTEP(a1, 13, xa, 13); QSTEP(a0, 14, xa, 14); QSTEP(a1, 15, xa, 15); QSTEP(a0, 16, xb, 0); QSTEP(a1, 17, xb, 1);
    if ((g >> 1) == (n >> 3)) *(f32x4*)(lds + L_QP + (hw * 2 + (n >> 3)) * 64 + 8 * (n & 7) + 4 * (g & 1)) = a0 + a1;
    const unsigned tag = __builtin_amdgcn_readfirstlane(ep) + TAG_STEP;
    STAMP(1);
    lds_arrive(l_cnt + 0, lane);
    if (hw == 0) {
      lds_wait<1>(l_cnt + 0, 2u);
      const float q = (lds[L_QP + lane] + lds[L_QP + 64 + lane]) + (lds[L_QP + 128 + lane] + lds[L_QP + 192 + lane]);
      st_gran(ws, WS_QG + ((rb * 16 + kvh) * 512 + kh * 256 + cg * 64 + lane) * 8, tag, q);
      if (lane == 0) l_cnt[5] = tag;
    }
    STAMP(2);
    pause(QGA);
    if (hw) pause(QGB - QGA);
    {
      v2u x[8];
      sweep<8, 512, 8>(rw, WS_QG + ((hw * 16 + kvh) * 512 + lane) * 8, tag, x);
#pragma unroll
      for (int k = 0; k < 4; ++k) ((unsigned short*)(lds + L_Q))[hw * 256 + k * 64 + lane] = f2bf(__uint_as_float(x[k].x) + __uint_as_float(x[k + 4].x));
      lds_arrive(hw ? l_cnt + 7 : l_cnt + 1, lane);
    }
    STAMP(3);
#pragma nounroll
    for (int br = 0; br < 2; ++br) {
      lds_wait<8>(l_cnt + 2 + br, 8u);
      const float* ml = lds + L_ML + br * 32 + hw * 8;
      const f32x4 m0 = *(const f32x4*)(ml), m1 = *(const f32x4*)(ml + 4), l0 = *(const f32x4*)(ml + 16), l1 = *(const f32x4*)(ml + 20);
      const float M = fmax_(fmax_(fmax_(m0.x, m0.y), fmax_(m0.z, m0.w)), fmax_(fmax_(m1.x, m1.y), fmax_(m1.z, m1.w)));
      float L = 0.f;
#pragma unroll
      for (int w = 0; w < 8; ++w) L = fmaf(__builtin_amdgcn_exp2f((w < 4 ? m0[w & 3] : m1[w & 3]) - M), w < 4 ? l0[w & 3] : l1[w & 3], L);
      const int mg = WS_MG + ((br * 16 + kvh) * 2 + hw) * 1024;
      if (lane < 8) st_gran(ws, mg + (ch & 15) * 64 + lane * 8, tag, (lane & 1) ? L : M);
      pause(MLD);
      v2u x[1];
      sweep<1, 0, 8>(rw, mg + n * 64 + (g & 1) * 8, tag, x);
      auto sw = __builtin_amdgcn_permlane16_swap(x[0].x, x[0].x, false, false);
      const float Ms = __uint_as_float(sw[0]), Ls = __uint_as_float(sw[1]);
      float Mx = fmax_(Ms, dpp<XOR1>(Ms)); Mx = fmax_(Mx, dpp<XOR2>(Mx)); Mx = fmax_(Mx, dpp<HMIR>(Mx)); Mx = fmax_(Mx, dpp<MIR>(Mx));
      float Lt = __builtin_amdgcn_exp2f(Ms - Mx) * Ls; Lt += dpp<XOR1>(Lt); Lt += dpp<XOR2>(Lt); Lt += dpp<HMIR>(Lt); Lt += dpp<MIR>(Lt);
      const float inv = 1.0f / Lt;
      float c[8];
#pragma unroll
      for (int w = 0; w < 8; ++w) c[w] = __builtin_amdgcn_exp2f((w < 4 ? m0[w & 3] : m1[w & 3]) - Mx) * inv;
      const unsigned oa = lds_addr(lds + L_O + br * 2048 + hw * 128 + 2 * lane);
      int dst = WS_PG + (br * 256 + ch) * PG_ROW + (hw * 128 + 2 * lane) * 8;
      asm volatile("" : "+v"(dst));
      float o0 = 0.f, o1 = 0.f;
      { const v2u v = lds_take2<8>(oa); o0 = c[0] * __uint_as_float(v.x); o1 = c[0] * __uint_as_float(v.y); }
#pragma unroll
      for (int w = 1; w < 8; ++w) { const v2u v = lds_take2<0>(oa + w * 1024); o0 = fmaf(c[w], __uint_as_float(v.x), o0); o1 = fmaf(c[w], __uint_as_float(v.y), o1); }
      st_gran2(rw, dst, tag, o0, o1);
      if (hw == 0 && lane == 0) __hip_atomic_store(l_cnt + 4, (unsigned)br + 1u, RLX_WG);
    }
    STAMP(8);
  } else {
    if (wave == 0) {
      unsigned long long ta;
      asm volatile("s_waitcnt vmcnt(0)\n\ts_memrealtime %0\n\ts_waitcnt lgkmcnt(0)" : "=s"(ta) : "v"(probe) : "memory");
      if (lane == 0) __hip_atomic_store(l_cnt + 6, ((unsigned)ta << 4) | 1u, RLX_WG);
    }
    St z;
    const int phs = PAT == 2 ? (wg >> 3) & 7 : wave;
    z.lds = lds; z.lane = lane; z.wave = wave; z.mask = mask;
    z.rs[0] = rk0;
    z.rs[1] = mkrs(ptr64(ka[8], ka[9]) + (size_t)ch * 262144, 262144);
    z.rs[2] = mkrs(ptr64(ka[6], ka[7]) + (size_t)ch * 262144, 262144);
    z.rs[3] = mkrs(ptr64(ka[10], ka[11]) + (size_t)ch * 262144, 262144);
    z.vk = (ABL & 8) ? lane * 16 : ((lane >> 2) & 3) * 256 + g * 64 + (lane & 3) * 16; z.vv = lane * 16; z.so = wave * WST;
    GAS unsigned short* outp = (GAS unsigned short*)ptr64(kb[4], kb[5]);
    z.tb = lds_wait<1>(l_cnt + 6, 1u) + (unsigned)(OFF16 + ((phs * DT16) >> 3));
    z.nx = z.tb;
    STAMP(1);
    Req<0, PRE>::go(z);
    STAMP(4);
    lds_wait<1>(l_cnt + 1, 1u);
    const unsigned tag = __builtin_amdgcn_readfirstlane(l_cnt[5]);
    STAMP(5);
    Con<0, PRE - DEP + 1, PRE - 1>::go(z);
    Run<PRE, 17>::go(z);
    STAMP(14);
    Run<17, 25>::go(z);
    STAMP(15);
    Run<25, 32>::go(z);
    STAMP(6);
    Con<32 - DEP + 1, 32, 31>::go(z);
    STAMP(7);
    const unsigned oba = lds_addr(lds + L_OB + g * 8);
    if (wave < 5) {
      const __amdgpu_buffer_rsrc_t ro = __builtin_amdgcn_make_buffer_rsrc((unsigned char*)wop + ((size_t)(kvh * 256 + s8 * 32) * DM) * 2, 0, 32 * DM * 2, RSRC3);
      const int wcol = wave * 128 + n * 8;
      const int vwo = (!NOWO && wcol < DM) ? g * (DM * 2) + wcol * 2 : OOB;
      v4u Wo[8];
      z.tb += (unsigned)((rb ? WO16 + wave * WOS16 : WOA16 + wave * WOSA16) - ((phs * DT16) >> 3));
      gate_next(z.tb);
#pragma unroll
      for (int i = 0; i < 8; ++i) Wo[i] = __builtin_amdgcn_raw_buffer_load_b128(ro, vwo, i * 4 * DM * 2, AUX_NT);
      int dst = wcol < DM ? WS_YG + (rb * 128 + pidx) * YG_ROW + (wcol + 4 * (g & 1) + 2 * (g >> 1)) * 8 : OOB;
      asm volatile("" : "+v"(dst), "+v"(Wo[0]), "+v"(Wo[1]), "+v"(Wo[2]), "+v"(Wo[3]), "+v"(Wo[4]), "+v"(Wo[5]), "+v"(Wo[6]), "+v"(Wo[7]));
      STAMP(2);
      v4u oa, ob;
#pragma nounroll
      for (unsigned spins = 0; spins < LSPIN_CAP; ++spins) {
        asm volatile("ds_read_b128 %0, %2\n\tds_read_b128 %1, %2 offset:16\n\ts_waitcnt lgkmcnt(0)" : "=&v"(oa), "=&v"(ob) : "v"(oba) : "memory");
        if (__all(oa.x != EMPTY_B && oa.y != EMPTY_B && oa.z != EMPTY_B && oa.w != EMPTY_B && ob.x != EMPTY_B && ob.y != EMPTY_B && ob.z != EMPTY_B && ob.w != EMPTY_B)) break;
      }
      STAMP(10);
      f32x4 Y0 = {0.f, 0.f, 0.f, 0.f}, Y1 = {0.f, 0.f, 0.f, 0.f};
#pragma unroll
      for (int i = 0; i < 4; ++i) { Y0 = mfma(diag(oa[i], mask), Wo[i], Y0); Y1 = mfma(diag(ob[i], mask), Wo[4 + i], Y1); }
      const f32x4 Y = Y0 + Y1;
      st_gran2(rw, dst, tag, g < 2 ? Y.x : Y.z, g < 2 ? Y.y : Y.w);
      STAMP(11);
    } else if (wave == 5) {
      const int po = WS_PG + (rb * 256 + kvh * 16 + (lane >> 5)) * PG_ROW + (32 * s8 + (lane & 31)) * 8;
      v2u x[8];
      lds_wait<0>(l_cnt + 4, (unsigned)rb + 1u);
      if (rb) pause(H1D);
      sweep<8, 2 * PG_ROW, 1>(rw, po, tag, x);
      STAMP(9);
      const float o = x32_sum(((__uint_as_float(x[0].x) + __uint_as_float(x[1].x)) + (__uint_as_float(x[2].x) + __uint_as_float(x[3].x)))
                            + ((__uint_as_float(x[4].x) + __uint_as_float(x[5].x)) + (__uint_as_float(x[6].x) + __uint_as_float(x[7].x))));
      if (lane < 32) ((unsigned*)(lds + L_OB))[(lane & 3) * 8 + (lane >> 2)] = dup_bf16(o);
    } else if (wave == 6 || pidx < 16) {
      const int pos = wave == 6 ? pidx * 4 : 512 + pidx * 4;
      const int so = WS_YG + (rb * 128 + (lane >> 2)) * YG_ROW + (pos + (lane & 3)) * 8;
      const int col = pos + (lane & 3);
      GAS unsigned short* op = outp + rb * DM + col;
      asm volatile("" : "+v"(op));
      v2u x[8];
      z.tb += (unsigned)((rb ? WO16 + 4 * WOS16 : WOA16 + 4 * WOSA16) + H2G16 - ((phs * DT16) >> 3));
#pragma nounroll
      for (unsigned spins = 0; spins < LSPIN_CAP; ++spins) {
        if (__builtin_amdgcn_readfirstlane(__hip_atomic_load((unsigned*)(lds + L_OB), RLX_WG)) != EMPTY_B) break;
        __builtin_amdgcn_s_sleep(1);
      }
      pause(H2D);
      gate_next(z.tb);
      sweep<8, 16 * YG_ROW, 1>(rw, so, tag, x);
      STAMP(12);
      float y = ((__uint_as_float(x[0].x) + __uint_as_float(x[1].x)) + (__uint_as_float(x[2].x) + __uint_as_float(x[3].x)))
              + ((__uint_as_float(x[4].x) + __uint_as_float(x[5].x)) + (__uint_as_float(x[6].x) + __uint_as_float(x[7].x)));
      y += dpp<ROR4>(y); y += dpp<ROR8>(y);
      y = xg_sum(y);
      if (lane < 4) *op = f2bf(y);
      if (rb == 0 && pos == 0 && lane == 0) __hip_atomic_store((gu32*)(ws + WS_EPOCH), tag, RLX_AGENT);
      STAMP(13);
    }
  }
  STAMP_OUT();
}

extern "C" void kernel_launch(void* const* d_in, const int* in_sizes, int n_in, void* d_out, int out_size, void* d_ws, size_t ws_size, hipStream_t stream) {
  Args a;
  a.x[0] = d_in[0]; a.k[0] = d_in[1]; a.v[0] = d_in[2]; a.wq[0] = d_in[3]; a.wo[0] = d_in[4];
  a.x[1] = d_in[5]; a.k[1] = d_in[6]; a.v[1] = d_in[7]; a.wq[1] = d_in[8]; a.wo[1] = d_in[9];
  a.out = d_out; a.ws = d_ws;
  gqa2_fused<<<NWG, NTHR, 0, stream>>>(a);
}
```

```cpp
#define EVT 1
#define OFF16 4000
#include <hip/hip_runtime.h>
#include <cstdint>

typedef unsigned v4u __attribute__((ext_vector_type(4)));
typedef unsigned v2u __attribute__((ext_vector_type(2)));
typedef float f32x4 __attribute__((ext_vector_type(4)));
typedef __bf16 bf16x8 __attribute__((ext_vector_type(8)));
typedef __bf16 bf16x2 __attribute__((ext_vector_type(2)));
typedef float f32x2 __attribute__((ext_vector_type(2)));
#define GAS __attribute__((address_space(1)))
#define LAS __attribute__((address_space(3)))
typedef GAS unsigned gu32;
typedef GAS unsigned long long gu64;
#define RLX_AGENT __ATOMIC_RELAXED, __HIP_MEMORY_SCOPE_AGENT
#define RLX_WG __ATOMIC_RELAXED, __HIP_MEMORY_SCOPE_WORKGROUP

#define STAMPS 0
#define DT16 1792
#define ANC -2
#define PRE 8
#define DEP 3
#define LIM 2
#define WO16 0
#define WOS16 600
#define QGD 75
#define PTH16 400
#define SLK16 100000
#define H1D 40
#define H2D 70
#define PREW 0
#define HBAR 1
#define PAT 1
#define JST 8192
#define WST 1024
#define SOFT16 0
#define NSOFT 6
#define H2G16 2000
#define WOA16 0
#define WOSA16 600
#define OUTSC 0
#define SLOWSTART 0
#define P016 2400
#define NLIM 32
#define LIMB 8
#define DF16 0
#define JIT16 160
#define ABL 0
#define QGA 280
#define QGB 600
#define MLD 350
#define PIPE 0
#define GAP1 8
#define GAP2 8
#define NOWO 0

constexpr int DM = 576, NQ = 4096;
constexpr int NWG = 256, NTHR = 640;
constexpr int AUX_NT = 2, AUX_SC1 = 16;
constexpr int RSRC3 = 0x00020000;
constexpr int OOB = 0x7fff0000;
constexpr int WS_EPOCH = 0;
constexpr int WS_QG = 4096;
constexpr int WS_MG = WS_QG + 32 * 4096;
constexpr int WS_PG = WS_MG + 64 * 1024;
constexpr int PG_ROW = 256 * 8;
constexpr int WS_YG = WS_PG + 512 * PG_ROW;
constexpr int YG_ROW = 576 * 8;
constexpr int WS_ST = WS_YG + 256 * YG_ROW;
constexpr unsigned TAG_STEP = 0x9E3779B9u;
constexpr unsigned SPIN_CAP = 1u << 17, LSPIN_CAP = 1u << 22;
constexpr float C2 = 0.088388347648318440f * 1.4426950408889634f;
constexpr unsigned EMPTY_O = 0xffffffffu, EMPTY_B = 0x0000ffffu;

struct Args {
  const void* x[2]; const void* k[2]; const void* v[2]; const void* wq[2]; const void* wo[2];
  void* out; void* ws;
};

#define SB() __builtin_amdgcn_sched_barrier(0)
__device__ __forceinline__ f32x4 mfma(v4u a, v4u b, f32x4 c) {
  return __builtin_amdgcn_mfma_f32_16x16x32_bf16(__builtin_bit_cast(bf16x8, a), __builtin_bit_cast(bf16x8, b), c, 0, 0, 0);
}
__device__ __forceinline__ unsigned dup_bf16(float f) {
  f32x2 v = {f, f};
  return __builtin_bit_cast(unsigned, __builtin_convertvector(v, bf16x2));
}
__device__ __forceinline__ unsigned short f2bf(float f) { return __builtin_bit_cast(unsigned short, (__bf16)f); }
__device__ __forceinline__ v4u diag(unsigned pp, v4u mask) { v4u r = {pp & mask.x, pp & mask.y, pp & mask.z, pp & mask.w}; return r; }
template <int CTRL> __device__ __forceinline__ float dpp(float x) {
  return __builtin_bit_cast(float, __builtin_amdgcn_mov_dpp(__builtin_bit_cast(int, x), CTRL, 0xf, 0xf, true));
}
constexpr int XOR1 = 0xB1, XOR2 = 0x4E, HMIR = 0x141, MIR = 0x140, ROR8 = 0x128, ROR4 = 0x124;
__device__ __forceinline__ float fmax_(float a, float b) { return __builtin_fmaxf(a, b); }
__device__ __forceinline__ float xg_max(float x) {
  auto s = __builtin_amdgcn_permlane16_swap(__float_as_uint(x), __float_as_uint(x), false, false);
  x = fmax_(__uint_as_float(s[0]), __uint_as_float(s[1]));
  auto t = __builtin_amdgcn_permlane32_swap(__float_as_uint(x), __float_as_uint(x), false, false);
  return fmax_(__uint_as_float(t[0]), __uint_as_float(t[1]));
}
__device__ __forceinline__ float x32_sum(float x) {
  auto t = __builtin_amdgcn_permlane32_swap(__float_as_uint(x), __float_as_uint(x), false, false);
  return __uint_as_float(t[0]) + __uint_as_float(t[1]);
}
__device__ __forceinline__ float xg_sum(float x) {
  auto s = __builtin_amdgcn_permlane16_swap(__float_as_uint(x), __float_as_uint(x), false, false);
  return x32_sum(__uint_as_float(s[0]) + __uint_as_float(s[1]));
}
__device__ __forceinline__ void st_gran(unsigned char* ws, int off, unsigned tag, float v) {
  __hip_atomic_store((gu64*)(ws + off), ((unsigned long long)tag << 32) | __float_as_uint(v), RLX_AGENT);
}
__device__ __forceinline__ void st_gran2(__amdgpu_buffer_rsrc_t rs, int off, unsigned tag, float a, float b) {
  __builtin_amdgcn_raw_buffer_store_b128(v4u{__float_as_uint(a), tag, __float_as_uint(b), tag}, rs, off, 0, AUX_SC1);
}
__device__ __forceinline__ v2u ldg(__amdgpu_buffer_rsrc_t rs, int off) { return __builtin_amdgcn_raw_buffer_load_b64(rs, off, 0, AUX_SC1); }
template <int N, int STEP, int SLEEP> __device__ __forceinline__ void sweep(__amdgpu_buffer_rsrc_t rs, int off, unsigned tag, v2u (&x)[N]) {
#pragma unroll
  for (int k = 0; k < N; ++k) x[k] = ldg(rs, off + k * STEP);
  SB();
  bool ok = true;
#pragma unroll
  for (int k = 0; k < N; ++k) ok &= x[k].y == tag;
  if (__builtin_expect(__all(ok), 1)) return;
#pragma nounroll
  for (unsigned spins = 0; spins < SPIN_CAP; ++spins) {
    __builtin_amdgcn_s_sleep(SLEEP);
    v2u y[N];
#pragma unroll
    for (int k = 0; k < N; ++k) y[k] = ldg(rs, x[k].y == tag ? OOB : off + k * STEP);
    SB();
    ok = true;
#pragma unroll
    for (int k = 0; k < N; ++k) { x[k] = x[k].y == tag ? x[k] : y[k]; ok &= x[k].y == tag; }
    if (__all(ok)) break;
  }
}
template <int STEP> __device__ __forceinline__ void pass4(v4u (&y)[4], const v4u (&x)[4], int off, v4u rs, unsigned tag) {
  int o[4];
#pragma unroll
  for (int k = 0; k < 4; ++k) o[k] = (x[k].y == tag && x[k].w == tag) ? OOB : off + k * STEP;
  asm volatile("buffer_load_dwordx4 %0, %4, %8, 0 offen sc1\n\t"
               "buffer_load_dwordx4 %1, %5, %8, 0 offen sc1\n\t"
               "buffer_load_dwordx4 %2, %6, %8, 0 offen sc1\n\t"
               "buffer_load_dwordx4 %3, %7, %8, 0 offen sc1"
               : "=&v"(y[0]), "=&v"(y[1]), "=&v"(y[2]), "=&v"(y[3]) : "v"(o[0]), "v"(o[1]), "v"(o[2]), "v"(o[3]), "s"(rs) : "memory");
}
__device__ __forceinline__ bool take4(v4u (&x)[4], v4u (&y)[4], unsigned tag) {
  asm volatile("s_waitcnt vmcnt(4)" : "+v"(y[0]), "+v"(y[1]), "+v"(y[2]), "+v"(y[3]));
  bool ok = true;
#pragma unroll
  for (int k = 0; k < 4; ++k) {
    const bool have = x[k].y == tag && x[k].w == tag;
    x[k] = have ? x[k] : y[k];
    ok &= x[k].y == tag && x[k].w == tag;
  }
  return __all(ok);
}
__device__ __forceinline__ void pause(int ticks);
template <int STEP, int GAP> __device__ __forceinline__ void sweep2(v4u rs, int off, unsigned tag, v4u (&x)[4], v4u (&ya)[4], v4u (&yb)[4]) {
#pragma unroll
  for (int k = 0; k < 4; ++k) x[k] = v4u{0u, ~tag, 0u, ~tag};
  pass4<STEP>(ya, x, off, rs, tag);
  __builtin_amdgcn_s_sleep(GAP);
#pragma nounroll
  for (unsigned spins = 0; spins < SPIN_CAP; ++spins) {
    pass4<STEP>(yb, x, off, rs, tag);
    if (take4(x, ya, tag)) break;
    pass4<STEP>(ya, x, off, rs, tag);
    if (take4(x, yb, tag)) break;
  }
}
#define KEEP8(A, B) asm volatile("s_waitcnt vmcnt(0)" :: "v"(A[0]), "v"(A[1]), "v"(A[2]), "v"(A[3]), "v"(B[0]), "v"(B[1]), "v"(B[2]), "v"(B[3]) : "memory")
__device__ __forceinline__ v4u mkrs(const void* p, unsigned bytes) {
  const unsigned long long u = (unsigned long long)p;
  v4u r = {(unsigned)u, (unsigned)(u >> 32) & 0xffffu, bytes, (unsigned)RSRC3};
  return r;
}
__device__ __forceinline__ void tile_load(v4u (&d)[4], int voff, v4u rs, int soff) {
  asm volatile("buffer_load_dwordx4 %0, %4, %5, %6 offen nt\n\t"
               "buffer_load_dwordx4 %1, %4, %5, %7 offen nt\n\t"
               "buffer_load_dwordx4 %2, %4, %5, %8 offen nt\n\t"
               "buffer_load_dwordx4 %3, %4, %5, %9 offen nt"
               : "=&v"(d[0]), "=&v"(d[1]), "=&v"(d[2]), "=&v"(d[3]) : "v"(voff), "s"(rs), "s"(soff), "s"(soff + JST), "s"(soff + 2 * JST), "s"(soff + 3 * JST));
}
template <int N> __device__ __forceinline__ void tile_wait(v4u (&d)[4]) {
  asm volatile("s_waitcnt vmcnt(%4)" : "+v"(d[0]), "+v"(d[1]), "+v"(d[2]), "+v"(d[3]) : "i"(N));
}
template <int N> __device__ __forceinline__ void piece_wait(v4u& d) { asm volatile("s_waitcnt vmcnt(%1)" : "+v"(d) : "i"(N)); }
__device__ __forceinline__ void pause(int ticks) {
  unsigned due;
  asm volatile("s_memrealtime vcc\n\t"
               "s_waitcnt lgkmcnt(0)\n\t"
               "s_add_i32 %0, vcc_lo, %1\n"
               ".Lg0_%=:\n\t"
               "s_sleep 1\n\t"
               "s_memrealtime vcc\n\t"
               "s_waitcnt lgkmcnt(0)\n\t"
               "s_sub_i32 vcc_lo, vcc_lo, %0\n\t"
               "s_cmp_lt_i32 vcc_lo, 0\n\t"
               "s_cbranch_scc1 .Lg0_%="
               : "=&s"(due) : "i"(ticks) : "scc", "vcc", "memory");
}
__device__ __forceinline__ void gate_next(unsigned& due) {
  asm volatile("s_branch .Lg1_%=\n"
               ".Lg0_%=:\n\t"
               "s_sleep 1\n"
               ".Lg1_%=:\n\t"
               "s_memrealtime vcc\n\t"
               "s_waitcnt lgkmcnt(0)\n\t"
               "s_lshl_b32 vcc_lo, vcc_lo, 4\n\t"
               "s_sub_i32 vcc_lo, vcc_lo, %0\n\t"
               "s_cmp_lt_i32 vcc_lo, 0\n\t"
               "s_cbranch_scc1 .Lg0_%=\n\t"
               "s_setprio 0\n\t"
               "s_cmp_lt_i32 vcc_lo, %1\n\t"
               "s_cbranch_scc1 .Lg2_%=\n\t"
               "s_setprio 1\n"
               ".Lg2_%=:\n\t"
               "s_sub_i32 vcc_lo, vcc_lo, %2\n\t"
               "s_max_i32 vcc_lo, vcc_lo, 0\n\t"
               "s_add_i32 %0, %0, vcc_lo\n\t"
               "s_add_i32 %0, %0, %3"
               : "+s"(due) : "i"(PTH16), "i"(SLK16), "i"(DT16) : "scc", "vcc", "memory");
}
__device__ __forceinline__ void gate2(unsigned& line, unsigned& nxt) {
  asm volatile("s_sub_i32 vcc_lo, %1, %0\n\t"
               "s_max_i32 vcc_lo, vcc_lo, 0\n\t"
               "s_add_i32 vcc_hi, %0, vcc_lo\n\t"
               "s_mov_b32 %1, vcc_hi\n\t"
               "s_branch .Lg1_%=\n"
               ".Lg0_%=:\n\t"
               "s_sleep 1\n"
               ".Lg1_%=:\n\t"
               "s_memrealtime vcc\n\t"
               "s_waitcnt lgkmcnt(0)\n\t"
               "s_lshl_b32 vcc_lo, vcc_lo, 4\n\t"
               "s_sub_i32 vcc_hi, vcc_lo, %1\n\t"
               "s_cmp_lt_i32 vcc_hi, 0\n\t"
               "s_cbranch_scc1 .Lg0_%=\n\t"
               "s_sub_i32 vcc_hi, vcc_hi, %4\n\t"
               "s_max_i32 vcc_hi, vcc_hi, 0\n\t"
               "s_add_i32 %1, %1, vcc_hi\n\t"
               "s_add_i32 %1, %1, %3\n\t"
               "s_add_i32 %0, %0, %2"
               : "+s"(line), "+s"(nxt) : "i"(DT16), "i"(DF16), "i"(JIT16) : "scc", "vcc", "memory");
}
__device__ __forceinline__ void lds_barrier() {
  asm volatile("s_waitcnt lgkmcnt(0)" ::: "memory");
  __builtin_amdgcn_s_barrier();
  asm volatile("" ::: "memory");
}
__device__ __forceinline__ void lds_arrive(unsigned* c, int lane) {
  asm volatile("s_waitcnt lgkmcnt(0)" ::: "memory");
  if (lane == 0) __hip_atomic_fetch_add(c, 1u, RLX_WG);
}
template <int SLEEP> __device__ __forceinline__ unsigned lds_wait(unsigned* c, unsigned want) {
  unsigned v = 0;
#pragma nounroll
  for (unsigned spins = 0; spins < LSPIN_CAP; ++spins) {
    v = __builtin_amdgcn_readfirstlane(__hip_atomic_load(c, RLX_WG));
    if (v >= want) break;
    __builtin_amdgcn_s_sleep(SLEEP);
  }
  asm volatile("" ::: "memory");
  return v;
}
__device__ __forceinline__ unsigned lds_addr(const void* p) { return (unsigned)(size_t)(const LAS void*)p; }
template <int SLEEP> __device__ __forceinline__ v2u lds_take2(unsigned addr) {
  v2u v;
#pragma nounroll
  for (unsigned spins = 0; spins < LSPIN_CAP; ++spins) {
    asm volatile("ds_read_b64 %0, %1\n\ts_waitcnt lgkmcnt(0)" : "=&v"(v) : "v"(addr) : "memory");
    if (__all(v.x != EMPTY_O && v.y != EMPTY_O)) break;
    if (SLEEP) __builtin_amdgcn_s_sleep(SLEEP);
  }
  return v;
}
#define STAMP(i) do {} while (0)
#define STAMP_OUT() do {} while (0)

constexpr int L_O = 0;
constexpr int L_ML = L_O + 4096;
constexpr int L_QP = L_ML + 64;
constexpr int L_Q = L_QP + 256;
constexpr int L_OB = L_Q + 256;
constexpr int L_CNT = L_OB + 32;
constexpr int L_END = L_CNT + 8;

struct St {
  v4u T[32][4];
  float Sc[8][4];
  unsigned P[8][4];
  v4u Q, mask;
  f32x4 O;
  v4u rs[4];
  int vk, vv, so;
  unsigned tb, nx;
  float* lds;
  int lane, wave;
};

template <int I> __device__ __forceinline__ void request(St& z) {
  constexpr int lim = I < NLIM ? LIM : LIMB;
  if constexpr (4 * I > lim) asm volatile("s_waitcnt vmcnt(%0)" :: "i"(lim) : "memory");
  gate_next(z.tb);
  if constexpr (SOFT16 && I < NSOFT) z.tb += SOFT16;
  tile_load(z.T[I], (I & 8) ? z.vv : z.vk, z.rs[I >> 3], z.so + (I & 7) * 32768);
  SB();
}
template <int I, int W> __device__ __forceinline__ void consume(St& z) {
  const int g = z.lane >> 4, n = z.lane & 15;
  constexpr int br = I >> 4;
  if constexpr ((I & 15) == 0) {
    if constexpr (br) lds_wait<1>((unsigned*)(z.lds + L_CNT) + 7, 1u);
    z.Q = *(const v4u*)((const unsigned short*)(z.lds + L_Q) + br * 256 + (n >> 3) * 128 + 32 * g + 8 * (n & 3));
  }
  if constexpr ((I & 8) == 0) {
    tile_wait<W>(z.T[I]);
#pragma unroll
    for (int j = 0; j < 4; ++j) {
      const f32x4 d = mfma(z.T[I][j], z.Q, f32x4{0.f, 0.f, 0.f, 0.f});
      z.Sc[I & 7][j] = (dpp<0x00>(d.x) + dpp<0x55>(d.y)) + (dpp<0xAA>(d.z) + dpp<0xFF>(d.w));
    }
    if constexpr ((I & 7) == 7) {
      float m = z.Sc[0][0];
#pragma unroll
      for (int t = 0; t < 8; ++t)
#pragma unroll
        for (int j = 0; j < 4; ++j) m = fmax_(m, z.Sc[t][j]);
      const float m2 = xg_max(m) * C2;
      float l = 0.f;
#pragma unroll
      for (int t = 0; t < 8; ++t) {
        float p[4];
#pragma unroll
        for (int j = 0; j < 4; ++j) { p[j] = __builtin_amdgcn_exp2f(fmaf(z.Sc[t][j], C2, -m2)); z.P[t][j] = dup_bf16(p[j]); }
        l += (p[0] + p[1]) + (p[2] + p[3]);
        asm volatile("" : "+v"(z.P[t][0]), "+v"(z.P[t][1]), "+v"(z.P[t][2]), "+v"(z.P[t][3]));
      }
      l = xg_sum(l);
      if (g == 0 && (n & 7) == 0) { z.lds[L_ML + br * 32 + (n >> 3) * 8 + z.wave] = m2; z.lds[L_ML + br * 32 + 16 + (n >> 3) * 8 + z.wave] = l; }
      lds_arrive((unsigned*)(z.lds + L_CNT) + 2 + br, z.lane);
      z.O = f32x4{0.f, 0.f, 0.f, 0.f};
    } else {
      asm volatile("" : "+v"(z.Sc[I & 7][0]), "+v"(z.Sc[I & 7][1]), "+v"(z.Sc[I & 7][2]), "+v"(z.Sc[I & 7][3]));
    }
  } else if constexpr (W == 0) {
    v4u A[4];
#pragma unroll
    for (int j = 0; j < 4; ++j) A[j] = diag(z.P[I & 7][j], z.mask);
    float* dst = z.lds + L_O + br * 2048 + z.wave * 256 + (g >> 1) * 128 + 8 * n + 4 * (g & 1);
    unsigned da = lds_addr(dst);
    asm volatile("" : "+v"(A[0]), "+v"(A[1]), "+v"(A[2]), "+v"(A[3]), "+v"(da));
    piece_wait<3>(z.T[I][0]); z.O = mfma(A[0], z.T[I][0], z.O); SB();
    piece_wait<2>(z.T[I][1]); z.O = mfma(A[1], z.T[I][1], z.O); SB();
    piece_wait<1>(z.T[I][2]); z.O = mfma(A[2], z.T[I][2], z.O); SB();
    piece_wait<0>(z.T[I][3]); z.O = mfma(A[3], z.T[I][3], z.O);
    *(LAS f32x4*)(size_t)da = z.O;
  } else {
    tile_wait<W>(z.T[I]);
#pragma unroll
    for (int j = 0; j < 4; ++j) z.O = mfma(diag(z.P[I & 7][j], z.mask), z.T[I][j], z.O);
    asm volatile("" : "+v"(z.O));
    if constexpr ((I & 7) == 7) *(f32x4*)(z.lds + L_O + br * 2048 + z.wave * 256 + (g >> 1) * 128 + 8 * n + 4 * (g & 1)) = z.O;
  }
  SB();
}
template <int I, int N> struct Run {
  static __device__ __forceinline__ void go(St& z) { request<I>(z); consume<I - DEP + 1, 4 * (DEP - 1)>(z); Run<I + 1, N>::go(z); }
};
template <int N> struct Run<N, N> { static __device__ __forceinline__ void go(St&) {} };
template <int I, int N> struct Req { static __device__ __forceinline__ void go(St& z) { request<I>(z); Req<I + 1, N>::go(z); } };
template <int N> struct Req<N, N> { static __device__ __forceinline__ void go(St&) {} };
template <int I, int N, int LAST> struct Con {
  static __device__ __forceinline__ void go(St& z) { consume<I, 4 * (LAST - I)>(z); Con<I + 1, N, LAST>::go(z); }
};
template <int N, int LAST> struct Con<N, N, LAST> { static __device__ __forceinline__ void go(St&) {} };

typedef unsigned v16u __attribute__((ext_vector_type(16)));
typedef unsigned v8u __attribute__((ext_vector_type(8)));
__device__ __forceinline__ unsigned char* ptr64(unsigned lo, unsigned hi) { return (unsigned char*)(((unsigned long long)hi << 32) | lo); }

__global__ void __launch_bounds__(NTHR, 3) gqa2_fused(Args) {
  __shared__ __attribute__((aligned(16))) float lds[L_END];
  unsigned* const l_cnt = (unsigned*)(lds + L_CNT);
  v16u ka; v8u kb;
  asm volatile("s_load_dwordx16 %0, %2, 0x0\n\ts_load_dwordx8 %1, %2, 0x40" : "=&s"(ka), "=&s"(kb) : "s"(__builtin_amdgcn_kernarg_segment_ptr()));
  unsigned long long te0; asm volatile("s_memrealtime %0" : "=s"(te0));
  const int tid = threadIdx.x, lane = tid & 63, wave = __builtin_amdgcn_readfirstlane(tid >> 6);
  const int g = lane >> 4, n = lane & 15;
  const int wg = blockIdx.x;
  const int ch = (wg & 7) << 5 | wg >> 3;
  const int kvh = ch >> 4, rb = (ch >> 3) & 1, s8 = ch & 7;
  {
    const v4u e = {EMPTY_O, EMPTY_O, EMPTY_O, EMPTY_O};
    const int i = tid < 512 ? tid : 0;
    *(v4u*)(lds + L_O + i * 4) = e; *(v4u*)(lds + L_O + 2048 + i * 4) = e;
    const int j = tid < 40 ? tid : 0;
    ((unsigned*)(lds + L_OB))[j] = j < 32 ? EMPTY_B : 0u;
  }
  asm volatile("s_waitcnt lgkmcnt(0)" : "+s"(ka), "+s"(kb) :: "memory");
  unsigned long long te1; asm volatile("s_memrealtime %0\n\ts_waitcnt lgkmcnt(0)" : "=s"(te1), "+s"(te0));
  const v4u rk0 = mkrs(ptr64(ka[4], ka[5]) + (size_t)ch * 262144, 262144);
  unsigned probe;
  asm volatile("s_cmp_lg_u32 %2, 0\n\ts_cbranch_scc1 .Lp%=\n\ts_mov_b64 exec, 1\n\tbuffer_load_dword %0, off, %1, 0 nt\n\ts_mov_b64 exec, -1\n.Lp%=:"
               : "=v"(probe) : "s"(rk0), "s"(wave) : "scc");
  __builtin_amdgcn_s_barrier();
  asm volatile("" ::: "memory");
  unsigned char* const ws = ptr64(kb[6], kb[7]);
  const unsigned char* const wop = rb ? ptr64(kb[2], kb[3]) : ptr64(kb[0], kb[1]);
  const __amdgpu_buffer_rsrc_t rw = __builtin_amdgcn_make_buffer_rsrc(ws, 0, WS_ST, RSRC3);
  v4u mask;
  {
    const unsigned half = (n & 1) ? 0xffff0000u : 0x0000ffffu; const int r = (n & 7) >> 1;
    mask.x = r == 0 ? half : 0u; mask.y = r == 1 ? half : 0u; mask.z = r == 2 ? half : 0u; mask.w = r == 3 ? half : 0u;
  }
  const int pidx = kvh * 8 + s8;
  if (__builtin_expect(wave >= 8, 1)) {
    asm volatile("" ::: "memory");
    __builtin_amdgcn_s_setprio(3);
    const int hw = wave - 8, cg = s8 & 3, kh = s8 >> 2, sub = 2 * g + (n >> 3);
    const __amdgpu_buffer_rsrc_t rq = __builtin_amdgcn_make_buffer_rsrc((rb ? ptr64(ka[14], ka[15]) : ptr64(ka[12], ka[13])) + ((size_t)(kh * 288 + hw * 144) * NQ + kvh * 256 + cg * 64) * 2, 0, 143 * NQ * 2 + 128, RSRC3);
    const __amdgpu_buffer_rsrc_t rx = __builtin_amdgcn_make_buffer_rsrc((rb ? ptr64(ka[2], ka[3]) : ptr64(ka[0], ka[1])) + (kh * 288 + hw * 144) * 2, 0, 144 * 2, RSRC3);
    v4u wqv[18];
#pragma unroll
    for (int i = 0; i < 18; ++i) wqv[i] = __builtin_amdgcn_raw_buffer_load_b128(rq, (ABL & 4) ? OOB : sub * (NQ * 2) + (n & 7) * 16, i * 8 * NQ * 2, AUX_NT);
    const unsigned xa = __builtin_amdgcn_raw_buffer_load_b32(rx, n * 16 + g * 4, 0, 0), xb = __builtin_amdgcn_raw_buffer_load_b32(rx, n * 16 + g * 4 + 256, 0, 0);
    const unsigned ep = __builtin_amdgcn_raw_buffer_load_b32(rw, lane == 0 ? WS_EPOCH : OOB, 0, AUX_SC1);
    SB();
    f32x4 a0 = {0.f, 0.f, 0.f, 0.f}, a1 = {0.f, 0.f, 0.f, 0.f};
    const unsigned sel = n < 8 ? 0x01000100u : 0x03020302u;
#define QSTEP(acc, i, src, k) acc = mfma(diag(__builtin_amdgcn_perm(0u, (unsigned)__builtin_amdgcn_mov_dpp((int)(src), 0x150 + (k), 0xf, 0xf, true), sel), mask), wqv[i], acc)
    QSTEP(a0, 0, xa, 0); QSTEP(a1, 1, xa, 1); QSTEP(a0, 2, xa, 2); QSTEP(a1, 3, xa, 3); QSTEP(a0, 4, xa, 4); QSTEP(a1, 5, xa, 5);
    QSTEP(a0, 6, xa, 6); QSTEP(a1, 7, xa, 7); QSTEP(a0, 8, xa, 8); QSTEP(a1, 9, xa, 9); QSTEP(a0, 10, xa, 10); QSTEP(a1, 11, xa, 11);
    QSTEP(a0, 12, xa, 12); QSTEP(a1, 13, xa, 13); QSTEP(a0, 14, xa, 14); QSTEP(a1, 15, xa, 15); QSTEP(a0, 16, xb, 0); QSTEP(a1, 17, xb, 1);
    if ((g >> 1) == (n >> 3)) *(f32x4*)(lds + L_QP + (hw * 2 + (n >> 3)) * 64 + 8 * (n & 7) + 4 * (g & 1)) = a0 + a1;
    const unsigned tag = __builtin_amdgcn_readfirstlane(ep) + TAG_STEP;
    STAMP(1);
    unsigned long long te3; asm volatile("s_memrealtime %0\n\ts_waitcnt lgkmcnt(0)" : "=s"(te3) : "s"(tag) : "memory");
    lds_arrive(l_cnt + 0, lane);
    if (hw == 0) {
      lds_wait<1>(l_cnt + 0, 2u);
      const float q = (lds[L_QP + lane] + lds[L_QP + 64 + lane]) + (lds[L_QP + 128 + lane] + lds[L_QP + 192 + lane]);
      st_gran(ws, WS_QG + ((rb * 16 + kvh) * 512 + kh * 256 + cg * 64 + lane) * 8, tag, q);
      if (lane == 0) l_cnt[5] = tag;
    }
    STAMP(2);
    pause(QGA);
    if (hw) pause(QGB - QGA);
    {
      v2u x[8];
      sweep<8, 512, 8>(rw, WS_QG + ((hw * 16 + kvh) * 512 + lane) * 8, tag, x);
#pragma unroll
      for (int k = 0; k < 4; ++k) ((unsigned short*)(lds + L_Q))[hw * 256 + k * 64 + lane] = f2bf(__uint_as_float(x[k].x) + __uint_as_float(x[k + 4].x));
      lds_arrive(hw ? l_cnt + 7 : l_cnt + 1, lane);
    }
    STAMP(3);
#pragma nounroll
    for (int br = 0; br < 2; ++br) {
      lds_wait<8>(l_cnt + 2 + br, 8u);
      const float* ml = lds + L_ML + br * 32 + hw * 8;
      const f32x4 m0 = *(const f32x4*)(ml), m1 = *(const f32x4*)(ml + 4), l0 = *(const f32x4*)(ml + 16), l1 = *(const f32x4*)(ml + 20);
      const float M = fmax_(fmax_(fmax_(m0.x, m0.y), fmax_(m0.z, m0.w)), fmax_(fmax_(m1.x, m1.y), fmax_(m1.z, m1.w)));
      float L = 0.f;
#pragma unroll
      for (int w = 0; w < 8; ++w) L = fmaf(__builtin_amdgcn_exp2f((w < 4 ? m0[w & 3] : m1[w & 3]) - M), w < 4 ? l0[w & 3] : l1[w & 3], L);
      const int mg = WS_MG + ((br * 16 + kvh) * 2 + hw) * 1024;
      if (lane < 8) st_gran(ws, mg + (ch & 15) * 64 + lane * 8, tag, (lane & 1) ? L : M);
      pause(MLD);
      v2u x[1];
      sweep<1, 0, 8>(rw, mg + n * 64 + (g & 1) * 8, tag, x);
      auto sw = __builtin_amdgcn_permlane16_swap(x[0].x, x[0].x, false, false);
      const float Ms = __uint_as_float(sw[0]), Ls = __uint_as_float(sw[1]);
      float Mx = fmax_(Ms, dpp<XOR1>(Ms)); Mx = fmax_(Mx, dpp<XOR2>(Mx)); Mx = fmax_(Mx, dpp<HMIR>(Mx)); Mx = fmax_(Mx, dpp<MIR>(Mx));
      float Lt = __builtin_amdgcn_exp2f(Ms - Mx) * Ls; Lt += dpp<XOR1>(Lt); Lt += dpp<XOR2>(Lt); Lt += dpp<HMIR>(Lt); Lt += dpp<MIR>(Lt);
      const float inv = 1.0f / Lt;
      float c[8];
#pragma unroll
      for (int w = 0; w < 8; ++w) c[w] = __builtin_amdgcn_exp2f((w < 4 ? m0[w & 3] : m1[w & 3]) - Mx) * inv;
      const unsigned oa = lds_addr(lds + L_O + br * 2048 + hw * 128 + 2 * lane);
      int dst = WS_PG + (br * 256 + ch) * PG_ROW + (hw * 128 + 2 * lane) * 8;
      asm volatile("" : "+v"(dst));
      float o0 = 0.f, o1 = 0.f;
      { const v2u v = lds_take2<8>(oa); o0 = c[0] * __uint_as_float(v.x); o1 = c[0] * __uint_as_float(v.y); }
#pragma unroll
      for (int w = 1; w < 8; ++w) { const v2u v = lds_take2<0>(oa + w * 1024); o0 = fmaf(c[w], __uint_as_float(v.x), o0); o1 = fmaf(c[w], __uint_as_float(v.y), o1); }
      st_gran2(rw, dst, tag, o0, o1);
      if (hw == 0 && lane == 0) __hip_atomic_store(l_cnt + 4, (unsigned)br + 1u, RLX_WG);
    }
    STAMP(8);
    {
      unsigned due = EVT == 1 ? (unsigned)te0 << 4 : EVT == 2 ? (unsigned)te1 << 4 : EVT == 3 ? __builtin_amdgcn_readfirstlane(l_cnt[6]) : (unsigned)te3 << 4;
      due += 6000u << 4;
      gate_next(due);
    }
  } else {
    if (wave == 0) {
      unsigned long long ta;
      asm volatile("s_waitcnt vmcnt(0)\n\ts_memrealtime %0\n\ts_waitcnt lgkmcnt(0)" : "=s"(ta) : "v"(probe) : "memory");
      if (lane == 0) __hip_atomic_store(l_cnt + 6, ((unsigned)ta << 4) | 1u, RLX_WG);
    }
    St z;
    const int phs = PAT == 2 ? (wg >> 3) & 7 : wave;
    z.lds = lds; z.lane = lane; z.wave = wave; z.mask = mask;
    z.rs[0] = rk0;
    z.rs[1] = mkrs(ptr64(ka[8], ka[9]) + (size_t)ch * 262144, 262144);
    z.rs[2] = mkrs(ptr64(ka[6], ka[7]) + (size_t)ch * 262144, 262144);
    z.rs[3] = mkrs(ptr64(ka[10], ka[11]) + (size_t)ch * 262144, 262144);
    z.vk = (ABL & 8) ? lane * 16 : ((lane >> 2) & 3) * 256 + g * 64 + (lane & 3) * 16; z.vv = lane * 16; z.so = wave * WST;
    GAS unsigned short* outp = (GAS unsigned short*)ptr64(kb[4], kb[5]);
    z.tb = lds_wait<1>(l_cnt + 6, 1u) + (unsigned)(OFF16 + ((phs * DT16) >> 3));
    z.nx = z.tb;
    STAMP(1);
    Req<0, PRE>::go(z);
    STAMP(4);
    lds_wait<1>(l_cnt + 1, 1u);
    const unsigned tag = __builtin_amdgcn_readfirstlane(l_cnt[5]);
    STAMP(5);
    Con<0, PRE - DEP + 1, PRE - 1>::go(z);
    Run<PRE, 17>::go(z);
    STAMP(14);
    Run<17, 25>::go(z);
    STAMP(15);
    Run<25, 32>::go(z);
    STAMP(6);
    Con<32 - DEP + 1, 32, 31>::go(z);
    STAMP(7);
    const unsigned oba = lds_addr(lds + L_OB + g * 8);
    if (wave < 5) {
      const __amdgpu_buffer_rsrc_t ro = __builtin_amdgcn_make_buffer_rsrc((unsigned char*)wop + ((size_t)(kvh * 256 + s8 * 32) * DM) * 2, 0, 32 * DM * 2, RSRC3);
      const int wcol = wave * 128 + n * 8;
      const int vwo = (!NOWO && wcol < DM) ? g * (DM * 2) + wcol * 2 : OOB;
      v4u Wo[8];
      z.tb += (unsigned)((rb ? WO16 + wave * WOS16 : WOA16 + wave * WOSA16) - ((phs * DT16) >> 3));
      gate_next(z.tb);
#pragma unroll
      for (int i = 0; i < 8; ++i) Wo[i] = __builtin_amdgcn_raw_buffer_load_b128(ro, vwo, i * 4 * DM * 2, AUX_NT);
      int dst = wcol < DM ? WS_YG + (rb * 128 + pidx) * YG_ROW + (wcol + 4 * (g & 1) + 2 * (g >> 1)) * 8 : OOB;
      asm volatile("" : "+v"(dst), "+v"(Wo[0]), "+v"(Wo[1]), "+v"(Wo[2]), "+v"(Wo[3]), "+v"(Wo[4]), "+v"(Wo[5]), "+v"(Wo[6]), "+v"(Wo[7]));
      STAMP(2);
      v4u oa, ob;
#pragma nounroll
      for (unsigned spins = 0; spins < LSPIN_CAP; ++spins) {
        asm volatile("ds_read_b128 %0, %2\n\tds_read_b128 %1, %2 offset:16\n\ts_waitcnt lgkmcnt(0)" : "=&v"(oa), "=&v"(ob) : "v"(oba) : "memory");
        if (__all(oa.x != EMPTY_B && oa.y != EMPTY_B && oa.z != EMPTY_B && oa.w != EMPTY_B && ob.x != EMPTY_B && ob.y != EMPTY_B && ob.z != EMPTY_B && ob.w != EMPTY_B)) break;
      }
      STAMP(10);
      f32x4 Y0 = {0.f, 0.f, 0.f, 0.f}, Y1 = {0.f, 0.f, 0.f, 0.f};
#pragma unroll
      for (int i = 0; i < 4; ++i) { Y0 = mfma(diag(oa[i], mask), Wo[i], Y0); Y1 = mfma(diag(ob[i], mask), Wo[4 + i], Y1); }
      const f32x4 Y = Y0 + Y1;
      st_gran2(rw, dst, tag, g < 2 ? Y.x : Y.z, g < 2 ? Y.y : Y.w);
      STAMP(11);
    } else if (wave == 5) {
      const int po = WS_PG + (rb * 256 + kvh * 16 + (lane >> 5)) * PG_ROW + (32 * s8 + (lane & 31)) * 8;
      v2u x[8];
      lds_wait<0>(l_cnt + 4, (unsigned)rb + 1u);
      if (rb) pause(H1D);
      sweep<8, 2 * PG_ROW, 1>(rw, po, tag, x);
      STAMP(9);
      const float o = x32_sum(((__uint_as_float(x[0].x) + __uint_as_float(x[1].x)) + (__uint_as_float(x[2].x) + __uint_as_float(x[3].x)))
                            + ((__uint_as_float(x[4].x) + __uint_as_float(x[5].x)) + (__uint_as_float(x[6].x) + __uint_as_float(x[7].x))));
      if (lane < 32) ((unsigned*)(lds + L_OB))[(lane & 3) * 8 + (lane >> 2)] = dup_bf16(o);
    } else if (wave == 6 || pidx < 16) {
      const int pos = wave == 6 ? pidx * 4 : 512 + pidx * 4;
      const int so = WS_YG + (rb * 128 + (lane >> 2)) * YG_ROW + (pos + (lane & 3)) * 8;
      const int col = pos + (lane & 3);
      GAS unsigned short* op = outp + rb * DM + col;
      asm volatile("" : "+v"(op));
      v2u x[8];
      z.tb += (unsigned)((rb ? WO16 + 4 * WOS16 : WOA16 + 4 * WOSA16) + H2G16 - ((phs * DT16) >> 3));
#pragma nounroll
      for (unsigned spins = 0; spins < LSPIN_CAP; ++spins) {
        if (__builtin_amdgcn_readfirstlane(__hip_atomic_load((unsigned*)(lds + L_OB), RLX_WG)) != EMPTY_B) break;
        __builtin_amdgcn_s_sleep(1);
      }
      pause(H2D);
      gate_next(z.tb);
      sweep<8, 16 * YG_ROW, 1>(rw, so, tag, x);
      STAMP(12);
      float y = ((__uint_as_float(x[0].x) + __uint_as_float(x[1].x)) + (__uint_as_float(x[2].x) + __uint_as_float(x[3].x)))
              + ((__uint_as_float(x[4].x) + __uint_as_float(x[5].x)) + (__uint_as_float(x[6].x) + __uint_as_float(x[7].x)));
      y += dpp<ROR4>(y); y += dpp<ROR8>(y);
      y = xg_sum(y);
      if (lane < 4) *op = f2bf(y);
      if (rb == 0 && pos == 0 && lane == 0) __hip_atomic_store((gu32*)(ws + WS_EPOCH), tag, RLX_AGENT);
      STAMP(13);
    }
  }
  STAMP_OUT();
}

extern "C" void kernel_launch(void* const* d_in, const int* in_sizes, int n_in, void* d_out, int out_size, void* d_ws, size_t ws_size, hipStream_t stream) {
  Args a;
  a.x[0] = d_in[0]; a.k[0] = d_in[1]; a.v[0] = d_in[2]; a.wq[0] = d_in[3]; a.wo[0] = d_in[4];
  a.x[1] = d_in[5]; a.k[1] = d_in[6]; a.v[1] = d_in[7]; a.wq[1] = d_in[8]; a.wo[1] = d_in[9];
  a.out = d_out; a.ws = d_ws;
  gqa2_fused<<<NWG, NTHR, 0, stream>>>(a);
}
```

```cpp
#define OFF16 800
#define DT16 1792
#include <hip/hip_runtime.h>
#include <cstdint>

typedef unsigned v4u __attribute__((ext_vector_type(4)));
typedef unsigned v2u __attribute__((ext_vector_type(2)));
typedef float f32x4 __attribute__((ext_vector_type(4)));
typedef __bf16 bf16x8 __attribute__((ext_vector_type(8)));
typedef __bf16 bf16x2 __attribute__((ext_vector_type(2)));
typedef float f32x2 __attribute__((ext_vector_type(2)));
#define GAS __attribute__((address_space(1)))
#define LAS __attribute__((address_space(3)))
typedef GAS unsigned gu32;
typedef GAS unsigned long long gu64;
#define RLX_AGENT __ATOMIC_RELAXED, __HIP_MEMORY_SCOPE_AGENT
#define RLX_WG __ATOMIC_RELAXED, __HIP_MEMORY_SCOPE_WORKGROUP

#define STAMPS 0
#define ANC -2
#define PRE 8
#define DEP 3
#define LIM 2
#define WO16 0
#define WOS16 600
#define QGD 75
#define PTH16 400
#define SLK16 100000
#define H1D 40
#define H2D 70
#define PREW 0
#define HBAR 1
#define PAT 1
#define JST 8192
#define WST 1024
#define SOFT16 0
#define NSOFT 6
#define H2G16 2000
#define WOA16 0
#define WOSA16 600
#define OUTSC 0
#define SLOWSTART 0
#define P016 2400
#define NLIM 32
#define LIMB 8
#define DF16 0
#define JIT16 160
#define ABL 0
#define QGA 280
#define QGB 600
#define MLD 350
#define PIPE 0
#define GAP1 8
#define GAP2 8
#define ALIGN 1
#define NW1 3
#define WG16 1800
#define WOSB16 600
#define NOWO 0

constexpr int DM = 576, NQ = 4096;
constexpr int NWG = 256, NTHR = 640;
constexpr int AUX_NT = 2, AUX_SC1 = 16;
constexpr int RSRC3 = 0x00020000;
constexpr int OOB = 0x7fff0000;
constexpr int WS_EPOCH = 0;
constexpr int WS_AG = 1024;
constexpr int WS_QG = 4096;
constexpr int WS_MG = WS_QG + 32 * 4096;
constexpr int WS_PG = WS_MG + 64 * 1024;
constexpr int PG_ROW = 256 * 8;
constexpr int WS_YG = WS_PG + 512 * PG_ROW;
constexpr int YG_ROW = 576 * 8;
constexpr int WS_ST = WS_YG + 256 * YG_ROW;
constexpr unsigned TAG_STEP = 0x9E3779B9u;
constexpr unsigned SPIN_CAP = 1u << 17, LSPIN_CAP = 1u << 22;
constexpr float C2 = 0.088388347648318440f * 1.4426950408889634f;
constexpr unsigned EMPTY_O = 0xffffffffu, EMPTY_B = 0x0000ffffu;

struct Args {
  const void* x[2]; const void* k[2]; const void* v[2]; const void* wq[2]; const void* wo[2];
  void* out; void* ws;
};

#define SB() __builtin_amdgcn_sched_barrier(0)
__device__ __forceinline__ f32x4 mfma(v4u a, v4u b, f32x4 c) {
  return __builtin_amdgcn_mfma_f32_16x16x32_bf16(__builtin_bit_cast(bf16x8, a), __builtin_bit_cast(bf16x8, b), c, 0, 0, 0);
}
__device__ __forceinline__ unsigned dup_bf16(float f) {
  f32x2 v = {f, f};
  return __builtin_bit_cast(unsigned, __builtin_convertvector(v, bf16x2));
}
__device__ __forceinline__ unsigned short f2bf(float f) { return __builtin_bit_cast(unsigned short, (__bf16)f); }
__device__ __forceinline__ v4u diag(unsigned pp, v4u mask) { v4u r = {pp & mask.x, pp & mask.y, pp & mask.z, pp & mask.w}; return r; }
template <int CTRL> __device__ __forceinline__ float dpp(float x) {
  return __builtin_bit_cast(float, __builtin_amdgcn_mov_dpp(__builtin_bit_cast(int, x), CTRL, 0xf, 0xf, true));
}
constexpr int XOR1 = 0xB1, XOR2 = 0x4E, HMIR = 0x141, MIR = 0x140, ROR8 = 0x128, ROR4 = 0x124;
__device__ __forceinline__ float fmax_(float a, float b) { return __builtin_fmaxf(a, b); }
__device__ __forceinline__ float xg_max(float x) {
  auto s = __builtin_amdgcn_permlane16_swap(__float_as_uint(x), __float_as_uint(x), false, false);
  x = fmax_(__uint_as_float(s[0]), __uint_as_float(s[1]));
  auto t = __builtin_amdgcn_permlane32_swap(__float_as_uint(x), __float_as_uint(x), false, false);
  return fmax_(__uint_as_float(t[0]), __uint_as_float(t[1]));
}
__device__ __forceinline__ float x32_sum(float x) {
  auto t = __builtin_amdgcn_permlane32_swap(__float_as_uint(x), __float_as_uint(x), false, false);
  return __uint_as_float(t[0]) + __uint_as_float(t[1]);
}
__device__ __forceinline__ float xg_sum(float x) {
  auto s = __builtin_amdgcn_permlane16_swap(__float_as_uint(x), __float_as_uint(x), false, false);
  return x32_sum(__uint_as_float(s[0]) + __uint_as_float(s[1]));
}
__device__ __forceinline__ void st_gran(unsigned char* ws, int off, unsigned tag, float v) {
  __hip_atomic_store((gu64*)(ws + off), ((unsigned long long)tag << 32) | __float_as_uint(v), RLX_AGENT);
}
__device__ __forceinline__ void st_gran2(__amdgpu_buffer_rsrc_t rs, int off, unsigned tag, float a, float b) {
  __builtin_amdgcn_raw_buffer_store_b128(v4u{__float_as_uint(a), tag, __float_as_uint(b), tag}, rs, off, 0, AUX_SC1);
}
__device__ __forceinline__ v2u ldg(__amdgpu_buffer_rsrc_t rs, int off) { return __builtin_amdgcn_raw_buffer_load_b64(rs, off, 0, AUX_SC1); }
template <int N, int STEP, int SLEEP> __device__ __forceinline__ void sweep(__amdgpu_buffer_rsrc_t rs, int off, unsigned tag, v2u (&x)[N]) {
#pragma unroll
  for (int k = 0; k < N; ++k) x[k] = ldg(rs, off + k * STEP);
  SB();
  bool ok = true;
#pragma unroll
  for (int k = 0; k < N; ++k) ok &= x[k].y == tag;
  if (__builtin_expect(__all(ok), 1)) return;
#pragma nounroll
  for (unsigned spins = 0; spins < SPIN_CAP; ++spins) {
    __builtin_amdgcn_s_sleep(SLEEP);
    v2u y[N];
#pragma unroll
    for (int k = 0; k < N; ++k) y[k] = ldg(rs, x[k].y == tag ? OOB : off + k * STEP);
    SB();
    ok = true;
#pragma unroll
    for (int k = 0; k < N; ++k) { x[k] = x[k].y == tag ? x[k] : y[k]; ok &= x[k].y == tag; }
    if (__all(ok)) break;
  }
}
template <int STEP> __device__ __forceinline__ void pass4(v4u (&y)[4], const v4u (&x)[4], int off, v4u rs, unsigned tag) {
  int o[4];
#pragma unroll
  for (int k = 0; k < 4; ++k) o[k] = (x[k].y == tag && x[k].w == tag) ? OOB : off + k * STEP;
  asm volatile("buffer_load_dwordx4 %0, %4, %8, 0 offen sc1\n\t"
               "buffer_load_dwordx4 %1, %5, %8, 0 offen sc1\n\t"
               "buffer_load_dwordx4 %2, %6, %8, 0 offen sc1\n\t"
               "buffer_load_dwordx4 %3, %7, %8, 0 offen sc1"
               : "=&v"(y[0]), "=&v"(y[1]), "=&v"(y[2]), "=&v"(y[3]) : "v"(o[0]), "v"(o[1]), "v"(o[2]), "v"(o[3]), "s"(rs) : "memory");
}
__device__ __forceinline__ bool take4(v4u (&x)[4], v4u (&y)[4], unsigned tag) {
  asm volatile("s_waitcnt vmcnt(4)" : "+v"(y[0]), "+v"(y[1]), "+v"(y[2]), "+v"(y[3]));
  bool ok = true;
#pragma unroll
  for (int k = 0; k < 4; ++k) {
    const bool have = x[k].y == tag && x[k].w == tag;
    x[k] = have ? x[k] : y[k];
    ok &= x[k].y == tag && x[k].w == tag;
  }
  return __all(ok);
}
__device__ __forceinline__ void pause(int ticks);
template <int STEP, int GAP> __device__ __forceinline__ void sweep2(v4u rs, int off, unsigned tag, v4u (&x)[4], v4u (&ya)[4], v4u (&yb)[4]) {
#pragma unroll
  for (int k = 0; k < 4; ++k) x[k] = v4u{0u, ~tag, 0u, ~tag};
  pass4<STEP>(ya, x, off, rs, tag);
  __builtin_amdgcn_s_sleep(GAP);
#pragma nounroll
  for (unsigned spins = 0; spins < SPIN_CAP; ++spins) {
    pass4<STEP>(yb, x, off, rs, tag);
    if (take4(x, ya, tag)) break;
    pass4<STEP>(ya, x, off, rs, tag);
    if (take4(x, yb, tag)) break;
  }
}
#define KEEP8(A, B) asm volatile("s_waitcnt vmcnt(0)" :: "v"(A[0]), "v"(A[1]), "v"(A[2]), "v"(A[3]), "v"(B[0]), "v"(B[1]), "v"(B[2]), "v"(B[3]) : "memory")
__device__ __forceinline__ v4u mkrs(const void* p, unsigned bytes) {
  const unsigned long long u = (unsigned long long)p;
  v4u r = {(unsigned)u, (unsigned)(u >> 32) & 0xffffu, bytes, (unsigned)RSRC3};
  return r;
}
__device__ __forceinline__ void tile_load(v4u (&d)[4], int voff, v4u rs, int soff) {
  asm volatile("buffer_load_dwordx4 %0, %4, %5, %6 offen nt\n\t"
               "buffer_load_dwordx4 %1, %4, %5, %7 offen nt\n\t"
               "buffer_load_dwordx4 %2, %4, %5, %8 offen nt\n\t"
               "buffer_load_dwordx4 %3, %4, %5, %9 offen nt"
               : "=&v"(d[0]), "=&v"(d[1]), "=&v"(d[2]), "=&v"(d[3]) : "v"(voff), "s"(rs), "s"(soff), "s"(soff + JST), "s"(soff + 2 * JST), "s"(soff + 3 * JST));
}
template <int N> __device__ __forceinline__ void tile_wait(v4u (&d)[4]) {
  asm volatile("s_waitcnt vmcnt(%4)" : "+v"(d[0]), "+v"(d[1]), "+v"(d[2]), "+v"(d[3]) : "i"(N));
}
template <int N> __device__ __forceinline__ void piece_wait(v4u& d) { asm volatile("s_waitcnt vmcnt(%1)" : "+v"(d) : "i"(N)); }
__device__ __forceinline__ void pause(int ticks) {
  unsigned due;
  asm volatile("s_memrealtime vcc\n\t"
               "s_waitcnt lgkmcnt(0)\n\t"
               "s_add_i32 %0, vcc_lo, %1\n"
               ".Lg0_%=:\n\t"
               "s_sleep 1\n\t"
               "s_memrealtime vcc\n\t"
               "s_waitcnt lgkmcnt(0)\n\t"
               "s_sub_i32 vcc_lo, vcc_lo, %0\n\t"
               "s_cmp_lt_i32 vcc_lo, 0\n\t"
               "s_cbranch_scc1 .Lg0_%="
               : "=&s"(due) : "i"(ticks) : "scc", "vcc", "memory");
}
__device__ __forceinline__ void gate_next(unsigned& due) {
  asm volatile("s_branch .Lg1_%=\n"
               ".Lg0_%=:\n\t"
               "s_sleep 1\n"
               ".Lg1_%=:\n\t"
               "s_memrealtime vcc\n\t"
               "s_waitcnt lgkmcnt(0)\n\t"
               "s_lshl_b32 vcc_lo, vcc_lo, 4\n\t"
               "s_sub_i32 vcc_lo, vcc_lo, %0\n\t"
               "s_cmp_lt_i32 vcc_lo, 0\n\t"
               "s_cbranch_scc1 .Lg0_%=\n\t"
               "s_setprio 0\n\t"
               "s_cmp_lt_i32 vcc_lo, %1\n\t"
               "s_cbranch_scc1 .Lg2_%=\n\t"
               "s_setprio 1\n"
               ".Lg2_%=:\n\t"
               "s_sub_i32 vcc_lo, vcc_lo, %2\n\t"
               "s_max_i32 vcc_lo, vcc_lo, 0\n\t"
               "s_add_i32 %0, %0, vcc_lo\n\t"
               "s_add_i32 %0, %0, %3"
               : "+s"(due) : "i"(PTH16), "i"(SLK16), "i"(DT16) : "scc", "vcc", "memory");
}
__device__ __forceinline__ void gate2(unsigned& line, unsigned& nxt) {
  asm volatile("s_sub_i32 vcc_lo, %1, %0\n\t"
               "s_max_i32 vcc_lo, vcc_lo, 0\n\t"
               "s_add_i32 vcc_hi, %0, vcc_lo\n\t"
               "s_mov_b32 %1, vcc_hi\n\t"
               "s_branch .Lg1_%=\n"
               ".Lg0_%=:\n\t"
               "s_sleep 1\n"
               ".Lg1_%=:\n\t"
               "s_memrealtime vcc\n\t"
               "s_waitcnt lgkmcnt(0)\n\t"
               "s_lshl_b32 vcc_lo, vcc_lo, 4\n\t"
               "s_sub_i32 vcc_hi, vcc_lo, %1\n\t"
               "s_cmp_lt_i32 vcc_hi, 0\n\t"
               "s_cbranch_scc1 .Lg0_%=\n\t"
               "s_sub_i32 vcc_hi, vcc_hi, %4\n\t"
               "s_max_i32 vcc_hi, vcc_hi, 0\n\t"
               "s_add_i32 %1, %1, vcc_hi\n\t"
               "s_add_i32 %1, %1, %3\n\t"
               "s_add_i32 %0, %0, %2"
               : "+s"(line), "+s"(nxt) : "i"(DT16), "i"(DF16), "i"(JIT16) : "scc", "vcc", "memory");
}
__device__ __forceinline__ void lds_barrier() {
  asm volatile("s_waitcnt lgkmcnt(0)" ::: "memory");
  __builtin_amdgcn_s_barrier();
  asm volatile("" ::: "memory");
}
__device__ __forceinline__ void lds_arrive(unsigned* c, int lane) {
  asm volatile("s_waitcnt lgkmcnt(0)" ::: "memory");
  if (lane == 0) __hip_atomic_fetch_add(c, 1u, RLX_WG);
}
template <int SLEEP> __device__ __forceinline__ unsigned lds_wait(unsigned* c, unsigned want) {
  unsigned v = 0;
#pragma nounroll
  for (unsigned spins = 0; spins < LSPIN_CAP; ++spins) {
    v = __builtin_amdgcn_readfirstlane(__hip_atomic_load(c, RLX_WG));
    if (v >= want) break;
    __builtin_amdgcn_s_sleep(SLEEP);
  }
  asm volatile("" ::: "memory");
  return v;
}
__device__ __forceinline__ unsigned lds_addr(const void* p) { return (unsigned)(size_t)(const LAS void*)p; }
template <int SLEEP> __device__ __forceinline__ v2u lds_take2(unsigned addr) {
  v2u v;
#pragma nounroll
  for (unsigned spins = 0; spins < LSPIN_CAP; ++spins) {
    asm volatile("ds_read_b64 %0, %1\n\ts_waitcnt lgkmcnt(0)" : "=&v"(v) : "v"(addr) : "memory");
    if (__all(v.x != EMPTY_O && v.y != EMPTY_O)) break;
    if (SLEEP) __builtin_amdgcn_s_sleep(SLEEP);
  }
  return v;
}
#define STAMP(i) do {} while (0)
#define STAMP_OUT() do {} while (0)

constexpr int L_O = 0;
constexpr int L_ML = L_O + 4096;
constexpr int L_QP = L_ML + 64;
constexpr int L_Q = L_QP + 256;
constexpr int L_OB = L_Q + 256;
constexpr int L_CNT = L_OB + 32;
constexpr int L_END = L_CNT + 16;

struct St {
  v4u T[32][4];
  float Sc[8][4];
  unsigned P[8][4];
  v4u Q, mask;
  f32x4 O;
  v4u rs[4];
  int vk, vv, so;
  unsigned tb, nx, adj;
  float* lds;
  int lane, wave;
};

template <int I> __device__ __forceinline__ void request(St& z) {
  constexpr int lim = I < NLIM ? LIM : LIMB;
  if constexpr (4 * I > lim) asm volatile("s_waitcnt vmcnt(%0)" :: "i"(lim) : "memory");
  gate_next(z.tb);
  if constexpr (I >= PRE && I < 31) z.tb += z.adj;
  tile_load(z.T[I], (I & 8) ? z.vv : z.vk, z.rs[I >> 3], z.so + (I & 7) * 32768);
  SB();
}
template <int I, int W> __device__ __forceinline__ void consume(St& z) {
  const int g = z.lane >> 4, n = z.lane & 15;
  constexpr int br = I >> 4;
  if constexpr ((I & 15) == 0) {
    if constexpr (br) lds_wait<1>((unsigned*)(z.lds + L_CNT) + 7, 1u);
    z.Q = *(const v4u*)((const unsigned short*)(z.lds + L_Q) + br * 256 + (n >> 3) * 128 + 32 * g + 8 * (n & 3));
  }
  if constexpr ((I & 8) == 0) {
    tile_wait<W>(z.T[I]);
#pragma unroll
    for (int j = 0; j < 4; ++j) {
      const f32x4 d = mfma(z.T[I][j], z.Q, f32x4{0.f, 0.f, 0.f, 0.f});
      z.Sc[I & 7][j] = (dpp<0x00>(d.x) + dpp<0x55>(d.y)) + (dpp<0xAA>(d.z) + dpp<0xFF>(d.w));
    }
    if constexpr ((I & 7) == 7) {
      float m = z.Sc[0][0];
#pragma unroll
      for (int t = 0; t < 8; ++t)
#pragma unroll
        for (int j = 0; j < 4; ++j) m = fmax_(m, z.Sc[t][j]);
      const float m2 = xg_max(m) * C2;
      float l = 0.f;
#pragma unroll
      for (int t = 0; t < 8; ++t) {
        float p[4];
#pragma unroll
        for (int j = 0; j < 4; ++j) { p[j] = __builtin_amdgcn_exp2f(fmaf(z.Sc[t][j], C2, -m2)); z.P[t][j] = dup_bf16(p[j]); }
        l += (p[0] + p[1]) + (p[2] + p[3]);
        asm volatile("" : "+v"(z.P[t][0]), "+v"(z.P[t][1]), "+v"(z.P[t][2]), "+v"(z.P[t][3]));
      }
      l = xg_sum(l);
      if (g == 0 && (n & 7) == 0) { z.lds[L_ML + br * 32 + (n >> 3) * 8 + z.wave] = m2; z.lds[L_ML + br * 32 + 16 + (n >> 3) * 8 + z.wave] = l; }
      lds_arrive((unsigned*)(z.lds + L_CNT) + 2 + br, z.lane);
      z.O = f32x4{0.f, 0.f, 0.f, 0.f};
    } else {
      asm volatile("" : "+v"(z.Sc[I & 7][0]), "+v"(z.Sc[I & 7][1]), "+v"(z.Sc[I & 7][2]), "+v"(z.Sc[I & 7][3]));
    }
  } else if constexpr (W == 0) {
    v4u A[4];
#pragma unroll
    for (int j = 0; j < 4; ++j) A[j] = diag(z.P[I & 7][j], z.mask);
    float* dst = z.lds + L_O + br * 2048 + z.wave * 256 + (g >> 1) * 128 + 8 * n + 4 * (g & 1);
    unsigned da = lds_addr(dst);
    asm volatile("" : "+v"(A[0]), "+v"(A[1]), "+v"(A[2]), "+v"(A[3]), "+v"(da));
    piece_wait<3>(z.T[I][0]); z.O = mfma(A[0], z.T[I][0], z.O); SB();
    piece_wait<2>(z.T[I][1]); z.O = mfma(A[1], z.T[I][1], z.O); SB();
    piece_wait<1>(z.T[I][2]); z.O = mfma(A[2], z.T[I][2], z.O); SB();
    piece_wait<0>(z.T[I][3]); z.O = mfma(A[3], z.T[I][3], z.O);
    *(LAS f32x4*)(size_t)da = z.O;
  } else {
    tile_wait<W>(z.T[I]);
#pragma unroll
    for (int j = 0; j < 4; ++j) z.O = mfma(diag(z.P[I & 7][j], z.mask), z.T[I][j], z.O);
    asm volatile("" : "+v"(z.O));
    if constexpr ((I & 7) == 7) *(f32x4*)(z.lds + L_O + br * 2048 + z.wave * 256 + (g >> 1) * 128 + 8 * n + 4 * (g & 1)) = z.O;
  }
  SB();
}
template <int I, int N> struct Run {
  static __device__ __forceinline__ void go(St& z) { request<I>(z); consume<I - DEP + 1, 4 * (DEP - 1)>(z); Run<I + 1, N>::go(z); }
};
template <int N> struct Run<N, N> { static __device__ __forceinline__ void go(St&) {} };
template <int I, int N> struct Req { static __device__ __forceinline__ void go(St& z) { request<I>(z); Req<I + 1, N>::go(z); } };
template <int N> struct Req<N, N> { static __device__ __forceinline__ void go(St&) {} };
template <int I, int N, int LAST> struct Con {
  static __device__ __forceinline__ void go(St& z) { consume<I, 4 * (LAST - I)>(z); Con<I + 1, N, LAST>::go(z); }
};
template <int N, int LAST> struct Con<N, N, LAST> { static __device__ __forceinline__ void go(St&) {} };

typedef unsigned v16u __attribute__((ext_vector_type(16)));
typedef unsigned v8u __attribute__((ext_vector_type(8)));
__device__ __forceinline__ unsigned char* ptr64(unsigned lo, unsigned hi) { return (unsigned char*)(((unsigned long long)hi << 32) | lo); }

__global__ void __launch_bounds__(NTHR, 3) gqa2_fused(Args) {
  __shared__ __attribute__((aligned(16))) float lds[L_END];
  unsigned* const l_cnt = (unsigned*)(lds + L_CNT);
  v16u ka; v8u kb;
  asm volatile("s_load_dwordx16 %0, %2, 0x0\n\ts_load_dwordx8 %1, %2, 0x40" : "=&s"(ka), "=&s"(kb) : "s"(__builtin_amdgcn_kernarg_segment_ptr()));
  const int tid = threadIdx.x, lane = tid & 63, wave = __builtin_amdgcn_readfirstlane(tid >> 6);
  const int g = lane >> 4, n = lane & 15;
  const int wg = blockIdx.x;
  const int ch = (wg & 7) << 5 | wg >> 3;
  const int kvh = ch >> 4, rb = (ch >> 3) & 1, s8 = ch & 7;
  {
    const v4u e = {EMPTY_O, EMPTY_O, EMPTY_O, EMPTY_O};
    const int i = tid < 512 ? tid : 0;
    *(v4u*)(lds + L_O + i * 4) = e; *(v4u*)(lds + L_O + 2048 + i * 4) = e;
    const int j = tid < 48 ? tid : 0;
    ((unsigned*)(lds + L_OB))[j] = j < 32 ? EMPTY_B : 0u;
  }
  asm volatile("s_waitcnt lgkmcnt(0)" : "+s"(ka), "+s"(kb) :: "memory");
  const v4u rk0 = mkrs(ptr64(ka[4], ka[5]) + (size_t)ch * 262144, 262144);
  unsigned probe;
  asm volatile("s_cmp_lg_u32 %2, 0\n\ts_cbranch_scc1 .Lp%=\n\ts_mov_b64 exec, 1\n\tbuffer_load_dword %0, off, %1, 0 nt\n\ts_mov_b64 exec, -1\n.Lp%=:"
               : "=v"(probe) : "s"(rk0), "s"(wave) : "scc");
  __builtin_amdgcn_s_barrier();
  asm volatile("" ::: "memory");
  unsigned char* const ws = ptr64(kb[6], kb[7]);
  const unsigned char* const wop = rb ? ptr64(kb[2], kb[3]) : ptr64(kb[0], kb[1]);
  const __amdgpu_buffer_rsrc_t rw = __builtin_amdgcn_make_buffer_rsrc(ws, 0, WS_ST, RSRC3);
  v4u mask;
  {
    const unsigned half = (n & 1) ? 0xffff0000u : 0x0000ffffu; const int r = (n & 7) >> 1;
    mask.x = r == 0 ? half : 0u; mask.y = r == 1 ? half : 0u; mask.z = r == 2 ? half : 0u; mask.w = r == 3 ? half : 0u;
  }
  const int pidx = kvh * 8 + s8;
  if (__builtin_expect(wave >= 8, 1)) {
    asm volatile("" ::: "memory");
    __builtin_amdgcn_s_setprio(3);
    const int hw = wave - 8, cg = s8 & 3, kh = s8 >> 2, sub = 2 * g + (n >> 3);
    const __amdgpu_buffer_rsrc_t rq = __builtin_amdgcn_make_buffer_rsrc((rb ? ptr64(ka[14], ka[15]) : ptr64(ka[12], ka[13])) + ((size_t)(kh * 288 + hw * 144) * NQ + kvh * 256 + cg * 64) * 2, 0, 143 * NQ * 2 + 128, RSRC3);
    const __amdgpu_buffer_rsrc_t rx = __builtin_amdgcn_make_buffer_rsrc((rb ? ptr64(ka[2], ka[3]) : ptr64(ka[0], ka[1])) + (kh * 288 + hw * 144) * 2, 0, 144 * 2, RSRC3);
    v4u wqv[18];
#pragma unroll
    for (int i = 0; i < 18; ++i) wqv[i] = __builtin_amdgcn_raw_buffer_load_b128(rq, (ABL & 4) ? OOB : sub * (NQ * 2) + (n & 7) * 16, i * 8 * NQ * 2, AUX_NT);
    const unsigned xa = __builtin_amdgcn_raw_buffer_load_b32(rx, n * 16 + g * 4, 0, 0), xb = __builtin_amdgcn_raw_buffer_load_b32(rx, n * 16 + g * 4 + 256, 0, 0);
    const unsigned ep = __builtin_amdgcn_raw_buffer_load_b32(rw, lane == 0 ? WS_EPOCH : OOB, 0, AUX_SC1);
    SB();
    f32x4 a0 = {0.f, 0.f, 0.f, 0.f}, a1 = {0.f, 0.f, 0.f, 0.f};
    const unsigned sel = n < 8 ? 0x01000100u : 0x03020302u;
#define QSTEP(acc, i, src, k) acc = mfma(diag(__builtin_amdgcn_perm(0u, (unsigned)__builtin_amdgcn_mov_dpp((int)(src), 0x150 + (k), 0xf, 0xf, true), sel), mask), wqv[i], acc)
    QSTEP(a0, 0, xa, 0); QSTEP(a1, 1, xa, 1); QSTEP(a0, 2, xa, 2); QSTEP(a1, 3, xa, 3); QSTEP(a0, 4, xa, 4); QSTEP(a1, 5, xa, 5);
    QSTEP(a0, 6, xa, 6); QSTEP(a1, 7, xa, 7); QSTEP(a0, 8, xa, 8); QSTEP(a1, 9, xa, 9); QSTEP(a0, 10, xa, 10); QSTEP(a1, 11, xa, 11);
    QSTEP(a0, 12, xa, 12); QSTEP(a1, 13, xa, 13); QSTEP(a0, 14, xa, 14); QSTEP(a1, 15, xa, 15); QSTEP(a0, 16, xb, 0); QSTEP(a1, 17, xb, 1);
    if ((g >> 1) == (n >> 3)) *(f32x4*)(lds + L_QP + (hw * 2 + (n >> 3)) * 64 + 8 * (n & 7) + 4 * (g & 1)) = a0 + a1;
    const unsigned tag = __builtin_amdgcn_readfirstlane(ep) + TAG_STEP;
    STAMP(1);
    lds_arrive(l_cnt + 0, lane);
    unsigned anc = 0;
    if (hw == 0) {
      lds_wait<1>(l_cnt + 0, 2u);
      const float q = (lds[L_QP + lane] + lds[L_QP + 64 + lane]) + (lds[L_QP + 128 + lane] + lds[L_QP + 192 + lane]);
      st_gran(ws, WS_QG + ((rb * 16 + kvh) * 512 + kh * 256 + cg * 64 + lane) * 8, tag, q);
      if (lane == 0) l_cnt[5] = tag;
      anc = lds_wait<1>(l_cnt + 6, 1u);
      if (lane == 0) __hip_atomic_store((gu64*)(ws + WS_AG + wg * 8), ((unsigned long long)tag << 32) | anc, RLX_AGENT);
    }
    STAMP(2);
    pause(QGA);
    if (hw) pause(QGB - QGA);
    {
      v2u x[8];
      const v2u ag = ldg(rw, (hw == 0 && lane < 8) ? WS_AG + (wg & ~7) * 8 + lane * 8 : OOB);
      sweep<8, 512, 8>(rw, WS_QG + ((hw * 16 + kvh) * 512 + lane) * 8, tag, x);
#pragma unroll
      for (int k = 0; k < 4; ++k) ((unsigned short*)(lds + L_Q))[hw * 256 + k * 64 + lane] = f2bf(__uint_as_float(x[k].x) + __uint_as_float(x[k + 4].x));
      if (hw == 0) {
        int d = ag.y == tag ? (int)(ag.x - anc) : 0;
        d = d > 1600 ? 1600 : d < -1600 ? -1600 : d;
        d += __builtin_amdgcn_mov_dpp(d, XOR1, 0xf, 0xf, true); d += __builtin_amdgcn_mov_dpp(d, XOR2, 0xf, 0xf, true); d += __builtin_amdgcn_mov_dpp(d, HMIR, 0xf, 0xf, true);
        d /= 8 * (31 - PRE);
        if (lane == 0) l_cnt[8] = (unsigned)d;
      }
      lds_arrive(hw ? l_cnt + 7 : l_cnt + 1, lane);
    }
    STAMP(3);
#pragma nounroll
    for (int br = 0; br < 2; ++br) {
      lds_wait<8>(l_cnt + 2 + br, 8u);
      const float* ml = lds + L_ML + br * 32 + hw * 8;
      const f32x4 m0 = *(const f32x4*)(ml), m1 = *(const f32x4*)(ml + 4), l0 = *(const f32x4*)(ml + 16), l1 = *(const f32x4*)(ml + 20);
      const float M = fmax_(fmax_(fmax_(m0.x, m0.y), fmax_(m0.z, m0.w)), fmax_(fmax_(m1.x, m1.y), fmax_(m1.z, m1.w)));
      float L = 0.f;
#pragma unroll
      for (int w = 0; w < 8; ++w) L = fmaf(__builtin_amdgcn_exp2f((w < 4 ? m0[w & 3] : m1[w & 3]) - M), w < 4 ? l0[w & 3] : l1[w & 3], L);
      const int mg = WS_MG + ((br * 16 + kvh) * 2 + hw) * 1024;
      if (lane < 8) st_gran(ws, mg + (ch & 15) * 64 + lane * 8, tag, (lane & 1) ? L : M);
      pause(MLD);
      v2u x[1];
      sweep<1, 0, 8>(rw, mg + n * 64 + (g & 1) * 8, tag, x);
      auto sw = __builtin_amdgcn_permlane16_swap(x[0].x, x[0].x, false, false);
      const float Ms = __uint_as_float(sw[0]), Ls = __uint_as_float(sw[1]);
      float Mx = fmax_(Ms, dpp<XOR1>(Ms)); Mx = fmax_(Mx, dpp<XOR2>(Mx)); Mx = fmax_(Mx, dpp<HMIR>(Mx)); Mx = fmax_(Mx, dpp<MIR>(Mx));
      float Lt = __builtin_amdgcn_exp2f(Ms - Mx) * Ls; Lt += dpp<XOR1>(Lt); Lt += dpp<XOR2>(Lt); Lt += dpp<HMIR>(Lt); Lt += dpp<MIR>(Lt);
      const float inv = 1.0f / Lt;
      float c[8];
#pragma unroll
      for (int w = 0; w < 8; ++w) c[w] = __builtin_amdgcn_exp2f((w < 4 ? m0[w & 3] : m1[w & 3]) - Mx) * inv;
      const unsigned oa = lds_addr(lds + L_O + br * 2048 + hw * 128 + 2 * lane);
      int dst = WS_PG + (br * 256 + ch) * PG_ROW + (hw * 128 + 2 * lane) * 8;
      asm volatile("" : "+v"(dst));
      float o0 = 0.f, o1 = 0.f;
      { const v2u v = lds_take2<8>(oa); o0 = c[0] * __uint_as_float(v.x); o1 = c[0] * __uint_as_float(v.y); }
#pragma unroll
      for (int w = 1; w < 8; ++w) { const v2u v = lds_take2<0>(oa + w * 1024); o0 = fmaf(c[w], __uint_as_float(v.x), o0); o1 = fmaf(c[w], __uint_as_float(v.y), o1); }
      st_gran2(rw, dst, tag, o0, o1);
      if (hw == 0 && lane == 0) __hip_atomic_store(l_cnt + 4, (unsigned)br + 1u, RLX_WG);
    }
    STAMP(8);
  } else {
    if (wave == 0) {
      unsigned long long ta;
      asm volatile("s_waitcnt vmcnt(0)\n\ts_memrealtime %0\n\ts_waitcnt lgkmcnt(0)" : "=s"(ta) : "v"(probe) : "memory");
      if (lane == 0) __hip_atomic_store(l_cnt + 6, ((unsigned)ta << 4) | 1u, RLX_WG);
    }
    St z;
    const int phs = PAT == 2 ? (wg >> 3) & 7 : wave;
    z.lds = lds; z.lane = lane; z.wave = wave; z.mask = mask;
    z.rs[0] = rk0;
    z.rs[1] = mkrs(ptr64(ka[8], ka[9]) + (size_t)ch * 262144, 262144);
    z.rs[2] = mkrs(ptr64(ka[6], ka[7]) + (size_t)ch * 262144, 262144);
    z.rs[3] = mkrs(ptr64(ka[10], ka[11]) + (size_t)ch * 262144, 262144);
    z.vk = (ABL & 8) ? lane * 16 : ((lane >> 2) & 3) * 256 + g * 64 + (lane & 3) * 16; z.vv = lane * 16; z.so = wave * WST;
    GAS unsigned short* outp = (GAS unsigned short*)ptr64(kb[4], kb[5]);
    z.tb = lds_wait<1>(l_cnt + 6, 1u) + (unsigned)(OFF16 + ((phs * DT16) >> 3));
    z.nx = z.tb;
    STAMP(1);
    Req<0, PRE>::go(z);
    STAMP(4);
    lds_wait<1>(l_cnt + 1, 1u);
    const unsigned tag = __builtin_amdgcn_readfirstlane(l_cnt[5]);
    z.adj = __builtin_amdgcn_readfirstlane(l_cnt[8]);
    STAMP(5);
    Con<0, PRE - DEP + 1, PRE - 1>::go(z);
    Run<PRE, 17>::go(z);
    STAMP(14);
    Run<17, 25>::go(z);
    STAMP(15);
    Run<25, 32>::go(z);
    STAMP(6);
    Con<32 - DEP + 1, 32, 31>::go(z);
    STAMP(7);
    const unsigned oba = lds_addr(lds + L_OB + g * 8);
    if (wave < 5) {
      const __amdgpu_buffer_rsrc_t ro = __builtin_amdgcn_make_buffer_rsrc((unsigned char*)wop + ((size_t)(kvh * 256 + s8 * 32) * DM) * 2, 0, 32 * DM * 2, RSRC3);
      const int wcol = wave * 128 + n * 8;
      const int vwo = (!NOWO && wcol < DM) ? g * (DM * 2) + wcol * 2 : OOB;
      v4u Wo[8];
      z.tb += (unsigned)((wave < NW1 ? wave * WOS16 : WG16 + (wave - NW1) * WOSB16) - ((phs * DT16) >> 3));
      gate_next(z.tb);
#pragma unroll
      for (int i = 0; i < 8; ++i) Wo[i] = __builtin_amdgcn_raw_buffer_load_b128(ro, vwo, i * 4 * DM * 2, AUX_NT);
      int dst = wcol < DM ? WS_YG + (rb * 128 + pidx) * YG_ROW + (wcol + 4 * (g & 1) + 2 * (g >> 1)) * 8 : OOB;
      asm volatile("" : "+v"(dst), "+v"(Wo[0]), "+v"(Wo[1]), "+v"(Wo[2]), "+v"(Wo[3]), "+v"(Wo[4]), "+v"(Wo[5]), "+v"(Wo[6]), "+v"(Wo[7]));
      STAMP(2);
      v4u oa, ob;
#pragma nounroll
      for (unsigned spins = 0; spins < LSPIN_CAP; ++spins) {
        asm volatile("ds_read_b128 %0, %2\n\tds_read_b128 %1, %2 offset:16\n\ts_waitcnt lgkmcnt(0)" : "=&v"(oa), "=&v"(ob) : "v"(oba) : "memory");
        if (__all(oa.x != EMPTY_B && oa.y != EMPTY_B && oa.z != EMPTY_B && oa.w != EMPTY_B && ob.x != EMPTY_B && ob.y != EMPTY_B && ob.z != EMPTY_B && ob.w != EMPTY_B)) break;
      }
      STAMP(10);
      f32x4 Y0 = {0.f, 0.f, 0.f, 0.f}, Y1 = {0.f, 0.f, 0.f, 0.f};
#pragma unroll
      for (int i = 0; i < 4; ++i) { Y0 = mfma(diag(oa[i], mask), Wo[i], Y0); Y1 = mfma(diag(ob[i], mask), Wo[4 + i], Y1); }
      const f32x4 Y = Y0 + Y1;
      st_gran2(rw, dst, tag, g < 2 ? Y.x : Y.z, g < 2 ? Y.y : Y.w);
      STAMP(11);
    } else if (wave == 5) {
      const int po = WS_PG + (rb * 256 + kvh * 16 + (lane >> 5)) * PG_ROW + (32 * s8 + (lane & 31)) * 8;
      v2u x[8];
      lds_wait<0>(l_cnt + 4, (unsigned)rb + 1u);
      if (rb) pause(H1D);
      sweep<8, 2 * PG_ROW, 1>(rw, po, tag, x);
      STAMP(9);
      const float o = x32_sum(((__uint_as_float(x[0].x) + __uint_as_float(x[1].x)) + (__uint_as_float(x[2].x) + __uint_as_float(x[3].x)))
                            + ((__uint_as_float(x[4].x) + __uint_as_float(x[5].x)) + (__uint_as_float(x[6].x) + __uint_as_float(x[7].x))));
      if (lane < 32) ((unsigned*)(lds + L_OB))[(lane & 3) * 8 + (lane >> 2)] = dup_bf16(o);
    } else if (wave == 6 || pidx < 16) {
      const int pos = wave == 6 ? pidx * 4 : 512 + pidx * 4;
      const int so = WS_YG + (rb * 128 + (lane >> 2)) * YG_ROW + (pos + (lane & 3)) * 8;
      const int col = pos + (lane & 3);
      GAS unsigned short* op = outp + rb * DM + col;
      asm volatile("" : "+v"(op));
      v2u x[8];
      z.tb += (unsigned)(WG16 + (4 - NW1) * WOSB16 + H2G16 - ((phs * DT16) >> 3));
#pragma nounroll
      for (unsigned spins = 0; spins < LSPIN_CAP; ++spins) {
        if (__builtin_amdgcn_readfirstlane(__hip_atomic_load((unsigned*)(lds + L_OB), RLX_WG)) != EMPTY_B) break;
        __builtin_amdgcn_s_sleep(1);
      }
      pause(H2D);
      gate_next(z.tb);
      sweep<8, 16 * YG_ROW, 1>(rw, so, tag, x);
      STAMP(12);
      float y = ((__uint_as_float(x[0].x) + __uint_as_float(x[1].x)) + (__uint_as_float(x[2].x) + __uint_as_float(x[3].x)))
              + ((__uint_as_float(x[4].x) + __uint_as_float(x[5].x)) + (__uint_as_float(x[6].x) + __uint_as_float(x[7].x)));
      y += dpp<ROR4>(y); y += dpp<ROR8>(y);
      y = xg_sum(y);
      if (lane < 4) *op = f2bf(y);
      if (rb == 0 && pos == 0 && lane == 0) __hip_atomic_store((gu32*)(ws + WS_EPOCH), tag, RLX_AGENT);
      STAMP(13);
    }
  }
  STAMP_OUT();
}

extern "C" void kernel_launch(void* const* d_in, const int* in_sizes, int n_in, void* d_out, int out_size, void* d_ws, size_t ws_size, hipStream_t stream) {
  Args a;
  a.x[0] = d_in[0]; a.k[0] = d_in[1]; a.v[0] = d_in[2]; a.wq[0] = d_in[3]; a.wo[0] = d_in[4];
  a.x[1] = d_in[5]; a.k[1] = d_in[6]; a.v[1] = d_in[7]; a.wq[1] = d_in[8]; a.wo[1] = d_in[9];
  a.out = d_out; a.ws = d_ws;
  gqa2_fused<<<NWG, NTHR, 0, stream>>>(a);
}
```

```cpp
#define EVT 2
#include <hip/hip_runtime.h>
#include <cstdint>

typedef unsigned v4u __attribute__((ext_vector_type(4)));
typedef unsigned v2u __attribute__((ext_vector_type(2)));
typedef float f32x4 __attribute__((ext_vector_type(4)));
typedef __bf16 bf16x8 __attribute__((ext_vector_type(8)));
typedef __bf16 bf16x2 __attribute__((ext_vector_type(2)));
typedef float f32x2 __attribute__((ext_vector_type(2)));
#define GAS __attribute__((address_space(1)))
#define LAS __attribute__((address_space(3)))
typedef GAS unsigned gu32;
typedef GAS unsigned long long gu64;
#define RLX_AGENT __ATOMIC_RELAXED, __HIP_MEMORY_SCOPE_AGENT
#define RLX_WG __ATOMIC_RELAXED, __HIP_MEMORY_SCOPE_WORKGROUP

#define STAMPS 0
#define DT16 1808
#define ANC -2
#define OFF16 600
#define PRE 8
#define DEP 3
#define LIM 2
#define WO16 0
#define WOS16 600
#define QGD 75
#define PTH16 400
#define SLK16 100000
#define H1D 40
#define H2D 70
#define PREW 0
#define HBAR 1
#define PAT 1
#define JST 8192
#define WST 1024
#define SOFT16 0
#define NSOFT 6
#define H2G16 2000
#define WOA16 0
#define WOSA16 600
#define OUTSC 0
#define SLOWSTART 0
#define P016 2400
#define NLIM 32
#define LIMB 8
#define DF16 0
#define JIT16 160
#define ABL 0
#define QGA 280
#define QGB 600
#define MLD 350
#define PIPE 0
#define GAP1 8
#define GAP2 8
#define ALIGN 1
#define NW1 3
#define WG16 1800
#define WOSB16 600
#define NOWO 0

constexpr int DM = 576, NQ = 4096;
constexpr int NWG = 256, NTHR = 640;
constexpr int AUX_NT = 2, AUX_SC1 = 16;
constexpr int RSRC3 = 0x00020000;
constexpr int OOB = 0x7fff0000;
constexpr int WS_EPOCH = 0;
constexpr int WS_AG = 1024;
constexpr int WS_QG = 4096;
constexpr int WS_MG = WS_QG + 32 * 4096;
constexpr int WS_PG = WS_MG + 64 * 1024;
constexpr int PG_ROW = 256 * 8;
constexpr int WS_YG = WS_PG + 512 * PG_ROW;
constexpr int YG_ROW = 576 * 8;
constexpr int WS_ST = WS_YG + 256 * YG_ROW;
constexpr unsigned TAG_STEP = 0x9E3779B9u;
constexpr unsigned SPIN_CAP = 1u << 17, LSPIN_CAP = 1u << 22;
constexpr float C2 = 0.088388347648318440f * 1.4426950408889634f;
constexpr unsigned EMPTY_O = 0xffffffffu, EMPTY_B = 0x0000ffffu;

struct Args {
  const void* x[2]; const void* k[2]; const void* v[2]; const void* wq[2]; const void* wo[2];
  void* out; void* ws;
};

#define SB() __builtin_amdgcn_sched_barrier(0)
__device__ __forceinline__ f32x4 mfma(v4u a, v4u b, f32x4 c) {
  return __builtin_amdgcn_mfma_f32_16x16x32_bf16(__builtin_bit_cast(bf16x8, a), __builtin_bit_cast(bf16x8, b), c, 0, 0, 0);
}
__device__ __forceinline__ unsigned dup_bf16(float f) {
  f32x2 v = {f, f};
  return __builtin_bit_cast(unsigned, __builtin_convertvector(v, bf16x2));
}
__device__ __forceinline__ unsigned short f2bf(float f) { return __builtin_bit_cast(unsigned short, (__bf16)f); }
__device__ __forceinline__ v4u diag(unsigned pp, v4u mask) { v4u r = {pp & mask.x, pp & mask.y, pp & mask.z, pp & mask.w}; return r; }
template <int CTRL> __device__ __forceinline__ float dpp(float x) {
  return __builtin_bit_cast(float, __builtin_amdgcn_mov_dpp(__builtin_bit_cast(int, x), CTRL, 0xf, 0xf, true));
}
constexpr int XOR1 = 0xB1, XOR2 = 0x4E, HMIR = 0x141, MIR = 0x140, ROR8 = 0x128, ROR4 = 0x124;
__device__ __forceinline__ float fmax_(float a, float b) { return __builtin_fmaxf(a, b); }
__device__ __forceinline__ float xg_max(float x) {
  auto s = __builtin_amdgcn_permlane16_swap(__float_as_uint(x), __float_as_uint(x), false, false);
  x = fmax_(__uint_as_float(s[0]), __uint_as_float(s[1]));
  auto t = __builtin_amdgcn_permlane32_swap(__float_as_uint(x), __float_as_uint(x), false, false);
  return fmax_(__uint_as_float(t[0]), __uint_as_float(t[1]));
}
__device__ __forceinline__ float x32_sum(float x) {
  auto t = __builtin_amdgcn_permlane32_swap(__float_as_uint(x), __float_as_uint(x), false, false);
  return __uint_as_float(t[0]) + __uint_as_float(t[1]);
}
__device__ __forceinline__ float xg_sum(float x) {
  auto s = __builtin_amdgcn_permlane16_swap(__float_as_uint(x), __float_as_uint(x), false, false);
  return x32_sum(__uint_as_float(s[0]) + __uint_as_float(s[1]));
}
__device__ __forceinline__ void st_gran(unsigned char* ws, int off, unsigned tag, float v) {
  __hip_atomic_store((gu64*)(ws + off), ((unsigned long long)tag << 32) | __float_as_uint(v), RLX_AGENT);
}
__device__ __forceinline__ void st_gran2(__amdgpu_buffer_rsrc_t rs, int off, unsigned tag, float a, float b) {
  __builtin_amdgcn_raw_buffer_store_b128(v4u{__float_as_uint(a), tag, __float_as_uint(b), tag}, rs, off, 0, AUX_SC1);
}
__device__ __forceinline__ v2u ldg(__amdgpu_buffer_rsrc_t rs, int off) { return __builtin_amdgcn_raw_buffer_load_b64(rs, off, 0, AUX_SC1); }
template <int N, int STEP, int SLEEP> __device__ __forceinline__ void sweep(__amdgpu_buffer_rsrc_t rs, int off, unsigned tag, v2u (&x)[N]) {
#pragma unroll
  for (int k = 0; k < N; ++k) x[k] = ldg(rs, off + k * STEP);
  SB();
  bool ok = true;
#pragma unroll
  for (int k = 0; k < N; ++k) ok &= x[k].y == tag;
  if (__builtin_expect(__all(ok), 1)) return;
#pragma nounroll
  for (unsigned spins = 0; spins < SPIN_CAP; ++spins) {
    __builtin_amdgcn_s_sleep(SLEEP);
    v2u y[N];
#pragma unroll
    for (int k = 0; k < N; ++k) y[k] = ldg(rs, x[k].y == tag ? OOB : off + k * STEP);
    SB();
    ok = true;
#pragma unroll
    for (int k = 0; k < N; ++k) { x[k] = x[k].y == tag ? x[k] : y[k]; ok &= x[k].y == tag; }
    if (__all(ok)) break;
  }
}
template <int STEP> __device__ __forceinline__ void pass4(v4u (&y)[4], const v4u (&x)[4], int off, v4u rs, unsigned tag) {
  int o[4];
#pragma unroll
  for (int k = 0; k < 4; ++k) o[k] = (x[k].y == tag && x[k].w == tag) ? OOB : off + k * STEP;
  asm volatile("buffer_load_dwordx4 %0, %4, %8, 0 offen sc1\n\t"
               "buffer_load_dwordx4 %1, %5, %8, 0 offen sc1\n\t"
               "buffer_load_dwordx4 %2, %6, %8, 0 offen sc1\n\t"
               "buffer_load_dwordx4 %3, %7, %8, 0 offen sc1"
               : "=&v"(y[0]), "=&v"(y[1]), "=&v"(y[2]), "=&v"(y[3]) : "v"(o[0]), "v"(o[1]), "v"(o[2]), "v"(o[3]), "s"(rs) : "memory");
}
__device__ __forceinline__ bool take4(v4u (&x)[4], v4u (&y)[4], unsigned tag) {
  asm volatile("s_waitcnt vmcnt(4)" : "+v"(y[0]), "+v"(y[1]), "+v"(y[2]), "+v"(y[3]));
  bool ok = true;
#pragma unroll
  for (int k = 0; k < 4; ++k) {
    const bool have = x[k].y == tag && x[k].w == tag;
    x[k] = have ? x[k] : y[k];
    ok &= x[k].y == tag && x[k].w == tag;
  }
  return __all(ok);
}
__device__ __forceinline__ void pause(int ticks);
template <int STEP, int GAP> __device__ __forceinline__ void sweep2(v4u rs, int off, unsigned tag, v4u (&x)[4], v4u (&ya)[4], v4u (&yb)[4]) {
#pragma unroll
  for (int k = 0; k < 4; ++k) x[k] = v4u{0u, ~tag, 0u, ~tag};
  pass4<STEP>(ya, x, off, rs, tag);
  __builtin_amdgcn_s_sleep(GAP);
#pragma nounroll
  for (unsigned spins = 0; spins < SPIN_CAP; ++spins) {
    pass4<STEP>(yb, x, off, rs, tag);
    if (take4(x, ya, tag)) break;
    pass4<STEP>(ya, x, off, rs, tag);
    if (take4(x, yb, tag)) break;
  }
}
#define KEEP8(A, B) asm volatile("s_waitcnt vmcnt(0)" :: "v"(A[0]), "v"(A[1]), "v"(A[2]), "v"(A[3]), "v"(B[0]), "v"(B[1]), "v"(B[2]), "v"(B[3]) : "memory")
__device__ __forceinline__ v4u mkrs(const void* p, unsigned bytes) {
  const unsigned long long u = (unsigned long long)p;
  v4u r = {(unsigned)u, (unsigned)(u >> 32) & 0xffffu, bytes, (unsigned)RSRC3};
  return r;
}
__device__ __forceinline__ void tile_load(v4u (&d)[4], int voff, v4u rs, int soff) {
  asm volatile("buffer_load_dwordx4 %0, %4, %5, %6 offen nt\n\t"
               "buffer_load_dwordx4 %1, %4, %5, %7 offen nt\n\t"
               "buffer_load_dwordx4 %2, %4, %5, %8 offen nt\n\t"
               "buffer_load_dwordx4 %3, %4, %5, %9 offen nt"
               : "=&v"(d[0]), "=&v"(d[1]), "=&v"(d[2]), "=&v"(d[3]) : "v"(voff), "s"(rs), "s"(soff), "s"(soff + JST), "s"(soff + 2 * JST), "s"(soff + 3 * JST));
}
template <int N> __device__ __forceinline__ void tile_wait(v4u (&d)[4]) {
  asm volatile("s_waitcnt vmcnt(%4)" : "+v"(d[0]), "+v"(d[1]), "+v"(d[2]), "+v"(d[3]) : "i"(N));
}
template <int N> __device__ __forceinline__ void piece_wait(v4u& d) { asm volatile("s_waitcnt vmcnt(%1)" : "+v"(d) : "i"(N)); }
__device__ __forceinline__ void pause(int ticks) {
  unsigned due;
  asm volatile("s_memrealtime vcc\n\t"
               "s_waitcnt lgkmcnt(0)\n\t"
               "s_add_i32 %0, vcc_lo, %1\n"
               ".Lg0_%=:\n\t"
               "s_sleep 1\n\t"
               "s_memrealtime vcc\n\t"
               "s_waitcnt lgkmcnt(0)\n\t"
               "s_sub_i32 vcc_lo, vcc_lo, %0\n\t"
               "s_cmp_lt_i32 vcc_lo, 0\n\t"
               "s_cbranch_scc1 .Lg0_%="
               : "=&s"(due) : "i"(ticks) : "scc", "vcc", "memory");
}
__device__ __forceinline__ void gate_next(unsigned& due) {
  asm volatile("s_branch .Lg1_%=\n"
               ".Lg0_%=:\n\t"
               "s_sleep 1\n"
               ".Lg1_%=:\n\t"
               "s_memrealtime vcc\n\t"
               "s_waitcnt lgkmcnt(0)\n\t"
               "s_lshl_b32 vcc_lo, vcc_lo, 4\n\t"
               "s_sub_i32 vcc_lo, vcc_lo, %0\n\t"
               "s_cmp_lt_i32 vcc_lo, 0\n\t"
               "s_cbranch_scc1 .Lg0_%=\n\t"
               "s_setprio 0\n\t"
               "s_cmp_lt_i32 vcc_lo, %1\n\t"
               "s_cbranch_scc1 .Lg2_%=\n\t"
               "s_setprio 1\n"
               ".Lg2_%=:\n\t"
               "s_sub_i32 vcc_lo, vcc_lo, %2\n\t"
               "s_max_i32 vcc_lo, vcc_lo, 0\n\t"
               "s_add_i32 %0, %0, vcc_lo\n\t"
               "s_add_i32 %0, %0, %3"
               : "+s"(due) : "i"(PTH16), "i"(SLK16), "i"(DT16) : "scc", "vcc", "memory");
}
__device__ __forceinline__ void gate2(unsigned& line, unsigned& nxt) {
  asm volatile("s_sub_i32 vcc_lo, %1, %0\n\t"
               "s_max_i32 vcc_lo, vcc_lo, 0\n\t"
               "s_add_i32 vcc_hi, %0, vcc_lo\n\t"
               "s_mov_b32 %1, vcc_hi\n\t"
               "s_branch .Lg1_%=\n"
               ".Lg0_%=:\n\t"
               "s_sleep 1\n"
               ".Lg1_%=:\n\t"
               "s_memrealtime vcc\n\t"
               "s_waitcnt lgkmcnt(0)\n\t"
               "s_lshl_b32 vcc_lo, vcc_lo, 4\n\t"
               "s_sub_i32 vcc_hi, vcc_lo, %1\n\t"
               "s_cmp_lt_i32 vcc_hi, 0\n\t"
               "s_cbranch_scc1 .Lg0_%=\n\t"
               "s_sub_i32 vcc_hi, vcc_hi, %4\n\t"
               "s_max_i32 vcc_hi, vcc_hi, 0\n\t"
               "s_add_i32 %1, %1, vcc_hi\n\t"
               "s_add_i32 %1, %1, %3\n\t"
               "s_add_i32 %0, %0, %2"
               : "+s"(line), "+s"(nxt) : "i"(DT16), "i"(DF16), "i"(JIT16) : "scc", "vcc", "memory");
}
__device__ __forceinline__ void lds_barrier() {
  asm volatile("s_waitcnt lgkmcnt(0)" ::: "memory");
  __builtin_amdgcn_s_barrier();
  asm volatile("" ::: "memory");
}
__device__ __forceinline__ void lds_arrive(unsigned* c, int lane) {
  asm volatile("s_waitcnt lgkmcnt(0)" ::: "memory");
  if (lane == 0) __hip_atomic_fetch_add(c, 1u, RLX_WG);
}
template <int SLEEP> __device__ __forceinline__ unsigned lds_wait(unsigned* c, unsigned want) {
  unsigned v = 0;
#pragma nounroll
  for (unsigned spins = 0; spins < LSPIN_CAP; ++spins) {
    v = __builtin_amdgcn_readfirstlane(__hip_atomic_load(c, RLX_WG));
    if (v >= want) break;
    __builtin_amdgcn_s_sleep(SLEEP);
  }
  asm volatile("" ::: "memory");
  return v;
}
__device__ __forceinline__ unsigned lds_addr(const void* p) { return (unsigned)(size_t)(const LAS void*)p; }
template <int SLEEP> __device__ __forceinline__ v2u lds_take2(unsigned addr) {
  v2u v;
#pragma nounroll
  for (unsigned spins = 0; spins < LSPIN_CAP; ++spins) {
    asm volatile("ds_read_b64 %0, %1\n\ts_waitcnt lgkmcnt(0)" : "=&v"(v) : "v"(addr) : "memory");
    if (__all(v.x != EMPTY_O && v.y != EMPTY_O)) break;
    if (SLEEP) __builtin_amdgcn_s_sleep(SLEEP);
  }
  return v;
}
#define STAMP(i) do {} while (0)
#define STAMP_OUT() do {} while (0)

constexpr int L_O = 0;
constexpr int L_ML = L_O + 4096;
constexpr int L_QP = L_ML + 64;
constexpr int L_Q = L_QP + 256;
constexpr int L_OB = L_Q + 256;
constexpr int L_CNT = L_OB + 32;
constexpr int L_END = L_CNT + 16;

struct St {
  v4u T[32][4];
  float Sc[8][4];
  unsigned P[8][4];
  v4u Q, mask;
  f32x4 O;
  v4u rs[4];
  int vk, vv, so;
  unsigned tb, nx, adj;
  float* lds;
  int lane, wave;
};

template <int I> __device__ __forceinline__ void request(St& z) {
  constexpr int lim = I < NLIM ? LIM : LIMB;
  if constexpr (4 * I > lim) asm volatile("s_waitcnt vmcnt(%0)" :: "i"(lim) : "memory");
  gate_next(z.tb);
  if constexpr (I >= PRE && I < 31) z.tb += z.adj;
  tile_load(z.T[I], (I & 8) ? z.vv : z.vk, z.rs[I >> 3], z.so + (I & 7) * 32768);
  SB();
}
template <int I, int W> __device__ __forceinline__ void consume(St& z) {
  const int g = z.lane >> 4, n = z.lane & 15;
  constexpr int br = I >> 4;
  if constexpr ((I & 15) == 0) {
    if constexpr (br) lds_wait<1>((unsigned*)(z.lds + L_CNT) + 7, 1u);
    z.Q = *(const v4u*)((const unsigned short*)(z.lds + L_Q) + br * 256 + (n >> 3) * 128 + 32 * g + 8 * (n & 3));
  }
  if constexpr ((I & 8) == 0) {
    tile_wait<W>(z.T[I]);
#pragma unroll
    for (int j = 0; j < 4; ++j) {
      const f32x4 d = mfma(z.T[I][j], z.Q, f32x4{0.f, 0.f, 0.f, 0.f});
      z.Sc[I & 7][j] = (dpp<0x00>(d.x) + dpp<0x55>(d.y)) + (dpp<0xAA>(d.z) + dpp<0xFF>(d.w));
    }
    if constexpr ((I & 7) == 7) {
      float m = z.Sc[0][0];
#pragma unroll
      for (int t = 0; t < 8; ++t)
#pragma unroll
        for (int j = 0; j < 4; ++j) m = fmax_(m, z.Sc[t][j]);
      const float m2 = xg_max(m) * C2;
      float l = 0.f;
#pragma unroll
      for (int t = 0; t < 8; ++t) {
        float p[4];
#pragma unroll
        for (int j = 0; j < 4; ++j) { p[j] = __builtin_amdgcn_exp2f(fmaf(z.Sc[t][j], C2, -m2)); z.P[t][j] = dup_bf16(p[j]); }
        l += (p[0] + p[1]) + (p[2] + p[3]);
        asm volatile("" : "+v"(z.P[t][0]), "+v"(z.P[t][1]), "+v"(z.P[t][2]), "+v"(z.P[t][3]));
      }
      l = xg_sum(l);
      if (g == 0 && (n & 7) == 0) { z.lds[L_ML + br * 32 + (n >> 3) * 8 + z.wave] = m2; z.lds[L_ML + br * 32 + 16 + (n >> 3) * 8 + z.wave] = l; }
      lds_arrive((unsigned*)(z.lds + L_CNT) + 2 + br, z.lane);
      z.O = f32x4{0.f, 0.f, 0.f, 0.f};
    } else {
      asm volatile("" : "+v"(z.Sc[I & 7][0]), "+v"(z.Sc[I & 7][1]), "+v"(z.Sc[I & 7][2]), "+v"(z.Sc[I & 7][3]));
    }
  } else if constexpr (W == 0) {
    v4u A[4];
#pragma unroll
    for (int j = 0; j < 4; ++j) A[j] = diag(z.P[I & 7][j], z.mask);
    float* dst = z.lds + L_O + br * 2048 + z.wave * 256 + (g >> 1) * 128 + 8 * n + 4 * (g & 1);
    unsigned da = lds_addr(dst);
    asm volatile("" : "+v"(A[0]), "+v"(A[1]), "+v"(A[2]), "+v"(A[3]), "+v"(da));
    piece_wait<3>(z.T[I][0]); z.O = mfma(A[0], z.T[I][0], z.O); SB();
    piece_wait<2>(z.T[I][1]); z.O = mfma(A[1], z.T[I][1], z.O); SB();
    piece_wait<1>(z.T[I][2]); z.O = mfma(A[2], z.T[I][2], z.O); SB();
    piece_wait<0>(z.T[I][3]); z.O = mfma(A[3], z.T[I][3], z.O);
    *(LAS f32x4*)(size_t)da = z.O;
  } else {
    tile_wait<W>(z.T[I]);
#pragma unroll
    for (int j = 0; j < 4; ++j) z.O = mfma(diag(z.P[I & 7][j], z.mask), z.T[I][j], z.O);
    asm volatile("" : "+v"(z.O));
    if constexpr ((I & 7) == 7) *(f32x4*)(z.lds + L_O + br * 2048 + z.wave * 256 + (g >> 1) * 128 + 8 * n + 4 * (g & 1)) = z.O;
  }
  SB();
}
template <int I, int N> struct Run {
  static __device__ __forceinline__ void go(St& z) { request<I>(z); consume<I - DEP + 1, 4 * (DEP - 1)>(z); Run<I + 1, N>::go(z); }
};
template <int N> struct Run<N, N> { static __device__ __forceinline__ void go(St&) {} };
template <int I, int N> struct Req { static __device__ __forceinline__ void go(St& z) { request<I>(z); Req<I + 1, N>::go(z); } };
template <int N> struct Req<N, N> { static __device__ __forceinline__ void go(St&) {} };
template <int I, int N, int LAST> struct Con {
  static __device__ __forceinline__ void go(St& z) { consume<I, 4 * (LAST - I)>(z); Con<I + 1, N, LAST>::go(z); }
};
template <int N, int LAST> struct Con<N, N, LAST> { static __device__ __forceinline__ void go(St&) {} };

typedef unsigned v16u __attribute__((ext_vector_type(16)));
typedef unsigned v8u __attribute__((ext_vector_type(8)));
__device__ __forceinline__ unsigned char* ptr64(unsigned lo, unsigned hi) { return (unsigned char*)(((unsigned long long)hi << 32) | lo); }

__global__ void __launch_bounds__(NTHR, 3) gqa2_fused(Args) {
  __shared__ __attribute__((aligned(16))) float lds[L_END];
  unsigned* const l_cnt = (unsigned*)(lds + L_CNT);
  v16u ka; v8u kb;
  unsigned long long te;
#define N8 "s_nop 0\n\ts_nop 0\n\ts_nop 0\n\ts_nop 0\n\ts_nop 0\n\ts_nop 0\n\ts_nop 0\n\ts_nop 0\n\t"
#define N64 N8 N8 N8 N8 N8 N8 N8 N8
  asm volatile(N64 N64 "s_memrealtime %2\n\ts_load_dwordx16 %0, %3, 0x0\n\ts_load_dwordx8 %1, %3, 0x40" : "=&s"(ka), "=&s"(kb), "=&s"(te) : "s"(__builtin_amdgcn_kernarg_segment_ptr()));
  const int tid = threadIdx.x, lane = tid & 63, wave = __builtin_amdgcn_readfirstlane(tid >> 6);
  const int g = lane >> 4, n = lane & 15;
  const int wg = blockIdx.x;
  const int ch = (wg & 7) << 5 | wg >> 3;
  const int kvh = ch >> 4, rb = (ch >> 3) & 1, s8 = ch & 7;
  {
    const v4u e = {EMPTY_O, EMPTY_O, EMPTY_O, EMPTY_O};
    const int i = tid < 512 ? tid : 0;
    *(v4u*)(lds + L_O + i * 4) = e; *(v4u*)(lds + L_O + 2048 + i * 4) = e;
    const int j = tid < 48 ? tid : 0;
    ((unsigned*)(lds + L_OB))[j] = j < 32 ? EMPTY_B : 0u;
  }
  asm volatile("s_waitcnt lgkmcnt(0)" : "+s"(ka), "+s"(kb), "+s"(te) :: "memory");
  const v4u rk0 = mkrs(ptr64(ka[4], ka[5]) + (size_t)ch * 262144, 262144);
  unsigned probe;
  asm volatile("s_cmp_lg_u32 %2, 0\n\ts_cbranch_scc1 .Lp%=\n\ts_mov_b64 exec, 1\n\tbuffer_load_dword %0, off, %1, 0 nt\n\ts_mov_b64 exec, -1\n.Lp%=:"
               : "=v"(probe) : "s"(rk0), "s"(wave) : "scc");
  __builtin_amdgcn_s_barrier();
  asm volatile("" ::: "memory");
  unsigned char* const ws = ptr64(kb[6], kb[7]);
  const unsigned char* const wop = rb ? ptr64(kb[2], kb[3]) : ptr64(kb[0], kb[1]);
  const __amdgpu_buffer_rsrc_t rw = __builtin_amdgcn_make_buffer_rsrc(ws, 0, WS_ST, RSRC3);
  v4u mask;
  {
    const unsigned half = (n & 1) ? 0xffff0000u : 0x0000ffffu; const int r = (n & 7) >> 1;
    mask.x = r == 0 ? half : 0u; mask.y = r == 1 ? half : 0u; mask.z = r == 2 ? half : 0u; mask.w = r == 3 ? half : 0u;
  }
  const int pidx = kvh * 8 + s8;
  if (__builtin_expect(wave >= 8, 1)) {
    asm volatile("" ::: "memory");
    __builtin_amdgcn_s_setprio(3);
    const int hw = wave - 8, cg = s8 & 3, kh = s8 >> 2, sub = 2 * g + (n >> 3);
    const __amdgpu_buffer_rsrc_t rq = __builtin_amdgcn_make_buffer_rsrc((rb ? ptr64(ka[14], ka[15]) : ptr64(ka[12], ka[13])) + ((size_t)(kh * 288 + hw * 144) * NQ + kvh * 256 + cg * 64) * 2, 0, 143 * NQ * 2 + 128, RSRC3);
    const __amdgpu_buffer_rsrc_t rx = __builtin_amdgcn_make_buffer_rsrc((rb ? ptr64(ka[2], ka[3]) : ptr64(ka[0], ka[1])) + (kh * 288 + hw * 144) * 2, 0, 144 * 2, RSRC3);
    v4u wqv[18];
#pragma unroll
    for (int i = 0; i < 18; ++i) wqv[i] = __builtin_amdgcn_raw_buffer_load_b128(rq, (ABL & 4) ? OOB : sub * (NQ * 2) + (n & 7) * 16, i * 8 * NQ * 2, AUX_NT);
    const unsigned xa = __builtin_amdgcn_raw_buffer_load_b32(rx, n * 16 + g * 4, 0, 0), xb = __builtin_amdgcn_raw_buffer_load_b32(rx, n * 16 + g * 4 + 256, 0, 0);
    const unsigned ep = __builtin_amdgcn_raw_buffer_load_b32(rw, lane == 0 ? WS_EPOCH : OOB, 0, AUX_SC1);
    SB();
    f32x4 a0 = {0.f, 0.f, 0.f, 0.f}, a1 = {0.f, 0.f, 0.f, 0.f};
    const unsigned sel = n < 8 ? 0x01000100u : 0x03020302u;
#define QSTEP(acc, i, src, k) acc = mfma(diag(__builtin_amdgcn_perm(0u, (unsigned)__builtin_amdgcn_mov_dpp((int)(src), 0x150 + (k), 0xf, 0xf, true), sel), mask), wqv[i], acc)
    QSTEP(a0, 0, xa, 0); QSTEP(a1, 1, xa, 1); QSTEP(a0, 2, xa, 2); QSTEP(a1, 3, xa, 3); QSTEP(a0, 4, xa, 4); QSTEP(a1, 5, xa, 5);
    QSTEP(a0, 6, xa, 6); QSTEP(a1, 7, xa, 7); QSTEP(a0, 8, xa, 8); QSTEP(a1, 9, xa, 9); QSTEP(a0, 10, xa, 10); QSTEP(a1, 11, xa, 11);
    QSTEP(a0, 12, xa, 12); QSTEP(a1, 13, xa, 13); QSTEP(a0, 14, xa, 14); QSTEP(a1, 15, xa, 15); QSTEP(a0, 16, xb, 0); QSTEP(a1, 17, xb, 1);
    if ((g >> 1) == (n >> 3)) *(f32x4*)(lds + L_QP + (hw * 2 + (n >> 3)) * 64 + 8 * (n & 7) + 4 * (g & 1)) = a0 + a1;
    const unsigned tag = __builtin_amdgcn_readfirstlane(ep) + TAG_STEP;
    STAMP(1);
    lds_arrive(l_cnt + 0, lane);
    unsigned anc = 0;
    if (hw == 0) {
      lds_wait<1>(l_cnt + 0, 2u);
      const float q = (lds[L_QP + lane] + lds[L_QP + 64 + lane]) + (lds[L_QP + 128 + lane] + lds[L_QP + 192 + lane]);
      st_gran(ws, WS_QG + ((rb * 16 + kvh) * 512 + kh * 256 + cg * 64 + lane) * 8, tag, q);
      if (lane == 0) l_cnt[5] = tag;
      anc = lds_wait<1>(l_cnt + 6, 1u);
      if (lane == 0) __hip_atomic_store((gu64*)(ws + WS_AG + wg * 8), ((unsigned long long)tag << 32) | anc, RLX_AGENT);
    }
    STAMP(2);
    pause(QGA);
    if (hw) pause(QGB - QGA);
    {
      v2u x[8];
      const v2u ag = ldg(rw, (hw == 0 && lane < 8) ? WS_AG + (wg & ~7) * 8 + lane * 8 : OOB);
      sweep<8, 512, 8>(rw, WS_QG + ((hw * 16 + kvh) * 512 + lane) * 8, tag, x);
#pragma unroll
      for (int k = 0; k < 4; ++k) ((unsigned short*)(lds + L_Q))[hw * 256 + k * 64 + lane] = f2bf(__uint_as_float(x[k].x) + __uint_as_float(x[k + 4].x));
      if (hw == 0) {
        int d = ag.y == tag ? (int)(ag.x - anc) : 0;
        d = d > 1600 ? 1600 : d < -1600 ? -1600 : d;
        d += __builtin_amdgcn_mov_dpp(d, XOR1, 0xf, 0xf, true); d += __builtin_amdgcn_mov_dpp(d, XOR2, 0xf, 0xf, true); d += __builtin_amdgcn_mov_dpp(d, HMIR, 0xf, 0xf, true);
        d /= 8 * (31 - PRE);
        if (lane == 0) l_cnt[8] = (unsigned)d;
      }
      lds_arrive(hw ? l_cnt + 7 : l_cnt + 1, lane);
    }
    STAMP(3);
#pragma nounroll
    for (int br = 0; br < 2; ++br) {
      lds_wait<8>(l_cnt + 2 + br, 8u);
      const float* ml = lds + L_ML + br * 32 + hw * 8;
      const f32x4 m0 = *(const f32x4*)(ml), m1 = *(const f32x4*)(ml + 4), l0 = *(const f32x4*)(ml + 16), l1 = *(const f32x4*)(ml + 20);
      const float M = fmax_(fmax_(fmax_(m0.x, m0.y), fmax_(m0.z, m0.w)), fmax_(fmax_(m1.x, m1.y), fmax_(m1.z, m1.w)));
      float L = 0.f;
#pragma unroll
      for (int w = 0; w < 8; ++w) L = fmaf(__builtin_amdgcn_exp2f((w < 4 ? m0[w & 3] : m1[w & 3]) - M), w < 4 ? l0[w & 3] : l1[w & 3], L);
      const int mg = WS_MG + ((br * 16 + kvh) * 2 + hw) * 1024;
      if (lane < 8) st_gran(ws, mg + (ch & 15) * 64 + lane * 8, tag, (lane & 1) ? L : M);
      pause(MLD);
      v2u x[1];
      sweep<1, 0, 8>(rw, mg + n * 64 + (g & 1) * 8, tag, x);
      auto sw = __builtin_amdgcn_permlane16_swap(x[0].x, x[0].x, false, false);
      const float Ms = __uint_as_float(sw[0]), Ls = __uint_as_float(sw[1]);
      float Mx = fmax_(Ms, dpp<XOR1>(Ms)); Mx = fmax_(Mx, dpp<XOR2>(Mx)); Mx = fmax_(Mx, dpp<HMIR>(Mx)); Mx = fmax_(Mx, dpp<MIR>(Mx));
      float Lt = __builtin_amdgcn_exp2f(Ms - Mx) * Ls; Lt += dpp<XOR1>(Lt); Lt += dpp<XOR2>(Lt); Lt += dpp<HMIR>(Lt); Lt += dpp<MIR>(Lt);
      const float inv = 1.0f / Lt;
      float c[8];
#pragma unroll
      for (int w = 0; w < 8; ++w) c[w] = __builtin_amdgcn_exp2f((w < 4 ? m0[w & 3] : m1[w & 3]) - Mx) * inv;
      const unsigned oa = lds_addr(lds + L_O + br * 2048 + hw * 128 + 2 * lane);
      int dst = WS_PG + (br * 256 + ch) * PG_ROW + (hw * 128 + 2 * lane) * 8;
      asm volatile("" : "+v"(dst));
      float o0 = 0.f, o1 = 0.f;
      { const v2u v = lds_take2<8>(oa); o0 = c[0] * __uint_as_float(v.x); o1 = c[0] * __uint_as_float(v.y); }
#pragma unroll
      for (int w = 1; w < 8; ++w) { const v2u v = lds_take2<0>(oa + w * 1024); o0 = fmaf(c[w], __uint_as_float(v.x), o0); o1 = fmaf(c[w], __uint_as_float(v.y), o1); }
      st_gran2(rw, dst, tag, o0, o1);
      if (hw == 0 && lane == 0) __hip_atomic_store(l_cnt + 4, (unsigned)br + 1u, RLX_WG);
    }
    STAMP(8);
    { unsigned due = ((unsigned)te + 6000u) << 4; gate_next(due); }
  } else {
    if (wave == 0) {
      unsigned long long ta;
      asm volatile("s_waitcnt vmcnt(0)\n\ts_memrealtime %0\n\ts_waitcnt lgkmcnt(0)" : "=s"(ta) : "v"(probe) : "memory");
      if (lane == 0) __hip_atomic_store(l_cnt + 6, ((unsigned)ta << 4) | 1u, RLX_WG);
    }
    St z;
    const int phs = PAT == 2 ? (wg >> 3) & 7 : wave;
    z.lds = lds; z.lane = lane; z.wave = wave; z.mask = mask;
    z.rs[0] = rk0;
    z.rs[1] = mkrs(ptr64(ka[8], ka[9]) + (size_t)ch * 262144, 262144);
    z.rs[2] = mkrs(ptr64(ka[6], ka[7]) + (size_t)ch * 262144, 262144);
    z.rs[3] = mkrs(ptr64(ka[10], ka[11]) + (size_t)ch * 262144, 262144);
    z.vk = (ABL & 8) ? lane * 16 : ((lane >> 2) & 3) * 256 + g * 64 + (lane & 3) * 16; z.vv = lane * 16; z.so = wave * WST;
    GAS unsigned short* outp = (GAS unsigned short*)ptr64(kb[4], kb[5]);
    z.tb = lds_wait<1>(l_cnt + 6, 1u) + (unsigned)(OFF16 + ((phs * DT16) >> 3));
    z.nx = z.tb;
    STAMP(1);
    Req<0, PRE>::go(z);
    STAMP(4);
    lds_wait<1>(l_cnt + 1, 1u);
    const unsigned tag = __builtin_amdgcn_readfirstlane(l_cnt[5]);
    z.adj = __builtin_amdgcn_readfirstlane(l_cnt[8]);
    STAMP(5);
    Con<0, PRE - DEP + 1, PRE - 1>::go(z);
    Run<PRE, 17>::go(z);
    STAMP(14);
    Run<17, 25>::go(z);
    STAMP(15);
    Run<25, 32>::go(z);
    STAMP(6);
    Con<32 - DEP + 1, 32, 31>::go(z);
    STAMP(7);
    const unsigned oba = lds_addr(lds + L_OB + g * 8);
    if (wave < 5) {
      const __amdgpu_buffer_rsrc_t ro = __builtin_amdgcn_make_buffer_rsrc((unsigned char*)wop + ((size_t)(kvh * 256 + s8 * 32) * DM) * 2, 0, 32 * DM * 2, RSRC3);
      const int wcol = wave * 128 + n * 8;
      const int vwo = (!NOWO && wcol < DM) ? g * (DM * 2) + wcol * 2 : OOB;
      v4u Wo[8];
      z.tb += (unsigned)((wave < NW1 ? wave * WOS16 : WG16 + (wave - NW1) * WOSB16) - ((phs * DT16) >> 3));
      gate_next(z.tb);
#pragma unroll
      for (int i = 0; i < 8; ++i) Wo[i] = __builtin_amdgcn_raw_buffer_load_b128(ro, vwo, i * 4 * DM * 2, AUX_NT);
      int dst = wcol < DM ? WS_YG + (rb * 128 + pidx) * YG_ROW + (wcol + 4 * (g & 1) + 2 * (g >> 1)) * 8 : OOB;
      asm volatile("" : "+v"(dst), "+v"(Wo[0]), "+v"(Wo[1]), "+v"(Wo[2]), "+v"(Wo[3]), "+v"(Wo[4]), "+v"(Wo[5]), "+v"(Wo[6]), "+v"(Wo[7]));
      STAMP(2);
      v4u oa, ob;
#pragma nounroll
      for (unsigned spins = 0; spins < LSPIN_CAP; ++spins) {
        asm volatile("ds_read_b128 %0, %2\n\tds_read_b128 %1, %2 offset:16\n\ts_waitcnt lgkmcnt(0)" : "=&v"(oa), "=&v"(ob) : "v"(oba) : "memory");
        if (__all(oa.x != EMPTY_B && oa.y != EMPTY_B && oa.z != EMPTY_B && oa.w != EMPTY_B && ob.x != EMPTY_B && ob.y != EMPTY_B && ob.z != EMPTY_B && ob.w != EMPTY_B)) break;
      }
      STAMP(10);
      f32x4 Y0 = {0.f, 0.f, 0.f, 0.f}, Y1 = {0.f, 0.f, 0.f, 0.f};
#pragma unroll
      for (int i = 0; i < 4; ++i) { Y0 = mfma(diag(oa[i], mask), Wo[i], Y0); Y1 = mfma(diag(ob[i], mask), Wo[4 + i], Y1); }
      const f32x4 Y = Y0 + Y1;
      st_gran2(rw, dst, tag, g < 2 ? Y.x : Y.z, g < 2 ? Y.y : Y.w);
      STAMP(11);
    } else if (wave == 5) {
      const int po = WS_PG + (rb * 256 + kvh * 16 + (lane >> 5)) * PG_ROW + (32 * s8 + (lane & 31)) * 8;
      v2u x[8];
      lds_wait<0>(l_cnt + 4, (unsigned)rb + 1u);
      if (rb) pause(H1D);
      sweep<8, 2 * PG_ROW, 1>(rw, po, tag, x);
      STAMP(9);
      const float o = x32_sum(((__uint_as_float(x[0].x) + __uint_as_float(x[1].x)) + (__uint_as_float(x[2].x) + __uint_as_float(x[3].x)))
                            + ((__uint_as_float(x[4].x) + __uint_as_float(x[5].x)) + (__uint_as_float(x[6].x) + __uint_as_float(x[7].x))));
      if (lane < 32) ((unsigned*)(lds + L_OB))[(lane & 3) * 8 + (lane >> 2)] = dup_bf16(o);
    } else if (wave == 6 || pidx < 16) {
      const int pos = wave == 6 ? pidx * 4 : 512 + pidx * 4;
      const int so = WS_YG + (rb * 128 + (lane >> 2)) * YG_ROW + (pos + (lane & 3)) * 8;
      const int col = pos + (lane & 3);
      GAS unsigned short* op = outp + rb * DM + col;
      asm volatile("" : "+v"(op));
      v2u x[8];
      z.tb += (unsigned)(WG16 + (4 - NW1) * WOSB16 + H2G16 - ((phs * DT16) >> 3));
#pragma nounroll
      for (unsigned spins = 0; spins < LSPIN_CAP; ++spins) {
        if (__builtin_amdgcn_readfirstlane(__hip_atomic_load((unsigned*)(lds + L_OB), RLX_WG)) != EMPTY_B) break;
        __builtin_amdgcn_s_sleep(1);
      }
      pause(H2D);
      gate_next(z.tb);
      sweep<8, 16 * YG_ROW, 1>(rw, so, tag, x);
      STAMP(12);
      float y = ((__uint_as_float(x[0].x) + __uint_as_float(x[1].x)) + (__uint_as_float(x[2].x) + __uint_as_float(x[3].x)))
              + ((__uint_as_float(x[4].x) + __uint_as_float(x[5].x)) + (__uint_as_float(x[6].x) + __uint_as_float(x[7].x)));
      y += dpp<ROR4>(y); y += dpp<ROR8>(y);
      y = xg_sum(y);
      if (lane < 4) *op = f2bf(y);
      if (rb == 0 && pos == 0 && lane == 0) __hip_atomic_store((gu32*)(ws + WS_EPOCH), tag, RLX_AGENT);
      STAMP(13);
    }
  }
  STAMP_OUT();
}

extern "C" void kernel_launch(void* const* d_in, const int* in_sizes, int n_in, void* d_out, int out_size, void* d_ws, size_t ws_size, hipStream_t stream) {
  Args a;
  a.x[0] = d_in[0]; a.k[0] = d_in[1]; a.v[0] = d_in[2]; a.wq[0] = d_in[3]; a.wo[0] = d_in[4];
  a.x[1] = d_in[5]; a.k[1] = d_in[6]; a.v[1] = d_in[7]; a.wq[1] = d_in[8]; a.wo[1] = d_in[9];
  a.out = d_out; a.ws = d_ws;
  gqa2_fused<<<NWG, NTHR, 0, stream>>>(a);
}
```

```cpp
#define CODEWARM 16
#include <hip/hip_runtime.h>
#include <cstdint>

typedef unsigned v4u __attribute__((ext_vector_type(4)));
typedef unsigned v2u __attribute__((ext_vector_type(2)));
typedef float f32x4 __attribute__((ext_vector_type(4)));
typedef __bf16 bf16x8 __attribute__((ext_vector_type(8)));
typedef __bf16 bf16x2 __attribute__((ext_vector_type(2)));
typedef float f32x2 __attribute__((ext_vector_type(2)));
#define GAS __attribute__((address_space(1)))
#define LAS __attribute__((address_space(3)))
typedef GAS unsigned gu32;
typedef GAS unsigned long long gu64;
#define RLX_AGENT __ATOMIC_RELAXED, __HIP_MEMORY_SCOPE_AGENT
#define RLX_WG __ATOMIC_RELAXED, __HIP_MEMORY_SCOPE_WORKGROUP

#define STAMPS 0
#define DT16 1808
#define ANC -2
#define OFF16 600
#define PRE 8
#define DEP 3
#define LIM 2
#define WO16 0
#define WOS16 600
#define QGD 75
#define PTH16 400
#define SLK16 100000
#define H1D 40
#define H2D 70
#define PREW 0
#define HBAR 1
#define PAT 1
#define JST 8192
#define WST 1024
#define SOFT16 0
#define NSOFT 6
#define H2G16 2000
#define WOA16 0
#define WOSA16 600
#define OUTSC 0
#define SLOWSTART 0
#define P016 2400
#define NLIM 32
#define LIMB 8
#define DF16 0
#define JIT16 160
#define ABL 0
#define QGA 280
#define QGB 600
#define MLD 350
#define PIPE 0
#define GAP1 8
#define GAP2 8
#define ALIGN 1
#define NW1 3
#define WG16 1800
#define WOSB16 600
#define CODEKB 20
#define NOWO 0

constexpr int DM = 576, NQ = 4096;
constexpr int NWG = 256, NTHR = 640;
constexpr int AUX_NT = 2, AUX_SC1 = 16;
constexpr int RSRC3 = 0x00020000;
constexpr int OOB = 0x7fff0000;
constexpr int WS_EPOCH = 0;
constexpr int WS_AG = 1024;
constexpr int WS_QG = 4096;
constexpr int WS_MG = WS_QG + 32 * 4096;
constexpr int WS_PG = WS_MG + 64 * 1024;
constexpr int PG_ROW = 256 * 8;
constexpr int WS_YG = WS_PG + 512 * PG_ROW;
constexpr int YG_ROW = 576 * 8;
constexpr int WS_ST = WS_YG + 256 * YG_ROW;
constexpr unsigned TAG_STEP = 0x9E3779B9u;
constexpr unsigned SPIN_CAP = 1u << 17, LSPIN_CAP = 1u << 22;
constexpr float C2 = 0.088388347648318440f * 1.4426950408889634f;
constexpr unsigned EMPTY_O = 0xffffffffu, EMPTY_B = 0x0000ffffu;

struct Args {
  const void* x[2]; const void* k[2]; const void* v[2]; const void* wq[2]; const void* wo[2];
  void* out; void* ws;
};

#define SB() __builtin_amdgcn_sched_barrier(0)
__device__ __forceinline__ f32x4 mfma(v4u a, v4u b, f32x4 c) {
  return __builtin_amdgcn_mfma_f32_16x16x32_bf16(__builtin_bit_cast(bf16x8, a), __builtin_bit_cast(bf16x8, b), c, 0, 0, 0);
}
__device__ __forceinline__ unsigned dup_bf16(float f) {
  f32x2 v = {f, f};
  return __builtin_bit_cast(unsigned, __builtin_convertvector(v, bf16x2));
}
__device__ __forceinline__ unsigned short f2bf(float f) { return __builtin_bit_cast(unsigned short, (__bf16)f); }
__device__ __forceinline__ v4u diag(unsigned pp, v4u mask) { v4u r = {pp & mask.x, pp & mask.y, pp & mask.z, pp & mask.w}; return r; }
template <int CTRL> __device__ __forceinline__ float dpp(float x) {
  return __builtin_bit_cast(float, __builtin_amdgcn_mov_dpp(__builtin_bit_cast(int, x), CTRL, 0xf, 0xf, true));
}
constexpr int XOR1 = 0xB1, XOR2 = 0x4E, HMIR = 0x141, MIR = 0x140, ROR8 = 0x128, ROR4 = 0x124;
__device__ __forceinline__ float fmax_(float a, float b) { return __builtin_fmaxf(a, b); }
__device__ __forceinline__ float xg_max(float x) {
  auto s = __builtin_amdgcn_permlane16_swap(__float_as_uint(x), __float_as_uint(x), false, false);
  x = fmax_(__uint_as_float(s[0]), __uint_as_float(s[1]));
  auto t = __builtin_amdgcn_permlane32_swap(__float_as_uint(x), __float_as_uint(x), false, false);
  return fmax_(__uint_as_float(t[0]), __uint_as_float(t[1]));
}
__device__ __forceinline__ float x32_sum(float x) {
  auto t = __builtin_amdgcn_permlane32_swap(__float_as_uint(x), __float_as_uint(x), false, false);
  return __uint_as_float(t[0]) + __uint_as_float(t[1]);
}
__device__ __forceinline__ float xg_sum(float x) {
  auto s = __builtin_amdgcn_permlane16_swap(__float_as_uint(x), __float_as_uint(x), false, false);
  return x32_sum(__uint_as_float(s[0]) + __uint_as_float(s[1]));
}
__device__ __forceinline__ void st_gran(unsigned char* ws, int off, unsigned tag, float v) {
  __hip_atomic_store((gu64*)(ws + off), ((unsigned long long)tag << 32) | __float_as_uint(v), RLX_AGENT);
}
__device__ __forceinline__ void st_gran2(__amdgpu_buffer_rsrc_t rs, int off, unsigned tag, float a, float b) {
  __builtin_amdgcn_raw_buffer_store_b128(v4u{__float_as_uint(a), tag, __float_as_uint(b), tag}, rs, off, 0, AUX_SC1);
}
__device__ __forceinline__ v2u ldg(__amdgpu_buffer_rsrc_t rs, int off) { return __builtin_amdgcn_raw_buffer_load_b64(rs, off, 0, AUX_SC1); }
template <int N, int STEP, int SLEEP> __device__ __forceinline__ void sweep(__amdgpu_buffer_rsrc_t rs, int off, unsigned tag, v2u (&x)[N]) {
#pragma unroll
  for (int k = 0; k < N; ++k) x[k] = ldg(rs, off + k * STEP);
  SB();
  bool ok = true;
#pragma unroll
  for (int k = 0; k < N; ++k) ok &= x[k].y == tag;
  if (__builtin_expect(__all(ok), 1)) return;
#pragma nounroll
  for (unsigned spins = 0; spins < SPIN_CAP; ++spins) {
    __builtin_amdgcn_s_sleep(SLEEP);
    v2u y[N];
#pragma unroll
    for (int k = 0; k < N; ++k) y[k] = ldg(rs, x[k].y == tag ? OOB : off + k * STEP);
    SB();
    ok = true;
#pragma unroll
    for (int k = 0; k < N; ++k) { x[k] = x[k].y == tag ? x[k] : y[k]; ok &= x[k].y == tag; }
    if (__all(ok)) break;
  }
}
template <int STEP> __device__ __forceinline__ void pass4(v4u (&y)[4], const v4u (&x)[4], int off, v4u rs, unsigned tag) {
  int o[4];
#pragma unroll
  for (int k = 0; k < 4; ++k) o[k] = (x[k].y == tag && x[k].w == tag) ? OOB : off + k * STEP;
  asm volatile("buffer_load_dwordx4 %0, %4, %8, 0 offen sc1\n\t"
               "buffer_load_dwordx4 %1, %5, %8, 0 offen sc1\n\t"
               "buffer_load_dwordx4 %2, %6, %8, 0 offen sc1\n\t"
               "buffer_load_dwordx4 %3, %7, %8, 0 offen sc1"
               : "=&v"(y[0]), "=&v"(y[1]), "=&v"(y[2]), "=&v"(y[3]) : "v"(o[0]), "v"(o[1]), "v"(o[2]), "v"(o[3]), "s"(rs) : "memory");
}
__device__ __forceinline__ bool take4(v4u (&x)[4], v4u (&y)[4], unsigned tag) {
  asm volatile("s_waitcnt vmcnt(4)" : "+v"(y[0]), "+v"(y[1]), "+v"(y[2]), "+v"(y[3]));
  bool ok = true;
#pragma unroll
  for (int k = 0; k < 4; ++k) {
    const bool have = x[k].y == tag && x[k].w == tag;
    x[k] = have ? x[k] : y[k];
    ok &= x[k].y == tag && x[k].w == tag;
  }
  return __all(ok);
}
__device__ __forceinline__ void pause(int ticks);
template <int STEP, int GAP> __device__ __forceinline__ void sweep2(v4u rs, int off, unsigned tag, v4u (&x)[4], v4u (&ya)[4], v4u (&yb)[4]) {
#pragma unroll
  for (int k = 0; k < 4; ++k) x[k] = v4u{0u, ~tag, 0u, ~tag};
  pass4<STEP>(ya, x, off, rs, tag);
  __builtin_amdgcn_s_sleep(GAP);
#pragma nounroll
  for (unsigned spins = 0; spins < SPIN_CAP; ++spins) {
    pass4<STEP>(yb, x, off, rs, tag);
    if (take4(x, ya, tag)) break;
    pass4<STEP>(ya, x, off, rs, tag);
    if (take4(x, yb, tag)) break;
  }
}
#define KEEP8(A, B) asm volatile("s_waitcnt vmcnt(0)" :: "v"(A[0]), "v"(A[1]), "v"(A[2]), "v"(A[3]), "v"(B[0]), "v"(B[1]), "v"(B[2]), "v"(B[3]) : "memory")
__device__ __forceinline__ v4u mkrs(const void* p, unsigned bytes) {
  const unsigned long long u = (unsigned long long)p;
  v4u r = {(unsigned)u, (unsigned)(u >> 32) & 0xffffu, bytes, (unsigned)RSRC3};
  return r;
}
__device__ __forceinline__ void tile_load(v4u (&d)[4], int voff, v4u rs, int soff) {
  asm volatile("buffer_load_dwordx4 %0, %4, %5, %6 offen nt\n\t"
               "buffer_load_dwordx4 %1, %4, %5, %7 offen nt\n\t"
               "buffer_load_dwordx4 %2, %4, %5, %8 offen nt\n\t"
               "buffer_load_dwordx4 %3, %4, %5, %9 offen nt"
               : "=&v"(d[0]), "=&v"(d[1]), "=&v"(d[2]), "=&v"(d[3]) : "v"(voff), "s"(rs), "s"(soff), "s"(soff + JST), "s"(soff + 2 * JST), "s"(soff + 3 * JST));
}
template <int N> __device__ __forceinline__ void tile_wait(v4u (&d)[4]) {
  asm volatile("s_waitcnt vmcnt(%4)" : "+v"(d[0]), "+v"(d[1]), "+v"(d[2]), "+v"(d[3]) : "i"(N));
}
template <int N> __device__ __forceinline__ void piece_wait(v4u& d) { asm volatile("s_waitcnt vmcnt(%1)" : "+v"(d) : "i"(N)); }
__device__ __forceinline__ void pause(int ticks) {
  unsigned due;
  asm volatile("s_memrealtime vcc\n\t"
               "s_waitcnt lgkmcnt(0)\n\t"
               "s_add_i32 %0, vcc_lo, %1\n"
               ".Lg0_%=:\n\t"
               "s_sleep 1\n\t"
               "s_memrealtime vcc\n\t"
               "s_waitcnt lgkmcnt(0)\n\t"
               "s_sub_i32 vcc_lo, vcc_lo, %0\n\t"
               "s_cmp_lt_i32 vcc_lo, 0\n\t"
               "s_cbranch_scc1 .Lg0_%="
               : "=&s"(due) : "i"(ticks) : "scc", "vcc", "memory");
}
__device__ __forceinline__ void gate_next(unsigned& due) {
  asm volatile("s_branch .Lg1_%=\n"
               ".Lg0_%=:\n\t"
               "s_sleep 1\n"
               ".Lg1_%=:\n\t"
               "s_memrealtime vcc\n\t"
               "s_waitcnt lgkmcnt(0)\n\t"
               "s_lshl_b32 vcc_lo, vcc_lo, 4\n\t"
               "s_sub_i32 vcc_lo, vcc_lo, %0\n\t"
               "s_cmp_lt_i32 vcc_lo, 0\n\t"
               "s_cbranch_scc1 .Lg0_%=\n\t"
               "s_setprio 0\n\t"
               "s_cmp_lt_i32 vcc_lo, %1\n\t"
               "s_cbranch_scc1 .Lg2_%=\n\t"
               "s_setprio 1\n"
               ".Lg2_%=:\n\t"
               "s_sub_i32 vcc_lo, vcc_lo, %2\n\t"
               "s_max_i32 vcc_lo, vcc_lo, 0\n\t"
               "s_add_i32 %0, %0, vcc_lo\n\t"
               "s_add_i32 %0, %0, %3"
               : "+s"(due) : "i"(PTH16), "i"(SLK16), "i"(DT16) : "scc", "vcc", "memory");
}
__device__ __forceinline__ void gate2(unsigned& line, unsigned& nxt) {
  asm volatile("s_sub_i32 vcc_lo, %1, %0\n\t"
               "s_max_i32 vcc_lo, vcc_lo, 0\n\t"
               "s_add_i32 vcc_hi, %0, vcc_lo\n\t"
               "s_mov_b32 %1, vcc_hi\n\t"
               "s_branch .Lg1_%=\n"
               ".Lg0_%=:\n\t"
               "s_sleep 1\n"
               ".Lg1_%=:\n\t"
               "s_memrealtime vcc\n\t"
               "s_waitcnt lgkmcnt(0)\n\t"
               "s_lshl_b32 vcc_lo, vcc_lo, 4\n\t"
               "s_sub_i32 vcc_hi, vcc_lo, %1\n\t"
               "s_cmp_lt_i32 vcc_hi, 0\n\t"
               "s_cbranch_scc1 .Lg0_%=\n\t"
               "s_sub_i32 vcc_hi, vcc_hi, %4\n\t"
               "s_max_i32 vcc_hi, vcc_hi, 0\n\t"
               "s_add_i32 %1, %1, vcc_hi\n\t"
               "s_add_i32 %1, %1, %3\n\t"
               "s_add_i32 %0, %0, %2"
               : "+s"(line), "+s"(nxt) : "i"(DT16), "i"(DF16), "i"(JIT16) : "scc", "vcc", "memory");
}
__device__ __forceinline__ void lds_barrier() {
  asm volatile("s_waitcnt lgkmcnt(0)" ::: "memory");
  __builtin_amdgcn_s_barrier();
  asm volatile("" ::: "memory");
}
__device__ __forceinline__ void lds_arrive(unsigned* c, int lane) {
  asm volatile("s_waitcnt lgkmcnt(0)" ::: "memory");
  if (lane == 0) __hip_atomic_fetch_add(c, 1u, RLX_WG);
}
template <int SLEEP> __device__ __forceinline__ unsigned lds_wait(unsigned* c, unsigned want) {
  unsigned v = 0;
#pragma nounroll
  for (unsigned spins = 0; spins < LSPIN_CAP; ++spins) {
    v = __builtin_amdgcn_readfirstlane(__hip_atomic_load(c, RLX_WG));
    if (v >= want) break;
    __builtin_amdgcn_s_sleep(SLEEP);
  }
  asm volatile("" ::: "memory");
  return v;
}
__device__ __forceinline__ unsigned lds_addr(const void* p) { return (unsigned)(size_t)(const LAS void*)p; }
template <int SLEEP> __device__ __forceinline__ v2u lds_take2(unsigned addr) {
  v2u v;
#pragma nounroll
  for (unsigned spins = 0; spins < LSPIN_CAP; ++spins) {
    asm volatile("ds_read_b64 %0, %1\n\ts_waitcnt lgkmcnt(0)" : "=&v"(v) : "v"(addr) : "memory");
    if (__all(v.x != EMPTY_O && v.y != EMPTY_O)) break;
    if (SLEEP) __builtin_amdgcn_s_sleep(SLEEP);
  }
  return v;
}
#define STAMP(i) do {} while (0)
#define STAMP_OUT() do {} while (0)

constexpr int L_O = 0;
constexpr int L_ML = L_O + 4096;
constexpr int L_QP = L_ML + 64;
constexpr int L_Q = L_QP + 256;
constexpr int L_OB = L_Q + 256;
constexpr int L_CNT = L_OB + 32;
constexpr int L_END = L_CNT + 16;

struct St {
  v4u T[32][4];
  float Sc[8][4];
  unsigned P[8][4];
  v4u Q, mask;
  f32x4 O;
  v4u rs[4];
  int vk, vv, so;
  unsigned tb, nx, adj;
  float* lds;
  int lane, wave;
};

template <int I> __device__ __forceinline__ void request(St& z) {
  constexpr int lim = I < NLIM ? LIM : LIMB;
  if constexpr (4 * I > lim) asm volatile("s_waitcnt vmcnt(%0)" :: "i"(lim) : "memory");
  gate_next(z.tb);
  if constexpr (I >= PRE && I < 31) z.tb += z.adj;
  tile_load(z.T[I], (I & 8) ? z.vv : z.vk, z.rs[I >> 3], z.so + (I & 7) * 32768);
  SB();
}
template <int I, int W> __device__ __forceinline__ void consume(St& z) {
  const int g = z.lane >> 4, n = z.lane & 15;
  constexpr int br = I >> 4;
  if constexpr ((I & 15) == 0) {
    if constexpr (br) lds_wait<1>((unsigned*)(z.lds + L_CNT) + 7, 1u);
    z.Q = *(const v4u*)((const unsigned short*)(z.lds + L_Q) + br * 256 + (n >> 3) * 128 + 32 * g + 8 * (n & 3));
  }
  if constexpr ((I & 8) == 0) {
    tile_wait<W>(z.T[I]);
#pragma unroll
    for (int j = 0; j < 4; ++j) {
      const f32x4 d = mfma(z.T[I][j], z.Q, f32x4{0.f, 0.f, 0.f, 0.f});
      z.Sc[I & 7][j] = (dpp<0x00>(d.x) + dpp<0x55>(d.y)) + (dpp<0xAA>(d.z) + dpp<0xFF>(d.w));
    }
    if constexpr ((I & 7) == 7) {
      float m = z.Sc[0][0];
#pragma unroll
      for (int t = 0; t < 8; ++t)
#pragma unroll
        for (int j = 0; j < 4; ++j) m = fmax_(m, z.Sc[t][j]);
      const float m2 = xg_max(m) * C2;
      float l = 0.f;
#pragma unroll
      for (int t = 0; t < 8; ++t) {
        float p[4];
#pragma unroll
        for (int j = 0; j < 4; ++j) { p[j] = __builtin_amdgcn_exp2f(fmaf(z.Sc[t][j], C2, -m2)); z.P[t][j] = dup_bf16(p[j]); }
        l += (p[0] + p[1]) + (p[2] + p[3]);
        asm volatile("" : "+v"(z.P[t][0]), "+v"(z.P[t][1]), "+v"(z.P[t][2]), "+v"(z.P[t][3]));
      }
      l = xg_sum(l);
      if (g == 0 && (n & 7) == 0) { z.lds[L_ML + br * 32 + (n >> 3) * 8 + z.wave] = m2; z.lds[L_ML + br * 32 + 16 + (n >> 3) * 8 + z.wave] = l; }
      lds_arrive((unsigned*)(z.lds + L_CNT) + 2 + br, z.lane);
      z.O = f32x4{0.f, 0.f, 0.f, 0.f};
    } else {
      asm volatile("" : "+v"(z.Sc[I & 7][0]), "+v"(z.Sc[I & 7][1]), "+v"(z.Sc[I & 7][2]), "+v"(z.Sc[I & 7][3]));
    }
  } else if constexpr (W == 0) {
    v4u A[4];
#pragma unroll
    for (int j = 0; j < 4; ++j) A[j] = diag(z.P[I & 7][j], z.mask);
    float* dst = z.lds + L_O + br * 2048 + z.wave * 256 + (g >> 1) * 128 + 8 * n + 4 * (g & 1);
    unsigned da = lds_addr(dst);
    asm volatile("" : "+v"(A[0]), "+v"(A[1]), "+v"(A[2]), "+v"(A[3]), "+v"(da));
    piece_wait<3>(z.T[I][0]); z.O = mfma(A[0], z.T[I][0], z.O); SB();
    piece_wait<2>(z.T[I][1]); z.O = mfma(A[1], z.T[I][1], z.O); SB();
    piece_wait<1>(z.T[I][2]); z.O = mfma(A[2], z.T[I][2], z.O); SB();
    piece_wait<0>(z.T[I][3]); z.O = mfma(A[3], z.T[I][3], z.O);
    *(LAS f32x4*)(size_t)da = z.O;
  } else {
    tile_wait<W>(z.T[I]);
#pragma unroll
    for (int j = 0; j < 4; ++j) z.O = mfma(diag(z.P[I & 7][j], z.mask), z.T[I][j], z.O);
    asm volatile("" : "+v"(z.O));
    if constexpr ((I & 7) == 7) *(f32x4*)(z.lds + L_O + br * 2048 + z.wave * 256 + (g >> 1) * 128 + 8 * n + 4 * (g & 1)) = z.O;
  }
  SB();
}
template <int I, int N> struct Run {
  static __device__ __forceinline__ void go(St& z) { request<I>(z); consume<I - DEP + 1, 4 * (DEP - 1)>(z); Run<I + 1, N>::go(z); }
};
template <int N> struct Run<N, N> { static __device__ __forceinline__ void go(St&) {} };
template <int I, int N> struct Req { static __device__ __forceinline__ void go(St& z) { request<I>(z); Req<I + 1, N>::go(z); } };
template <int N> struct Req<N, N> { static __device__ __forceinline__ void go(St&) {} };
template <int I, int N, int LAST> struct Con {
  static __device__ __forceinline__ void go(St& z) { consume<I, 4 * (LAST - I)>(z); Con<I + 1, N, LAST>::go(z); }
};
template <int N, int LAST> struct Con<N, N, LAST> { static __device__ __forceinline__ void go(St&) {} };

typedef unsigned v16u __attribute__((ext_vector_type(16)));
typedef unsigned v8u __attribute__((ext_vector_type(8)));
__device__ __forceinline__ unsigned char* ptr64(unsigned lo, unsigned hi) { return (unsigned char*)(((unsigned long long)hi << 32) | lo); }

__global__ void __launch_bounds__(NTHR, 3) gqa2_fused(Args) {
  __shared__ __attribute__((aligned(16))) float lds[L_END];
  unsigned* const l_cnt = (unsigned*)(lds + L_CNT);
  v16u ka; v8u kb;
  asm volatile("s_load_dwordx16 %0, %2, 0x0\n\ts_load_dwordx8 %1, %2, 0x40" : "=&s"(ka), "=&s"(kb) : "s"(__builtin_amdgcn_kernarg_segment_ptr()));
  const int tid = threadIdx.x, lane = tid & 63, wave = __builtin_amdgcn_readfirstlane(tid >> 6);
  const int g = lane >> 4, n = lane & 15;
  const int wg = blockIdx.x;
  const int ch = (wg & 7) << 5 | wg >> 3;
  const int kvh = ch >> 4, rb = (ch >> 3) & 1, s8 = ch & 7;
  {
    const v4u e = {EMPTY_O, EMPTY_O, EMPTY_O, EMPTY_O};
    const int i = tid < 512 ? tid : 0;
    *(v4u*)(lds + L_O + i * 4) = e; *(v4u*)(lds + L_O + 2048 + i * 4) = e;
    const int j = tid < 48 ? tid : 0;
    ((unsigned*)(lds + L_OB))[j] = j < 32 ? EMPTY_B : 0u;
  }
  asm volatile("s_waitcnt lgkmcnt(0)" : "+s"(ka), "+s"(kb) :: "memory");
  const v4u rk0 = mkrs(ptr64(ka[4], ka[5]) + (size_t)ch * 262144, 262144);
  unsigned probe;
  asm volatile("s_cmp_lg_u32 %2, 0\n\ts_cbranch_scc1 .Lp%=\n\ts_mov_b64 exec, 1\n\tbuffer_load_dword %0, off, %1, 0 nt\n\ts_mov_b64 exec, -1\n.Lp%=:"
               : "=v"(probe) : "s"(rk0), "s"(wave) : "scc");
  __builtin_amdgcn_s_barrier();
  asm volatile("" ::: "memory");
  unsigned char* const ws = ptr64(kb[6], kb[7]);
  const unsigned char* const wop = rb ? ptr64(kb[2], kb[3]) : ptr64(kb[0], kb[1]);
  const __amdgpu_buffer_rsrc_t rw = __builtin_amdgcn_make_buffer_rsrc(ws, 0, WS_ST, RSRC3);
  v4u mask;
  {
    const unsigned half = (n & 1) ? 0xffff0000u : 0x0000ffffu; const int r = (n & 7) >> 1;
    mask.x = r == 0 ? half : 0u; mask.y = r == 1 ? half : 0u; mask.z = r == 2 ? half : 0u; mask.w = r == 3 ? half : 0u;
  }
  const int pidx = kvh * 8 + s8;
  if (__builtin_expect(wave >= 8, 1)) {
    asm volatile("" ::: "memory");
    __builtin_amdgcn_s_setprio(3);
    const int hw = wave - 8, cg = s8 & 3, kh = s8 >> 2, sub = 2 * g + (n >> 3);
    const __amdgpu_buffer_rsrc_t rq = __builtin_amdgcn_make_buffer_rsrc((rb ? ptr64(ka[14], ka[15]) : ptr64(ka[12], ka[13])) + ((size_t)(kh * 288 + hw * 144) * NQ + kvh * 256 + cg * 64) * 2, 0, 143 * NQ * 2 + 128, RSRC3);
    const __amdgpu_buffer_rsrc_t rx = __builtin_amdgcn_make_buffer_rsrc((rb ? ptr64(ka[2], ka[3]) : ptr64(ka[0], ka[1])) + (kh * 288 + hw * 144) * 2, 0, 144 * 2, RSRC3);
    v4u wqv[18];
#pragma unroll
    for (int i = 0; i < 18; ++i) wqv[i] = __builtin_amdgcn_raw_buffer_load_b128(rq, (ABL & 4) ? OOB : sub * (NQ * 2) + (n & 7) * 16, i * 8 * NQ * 2, AUX_NT);
    const unsigned xa = __builtin_amdgcn_raw_buffer_load_b32(rx, n * 16 + g * 4, 0, 0), xb = __builtin_amdgcn_raw_buffer_load_b32(rx, n * 16 + g * 4 + 256, 0, 0);
    const unsigned ep = __builtin_amdgcn_raw_buffer_load_b32(rw, lane == 0 ? WS_EPOCH : OOB, 0, AUX_SC1);
    unsigned cw = 0;
    if (wg < CODEWARM && hw == 1) {
      unsigned long long pc;
      asm volatile("s_getpc_b64 %0" : "=s"(pc));
      const __amdgpu_buffer_rsrc_t rc = __builtin_amdgcn_make_buffer_rsrc((void*)(pc & ~127ull), 0, CODEKB * 1024, RSRC3);
      cw = __builtin_amdgcn_raw_buffer_load_b32(rc, lane * 128, 0, 0) ^ __builtin_amdgcn_raw_buffer_load_b32(rc, lane * 128, 8192, 0) ^ __builtin_amdgcn_raw_buffer_load_b32(rc, lane * 128, 16384, 0);
    }
    asm volatile("" :: "v"(cw));
    SB();
    f32x4 a0 = {0.f, 0.f, 0.f, 0.f}, a1 = {0.f, 0.f, 0.f, 0.f};
    const unsigned sel = n < 8 ? 0x01000100u : 0x03020302u;
#define QSTEP(acc, i, src, k) acc = mfma(diag(__builtin_amdgcn_perm(0u, (unsigned)__builtin_amdgcn_mov_dpp((int)(src), 0x150 + (k), 0xf, 0xf, true), sel), mask), wqv[i], acc)
    QSTEP(a0, 0, xa, 0); QSTEP(a1, 1, xa, 1); QSTEP(a0, 2, xa, 2); QSTEP(a1, 3, xa, 3); QSTEP(a0, 4, xa, 4); QSTEP(a1, 5, xa, 5);
    QSTEP(a0, 6, xa, 6); QSTEP(a1, 7, xa, 7); QSTEP(a0, 8, xa, 8); QSTEP(a1, 9, xa, 9); QSTEP(a0, 10, xa, 10); QSTEP(a1, 11, xa, 11);
    QSTEP(a0, 12, xa, 12); QSTEP(a1, 13, xa, 13); QSTEP(a0, 14, xa, 14); QSTEP(a1, 15, xa, 15); QSTEP(a0, 16, xb, 0); QSTEP(a1, 17, xb, 1);
    if ((g >> 1) == (n >> 3)) *(f32x4*)(lds + L_QP + (hw * 2 + (n >> 3)) * 64 + 8 * (n & 7) + 4 * (g & 1)) = a0 + a1;
    const unsigned tag = __builtin_amdgcn_readfirstlane(ep) + TAG_STEP;
    STAMP(1);
    lds_arrive(l_cnt + 0, lane);
    unsigned anc = 0;
    if (hw == 0) {
      lds_wait<1>(l_cnt + 0, 2u);
      const float q = (lds[L_QP + lane] + lds[L_QP + 64 + lane]) + (lds[L_QP + 128 + lane] + lds[L_QP + 192 + lane]);
      st_gran(ws, WS_QG + ((rb * 16 + kvh) * 512 + kh * 256 + cg * 64 + lane) * 8, tag, q);
      if (lane == 0) l_cnt[5] = tag;
      anc = lds_wait<1>(l_cnt + 6, 1u);
      if (lane == 0) __hip_atomic_store((gu64*)(ws + WS_AG + wg * 8), ((unsigned long long)tag << 32) | anc, RLX_AGENT);
    }
    STAMP(2);
    pause(QGA);
    if (hw) pause(QGB - QGA);
    {
      v2u x[8];
      const v2u ag = ldg(rw, (hw == 0 && lane < 8) ? WS_AG + (wg & ~7) * 8 + lane * 8 : OOB);
      sweep<8, 512, 8>(rw, WS_QG + ((hw * 16 + kvh) * 512 + lane) * 8, tag, x);
#pragma unroll
      for (int k = 0; k < 4; ++k) ((unsigned short*)(lds + L_Q))[hw * 256 + k * 64 + lane] = f2bf(__uint_as_float(x[k].x) + __uint_as_float(x[k + 4].x));
      if (hw == 0) {
        int d = ag.y == tag ? (int)(ag.x - anc) : 0;
        d = d > 1600 ? 1600 : d < -1600 ? -1600 : d;
        d += __builtin_amdgcn_mov_dpp(d, XOR1, 0xf, 0xf, true); d += __builtin_amdgcn_mov_dpp(d, XOR2, 0xf, 0xf, true); d += __builtin_amdgcn_mov_dpp(d, HMIR, 0xf, 0xf, true);
        d /= 8 * (31 - PRE);
        if (lane == 0) l_cnt[8] = (unsigned)d;
      }
      lds_arrive(hw ? l_cnt + 7 : l_cnt + 1, lane);
    }
    STAMP(3);
#pragma nounroll
    for (int br = 0; br < 2; ++br) {
      lds_wait<8>(l_cnt + 2 + br, 8u);
      const float* ml = lds + L_ML + br * 32 + hw * 8;
      const f32x4 m0 = *(const f32x4*)(ml), m1 = *(const f32x4*)(ml + 4), l0 = *(const f32x4*)(ml + 16), l1 = *(const f32x4*)(ml + 20);
      const float M = fmax_(fmax_(fmax_(m0.x, m0.y), fmax_(m0.z, m0.w)), fmax_(fmax_(m1.x, m1.y), fmax_(m1.z, m1.w)));
      float L = 0.f;
#pragma unroll
      for (int w = 0; w < 8; ++w) L = fmaf(__builtin_amdgcn_exp2f((w < 4 ? m0[w & 3] : m1[w & 3]) - M), w < 4 ? l0[w & 3] : l1[w & 3], L);
      const int mg = WS_MG + ((br * 16 + kvh) * 2 + hw) * 1024;
      if (lane < 8) st_gran(ws, mg + (ch & 15) * 64 + lane * 8, tag, (lane & 1) ? L : M);
      pause(MLD);
      v2u x[1];
      sweep<1, 0, 8>(rw, mg + n * 64 + (g & 1) * 8, tag, x);
      auto sw = __builtin_amdgcn_permlane16_swap(x[0].x, x[0].x, false, false);
      const float Ms = __uint_as_float(sw[0]), Ls = __uint_as_float(sw[1]);
      float Mx = fmax_(Ms, dpp<XOR1>(Ms)); Mx = fmax_(Mx, dpp<XOR2>(Mx)); Mx = fmax_(Mx, dpp<HMIR>(Mx)); Mx = fmax_(Mx, dpp<MIR>(Mx));
      float Lt = __builtin_amdgcn_exp2f(Ms - Mx) * Ls; Lt += dpp<XOR1>(Lt); Lt += dpp<XOR2>(Lt); Lt += dpp<HMIR>(Lt); Lt += dpp<MIR>(Lt);
      const float inv = 1.0f / Lt;
      float c[8];
#pragma unroll
      for (int w = 0; w < 8; ++w) c[w] = __builtin_amdgcn_exp2f((w < 4 ? m0[w & 3] : m1[w & 3]) - Mx) * inv;
      const unsigned oa = lds_addr(lds + L_O + br * 2048 + hw * 128 + 2 * lane);
      int dst = WS_PG + (br * 256 + ch) * PG_ROW + (hw * 128 + 2 * lane) * 8;
      asm volatile("" : "+v"(dst));
      float o0 = 0.f, o1 = 0.f;
      { const v2u v = lds_take2<8>(oa); o0 = c[0] * __uint_as_float(v.x); o1 = c[0] * __uint_as_float(v.y); }
#pragma unroll
      for (int w = 1; w < 8; ++w) { const v2u v = lds_take2<0>(oa + w * 1024); o0 = fmaf(c[w], __uint_as_float(v.x), o0); o1 = fmaf(c[w], __uint_as_float(v.y), o1); }
      st_gran2(rw, dst, tag, o0, o1);
      if (hw == 0 && lane == 0) __hip_atomic_store(l_cnt + 4, (unsigned)br + 1u, RLX_WG);
    }
    STAMP(8);
  } else {
    if (wave == 0) {
      unsigned long long ta;
      asm volatile("s_waitcnt vmcnt(0)\n\ts_memrealtime %0\n\ts_waitcnt lgkmcnt(0)" : "=s"(ta) : "v"(probe) : "memory");
      if (lane == 0) __hip_atomic_store(l_cnt + 6, ((unsigned)ta << 4) | 1u, RLX_WG);
    }
    St z;
    const int phs = PAT == 2 ? (wg >> 3) & 7 : wave;
    z.lds = lds; z.lane = lane; z.wave = wave; z.mask = mask;
    z.rs[0] = rk0;
    z.rs[1] = mkrs(ptr64(ka[8], ka[9]) + (size_t)ch * 262144, 262144);
    z.rs[2] = mkrs(ptr64(ka[6], ka[7]) + (size_t)ch * 262144, 262144);
    z.rs[3] = mkrs(ptr64(ka[10], ka[11]) + (size_t)ch * 262144, 262144);
    z.vk = (ABL & 8) ? lane * 16 : ((lane >> 2) & 3) * 256 + g * 64 + (lane & 3) * 16; z.vv = lane * 16; z.so = wave * WST;
    GAS unsigned short* outp = (GAS unsigned short*)ptr64(kb[4], kb[5]);
    z.tb = lds_wait<1>(l_cnt + 6, 1u) + (unsigned)(OFF16 + ((phs * DT16) >> 3));
    z.nx = z.tb;
    STAMP(1);
    Req<0, PRE>::go(z);
    STAMP(4);
    lds_wait<1>(l_cnt + 1, 1u);
    const unsigned tag = __builtin_amdgcn_readfirstlane(l_cnt[5]);
    z.adj = __builtin_amdgcn_readfirstlane(l_cnt[8]);
    STAMP(5);
    Con<0, PRE - DEP + 1, PRE - 1>::go(z);
    Run<PRE, 17>::go(z);
    STAMP(14);
    Run<17, 25>::go(z);
    STAMP(15);
    Run<25, 32>::go(z);
    STAMP(6);
    Con<32 - DEP + 1, 32, 31>::go(z);
    STAMP(7);
    const unsigned oba = lds_addr(lds + L_OB + g * 8);
    if (wave < 5) {
      const __amdgpu_buffer_rsrc_t ro = __builtin_amdgcn_make_buffer_rsrc((unsigned char*)wop + ((size_t)(kvh * 256 + s8 * 32) * DM) * 2, 0, 32 * DM * 2, RSRC3);
      const int wcol = wave * 128 + n * 8;
      const int vwo = (!NOWO && wcol < DM) ? g * (DM * 2) + wcol * 2 : OOB;
      v4u Wo[8];
      z.tb += (unsigned)((wave < NW1 ? wave * WOS16 : WG16 + (wave - NW1) * WOSB16) - ((phs * DT16) >> 3));
      gate_next(z.tb);
#pragma unroll
      for (int i = 0; i < 8; ++i) Wo[i] = __builtin_amdgcn_raw_buffer_load_b128(ro, vwo, i * 4 * DM * 2, AUX_NT);
      int dst = wcol < DM ? WS_YG + (rb * 128 + pidx) * YG_ROW + (wcol + 4 * (g & 1) + 2 * (g >> 1)) * 8 : OOB;
      asm volatile("" : "+v"(dst), "+v"(Wo[0]), "+v"(Wo[1]), "+v"(Wo[2]), "+v"(Wo[3]), "+v"(Wo[4]), "+v"(Wo[5]), "+v"(Wo[6]), "+v"(Wo[7]));
      STAMP(2);
      v4u oa, ob;
#pragma nounroll
      for (unsigned spins = 0; spins < LSPIN_CAP; ++spins) {
        asm volatile("ds_read_b128 %0, %2\n\tds_read_b128 %1, %2 offset:16\n\ts_waitcnt lgkmcnt(0)" : "=&v"(oa), "=&v"(ob) : "v"(oba) : "memory");
        if (__all(oa.x != EMPTY_B && oa.y != EMPTY_B && oa.z != EMPTY_B && oa.w != EMPTY_B && ob.x != EMPTY_B && ob.y != EMPTY_B && ob.z != EMPTY_B && ob.w != EMPTY_B)) break;
      }
      STAMP(10);
      f32x4 Y0 = {0.f, 0.f, 0.f, 0.f}, Y1 = {0.f, 0.f, 0.f, 0.f};
#pragma unroll
      for (int i = 0; i < 4; ++i) { Y0 = mfma(diag(oa[i], mask), Wo[i], Y0); Y1 = mfma(diag(ob[i], mask), Wo[4 + i], Y1); }
      const f32x4 Y = Y0 + Y1;
      st_gran2(rw, dst, tag, g < 2 ? Y.x : Y.z, g < 2 ? Y.y : Y.w);
      STAMP(11);
    } else if (wave == 5) {
      const int po = WS_PG + (rb * 256 + kvh * 16 + (lane >> 5)) * PG_ROW + (32 * s8 + (lane & 31)) * 8;
      v2u x[8];
      lds_wait<0>(l_cnt + 4, (unsigned)rb + 1u);
      if (rb) pause(H1D);
      sweep<8, 2 * PG_ROW, 1>(rw, po, tag, x);
      STAMP(9);
      const float o = x32_sum(((__uint_as_float(x[0].x) + __uint_as_float(x[1].x)) + (__uint_as_float(x[2].x) + __uint_as_float(x[3].x)))
                            + ((__uint_as_float(x[4].x) + __uint_as_float(x[5].x)) + (__uint_as_float(x[6].x) + __uint_as_float(x[7].x))));
      if (lane < 32) ((unsigned*)(lds + L_OB))[(lane & 3) * 8 + (lane >> 2)] = dup_bf16(o);
    } else if (wave == 6 || pidx < 16) {
      const int pos = wave == 6 ? pidx * 4 : 512 + pidx * 4;
      const int so = WS_YG + (rb * 128 + (lane >> 2)) * YG_ROW + (pos + (lane & 3)) * 8;
      const int col = pos + (lane & 3);
      GAS unsigned short* op = outp + rb * DM + col;
      asm volatile("" : "+v"(op));
      v2u x[8];
      z.tb += (unsigned)(WG16 + (4 - NW1) * WOSB16 + H2G16 - ((phs * DT16) >> 3));
#pragma nounroll
      for (unsigned spins = 0; spins < LSPIN_CAP; ++spins) {
        if (__builtin_amdgcn_readfirstlane(__hip_atomic_load((unsigned*)(lds + L_OB), RLX_WG)) != EMPTY_B) break;
        __builtin_amdgcn_s_sleep(1);
      }
      pause(H2D);
      gate_next(z.tb);
      sweep<8, 16 * YG_ROW, 1>(rw, so, tag, x);
      STAMP(12);
      float y = ((__uint_as_float(x[0].x) + __uint_as_float(x[1].x)) + (__uint_as_float(x[2].x) + __uint_as_float(x[3].x)))
              + ((__uint_as_float(x[4].x) + __uint_as_float(x[5].x)) + (__uint_as_float(x[6].x) + __uint_as_float(x[7].x)));
      y += dpp<ROR4>(y); y += dpp<ROR8>(y);
      y = xg_sum(y);
      if (lane < 4) *op = f2bf(y);
      if (rb == 0 && pos == 0 && lane == 0) __hip_atomic_store((gu32*)(ws + WS_EPOCH), tag, RLX_AGENT);
      STAMP(13);
    }
  }
  STAMP_OUT();
}

extern "C" void kernel_launch(void* const* d_in, const int* in_sizes, int n_in, void* d_out, int out_size, void* d_ws, size_t ws_size, hipStream_t stream) {
  Args a;
  a.x[0] = d_in[0]; a.k[0] = d_in[1]; a.v[0] = d_in[2]; a.wq[0] = d_in[3]; a.wo[0] = d_in[4];
  a.x[1] = d_in[5]; a.k[1] = d_in[6]; a.v[1] = d_in[7]; a.wq[1] = d_in[8]; a.wo[1] = d_in[9];
  a.out = d_out; a.ws = d_ws;
  gqa2_fused<<<NWG, NTHR, 0, stream>>>(a);
}
```

```cpp
#define CODEWARM 6
#include <hip/hip_runtime.h>
#include <cstdint>

typedef unsigned v4u __attribute__((ext_vector_type(4)));
typedef unsigned v2u __attribute__((ext_vector_type(2)));
typedef float f32x4 __attribute__((ext_vector_type(4)));
typedef __bf16 bf16x8 __attribute__((ext_vector_type(8)));
typedef __bf16 bf16x2 __attribute__((ext_vector_type(2)));
typedef float f32x2 __attribute__((ext_vector_type(2)));
#define GAS __attribute__((address_space(1)))
#define LAS __attribute__((address_space(3)))
typedef GAS unsigned gu32;
typedef GAS unsigned long long gu64;
#define RLX_AGENT __ATOMIC_RELAXED, __HIP_MEMORY_SCOPE_AGENT
#define RLX_WG __ATOMIC_RELAXED, __HIP_MEMORY_SCOPE_WORKGROUP

#define STAMPS 0
#define DT16 1808
#define ANC -2
#define OFF16 600
#define PRE 8
#define DEP 3
#define LIM 2
#define WO16 0
#define WOS16 600
#define QGD 75
#define PTH16 400
#define SLK16 100000
#define H1D 40
#define H2D 70
#define PREW 0
#define HBAR 1
#define PAT 1
#define JST 8192
#define WST 1024
#define SOFT16 0
#define NSOFT 6
#define H2G16 2000
#define WOA16 0
#define WOSA16 600
#define OUTSC 0
#define SLOWSTART 0
#define P016 2400
#define NLIM 32
#define LIMB 8
#define DF16 0
#define JIT16 160
#define ABL 0
#define QGA 280
#define QGB 600
#define MLD 350
#define PIPE 0
#define GAP1 8
#define GAP2 8
#define ALIGN 1
#define NW1 3
#define WG16 1800
#define WOSB16 600
#define CODEKB 21
#define NOWO 0

constexpr int DM = 576, NQ = 4096;
constexpr int NWG = 256, NTHR = 640;
constexpr int AUX_NT = 2, AUX_SC1 = 16;
constexpr int RSRC3 = 0x00020000;
constexpr int OOB = 0x7fff0000;
constexpr int WS_EPOCH = 0;
constexpr int WS_AG = 1024;
constexpr int WS_QG = 4096;
constexpr int WS_MG = WS_QG + 32 * 4096;
constexpr int WS_PG = WS_MG + 64 * 1024;
constexpr int PG_ROW = 256 * 8;
constexpr int WS_YG = WS_PG + 512 * PG_ROW;
constexpr int YG_ROW = 576 * 8;
constexpr int WS_ST = WS_YG + 256 * YG_ROW;
constexpr unsigned TAG_STEP = 0x9E3779B9u;
constexpr unsigned SPIN_CAP = 1u << 17, LSPIN_CAP = 1u << 22;
constexpr float C2 = 0.088388347648318440f * 1.4426950408889634f;
constexpr unsigned EMPTY_O = 0xffffffffu, EMPTY_B = 0x0000ffffu;

struct Args {
  const void* x[2]; const void* k[2]; const void* v[2]; const void* wq[2]; const void* wo[2];
  void* out; void* ws;
};

#define SB() __builtin_amdgcn_sched_barrier(0)
__device__ __forceinline__ f32x4 mfma(v4u a, v4u b, f32x4 c) {
  return __builtin_amdgcn_mfma_f32_16x16x32_bf16(__builtin_bit_cast(bf16x8, a), __builtin_bit_cast(bf16x8, b), c, 0, 0, 0);
}
__device__ __forceinline__ unsigned dup_bf16(float f) {
  f32x2 v = {f, f};
  return __builtin_bit_cast(unsigned, __builtin_convertvector(v, bf16x2));
}
__device__ __forceinline__ unsigned short f2bf(float f) { return __builtin_bit_cast(unsigned short, (__bf16)f); }
__device__ __forceinline__ v4u diag(unsigned pp, v4u mask) { v4u r = {pp & mask.x, pp & mask.y, pp & mask.z, pp & mask.w}; return r; }
template <int CTRL> __device__ __forceinline__ float dpp(float x) {
  return __builtin_bit_cast(float, __builtin_amdgcn_mov_dpp(__builtin_bit_cast(int, x), CTRL, 0xf, 0xf, true));
}
constexpr int XOR1 = 0xB1, XOR2 = 0x4E, HMIR = 0x141, MIR = 0x140, ROR8 = 0x128, ROR4 = 0x124;
__device__ __forceinline__ float fmax_(float a, float b) { return __builtin_fmaxf(a, b); }
__device__ __forceinline__ float xg_max(float x) {
  auto s = __builtin_amdgcn_permlane16_swap(__float_as_uint(x), __float_as_uint(x), false, false);
  x = fmax_(__uint_as_float(s[0]), __uint_as_float(s[1]));
  auto t = __builtin_amdgcn_permlane32_swap(__float_as_uint(x), __float_as_uint(x), false, false);
  return fmax_(__uint_as_float(t[0]), __uint_as_float(t[1]));
}
__device__ __forceinline__ float x32_sum(float x) {
  auto t = __builtin_amdgcn_permlane32_swap(__float_as_uint(x), __float_as_uint(x), false, false);
  return __uint_as_float(t[0]) + __uint_as_float(t[1]);
}
__device__ __forceinline__ float xg_sum(float x) {
  auto s = __builtin_amdgcn_permlane16_swap(__float_as_uint(x), __float_as_uint(x), false, false);
  return x32_sum(__uint_as_float(s[0]) + __uint_as_float(s[1]));
}
__device__ __forceinline__ void st_gran(unsigned char* ws, int off, unsigned tag, float v) {
  __hip_atomic_store((gu64*)(ws + off), ((unsigned long long)tag << 32) | __float_as_uint(v), RLX_AGENT);
}
__device__ __forceinline__ void st_gran2(__amdgpu_buffer_rsrc_t rs, int off, unsigned tag, float a, float b) {
  __builtin_amdgcn_raw_buffer_store_b128(v4u{__float_as_uint(a), tag, __float_as_uint(b), tag}, rs, off, 0, AUX_SC1);
}
__device__ __forceinline__ v2u ldg(__amdgpu_buffer_rsrc_t rs, int off) { return __builtin_amdgcn_raw_buffer_load_b64(rs, off, 0, AUX_SC1); }
template <int N, int STEP, int SLEEP> __device__ __forceinline__ void sweep(__amdgpu_buffer_rsrc_t rs, int off, unsigned tag, v2u (&x)[N]) {
#pragma unroll
  for (int k = 0; k < N; ++k) x[k] = ldg(rs, off + k * STEP);
  SB();
  bool ok = true;
#pragma unroll
  for (int k = 0; k < N; ++k) ok &= x[k].y == tag;
  if (__builtin_expect(__all(ok), 1)) return;
#pragma nounroll
  for (unsigned spins = 0; spins < SPIN_CAP; ++spins) {
    __builtin_amdgcn_s_sleep(SLEEP);
    v2u y[N];
#pragma unroll
    for (int k = 0; k < N; ++k) y[k] = ldg(rs, x[k].y == tag ? OOB : off + k * STEP);
    SB();
    ok = true;
#pragma unroll
    for (int k = 0; k < N; ++k) { x[k] = x[k].y == tag ? x[k] : y[k]; ok &= x[k].y == tag; }
    if (__all(ok)) break;
  }
}
template <int STEP> __device__ __forceinline__ void pass4(v4u (&y)[4], const v4u (&x)[4], int off, v4u rs, unsigned tag) {
  int o[4];
#pragma unroll
  for (int k = 0; k < 4; ++k) o[k] = (x[k].y == tag && x[k].w == tag) ? OOB : off + k * STEP;
  asm volatile("buffer_load_dwordx4 %0, %4, %8, 0 offen sc1\n\t"
               "buffer_load_dwordx4 %1, %5, %8, 0 offen sc1\n\t"
               "buffer_load_dwordx4 %2, %6, %8, 0 offen sc1\n\t"
               "buffer_load_dwordx4 %3, %7, %8, 0 offen sc1"
               : "=&v"(y[0]), "=&v"(y[1]), "=&v"(y[2]), "=&v"(y[3]) : "v"(o[0]), "v"(o[1]), "v"(o[2]), "v"(o[3]), "s"(rs) : "memory");
}
__device__ __forceinline__ bool take4(v4u (&x)[4], v4u (&y)[4], unsigned tag) {
  asm volatile("s_waitcnt vmcnt(4)" : "+v"(y[0]), "+v"(y[1]), "+v"(y[2]), "+v"(y[3]));
  bool ok = true;
#pragma unroll
  for (int k = 0; k < 4; ++k) {
    const bool have = x[k].y == tag && x[k].w == tag;
    x[k] = have ? x[k] : y[k];
    ok &= x[k].y == tag && x[k].w == tag;
  }
  return __all(ok);
}
__device__ __forceinline__ void pause(int ticks);
template <int STEP, int GAP> __device__ __forceinline__ void sweep2(v4u rs, int off, unsigned tag, v4u (&x)[4], v4u (&ya)[4], v4u (&yb)[4]) {
#pragma unroll
  for (int k = 0; k < 4; ++k) x[k] = v4u{0u, ~tag, 0u, ~tag};
  pass4<STEP>(ya, x, off, rs, tag);
  __builtin_amdgcn_s_sleep(GAP);
#pragma nounroll
  for (unsigned spins = 0; spins < SPIN_CAP; ++spins) {
    pass4<STEP>(yb, x, off, rs, tag);
    if (take4(x, ya, tag)) break;
    pass4<STEP>(ya, x, off, rs, tag);
    if (take4(x, yb, tag)) break;
  }
}
#define KEEP8(A, B) asm volatile("s_waitcnt vmcnt(0)" :: "v"(A[0]), "v"(A[1]), "v"(A[2]), "v"(A[3]), "v"(B[0]), "v"(B[1]), "v"(B[2]), "v"(B[3]) : "memory")
__device__ __forceinline__ v4u mkrs(const void* p, unsigned bytes) {
  const unsigned long long u = (unsigned long long)p;
  v4u r = {(unsigned)u, (unsigned)(u >> 32) & 0xffffu, bytes, (unsigned)RSRC3};
  return r;
}
__device__ __forceinline__ void tile_load(v4u (&d)[4], int voff, v4u rs, int soff) {
  asm volatile("buffer_load_dwordx4 %0, %4, %5, %6 offen nt\n\t"
               "buffer_load_dwordx4 %1, %4, %5, %7 offen nt\n\t"
               "buffer_load_dwordx4 %2, %4, %5, %8 offen nt\n\t"
               "buffer_load_dwordx4 %3, %4, %5, %9 offen nt"
               : "=&v"(d[0]), "=&v"(d[1]), "=&v"(d[2]), "=&v"(d[3]) : "v"(voff), "s"(rs), "s"(soff), "s"(soff + JST), "s"(soff + 2 * JST), "s"(soff + 3 * JST));
}
template <int N> __device__ __forceinline__ void tile_wait(v4u (&d)[4]) {
  asm volatile("s_waitcnt vmcnt(%4)" : "+v"(d[0]), "+v"(d[1]), "+v"(d[2]), "+v"(d[3]) : "i"(N));
}
template <int N> __device__ __forceinline__ void piece_wait(v4u& d) { asm volatile("s_waitcnt vmcnt(%1)" : "+v"(d) : "i"(N)); }
__device__ __forceinline__ void pause(int ticks) {
  unsigned due;
  asm volatile("s_memrealtime vcc\n\t"
               "s_waitcnt lgkmcnt(0)\n\t"
               "s_add_i32 %0, vcc_lo, %1\n"
               ".Lg0_%=:\n\t"
               "s_sleep 1\n\t"
               "s_memrealtime vcc\n\t"
               "s_waitcnt lgkmcnt(0)\n\t"
               "s_sub_i32 vcc_lo, vcc_lo, %0\n\t"
               "s_cmp_lt_i32 vcc_lo, 0\n\t"
               "s_cbranch_scc1 .Lg0_%="
               : "=&s"(due) : "i"(ticks) : "scc", "vcc", "memory");
}
__device__ __forceinline__ void gate_next(unsigned& due) {
  asm volatile("s_branch .Lg1_%=\n"
               ".Lg0_%=:\n\t"
               "s_sleep 1\n"
               ".Lg1_%=:\n\t"
               "s_memrealtime vcc\n\t"
               "s_waitcnt lgkmcnt(0)\n\t"
               "s_lshl_b32 vcc_lo, vcc_lo, 4\n\t"
               "s_sub_i32 vcc_lo, vcc_lo, %0\n\t"
               "s_cmp_lt_i32 vcc_lo, 0\n\t"
               "s_cbranch_scc1 .Lg0_%=\n\t"
               "s_setprio 0\n\t"
               "s_cmp_lt_i32 vcc_lo, %1\n\t"
               "s_cbranch_scc1 .Lg2_%=\n\t"
               "s_setprio 1\n"
               ".Lg2_%=:\n\t"
               "s_sub_i32 vcc_lo, vcc_lo, %2\n\t"
               "s_max_i32 vcc_lo, vcc_lo, 0\n\t"
               "s_add_i32 %0, %0, vcc_lo\n\t"
               "s_add_i32 %0, %0, %3"
               : "+s"(due) : "i"(PTH16), "i"(SLK16), "i"(DT16) : "scc", "vcc", "memory");
}
__device__ __forceinline__ void gate2(unsigned& line, unsigned& nxt) {
  asm volatile("s_sub_i32 vcc_lo, %1, %0\n\t"
               "s_max_i32 vcc_lo, vcc_lo, 0\n\t"
               "s_add_i32 vcc_hi, %0, vcc_lo\n\t"
               "s_mov_b32 %1, vcc_hi\n\t"
               "s_branch .Lg1_%=\n"
               ".Lg0_%=:\n\t"
               "s_sleep 1\n"
               ".Lg1_%=:\n\t"
               "s_memrealtime vcc\n\t"
               "s_waitcnt lgkmcnt(0)\n\t"
               "s_lshl_b32 vcc_lo, vcc_lo, 4\n\t"
               "s_sub_i32 vcc_hi, vcc_lo, %1\n\t"
               "s_cmp_lt_i32 vcc_hi, 0\n\t"
               "s_cbranch_scc1 .Lg0_%=\n\t"
               "s_sub_i32 vcc_hi, vcc_hi, %4\n\t"
               "s_max_i32 vcc_hi, vcc_hi, 0\n\t"
               "s_add_i32 %1, %1, vcc_hi\n\t"
               "s_add_i32 %1, %1, %3\n\t"
               "s_add_i32 %0, %0, %2"
               : "+s"(line), "+s"(nxt) : "i"(DT16), "i"(DF16), "i"(JIT16) : "scc", "vcc", "memory");
}
__device__ __forceinline__ void lds_barrier() {
  asm volatile("s_waitcnt lgkmcnt(0)" ::: "memory");
  __builtin_amdgcn_s_barrier();
  asm volatile("" ::: "memory");
}
__device__ __forceinline__ void lds_arrive(unsigned* c, int lane) {
  asm volatile("s_waitcnt lgkmcnt(0)" ::: "memory");
  if (lane == 0) __hip_atomic_fetch_add(c, 1u, RLX_WG);
}
template <int SLEEP> __device__ __forceinline__ unsigned lds_wait(unsigned* c, unsigned want) {
  unsigned v = 0;
#pragma nounroll
  for (unsigned spins = 0; spins < LSPIN_CAP; ++spins) {
    v = __builtin_amdgcn_readfirstlane(__hip_atomic_load(c, RLX_WG));
    if (v >= want) break;
    __builtin_amdgcn_s_sleep(SLEEP);
  }
  asm volatile("" ::: "memory");
  return v;
}
__device__ __forceinline__ unsigned lds_addr(const void* p) { return (unsigned)(size_t)(const LAS void*)p; }
template <int SLEEP> __device__ __forceinline__ v2u lds_take2(unsigned addr) {
  v2u v;
#pragma nounroll
  for (unsigned spins = 0; spins < LSPIN_CAP; ++spins) {
    asm volatile("ds_read_b64 %0, %1\n\ts_waitcnt lgkmcnt(0)" : "=&v"(v) : "v"(addr) : "memory");
    if (__all(v.x != EMPTY_O && v.y != EMPTY_O)) break;
    if (SLEEP) __builtin_amdgcn_s_sleep(SLEEP);
  }
  return v;
}
#define STAMP(i) do {} while (0)
#define STAMP_OUT() do {} while (0)

constexpr int L_O = 0;
constexpr int L_ML = L_O + 4096;
constexpr int L_QP = L_ML + 64;
constexpr int L_Q = L_QP + 256;
constexpr int L_OB = L_Q + 256;
constexpr int L_CNT = L_OB + 32;
constexpr int L_END = L_CNT + 16;

struct St {
  v4u T[32][4];
  float Sc[8][4];
  unsigned P[8][4];
  v4u Q, mask;
  f32x4 O;
  v4u rs[4];
  int vk, vv, so;
  unsigned tb, nx, adj;
  float* lds;
  int lane, wave;
};

template <int I> __device__ __forceinline__ void request(St& z) {
  constexpr int lim = I < NLIM ? LIM : LIMB;
  if constexpr (4 * I > lim) asm volatile("s_waitcnt vmcnt(%0)" :: "i"(lim) : "memory");
  gate_next(z.tb);
  if constexpr (I >= PRE && I < 31) z.tb += z.adj;
  tile_load(z.T[I], (I & 8) ? z.vv : z.vk, z.rs[I >> 3], z.so + (I & 7) * 32768);
  SB();
}
template <int I, int W> __device__ __forceinline__ void consume(St& z) {
  const int g = z.lane >> 4, n = z.lane & 15;
  constexpr int br = I >> 4;
  if constexpr ((I & 15) == 0) {
    if constexpr (br) lds_wait<1>((unsigned*)(z.lds + L_CNT) + 7, 1u);
    z.Q = *(const v4u*)((const unsigned short*)(z.lds + L_Q) + br * 256 + (n >> 3) * 128 + 32 * g + 8 * (n & 3));
  }
  if constexpr ((I & 8) == 0) {
    tile_wait<W>(z.T[I]);
#pragma unroll
    for (int j = 0; j < 4; ++j) {
      const f32x4 d = mfma(z.T[I][j], z.Q, f32x4{0.f, 0.f, 0.f, 0.f});
      z.Sc[I & 7][j] = (dpp<0x00>(d.x) + dpp<0x55>(d.y)) + (dpp<0xAA>(d.z) + dpp<0xFF>(d.w));
    }
    if constexpr ((I & 7) == 7) {
      float m = z.Sc[0][0];
#pragma unroll
      for (int t = 0; t < 8; ++t)
#pragma unroll
        for (int j = 0; j < 4; ++j) m = fmax_(m, z.Sc[t][j]);
      const float m2 = xg_max(m) * C2;
      float l = 0.f;
#pragma unroll
      for (int t = 0; t < 8; ++t) {
        float p[4];
#pragma unroll
        for (int j = 0; j < 4; ++j) { p[j] = __builtin_amdgcn_exp2f(fmaf(z.Sc[t][j], C2, -m2)); z.P[t][j] = dup_bf16(p[j]); }
        l += (p[0] + p[1]) + (p[2] + p[3]);
        asm volatile("" : "+v"(z.P[t][0]), "+v"(z.P[t][1]), "+v"(z.P[t][2]), "+v"(z.P[t][3]));
      }
      l = xg_sum(l);
      if (g == 0 && (n & 7) == 0) { z.lds[L_ML + br * 32 + (n >> 3) * 8 + z.wave] = m2; z.lds[L_ML + br * 32 + 16 + (n >> 3) * 8 + z.wave] = l; }
      lds_arrive((unsigned*)(z.lds + L_CNT) + 2 + br, z.lane);
      z.O = f32x4{0.f, 0.f, 0.f, 0.f};
    } else {
      asm volatile("" : "+v"(z.Sc[I & 7][0]), "+v"(z.Sc[I & 7][1]), "+v"(z.Sc[I & 7][2]), "+v"(z.Sc[I & 7][3]));
    }
  } else if constexpr (W == 0) {
    v4u A[4];
#pragma unroll
    for (int j = 0; j < 4; ++j) A[j] = diag(z.P[I & 7][j], z.mask);
    float* dst = z.lds + L_O + br * 2048 + z.wave * 256 + (g >> 1) * 128 + 8 * n + 4 * (g & 1);
    unsigned da = lds_addr(dst);
    asm volatile("" : "+v"(A[0]), "+v"(A[1]), "+v"(A[2]), "+v"(A[3]), "+v"(da));
    piece_wait<3>(z.T[I][0]); z.O = mfma(A[0], z.T[I][0], z.O); SB();
    piece_wait<2>(z.T[I][1]); z.O = mfma(A[1], z.T[I][1], z.O); SB();
    piece_wait<1>(z.T[I][2]); z.O = mfma(A[2], z.T[I][2], z.O); SB();
    piece_wait<0>(z.T[I][3]); z.O = mfma(A[3], z.T[I][3], z.O);
    *(LAS f32x4*)(size_t)da = z.O;
  } else {
    tile_wait<W>(z.T[I]);
#pragma unroll
    for (int j = 0; j < 4; ++j) z.O = mfma(diag(z.P[I & 7][j], z.mask), z.T[I][j], z.O);
    asm volatile("" : "+v"(z.O));
    if constexpr ((I & 7) == 7) *(f32x4*)(z.lds + L_O + br * 2048 + z.wave * 256 + (g >> 1) * 128 + 8 * n + 4 * (g & 1)) = z.O;
  }
  SB();
}
template <int I, int N> struct Run {
  static __device__ __forceinline__ void go(St& z) { request<I>(z); consume<I - DEP + 1, 4 * (DEP - 1)>(z); Run<I + 1, N>::go(z); }
};
template <int N> struct Run<N, N> { static __device__ __forceinline__ void go(St&) {} };
template <int I, int N> struct Req { static __device__ __forceinline__ void go(St& z) { request<I>(z); Req<I + 1, N>::go(z); } };
template <int N> struct Req<N, N> { static __device__ __forceinline__ void go(St&) {} };
template <int I, int N, int LAST> struct Con {
  static __device__ __forceinline__ void go(St& z) { consume<I, 4 * (LAST - I)>(z); Con<I + 1, N, LAST>::go(z); }
};
template <int N, int LAST> struct Con<N, N, LAST> { static __device__ __forceinline__ void go(St&) {} };

typedef unsigned v16u __attribute__((ext_vector_type(16)));
typedef unsigned v8u __attribute__((ext_vector_type(8)));
__device__ __forceinline__ unsigned char* ptr64(unsigned lo, unsigned hi) { return (unsigned char*)(((unsigned long long)hi << 32) | lo); }

__global__ void __launch_bounds__(NTHR, 3) gqa2_fused(Args) {
  __shared__ __attribute__((aligned(16))) float lds[L_END];
  unsigned* const l_cnt = (unsigned*)(lds + L_CNT);
  v16u ka; v8u kb;
  asm volatile("s_load_dwordx16 %0, %2, 0x0\n\ts_load_dwordx8 %1, %2, 0x40" : "=&s"(ka), "=&s"(kb) : "s"(__builtin_amdgcn_kernarg_segment_ptr()));
  const int tid = threadIdx.x, lane = tid & 63, wave = __builtin_amdgcn_readfirstlane(tid >> 6);
  const int g = lane >> 4, n = lane & 15;
  const int wg = blockIdx.x;
  const int ch = (wg & 7) << 5 | wg >> 3;
  unsigned cw;
  asm volatile("s_cmp_lg_u32 %2, 0\n\ts_cbranch_scc1 .Lc%=\n\ts_cmp_gt_u32 %3, 2\n\ts_cbranch_scc1 .Lc%=\n\t"
               "s_getpc_b64 vcc\n\tglobal_load_dword %0, %1, vcc\n.Lc%=:"
               : "=v"(cw) : "v"(min((tid - 64) * 128, CODEKB * 1024)), "s"((((wg >> 3) ^ wg) & 7) | (wg >> CODEWARM)), "s"((unsigned)(wave - 1)) : "scc", "vcc");
  const int kvh = ch >> 4, rb = (ch >> 3) & 1, s8 = ch & 7;
  {
    const v4u e = {EMPTY_O, EMPTY_O, EMPTY_O, EMPTY_O};
    const int i = tid < 512 ? tid : 0;
    *(v4u*)(lds + L_O + i * 4) = e; *(v4u*)(lds + L_O + 2048 + i * 4) = e;
    const int j = tid < 48 ? tid : 0;
    ((unsigned*)(lds + L_OB))[j] = j < 32 ? EMPTY_B : 0u;
  }
  asm volatile("s_waitcnt lgkmcnt(0)" : "+s"(ka), "+s"(kb) :: "memory");
  const v4u rk0 = mkrs(ptr64(ka[4], ka[5]) + (size_t)ch * 262144, 262144);
  unsigned probe;
  asm volatile("s_cmp_lg_u32 %2, 0\n\ts_cbranch_scc1 .Lp%=\n\ts_mov_b64 exec, 1\n\tbuffer_load_dword %0, off, %1, 0 nt\n\ts_mov_b64 exec, -1\n.Lp%=:"
               : "=v"(probe) : "s"(rk0), "s"(wave) : "scc");
  __builtin_amdgcn_s_barrier();
  asm volatile("" ::: "memory");
  unsigned char* const ws = ptr64(kb[6], kb[7]);
  const unsigned char* const wop = rb ? ptr64(kb[2], kb[3]) : ptr64(kb[0], kb[1]);
  const __amdgpu_buffer_rsrc_t rw = __builtin_amdgcn_make_buffer_rsrc(ws, 0, WS_ST, RSRC3);
  v4u mask;
  {
    const unsigned half = (n & 1) ? 0xffff0000u : 0x0000ffffu; const int r = (n & 7) >> 1;
    mask.x = r == 0 ? half : 0u; mask.y = r == 1 ? half : 0u; mask.z = r == 2 ? half : 0u; mask.w = r == 3 ? half : 0u;
  }
  const int pidx = kvh * 8 + s8;
  if (__builtin_expect(wave >= 8, 1)) {
    asm volatile("" ::: "memory");
    __builtin_amdgcn_s_setprio(3);
    const int hw = wave - 8, cg = s8 & 3, kh = s8 >> 2, sub = 2 * g + (n >> 3);
    const __amdgpu_buffer_rsrc_t rq = __builtin_amdgcn_make_buffer_rsrc((rb ? ptr64(ka[14], ka[15]) : ptr64(ka[12], ka[13])) + ((size_t)(kh * 288 + hw * 144) * NQ + kvh * 256 + cg * 64) * 2, 0, 143 * NQ * 2 + 128, RSRC3);
    const __amdgpu_buffer_rsrc_t rx = __builtin_amdgcn_make_buffer_rsrc((rb ? ptr64(ka[2], ka[3]) : ptr64(ka[0], ka[1])) + (kh * 288 + hw * 144) * 2, 0, 144 * 2, RSRC3);
    v4u wqv[18];
#pragma unroll
    for (int i = 0; i < 18; ++i) wqv[i] = __builtin_amdgcn_raw_buffer_load_b128(rq, (ABL & 4) ? OOB : sub * (NQ * 2) + (n & 7) * 16, i * 8 * NQ * 2, AUX_NT);
    const unsigned xa = __builtin_amdgcn_raw_buffer_load_b32(rx, n * 16 + g * 4, 0, 0), xb = __builtin_amdgcn_raw_buffer_load_b32(rx, n * 16 + g * 4 + 256, 0, 0);
    const unsigned ep = __builtin_amdgcn_raw_buffer_load_b32(rw, lane == 0 ? WS_EPOCH : OOB, 0, AUX_SC1);
    SB();
    f32x4 a0 = {0.f, 0.f, 0.f, 0.f}, a1 = {0.f, 0.f, 0.f, 0.f};
    const unsigned sel = n < 8 ? 0x01000100u : 0x03020302u;
#define QSTEP(acc, i, src, k) acc = mfma(diag(__builtin_amdgcn_perm(0u, (unsigned)__builtin_amdgcn_mov_dpp((int)(src), 0x150 + (k), 0xf, 0xf, true), sel), mask), wqv[i], acc)
    QSTEP(a0, 0, xa, 0); QSTEP(a1, 1, xa, 1); QSTEP(a0, 2, xa, 2); QSTEP(a1, 3, xa, 3); QSTEP(a0, 4, xa, 4); QSTEP(a1, 5, xa, 5);
    QSTEP(a0, 6, xa, 6); QSTEP(a1, 7, xa, 7); QSTEP(a0, 8, xa, 8); QSTEP(a1, 9, xa, 9); QSTEP(a0, 10, xa, 10); QSTEP(a1, 11, xa, 11);
    QSTEP(a0, 12, xa, 12); QSTEP(a1, 13, xa, 13); QSTEP(a0, 14, xa, 14); QSTEP(a1, 15, xa, 15); QSTEP(a0, 16, xb, 0); QSTEP(a1, 17, xb, 1);
    if ((g >> 1) == (n >> 3)) *(f32x4*)(lds + L_QP + (hw * 2 + (n >> 3)) * 64 + 8 * (n & 7) + 4 * (g & 1)) = a0 + a1;
    const unsigned tag = __builtin_amdgcn_readfirstlane(ep) + TAG_STEP;
    STAMP(1);
    lds_arrive(l_cnt + 0, lane);
    unsigned anc = 0;
    if (hw == 0) {
      lds_wait<1>(l_cnt + 0, 2u);
      const float q = (lds[L_QP + lane] + lds[L_QP + 64 + lane]) + (lds[L_QP + 128 + lane] + lds[L_QP + 192 + lane]);
      st_gran(ws, WS_QG + ((rb * 16 + kvh) * 512 + kh * 256 + cg * 64 + lane) * 8, tag, q);
      if (lane == 0) l_cnt[5] = tag;
      anc = lds_wait<1>(l_cnt + 6, 1u);
      if (lane == 0) __hip_atomic_store((gu64*)(ws + WS_AG + wg * 8), ((unsigned long long)tag << 32) | anc, RLX_AGENT);
    }
    STAMP(2);
    pause(QGA);
    if (hw) pause(QGB - QGA);
    {
      v2u x[8];
      const v2u ag = ldg(rw, (hw == 0 && lane < 8) ? WS_AG + (wg & ~7) * 8 + lane * 8 : OOB);
      sweep<8, 512, 8>(rw, WS_QG + ((hw * 16 + kvh) * 512 + lane) * 8, tag, x);
#pragma unroll
      for (int k = 0; k < 4; ++k) ((unsigned short*)(lds + L_Q))[hw * 256 + k * 64 + lane] = f2bf(__uint_as_float(x[k].x) + __uint_as_float(x[k + 4].x));
      if (hw == 0) {
        int d = ag.y == tag ? (int)(ag.x - anc) : 0;
        d = d > 1600 ? 1600 : d < -1600 ? -1600 : d;
        d += __builtin_amdgcn_mov_dpp(d, XOR1, 0xf, 0xf, true); d += __builtin_amdgcn_mov_dpp(d, XOR2, 0xf, 0xf, true); d += __builtin_amdgcn_mov_dpp(d, HMIR, 0xf, 0xf, true);
        d /= 8 * (31 - PRE);
        if (lane == 0) l_cnt[8] = (unsigned)d;
      }
      lds_arrive(hw ? l_cnt + 7 : l_cnt + 1, lane);
    }
    STAMP(3);
#pragma nounroll
    for (int br = 0; br < 2; ++br) {
      lds_wait<8>(l_cnt + 2 + br, 8u);
      const float* ml = lds + L_ML + br * 32 + hw * 8;
      const f32x4 m0 = *(const f32x4*)(ml), m1 = *(const f32x4*)(ml + 4), l0 = *(const f32x4*)(ml + 16), l1 = *(const f32x4*)(ml + 20);
      const float M = fmax_(fmax_(fmax_(m0.x, m0.y), fmax_(m0.z, m0.w)), fmax_(fmax_(m1.x, m1.y), fmax_(m1.z, m1.w)));
      float L = 0.f;
#pragma unroll
      for (int w = 0; w < 8; ++w) L = fmaf(__builtin_amdgcn_exp2f((w < 4 ? m0[w & 3] : m1[w & 3]) - M), w < 4 ? l0[w & 3] : l1[w & 3], L);
      const int mg = WS_MG + ((br * 16 + kvh) * 2 + hw) * 1024;
      if (lane < 8) st_gran(ws, mg + (ch & 15) * 64 + lane * 8, tag, (lane & 1) ? L : M);
      pause(MLD);
      v2u x[1];
      sweep<1, 0, 8>(rw, mg + n * 64 + (g & 1) * 8, tag, x);
      auto sw = __builtin_amdgcn_permlane16_swap(x[0].x, x[0].x, false, false);
      const float Ms = __uint_as_float(sw[0]), Ls = __uint_as_float(sw[1]);
      float Mx = fmax_(Ms, dpp<XOR1>(Ms)); Mx = fmax_(Mx, dpp<XOR2>(Mx)); Mx = fmax_(Mx, dpp<HMIR>(Mx)); Mx = fmax_(Mx, dpp<MIR>(Mx));
      float Lt = __builtin_amdgcn_exp2f(Ms - Mx) * Ls; Lt += dpp<XOR1>(Lt); Lt += dpp<XOR2>(Lt); Lt += dpp<HMIR>(Lt); Lt += dpp<MIR>(Lt);
      const float inv = 1.0f / Lt;
      float c[8];
#pragma unroll
      for (int w = 0; w < 8; ++w) c[w] = __builtin_amdgcn_exp2f((w < 4 ? m0[w & 3] : m1[w & 3]) - Mx) * inv;
      const unsigned oa = lds_addr(lds + L_O + br * 2048 + hw * 128 + 2 * lane);
      int dst = WS_PG + (br * 256 + ch) * PG_ROW + (hw * 128 + 2 * lane) * 8;
      asm volatile("" : "+v"(dst));
      float o0 = 0.f, o1 = 0.f;
      { const v2u v = lds_take2<8>(oa); o0 = c[0] * __uint_as_float(v.x); o1 = c[0] * __uint_as_float(v.y); }
#pragma unroll
      for (int w = 1; w < 8; ++w) { const v2u v = lds_take2<0>(oa + w * 1024); o0 = fmaf(c[w], __uint_as_float(v.x), o0); o1 = fmaf(c[w], __uint_as_float(v.y), o1); }
      st_gran2(rw, dst, tag, o0, o1);
      if (hw == 0 && lane == 0) __hip_atomic_store(l_cnt + 4, (unsigned)br + 1u, RLX_WG);
    }
    STAMP(8);
  } else {
    if (wave == 0) {
      unsigned long long ta;
      asm volatile("s_waitcnt vmcnt(0)\n\ts_memrealtime %0\n\ts_waitcnt lgkmcnt(0)" : "=s"(ta) : "v"(probe) : "memory");
      if (lane == 0) __hip_atomic_store(l_cnt + 6, ((unsigned)ta << 4) | 1u, RLX_WG);
    }
    St z;
    const int phs = PAT == 2 ? (wg >> 3) & 7 : wave;
    z.lds = lds; z.lane = lane; z.wave = wave; z.mask = mask;
    z.rs[0] = rk0;
    z.rs[1] = mkrs(ptr64(ka[8], ka[9]) + (size_t)ch * 262144, 262144);
    z.rs[2] = mkrs(ptr64(ka[6], ka[7]) + (size_t)ch * 262144, 262144);
    z.rs[3] = mkrs(ptr64(ka[10], ka[11]) + (size_t)ch * 262144, 262144);
    z.vk = (ABL & 8) ? lane * 16 : ((lane >> 2) & 3) * 256 + g * 64 + (lane & 3) * 16; z.vv = lane * 16; z.so = wave * WST;
    GAS unsigned short* outp = (GAS unsigned short*)ptr64(kb[4], kb[5]);
    asm volatile("s_waitcnt vmcnt(0)" :: "v"(cw) : "memory");
    z.tb = lds_wait<1>(l_cnt + 6, 1u) + (unsigned)(OFF16 + ((phs * DT16) >> 3));
    z.nx = z.tb;
    STAMP(1);
    Req<0, PRE>::go(z);
    STAMP(4);
    lds_wait<1>(l_cnt + 1, 1u);
    const unsigned tag = __builtin_amdgcn_readfirstlane(l_cnt[5]);
    z.adj = __builtin_amdgcn_readfirstlane(l_cnt[8]);
    STAMP(5);
    Con<0, PRE - DEP + 1, PRE - 1>::go(z);
    Run<PRE, 17>::go(z);
    STAMP(14);
    Run<17, 25>::go(z);
    STAMP(15);
    Run<25, 32>::go(z);
    STAMP(6);
    Con<32 - DEP + 1, 32, 31>::go(z);
    STAMP(7);
    const unsigned oba = lds_addr(lds + L_OB + g * 8);
    if (wave < 5) {
      const __amdgpu_buffer_rsrc_t ro = __builtin_amdgcn_make_buffer_rsrc((unsigned char*)wop + ((size_t)(kvh * 256 + s8 * 32) * DM) * 2, 0, 32 * DM * 2, RSRC3);
      const int wcol = wave * 128 + n * 8;
      const int vwo = (!NOWO && wcol < DM) ? g * (DM * 2) + wcol * 2 : OOB;
      v4u Wo[8];
      z.tb += (unsigned)((wave < NW1 ? wave * WOS16 : WG16 + (wave - NW1) * WOSB16) - ((phs * DT16) >> 3));
      gate_next(z.tb);
#pragma unroll
      for (int i = 0; i < 8; ++i) Wo[i] = __builtin_amdgcn_raw_buffer_load_b128(ro, vwo, i * 4 * DM * 2, AUX_NT);
      int dst = wcol < DM ? WS_YG + (rb * 128 + pidx) * YG_ROW + (wcol + 4 * (g & 1) + 2 * (g >> 1)) * 8 : OOB;
      asm volatile("" : "+v"(dst), "+v"(Wo[0]), "+v"(Wo[1]), "+v"(Wo[2]), "+v"(Wo[3]), "+v"(Wo[4]), "+v"(Wo[5]), "+v"(Wo[6]), "+v"(Wo[7]));
      STAMP(2);
      v4u oa, ob;
#pragma nounroll
      for (unsigned spins = 0; spins < LSPIN_CAP; ++spins) {
        asm volatile("ds_read_b128 %0, %2\n\tds_read_b128 %1, %2 offset:16\n\ts_waitcnt lgkmcnt(0)" : "=&v"(oa), "=&v"(ob) : "v"(oba) : "memory");
        if (__all(oa.x != EMPTY_B && oa.y != EMPTY_B && oa.z != EMPTY_B && oa.w != EMPTY_B && ob.x != EMPTY_B && ob.y != EMPTY_B && ob.z != EMPTY_B && ob.w != EMPTY_B)) break;
      }
      STAMP(10);
      f32x4 Y0 = {0.f, 0.f, 0.f, 0.f}, Y1 = {0.f, 0.f, 0.f, 0.f};
#pragma unroll
      for (int i = 0; i < 4; ++i) { Y0 = mfma(diag(oa[i], mask), Wo[i], Y0); Y1 = mfma(diag(ob[i], mask), Wo[4 + i], Y1); }
      const f32x4 Y = Y0 + Y1;
      st_gran2(rw, dst, tag, g < 2 ? Y.x : Y.z, g < 2 ? Y.y : Y.w);
      STAMP(11);
    } else if (wave == 5) {
      const int po = WS_PG + (rb * 256 + kvh * 16 + (lane >> 5)) * PG_ROW + (32 * s8 + (lane & 31)) * 8;
      v2u x[8];
      lds_wait<0>(l_cnt + 4, (unsigned)rb + 1u);
      if (rb) pause(H1D);
      sweep<8, 2 * PG_ROW, 1>(rw, po, tag, x);
      STAMP(9);
      const float o = x32_sum(((__uint_as_float(x[0].x) + __uint_as_float(x[1].x)) + (__uint_as_float(x[2].x) + __uint_as_float(x[3].x)))
                            + ((__uint_as_float(x[4].x) + __uint_as_float(x[5].x)) + (__uint_as_float(x[6].x) + __uint_as_float(x[7].x))));
      if (lane < 32) ((unsigned*)(lds + L_OB))[(lane & 3) * 8 + (lane >> 2)] = dup_bf16(o);
    } else if (wave == 6 || pidx < 16) {
      const int pos = wave == 6 ? pidx * 4 : 512 + pidx * 4;
      const int so = WS_YG + (rb * 128 + (lane >> 2)) * YG_ROW + (pos + (lane & 3)) * 8;
      const int col = pos + (lane & 3);
      GAS unsigned short* op = outp + rb * DM + col;
      asm volatile("" : "+v"(op));
      v2u x[8];
      z.tb += (unsigned)(WG16 + (4 - NW1) * WOSB16 + H2G16 - ((phs * DT16) >> 3));
#pragma nounroll
      for (unsigned spins = 0; spins < LSPIN_CAP; ++spins) {
        if (__builtin_amdgcn_readfirstlane(__hip_atomic_load((unsigned*)(lds + L_OB), RLX_WG)) != EMPTY_B) break;
        __builtin_amdgcn_s_sleep(1);
      }
      pause(H2D);
      gate_next(z.tb);
      sweep<8, 16 * YG_ROW, 1>(rw, so, tag, x);
      STAMP(12);
      float y = ((__uint_as_float(x[0].x) + __uint_as_float(x[1].x)) + (__uint_as_float(x[2].x) + __uint_as_float(x[3].x)))
              + ((__uint_as_float(x[4].x) + __uint_as_float(x[5].x)) + (__uint_as_float(x[6].x) + __uint_as_float(x[7].x)));
      y += dpp<ROR4>(y); y += dpp<ROR8>(y);
      y = xg_sum(y);
      if (lane < 4) *op = f2bf(y);
      if (rb == 0 && pos == 0 && lane == 0) __hip_atomic_store((gu32*)(ws + WS_EPOCH), tag, RLX_AGENT);
      STAMP(13);
    }
  }
  STAMP_OUT();
}

extern "C" void kernel_launch(void* const* d_in, const int* in_sizes, int n_in, void* d_out, int out_size, void* d_ws, size_t ws_size, hipStream_t stream) {
  Args a;
  a.x[0] = d_in[0]; a.k[0] = d_in[1]; a.v[0] = d_in[2]; a.wq[0] = d_in[3]; a.wo[0] = d_in[4];
  a.x[1] = d_in[5]; a.k[1] = d_in[6]; a.v[1] = d_in[7]; a.wq[1] = d_in[8]; a.wo[1] = d_in[9];
  a.out = d_out; a.ws = d_ws;
  gqa2_fused<<<NWG, NTHR, 0, stream>>>(a);
}
```

```cpp
#define OFF16 700
#define DT16 1784
#include <hip/hip_runtime.h>
#include <cstdint>

typedef unsigned v4u __attribute__((ext_vector_type(4)));
typedef unsigned v2u __attribute__((ext_vector_type(2)));
typedef float f32x4 __attribute__((ext_vector_type(4)));
typedef __bf16 bf16x8 __attribute__((ext_vector_type(8)));
typedef __bf16 bf16x2 __attribute__((ext_vector_type(2)));
typedef float f32x2 __attribute__((ext_vector_type(2)));
#define GAS __attribute__((address_space(1)))
#define LAS __attribute__((address_space(3)))
typedef GAS unsigned gu32;
typedef GAS unsigned long long gu64;
#define RLX_AGENT __ATOMIC_RELAXED, __HIP_MEMORY_SCOPE_AGENT
#define RLX_WG __ATOMIC_RELAXED, __HIP_MEMORY_SCOPE_WORKGROUP

#define STAMPS 0
#define ANC -2
#define PRE 8
#define DEP 3
#define LIM 2
#define WO16 0
#define WOS16 600
#define QGD 75
#define PTH16 400
#define SLK16 100000
#define H1D 40
#define H2D 70
#define PREW 0
#define HBAR 1
#define PAT 1
#define JST 8192
#define WST 1024
#define SOFT16 0
#define NSOFT 6
#define H2G16 2000
#define WOA16 0
#define WOSA16 600
#define OUTSC 0
#define SLOWSTART 0
#define P016 2400
#define NLIM 32
#define LIMB 8
#define DF16 0
#define JIT16 160
#define ABL 0
#define QGA 280
#define QGB 600
#define MLD 350
#define PIPE 0
#define GAP1 8
#define GAP2 8
#define ALIGN 1
#define NW1 3
#define WG16 1800
#define WOSB16 600
#define CODEWARM 0
#define CODEKB 20
#define NOWO 0

constexpr int DM = 576, NQ = 4096;
constexpr int NWG = 256, NTHR = 640;
constexpr int AUX_NT = 2, AUX_SC1 = 16;
constexpr int RSRC3 = 0x00020000;
constexpr int OOB = 0x7fff0000;
constexpr int WS_EPOCH = 0;
constexpr int WS_AG = 1024;
constexpr int WS_QG = 4096;
constexpr int WS_MG = WS_QG + 32 * 4096;
constexpr int WS_PG = WS_MG + 64 * 1024;
constexpr int PG_ROW = 256 * 8;
constexpr int WS_YG = WS_PG + 512 * PG_ROW;
constexpr int YG_ROW = 576 * 8;
constexpr int WS_ST = WS_YG + 256 * YG_ROW;
constexpr unsigned TAG_STEP = 0x9E3779B9u;
constexpr unsigned SPIN_CAP = 1u << 17, LSPIN_CAP = 1u << 22;
constexpr float C2 = 0.088388347648318440f * 1.4426950408889634f;
constexpr unsigned EMPTY_O = 0xffffffffu, EMPTY_B = 0x0000ffffu;

struct Args {
  const void* x[2]; const void* k[2]; const void* v[2]; const void* wq[2]; const void* wo[2];
  void* out; void* ws;
};

#define SB() __builtin_amdgcn_sched_barrier(0)
__device__ __forceinline__ f32x4 mfma(v4u a, v4u b, f32x4 c) {
  return __builtin_amdgcn_mfma_f32_16x16x32_bf16(__builtin_bit_cast(bf16x8, a), __builtin_bit_cast(bf16x8, b), c, 0, 0, 0);
}
__device__ __forceinline__ unsigned dup_bf16(float f) {
  f32x2 v = {f, f};
  return __builtin_bit_cast(unsigned, __builtin_convertvector(v, bf16x2));
}
__device__ __forceinline__ unsigned short f2bf(float f) { return __builtin_bit_cast(unsigned short, (__bf16)f); }
__device__ __forceinline__ v4u diag(unsigned pp, v4u mask) { v4u r = {pp & mask.x, pp & mask.y, pp & mask.z, pp & mask.w}; return r; }
template <int CTRL> __device__ __forceinline__ float dpp(float x) {
  return __builtin_bit_cast(float, __builtin_amdgcn_mov_dpp(__builtin_bit_cast(int, x), CTRL, 0xf, 0xf, true));
}
constexpr int XOR1 = 0xB1, XOR2 = 0x4E, HMIR = 0x141, MIR = 0x140, ROR8 = 0x128, ROR4 = 0x124;
__device__ __forceinline__ float fmax_(float a, float b) { return __builtin_fmaxf(a, b); }
__device__ __forceinline__ float xg_max(float x) {
  auto s = __builtin_amdgcn_permlane16_swap(__float_as_uint(x), __float_as_uint(x), false, false);
  x = fmax_(__uint_as_float(s[0]), __uint_as_float(s[1]));
  auto t = __builtin_amdgcn_permlane32_swap(__float_as_uint(x), __float_as_uint(x), false, false);
  return fmax_(__uint_as_float(t[0]), __uint_as_float(t[1]));
}
__device__ __forceinline__ float x32_sum(float x) {
  auto t = __builtin_amdgcn_permlane32_swap(__float_as_uint(x), __float_as_uint(x), false, false);
  return __uint_as_float(t[0]) + __uint_as_float(t[1]);
}
__device__ __forceinline__ float xg_sum(float x) {
  auto s = __builtin_amdgcn_permlane16_swap(__float_as_uint(x), __float_as_uint(x), false, false);
  return x32_sum(__uint_as_float(s[0]) + __uint_as_float(s[1]));
}
__device__ __forceinline__ void st_gran(unsigned char* ws, int off, unsigned tag, float v) {
  __hip_atomic_store((gu64*)(ws + off), ((unsigned long long)tag << 32) | __float_as_uint(v), RLX_AGENT);
}
__device__ __forceinline__ void st_gran2(__amdgpu_buffer_rsrc_t rs, int off, unsigned tag, float a, float b) {
  __builtin_amdgcn_raw_buffer_store_b128(v4u{__float_as_uint(a), tag, __float_as_uint(b), tag}, rs, off, 0, AUX_SC1);
}
__device__ __forceinline__ v2u ldg(__amdgpu_buffer_rsrc_t rs, int off) { return __builtin_amdgcn_raw_buffer_load_b64(rs, off, 0, AUX_SC1); }
template <int N, int STEP, int SLEEP> __device__ __forceinline__ void sweep(__amdgpu_buffer_rsrc_t rs, int off, unsigned tag, v2u (&x)[N]) {
#pragma unroll
  for (int k = 0; k < N; ++k) x[k] = ldg(rs, off + k * STEP);
  SB();
  bool ok = true;
#pragma unroll
  for (int k = 0; k < N; ++k) ok &= x[k].y == tag;
  if (__builtin_expect(__all(ok), 1)) return;
#pragma nounroll
  for (unsigned spins = 0; spins < SPIN_CAP; ++spins) {
    __builtin_amdgcn_s_sleep(SLEEP);
    v2u y[N];
#pragma unroll
    for (int k = 0; k < N; ++k) y[k] = ldg(rs, x[k].y == tag ? OOB : off + k * STEP);
    SB();
    ok = true;
#pragma unroll
    for (int k = 0; k < N; ++k) { x[k] = x[k].y == tag ? x[k] : y[k]; ok &= x[k].y == tag; }
    if (__all(ok)) break;
  }
}
template <int STEP> __device__ __forceinline__ void pass4(v4u (&y)[4], const v4u (&x)[4], int off, v4u rs, unsigned tag) {
  int o[4];
#pragma unroll
  for (int k = 0; k < 4; ++k) o[k] = (x[k].y == tag && x[k].w == tag) ? OOB : off + k * STEP;
  asm volatile("buffer_load_dwordx4 %0, %4, %8, 0 offen sc1\n\t"
               "buffer_load_dwordx4 %1, %5, %8, 0 offen sc1\n\t"
               "buffer_load_dwordx4 %2, %6, %8, 0 offen sc1\n\t"
               "buffer_load_dwordx4 %3, %7, %8, 0 offen sc1"
               : "=&v"(y[0]), "=&v"(y[1]), "=&v"(y[2]), "=&v"(y[3]) : "v"(o[0]), "v"(o[1]), "v"(o[2]), "v"(o[3]), "s"(rs) : "memory");
}
__device__ __forceinline__ bool take4(v4u (&x)[4], v4u (&y)[4], unsigned tag) {
  asm volatile("s_waitcnt vmcnt(4)" : "+v"(y[0]), "+v"(y[1]), "+v"(y[2]), "+v"(y[3]));
  bool ok = true;
#pragma unroll
  for (int k = 0; k < 4; ++k) {
    const bool have = x[k].y == tag && x[k].w == tag;
    x[k] = have ? x[k] : y[k];
    ok &= x[k].y == tag && x[k].w == tag;
  }
  return __all(ok);
}
__device__ __forceinline__ void pause(int ticks);
template <int STEP, int GAP> __device__ __forceinline__ void sweep2(v4u rs, int off, unsigned tag, v4u (&x)[4], v4u (&ya)[4], v4u (&yb)[4]) {
#pragma unroll
  for (int k = 0; k < 4; ++k) x[k] = v4u{0u, ~tag, 0u, ~tag};
  pass4<STEP>(ya, x, off, rs, tag);
  __builtin_amdgcn_s_sleep(GAP);
#pragma nounroll
  for (unsigned spins = 0; spins < SPIN_CAP; ++spins) {
    pass4<STEP>(yb, x, off, rs, tag);
    if (take4(x, ya, tag)) break;
    pass4<STEP>(ya, x, off, rs, tag);
    if (take4(x, yb, tag)) break;
  }
}
#define KEEP8(A, B) asm volatile("s_waitcnt vmcnt(0)" :: "v"(A[0]), "v"(A[1]), "v"(A[2]), "v"(A[3]), "v"(B[0]), "v"(B[1]), "v"(B[2]), "v"(B[3]) : "memory")
__device__ __forceinline__ v4u mkrs(const void* p, unsigned bytes) {
  const unsigned long long u = (unsigned long long)p;
  v4u r = {(unsigned)u, (unsigned)(u >> 32) & 0xffffu, bytes, (unsigned)RSRC3};
  return r;
}
__device__ __forceinline__ void tile_load(v4u (&d)[4], int voff, v4u rs, int soff) {
  asm volatile("buffer_load_dwordx4 %0, %4, %5, %6 offen nt\n\t"
               "buffer_load_dwordx4 %1, %4, %5, %7 offen nt\n\t"
               "buffer_load_dwordx4 %2, %4, %5, %8 offen nt\n\t"
               "buffer_load_dwordx4 %3, %4, %5, %9 offen nt"
               : "=&v"(d[0]), "=&v"(d[1]), "=&v"(d[2]), "=&v"(d[3]) : "v"(voff), "s"(rs), "s"(soff), "s"(soff + JST), "s"(soff + 2 * JST), "s"(soff + 3 * JST));
}
template <int N> __device__ __forceinline__ void tile_wait(v4u (&d)[4]) {
  asm volatile("s_waitcnt vmcnt(%4)" : "+v"(d[0]), "+v"(d[1]), "+v"(d[2]), "+v"(d[3]) : "i"(N));
}
template <int N> __device__ __forceinline__ void piece_wait(v4u& d) { asm volatile("s_waitcnt vmcnt(%1)" : "+v"(d) : "i"(N)); }
__device__ __forceinline__ void pause(int ticks) {
  unsigned due;
  asm volatile("s_memrealtime vcc\n\t"
               "s_waitcnt lgkmcnt(0)\n\t"
               "s_add_i32 %0, vcc_lo, %1\n"
               ".Lg0_%=:\n\t"
               "s_sleep 1\n\t"
               "s_memrealtime vcc\n\t"
               "s_waitcnt lgkmcnt(0)\n\t"
               "s_sub_i32 vcc_lo, vcc_lo, %0\n\t"
               "s_cmp_lt_i32 vcc_lo, 0\n\t"
               "s_cbranch_scc1 .Lg0_%="
               : "=&s"(due) : "i"(ticks) : "scc", "vcc", "memory");
}
__device__ __forceinline__ void gate_next(unsigned& due) {
  asm volatile("s_branch .Lg1_%=\n"
               ".Lg0_%=:\n\t"
               "s_sleep 1\n"
               ".Lg1_%=:\n\t"
               "s_memrealtime vcc\n\t"
               "s_waitcnt lgkmcnt(0)\n\t"
               "s_lshl_b32 vcc_lo, vcc_lo, 4\n\t"
               "s_sub_i32 vcc_lo, vcc_lo, %0\n\t"
               "s_cmp_lt_i32 vcc_lo, 0\n\t"
               "s_cbranch_scc1 .Lg0_%=\n\t"
               "s_setprio 0\n\t"
               "s_cmp_lt_i32 vcc_lo, %1\n\t"
               "s_cbranch_scc1 .Lg2_%=\n\t"
               "s_setprio 1\n"
               ".Lg2_%=:\n\t"
               "s_sub_i32 vcc_lo, vcc_lo, %2\n\t"
               "s_max_i32 vcc_lo, vcc_lo, 0\n\t"
               "s_add_i32 %0, %0, vcc_lo\n\t"
               "s_add_i32 %0, %0, %3"
               : "+s"(due) : "i"(PTH16), "i"(SLK16), "i"(DT16) : "scc", "vcc", "memory");
}
__device__ __forceinline__ void gate2(unsigned& line, unsigned& nxt) {
  asm volatile("s_sub_i32 vcc_lo, %1, %0\n\t"
               "s_max_i32 vcc_lo, vcc_lo, 0\n\t"
               "s_add_i32 vcc_hi, %0, vcc_lo\n\t"
               "s_mov_b32 %1, vcc_hi\n\t"
               "s_branch .Lg1_%=\n"
               ".Lg0_%=:\n\t"
               "s_sleep 1\n"
               ".Lg1_%=:\n\t"
               "s_memrealtime vcc\n\t"
               "s_waitcnt lgkmcnt(0)\n\t"
               "s_lshl_b32 vcc_lo, vcc_lo, 4\n\t"
               "s_sub_i32 vcc_hi, vcc_lo, %1\n\t"
               "s_cmp_lt_i32 vcc_hi, 0\n\t"
               "s_cbranch_scc1 .Lg0_%=\n\t"
               "s_sub_i32 vcc_hi, vcc_hi, %4\n\t"
               "s_max_i32 vcc_hi, vcc_hi, 0\n\t"
               "s_add_i32 %1, %1, vcc_hi\n\t"
               "s_add_i32 %1, %1, %3\n\t"
               "s_add_i32 %0, %0, %2"
               : "+s"(line), "+s"(nxt) : "i"(DT16), "i"(DF16), "i"(JIT16) : "scc", "vcc", "memory");
}
__device__ __forceinline__ void lds_barrier() {
  asm volatile("s_waitcnt lgkmcnt(0)" ::: "memory");
  __builtin_amdgcn_s_barrier();
  asm volatile("" ::: "memory");
}
__device__ __forceinline__ void lds_arrive(unsigned* c, int lane) {
  asm volatile("s_waitcnt lgkmcnt(0)" ::: "memory");
  if (lane == 0) __hip_atomic_fetch_add(c, 1u, RLX_WG);
}
template <int SLEEP> __device__ __forceinline__ unsigned lds_wait(unsigned* c, unsigned want) {
  unsigned v = 0;
#pragma nounroll
  for (unsigned spins = 0; spins < LSPIN_CAP; ++spins) {
    v = __builtin_amdgcn_readfirstlane(__hip_atomic_load(c, RLX_WG));
    if (v >= want) break;
    __builtin_amdgcn_s_sleep(SLEEP);
  }
  asm volatile("" ::: "memory");
  return v;
}
__device__ __forceinline__ unsigned lds_addr(const void* p) { return (unsigned)(size_t)(const LAS void*)p; }
template <int SLEEP> __device__ __forceinline__ v2u lds_take2(unsigned addr) {
  v2u v;
#pragma nounroll
  for (unsigned spins = 0; spins < LSPIN_CAP; ++spins) {
    asm volatile("ds_read_b64 %0, %1\n\ts_waitcnt lgkmcnt(0)" : "=&v"(v) : "v"(addr) : "memory");
    if (__all(v.x != EMPTY_O && v.y != EMPTY_O)) break;
    if (SLEEP) __builtin_amdgcn_s_sleep(SLEEP);
  }
  return v;
}
#define STAMP(i) do {} while (0)
#define STAMP_OUT() do {} while (0)

constexpr int L_O = 0;
constexpr int L_ML = L_O + 4096;
constexpr int L_QP = L_ML + 64;
constexpr int L_Q = L_QP + 256;
constexpr int L_OB = L_Q + 256;
constexpr int L_CNT = L_OB + 32;
constexpr int L_END = L_CNT + 16;

struct St {
  v4u T[32][4];
  float Sc[8][4];
  unsigned P[8][4];
  v4u Q, mask;
  f32x4 O;
  v4u rs[4];
  int vk, vv, so;
  unsigned tb, nx, adj;
  float* lds;
  int lane, wave;
};

template <int I> __device__ __forceinline__ void request(St& z) {
  constexpr int lim = I < NLIM ? LIM : LIMB;
  if constexpr (4 * I > lim) asm volatile("s_waitcnt vmcnt(%0)" :: "i"(lim) : "memory");
  gate_next(z.tb);
  if constexpr (I >= PRE && I < 31) z.tb += z.adj;
  tile_load(z.T[I], (I & 8) ? z.vv : z.vk, z.rs[I >> 3], z.so + (I & 7) * 32768);
  SB();
}
template <int I, int W> __device__ __forceinline__ void consume(St& z) {
  const int g = z.lane >> 4, n = z.lane & 15;
  constexpr int br = I >> 4;
  if constexpr ((I & 15) == 0) {
    if constexpr (br) lds_wait<1>((unsigned*)(z.lds + L_CNT) + 7, 1u);
    z.Q = *(const v4u*)((const unsigned short*)(z.lds + L_Q) + br * 256 + (n >> 3) * 128 + 32 * g + 8 * (n & 3));
  }
  if constexpr ((I & 8) == 0) {
    tile_wait<W>(z.T[I]);
#pragma unroll
    for (int j = 0; j < 4; ++j) {
      const f32x4 d = mfma(z.T[I][j], z.Q, f32x4{0.f, 0.f, 0.f, 0.f});
      z.Sc[I & 7][j] = (dpp<0x00>(d.x) + dpp<0x55>(d.y)) + (dpp<0xAA>(d.z) + dpp<0xFF>(d.w));
    }
    if constexpr ((I & 7) == 7) {
      float m = z.Sc[0][0];
#pragma unroll
      for (int t = 0; t < 8; ++t)
#pragma unroll
        for (int j = 0; j < 4; ++j) m = fmax_(m, z.Sc[t][j]);
      const float m2 = xg_max(m) * C2;
      float l = 0.f;
#pragma unroll
      for (int t = 0; t < 8; ++t) {
        float p[4];
#pragma unroll
        for (int j = 0; j < 4; ++j) { p[j] = __builtin_amdgcn_exp2f(fmaf(z.Sc[t][j], C2, -m2)); z.P[t][j] = dup_bf16(p[j]); }
        l += (p[0] + p[1]) + (p[2] + p[3]);
        asm volatile("" : "+v"(z.P[t][0]), "+v"(z.P[t][1]), "+v"(z.P[t][2]), "+v"(z.P[t][3]));
      }
      l = xg_sum(l);
      if (g == 0 && (n & 7) == 0) { z.lds[L_ML + br * 32 + (n >> 3) * 8 + z.wave] = m2; z.lds[L_ML + br * 32 + 16 + (n >> 3) * 8 + z.wave] = l; }
      lds_arrive((unsigned*)(z.lds + L_CNT) + 2 + br, z.lane);
      z.O = f32x4{0.f, 0.f, 0.f, 0.f};
    } else {
      asm volatile("" : "+v"(z.Sc[I & 7][0]), "+v"(z.Sc[I & 7][1]), "+v"(z.Sc[I & 7][2]), "+v"(z.Sc[I & 7][3]));
    }
  } else if constexpr (W == 0) {
    v4u A[4];
#pragma unroll
    for (int j = 0; j < 4; ++j) A[j] = diag(z.P[I & 7][j], z.mask);
    float* dst = z.lds + L_O + br * 2048 + z.wave * 256 + (g >> 1) * 128 + 8 * n + 4 * (g & 1);
    unsigned da = lds_addr(dst);
    asm volatile("" : "+v"(A[0]), "+v"(A[1]), "+v"(A[2]), "+v"(A[3]), "+v"(da));
    piece_wait<3>(z.T[I][0]); z.O = mfma(A[0], z.T[I][0], z.O); SB();
    piece_wait<2>(z.T[I][1]); z.O = mfma(A[1], z.T[I][1], z.O); SB();
    piece_wait<1>(z.T[I][2]); z.O = mfma(A[2], z.T[I][2], z.O); SB();
    piece_wait<0>(z.T[I][3]); z.O = mfma(A[3], z.T[I][3], z.O);
    *(LAS f32x4*)(size_t)da = z.O;
  } else {
    tile_wait<W>(z.T[I]);
#pragma unroll
    for (int j = 0; j < 4; ++j) z.O = mfma(diag(z.P[I & 7][j], z.mask), z.T[I][j], z.O);
    asm volatile("" : "+v"(z.O));
    if constexpr ((I & 7) == 7) *(f32x4*)(z.lds + L_O + br * 2048 + z.wave * 256 + (g >> 1) * 128 + 8 * n + 4 * (g & 1)) = z.O;
  }
  SB();
}
template <int I, int N> struct Run {
  static __device__ __forceinline__ void go(St& z) { request<I>(z); consume<I - DEP + 1, 4 * (DEP - 1)>(z); Run<I + 1, N>::go(z); }
};
template <int N> struct Run<N, N> { static __device__ __forceinline__ void go(St&) {} };
template <int I, int N> struct Req { static __device__ __forceinline__ void go(St& z) { request<I>(z); Req<I + 1, N>::go(z); } };
template <int N> struct Req<N, N> { static __device__ __forceinline__ void go(St&) {} };
template <int I, int N, int LAST> struct Con {
  static __device__ __forceinline__ void go(St& z) { consume<I, 4 * (LAST - I)>(z); Con<I + 1, N, LAST>::go(z); }
};
template <int N, int LAST> struct Con<N, N, LAST> { static __device__ __forceinline__ void go(St&) {} };

typedef unsigned v16u __attribute__((ext_vector_type(16)));
typedef unsigned v8u __attribute__((ext_vector_type(8)));
__device__ __forceinline__ unsigned char* ptr64(unsigned lo, unsigned hi) { return (unsigned char*)(((unsigned long long)hi << 32) | lo); }

__global__ void __launch_bounds__(NTHR, 3) gqa2_fused(Args) {
  __shared__ __attribute__((aligned(16))) float lds[L_END];
  unsigned* const l_cnt = (unsigned*)(lds + L_CNT);
  v16u ka; v8u kb;
  asm volatile("s_load_dwordx16 %0, %2, 0x0\n\ts_load_dwordx8 %1, %2, 0x40" : "=&s"(ka), "=&s"(kb) : "s"(__builtin_amdgcn_kernarg_segment_ptr()));
  const int tid = threadIdx.x, lane = tid & 63, wave = __builtin_amdgcn_readfirstlane(tid >> 6);
  const int g = lane >> 4, n = lane & 15;
  const int wg = blockIdx.x;
  const int ch = (wg & 7) << 5 | wg >> 3;
  const int kvh = ch >> 4, rb = (ch >> 3) & 1, s8 = ch & 7;
  {
    const v4u e = {EMPTY_O, EMPTY_O, EMPTY_O, EMPTY_O};
    const int i = tid < 512 ? tid : 0;
    *(v4u*)(lds + L_O + i * 4) = e; *(v4u*)(lds + L_O + 2048 + i * 4) = e;
    const int j = tid < 48 ? tid : 0;
    ((unsigned*)(lds + L_OB))[j] = j < 32 ? EMPTY_B : 0u;
  }
  asm volatile("s_waitcnt lgkmcnt(0)" : "+s"(ka), "+s"(kb) :: "memory");
  const v4u rk0 = mkrs(ptr64(ka[4], ka[5]) + (size_t)ch * 262144, 262144);
  unsigned probe;
  asm volatile("s_cmp_lg_u32 %2, 0\n\ts_cbranch_scc1 .Lp%=\n\ts_mov_b64 exec, 1\n\tbuffer_load_dword %0, off, %1, 0 nt\n\ts_mov_b64 exec, -1\n.Lp%=:"
               : "=v"(probe) : "s"(rk0), "s"(wave) : "scc");
  __builtin_amdgcn_s_barrier();
  asm volatile("" ::: "memory");
  unsigned char* const ws = ptr64(kb[6], kb[7]);
  const unsigned char* const wop = rb ? ptr64(kb[2], kb[3]) : ptr64(kb[0], kb[1]);
  const __amdgpu_buffer_rsrc_t rw = __builtin_amdgcn_make_buffer_rsrc(ws, 0, WS_ST, RSRC3);
  v4u mask;
  {
    const unsigned half = (n & 1) ? 0xffff0000u : 0x0000ffffu; const int r = (n & 7) >> 1;
    mask.x = r == 0 ? half : 0u; mask.y = r == 1 ? half : 0u; mask.z = r == 2 ? half : 0u; mask.w = r == 3 ? half : 0u;
  }
  const int pidx = kvh * 8 + s8;
  if (__builtin_expect(wave >= 8, 1)) {
    asm volatile("" ::: "memory");
    __builtin_amdgcn_s_setprio(3);
    const int hw = wave - 8, cg = s8 & 3, kh = s8 >> 2, sub = 2 * g + (n >> 3);
    const __amdgpu_buffer_rsrc_t rq = __builtin_amdgcn_make_buffer_rsrc((rb ? ptr64(ka[14], ka[15]) : ptr64(ka[12], ka[13])) + ((size_t)(kh * 288 + hw * 144) * NQ + kvh * 256 + cg * 64) * 2, 0, 143 * NQ * 2 + 128, RSRC3);
    const __amdgpu_buffer_rsrc_t rx = __builtin_amdgcn_make_buffer_rsrc((rb ? ptr64(ka[2], ka[3]) : ptr64(ka[0], ka[1])) + (kh * 288 + hw * 144) * 2, 0, 144 * 2, RSRC3);
    v4u wqv[18];
#pragma unroll
    for (int i = 0; i < 18; ++i) wqv[i] = __builtin_amdgcn_raw_buffer_load_b128(rq, (ABL & 4) ? OOB : sub * (NQ * 2) + (n & 7) * 16, i * 8 * NQ * 2, AUX_NT);
    const unsigned xa = __builtin_amdgcn_raw_buffer_load_b32(rx, n * 16 + g * 4, 0, 0), xb = __builtin_amdgcn_raw_buffer_load_b32(rx, n * 16 + g * 4 + 256, 0, 0);
    const unsigned ep = __builtin_amdgcn_raw_buffer_load_b32(rw, lane == 0 ? WS_EPOCH : OOB, 0, AUX_SC1);
    SB();
    f32x4 a0 = {0.f, 0.f, 0.f, 0.f}, a1 = {0.f, 0.f, 0.f, 0.f};
    const unsigned sel = n < 8 ? 0x01000100u : 0x03020302u;
#define QSTEP(acc, i, src, k) acc = mfma(diag(__builtin_amdgcn_perm(0u, (unsigned)__builtin_amdgcn_mov_dpp((int)(src), 0x150 + (k), 0xf, 0xf, true), sel), mask), wqv[i], acc)
    QSTEP(a0, 0, xa, 0); QSTEP(a1, 1, xa, 1); QSTEP(a0, 2, xa, 2); QSTEP(a1, 3, xa, 3); QSTEP(a0, 4, xa, 4); QSTEP(a1, 5, xa, 5);
    QSTEP(a0, 6, xa, 6); QSTEP(a1, 7, xa, 7); QSTEP(a0, 8, xa, 8); QSTEP(a1, 9, xa, 9); QSTEP(a0, 10, xa, 10); QSTEP(a1, 11, xa, 11);
    QSTEP(a0, 12, xa, 12); QSTEP(a1, 13, xa, 13); QSTEP(a0, 14, xa, 14); QSTEP(a1, 15, xa, 15); QSTEP(a0, 16, xb, 0); QSTEP(a1, 17, xb, 1);
    if ((g >> 1) == (n >> 3)) *(f32x4*)(lds + L_QP + (hw * 2 + (n >> 3)) * 64 + 8 * (n & 7) + 4 * (g & 1)) = a0 + a1;
    const unsigned tag = __builtin_amdgcn_readfirstlane(ep) + TAG_STEP;
    STAMP(1);
    lds_arrive(l_cnt + 0, lane);
    unsigned anc = 0;
    if (hw == 0) {
      lds_wait<1>(l_cnt + 0, 2u);
      const float q = (lds[L_QP + lane] + lds[L_QP + 64 + lane]) + (lds[L_QP + 128 + lane] + lds[L_QP + 192 + lane]);
      st_gran(ws, WS_QG + ((rb * 16 + kvh) * 512 + kh * 256 + cg * 64 + lane) * 8, tag, q);
      if (lane == 0) l_cnt[5] = tag;
      anc = lds_wait<1>(l_cnt + 6, 1u);
      if (lane == 0) __hip_atomic_store((gu64*)(ws + WS_AG + wg * 8), ((unsigned long long)tag << 32) | anc, RLX_AGENT);
    }
    STAMP(2);
    pause(QGA);
    if (hw) pause(QGB - QGA);
    {
      v2u x[8];
      const v2u ag = ldg(rw, (hw == 0 && lane < 8) ? WS_AG + (wg & ~7) * 8 + lane * 8 : OOB);
      sweep<8, 512, 8>(rw, WS_QG + ((hw * 16 + kvh) * 512 + lane) * 8, tag, x);
#pragma unroll
      for (int k = 0; k < 4; ++k) ((unsigned short*)(lds + L_Q))[hw * 256 + k * 64 + lane] = f2bf(__uint_as_float(x[k].x) + __uint_as_float(x[k + 4].x));
      if (hw == 0) {
        int d = ag.y == tag ? (int)(ag.x - anc) : 0;
        d = d > 1600 ? 1600 : d < -1600 ? -1600 : d;
        d += __builtin_amdgcn_mov_dpp(d, XOR1, 0xf, 0xf, true); d += __builtin_amdgcn_mov_dpp(d, XOR2, 0xf, 0xf, true); d += __builtin_amdgcn_mov_dpp(d, HMIR, 0xf, 0xf, true);
        d /= 8 * (31 - PRE);
        if (lane == 0) l_cnt[8] = (unsigned)d;
      }
      lds_arrive(hw ? l_cnt + 7 : l_cnt + 1, lane);
    }
    STAMP(3);
#pragma nounroll
    for (int br = 0; br < 2; ++br) {
      lds_wait<8>(l_cnt + 2 + br, 8u);
      const float* ml = lds + L_ML + br * 32 + hw * 8;
      const f32x4 m0 = *(const f32x4*)(ml), m1 = *(const f32x4*)(ml + 4), l0 = *(const f32x4*)(ml + 16), l1 = *(const f32x4*)(ml + 20);
      const float M = fmax_(fmax_(fmax_(m0.x, m0.y), fmax_(m0.z, m0.w)), fmax_(fmax_(m1.x, m1.y), fmax_(m1.z, m1.w)));
      float L = 0.f;
#pragma unroll
      for (int w = 0; w < 8; ++w) L = fmaf(__builtin_amdgcn_exp2f((w < 4 ? m0[w & 3] : m1[w & 3]) - M), w < 4 ? l0[w & 3] : l1[w & 3], L);
      const int mg = WS_MG + ((br * 16 + kvh) * 2 + hw) * 1024;
      if (lane < 8) st_gran(ws, mg + (ch & 15) * 64 + lane * 8, tag, (lane & 1) ? L : M);
      pause(MLD);
      v2u x[1];
      sweep<1, 0, 8>(rw, mg + n * 64 + (g & 1) * 8, tag, x);
      auto sw = __builtin_amdgcn_permlane16_swap(x[0].x, x[0].x, false, false);
      const float Ms = __uint_as_float(sw[0]), Ls = __uint_as_float(sw[1]);
      float Mx = fmax_(Ms, dpp<XOR1>(Ms)); Mx = fmax_(Mx, dpp<XOR2>(Mx)); Mx = fmax_(Mx, dpp<HMIR>(Mx)); Mx = fmax_(Mx, dpp<MIR>(Mx));
      float Lt = __builtin_amdgcn_exp2f(Ms - Mx) * Ls; Lt += dpp<XOR1>(Lt); Lt += dpp<XOR2>(Lt); Lt += dpp<HMIR>(Lt); Lt += dpp<MIR>(Lt);
      const float inv = 1.0f / Lt;
      float c[8];
#pragma unroll
      for (int w = 0; w < 8; ++w) c[w] = __builtin_amdgcn_exp2f((w < 4 ? m0[w & 3] : m1[w & 3]) - Mx) * inv;
      const unsigned oa = lds_addr(lds + L_O + br * 2048 + hw * 128 + 2 * lane);
      int dst = WS_PG + (br * 256 + ch) * PG_ROW + (hw * 128 + 2 * lane) * 8;
      asm volatile("" : "+v"(dst));
      float o0 = 0.f, o1 = 0.f;
      { const v2u v = lds_take2<8>(oa); o0 = c[0] * __uint_as_float(v.x); o1 = c[0] * __uint_as_float(v.y); }
#pragma unroll
      for (int w = 1; w < 8; ++w) { const v2u v = lds_take2<0>(oa + w * 1024); o0 = fmaf(c[w], __uint_as_float(v.x), o0); o1 = fmaf(c[w], __uint_as_float(v.y), o1); }
      st_gran2(rw, dst, tag, o0, o1);
      if (hw == 0 && lane == 0) __hip_atomic_store(l_cnt + 4, (unsigned)br + 1u, RLX_WG);
    }
    STAMP(8);
  } else {
    if (wave == 0) {
      unsigned long long ta;
      asm volatile("s_waitcnt vmcnt(0)\n\ts_memrealtime %0\n\ts_waitcnt lgkmcnt(0)" : "=s"(ta) : "v"(probe) : "memory");
      if (lane == 0) __hip_atomic_store(l_cnt + 6, ((unsigned)ta << 4) | 1u, RLX_WG);
    }
    St z;
    const int phs = PAT == 2 ? (wg >> 3) & 7 : wave;
    z.lds = lds; z.lane = lane; z.wave = wave; z.mask = mask;
    z.rs[0] = rk0;
    z.rs[1] = mkrs(ptr64(ka[8], ka[9]) + (size_t)ch * 262144, 262144);
    z.rs[2] = mkrs(ptr64(ka[6], ka[7]) + (size_t)ch * 262144, 262144);
    z.rs[3] = mkrs(ptr64(ka[10], ka[11]) + (size_t)ch * 262144, 262144);
    z.vk = (ABL & 8) ? lane * 16 : ((lane >> 2) & 3) * 256 + g * 64 + (lane & 3) * 16; z.vv = lane * 16; z.so = wave * WST;
    GAS unsigned short* outp = (GAS unsigned short*)ptr64(kb[4], kb[5]);
    z.tb = lds_wait<1>(l_cnt + 6, 1u) + (unsigned)(OFF16 + ((phs * DT16) >> 3));
    z.nx = z.tb;
    STAMP(1);
    Req<0, PRE>::go(z);
    STAMP(4);
    lds_wait<1>(l_cnt + 1, 1u);
    const unsigned tag = __builtin_amdgcn_readfirstlane(l_cnt[5]);
    z.adj = __builtin_amdgcn_readfirstlane(l_cnt[8]);
    STAMP(5);
    Con<0, PRE - DEP + 1, PRE - 1>::go(z);
    Run<PRE, 17>::go(z);
    STAMP(14);
    Run<17, 25>::go(z);
    STAMP(15);
    Run<25, 32>::go(z);
    STAMP(6);
    Con<32 - DEP + 1, 32, 31>::go(z);
    STAMP(7);
    const unsigned oba = lds_addr(lds + L_OB + g * 8);
    if (wave < 5) {
      const __amdgpu_buffer_rsrc_t ro = __builtin_amdgcn_make_buffer_rsrc((unsigned char*)wop + ((size_t)(kvh * 256 + s8 * 32) * DM) * 2, 0, 32 * DM * 2, RSRC3);
      const int wcol = wave * 128 + n * 8;
      const int vwo = (!NOWO && wcol < DM) ? g * (DM * 2) + wcol * 2 : OOB;
      v4u Wo[8];
      z.tb += (unsigned)((wave < NW1 ? wave * WOS16 : WG16 + (wave - NW1) * WOSB16) - ((phs * DT16) >> 3));
      gate_next(z.tb);
#pragma unroll
      for (int i = 0; i < 8; ++i) Wo[i] = __builtin_amdgcn_raw_buffer_load_b128(ro, vwo, i * 4 * DM * 2, AUX_NT);
      int dst = wcol < DM ? WS_YG + (rb * 128 + pidx) * YG_ROW + (wcol + 4 * (g & 1) + 2 * (g >> 1)) * 8 : OOB;
      asm volatile("" : "+v"(dst), "+v"(Wo[0]), "+v"(Wo[1]), "+v"(Wo[2]), "+v"(Wo[3]), "+v"(Wo[4]), "+v"(Wo[5]), "+v"(Wo[6]), "+v"(Wo[7]));
      STAMP(2);
      v4u oa, ob;
#pragma nounroll
      for (unsigned spins = 0; spins < LSPIN_CAP; ++spins) {
        asm volatile("ds_read_b128 %0, %2\n\tds_read_b128 %1, %2 offset:16\n\ts_waitcnt lgkmcnt(0)" : "=&v"(oa), "=&v"(ob) : "v"(oba) : "memory");
        if (__all(oa.x != EMPTY_B && oa.y != EMPTY_B && oa.z != EMPTY_B && oa.w != EMPTY_B && ob.x != EMPTY_B && ob.y != EMPTY_B && ob.z != EMPTY_B && ob.w != EMPTY_B)) break;
      }
      STAMP(10);
      f32x4 Y0 = {0.f, 0.f, 0.f, 0.f}, Y1 = {0.f, 0.f, 0.f, 0.f};
#pragma unroll
      for (int i = 0; i < 4; ++i) { Y0 = mfma(diag(oa[i], mask), Wo[i], Y0); Y1 = mfma(diag(ob[i], mask), Wo[4 + i], Y1); }
      const f32x4 Y = Y0 + Y1;
      st_gran2(rw, dst, tag, g < 2 ? Y.x : Y.z, g < 2 ? Y.y : Y.w);
      STAMP(11);
    } else if (wave == 5) {
      const int po = WS_PG + (rb * 256 + kvh * 16 + (lane >> 5)) * PG_ROW + (32 * s8 + (lane & 31)) * 8;
      v2u x[8];
      lds_wait<0>(l_cnt + 4, (unsigned)rb + 1u);
      if (rb) pause(H1D);
      sweep<8, 2 * PG_ROW, 1>(rw, po, tag, x);
      STAMP(9);
      const float o = x32_sum(((__uint_as_float(x[0].x) + __uint_as_float(x[1].x)) + (__uint_as_float(x[2].x) + __uint_as_float(x[3].x)))
                            + ((__uint_as_float(x[4].x) + __uint_as_float(x[5].x)) + (__uint_as_float(x[6].x) + __uint_as_float(x[7].x))));
      if (lane < 32) ((unsigned*)(lds + L_OB))[(lane & 3) * 8 + (lane >> 2)] = dup_bf16(o);
    } else if (wave == 6 || pidx < 16) {
      const int pos = wave == 6 ? pidx * 4 : 512 + pidx * 4;
      const int so = WS_YG + (rb * 128 + (lane >> 2)) * YG_ROW + (pos + (lane & 3)) * 8;
      const int col = pos + (lane & 3);
      GAS unsigned short* op = outp + rb * DM + col;
      asm volatile("" : "+v"(op));
      v2u x[8];
      z.tb += (unsigned)(WG16 + (4 - NW1) * WOSB16 + H2G16 - ((phs * DT16) >> 3));
#pragma nounroll
      for (unsigned spins = 0; spins < LSPIN_CAP; ++spins) {
        if (__builtin_amdgcn_readfirstlane(__hip_atomic_load((unsigned*)(lds + L_OB), RLX_WG)) != EMPTY_B) break;
        __builtin_amdgcn_s_sleep(1);
      }
      pause(H2D);
      gate_next(z.tb);
      sweep<8, 16 * YG_ROW, 1>(rw, so, tag, x);
      STAMP(12);
      float y = ((__uint_as_float(x[0].x) + __uint_as_float(x[1].x)) + (__uint_as_float(x[2].x) + __uint_as_float(x[3].x)))
              + ((__uint_as_float(x[4].x) + __uint_as_float(x[5].x)) + (__uint_as_float(x[6].x) + __uint_as_float(x[7].x)));
      y += dpp<ROR4>(y); y += dpp<ROR8>(y);
      y = xg_sum(y);
      if (lane < 4) *op = f2bf(y);
      if (rb == 0 && pos == 0 && lane == 0) __hip_atomic_store((gu32*)(ws + WS_EPOCH), tag, RLX_AGENT);
      STAMP(13);
    }
  }
  STAMP_OUT();
}

extern "C" void kernel_launch(void* const* d_in, const int* in_sizes, int n_in, void* d_out, int out_size, void* d_ws, size_t ws_size, hipStream_t stream) {
  Args a;
  a.x[0] = d_in[0]; a.k[0] = d_in[1]; a.v[0] = d_in[2]; a.wq[0] = d_in[3]; a.wo[0] = d_in[4];
  a.x[1] = d_in[5]; a.k[1] = d_in[6]; a.v[1] = d_in[7]; a.wq[1] = d_in[8]; a.wo[1] = d_in[9];
  a.out = d_out; a.ws = d_ws;
  gqa2_fused<<<NWG, NTHR, 0, stream>>>(a);
}
```

```cpp
#define OFF16 800
#define DT16 1800
#include <hip/hip_runtime.h>

typedef unsigned v2u __attribute__((ext_vector_type(2)));
typedef unsigned v4u __attribute__((ext_vector_type(4)));
typedef unsigned v8u __attribute__((ext_vector_type(8)));
typedef unsigned v16u __attribute__((ext_vector_type(16)));
typedef float f32x2 __attribute__((ext_vector_type(2)));
typedef float f32x4 __attribute__((ext_vector_type(4)));
typedef __bf16 bf16x2 __attribute__((ext_vector_type(2)));
typedef __bf16 bf16x8 __attribute__((ext_vector_type(8)));
#define GAS __attribute__((address_space(1)))
#define LAS __attribute__((address_space(3)))
#define RLX_AGENT __ATOMIC_RELAXED, __HIP_MEMORY_SCOPE_AGENT
#define RLX_WG __ATOMIC_RELAXED, __HIP_MEMORY_SCOPE_WORKGROUP
#define SB() __builtin_amdgcn_sched_barrier(0)

constexpr int DM = 576, NQ = 4096;
constexpr int NWG = 256, NTHR = 640;
constexpr float C2 = 0.088388347648318440f * 1.4426950408889634f;

constexpr int kSlot16 = DT16;
constexpr int kOff16 = OFF16;
constexpr int kPrio16 = 400;
constexpr int kWoStep16 = 600;
constexpr int kHop2Gate16 = 2000;
constexpr int kQA = 280, kQB = 600, kML = 350, kHop1 = 40, kHop2 = 70;
constexpr int PRE = 8;
constexpr int DEP = 3;
constexpr int LIM = 2;

constexpr int AUX_NT = 2, AUX_SC1 = 16, RSRC3 = 0x00020000, OOB = 0x7fff0000;
constexpr int WS_EPOCH = 0;
constexpr int WS_AG = 1024;
constexpr int WS_QG = 4096;
constexpr int WS_MG = WS_QG + 32 * 4096;
constexpr int WS_PG = WS_MG + 64 * 1024, PG_ROW = 256 * 8;
constexpr int WS_YG = WS_PG + 512 * PG_ROW, YG_ROW = DM * 8;
constexpr int WS_END = WS_YG + 256 * YG_ROW;
constexpr unsigned TAG_STEP = 0x9E3779B9u;
constexpr unsigned SPIN_CAP = 1u << 17, LSPIN_CAP = 1u << 22;
constexpr int L_O = 0;
constexpr int L_ML = L_O + 4096;
constexpr int L_QP = L_ML + 64;
constexpr int L_Q = L_QP + 256;
constexpr int L_OB = L_Q + 256;
constexpr int L_CNT = L_OB + 32, L_END = L_CNT + 16;
enum { C_QP, C_QA, C_ML  , C_PUB = 4, C_TAG, C_ANCHOR, C_QB, C_ADJ };
constexpr unsigned EMPTY_O = 0xffffffffu, EMPTY_B = 0x0000ffffu;

struct Args {
  const void* x[2]; const void* k[2]; const void* v[2]; const void* wq[2]; const void* wo[2];
  void* out; void* ws;
};

__device__ __forceinline__ f32x4 mfma(v4u a, v4u b, f32x4 c) {
  return __builtin_amdgcn_mfma_f32_16x16x32_bf16(__builtin_bit_cast(bf16x8, a), __builtin_bit_cast(bf16x8, b), c, 0, 0, 0);
}
__device__ __forceinline__ unsigned dup_bf16(float f) {
  f32x2 v = {f, f};
  return __builtin_bit_cast(unsigned, __builtin_convertvector(v, bf16x2));
}
__device__ __forceinline__ unsigned short f2bf(float f) { return __builtin_bit_cast(unsigned short, (__bf16)f); }
__device__ __forceinline__ float u2f(unsigned u) { return __uint_as_float(u); }
__device__ __forceinline__ v4u diag(unsigned pp, v4u mask) { v4u r = {pp & mask.x, pp & mask.y, pp & mask.z, pp & mask.w}; return r; }
template <int CTRL> __device__ __forceinline__ int dppi(int x) { return __builtin_amdgcn_mov_dpp(x, CTRL, 0xf, 0xf, true); }
template <int CTRL> __device__ __forceinline__ float dpp(float x) { return __builtin_bit_cast(float, dppi<CTRL>(__builtin_bit_cast(int, x))); }
constexpr int XOR1 = 0xB1, XOR2 = 0x4E, HMIR = 0x141, MIR = 0x140, ROR8 = 0x128, ROR4 = 0x124, BCAST = 0x150;
__device__ __forceinline__ float fmax_(float a, float b) { return __builtin_fmaxf(a, b); }
__device__ __forceinline__ float xg_max(float x) {
  auto s = __builtin_amdgcn_permlane16_swap(__float_as_uint(x), __float_as_uint(x), false, false);
  x = fmax_(u2f(s[0]), u2f(s[1]));
  auto t = __builtin_amdgcn_permlane32_swap(__float_as_uint(x), __float_as_uint(x), false, false);
  return fmax_(u2f(t[0]), u2f(t[1]));
}
__device__ __forceinline__ float x32_sum(float x) {
  auto t = __builtin_amdgcn_permlane32_swap(__float_as_uint(x), __float_as_uint(x), false, false);
  return u2f(t[0]) + u2f(t[1]);
}
__device__ __forceinline__ float xg_sum(float x) {
  auto s = __builtin_amdgcn_permlane16_swap(__float_as_uint(x), __float_as_uint(x), false, false);
  return x32_sum(u2f(s[0]) + u2f(s[1]));
}
__device__ __forceinline__ float sum8(const v2u (&x)[8]) {
  return ((u2f(x[0].x) + u2f(x[1].x)) + (u2f(x[2].x) + u2f(x[3].x))) + ((u2f(x[4].x) + u2f(x[5].x)) + (u2f(x[6].x) + u2f(x[7].x)));
}

__device__ __forceinline__ void st_gran2(__amdgpu_buffer_rsrc_t rs, int off, unsigned tag, float a, float b) {
  __builtin_amdgcn_raw_buffer_store_b128(v4u{__float_as_uint(a), tag, __float_as_uint(b), tag}, rs, off, 0, AUX_SC1);
}
__device__ __forceinline__ v2u ldg(__amdgpu_buffer_rsrc_t rs, int off) { return __builtin_amdgcn_raw_buffer_load_b64(rs, off, 0, AUX_SC1); }
template <int N, int STEP, int SLEEP> __device__ __forceinline__ void sweep(__amdgpu_buffer_rsrc_t rs, int off, unsigned tag, v2u (&x)[N]) {
#pragma unroll
  for (int k = 0; k < N; ++k) x[k] = ldg(rs, off + k * STEP);
  SB();
  bool ok = true;
#pragma unroll
  for (int k = 0; k < N; ++k) ok &= x[k].y == tag;
  if (__builtin_expect(__all(ok), 1)) return;
#pragma nounroll
  for (unsigned spins = 0; spins < SPIN_CAP; ++spins) {
    __builtin_amdgcn_s_sleep(SLEEP);
    v2u y[N];
#pragma unroll
    for (int k = 0; k < N; ++k) y[k] = ldg(rs, x[k].y == tag ? OOB : off + k * STEP);
    SB();
    ok = true;
#pragma unroll
    for (int k = 0; k < N; ++k) { x[k] = x[k].y == tag ? x[k] : y[k]; ok &= x[k].y == tag; }
    if (__all(ok)) break;
  }
}

__device__ __forceinline__ unsigned char* ptr64(unsigned lo, unsigned hi) { return (unsigned char*)(((unsigned long long)hi << 32) | lo); }
__device__ __forceinline__ v4u mkrs(const void* p, unsigned bytes) {
  const unsigned long long u = (unsigned long long)p;
  v4u r = {(unsigned)u, (unsigned)(u >> 32) & 0xffffu, bytes, (unsigned)RSRC3};
  return r;
}
__device__ __forceinline__ void tile_load(v4u (&d)[4], int voff, v4u rs, int soff) {
  asm volatile("buffer_load_dwordx4 %0, %4, %5, %6 offen nt\n\t"
               "buffer_load_dwordx4 %1, %4, %5, %7 offen nt\n\t"
               "buffer_load_dwordx4 %2, %4, %5, %8 offen nt\n\t"
               "buffer_load_dwordx4 %3, %4, %5, %9 offen nt"
               : "=&v"(d[0]), "=&v"(d[1]), "=&v"(d[2]), "=&v"(d[3])
               : "v"(voff), "s"(rs), "s"(soff), "s"(soff + 8192), "s"(soff + 16384), "s"(soff + 24576));
}
template <int N> __device__ __forceinline__ void tile_wait(v4u (&d)[4]) {
  asm volatile("s_waitcnt vmcnt(%4)" : "+v"(d[0]), "+v"(d[1]), "+v"(d[2]), "+v"(d[3]) : "i"(N));
}
template <int N> __device__ __forceinline__ void piece_wait(v4u& d) { asm volatile("s_waitcnt vmcnt(%1)" : "+v"(d) : "i"(N)); }

template <int TICKS> __device__ __forceinline__ void pause() {
  unsigned due;
  asm volatile("s_memrealtime vcc\n\t"
               "s_waitcnt lgkmcnt(0)\n\t"
               "s_add_i32 %0, vcc_lo, %1\n"
               ".Lg0_%=:\n\t"
               "s_sleep 1\n\t"
               "s_memrealtime vcc\n\t"
               "s_waitcnt lgkmcnt(0)\n\t"
               "s_sub_i32 vcc_lo, vcc_lo, %0\n\t"
               "s_cmp_lt_i32 vcc_lo, 0\n\t"
               "s_cbranch_scc1 .Lg0_%="
               : "=&s"(due) : "i"(TICKS) : "scc", "vcc", "memory");
}
__device__ __forceinline__ void gate(unsigned due16) {
  asm volatile("s_branch .Lg1_%=\n"
               ".Lg0_%=:\n\t"
               "s_sleep 1\n"
               ".Lg1_%=:\n\t"
               "s_memrealtime vcc\n\t"
               "s_waitcnt lgkmcnt(0)\n\t"
               "s_lshl_b32 vcc_lo, vcc_lo, 4\n\t"
               "s_sub_i32 vcc_lo, vcc_lo, %0\n\t"
               "s_cmp_lt_i32 vcc_lo, 0\n\t"
               "s_cbranch_scc1 .Lg0_%=\n\t"
               "s_setprio 0\n\t"
               "s_cmp_lt_i32 vcc_lo, %1\n\t"
               "s_cbranch_scc1 .Lg2_%=\n\t"
               "s_setprio 1\n"
               ".Lg2_%=:"
               :: "s"(due16), "i"(kPrio16) : "scc", "vcc", "memory");
}

__device__ __forceinline__ void lds_arrive(unsigned* c, int lane) {
  asm volatile("s_waitcnt lgkmcnt(0)" ::: "memory");
  if (lane == 0) __hip_atomic_fetch_add(c, 1u, RLX_WG);
}
template <int SLEEP> __device__ __forceinline__ unsigned lds_wait(unsigned* c, unsigned want) {
  unsigned v = 0;
#pragma nounroll
  for (unsigned spins = 0; spins < LSPIN_CAP; ++spins) {
    v = __builtin_amdgcn_readfirstlane(__hip_atomic_load(c, RLX_WG));
    if (v >= want) break;
    __builtin_amdgcn_s_sleep(SLEEP);
  }
  asm volatile("" ::: "memory");
  return v;
}
__device__ __forceinline__ unsigned lds_addr(const void* p) { return (unsigned)(size_t)(const LAS void*)p; }
template <int SLEEP> __device__ __forceinline__ v2u lds_take2(unsigned addr) {
  v2u v;
#pragma nounroll
  for (unsigned spins = 0; spins < LSPIN_CAP; ++spins) {
    asm volatile("ds_read_b64 %0, %1\n\ts_waitcnt lgkmcnt(0)" : "=&v"(v) : "v"(addr) : "memory");
    if (__all(v.x != EMPTY_O && v.y != EMPTY_O)) break;
    if (SLEEP) __builtin_amdgcn_s_sleep(SLEEP);
  }
  return v;
}

struct St {
  v4u T[32][4];
  float Sc[8][4];
  unsigned P[8][4];
  v4u Q, mask;
  f32x4 O;
  v4u rs[4];
  int vk, vv, so;
  unsigned due, adj;
  float* lds;
  int lane, wave;
};
template <int I> __device__ __forceinline__ void request(St& z) {
  if constexpr (I > 0) asm volatile("s_waitcnt vmcnt(%0)" :: "i"(LIM) : "memory");
  gate(z.due);
  z.due += kSlot16;
  if constexpr (I >= PRE && I < 31) z.due += z.adj;
  tile_load(z.T[I], (I & 8) ? z.vv : z.vk, z.rs[I >> 3], z.so + (I & 7) * 32768);
  SB();
}
template <int I, int W> __device__ __forceinline__ void consume(St& z) {
  const int g = z.lane >> 4, n = z.lane & 15;
  constexpr int br = I >> 4;
  unsigned* const cnt = (unsigned*)(z.lds + L_CNT);
  if constexpr ((I & 15) == 0) {
    if constexpr (br) lds_wait<1>(cnt + C_QB, 1u);
    z.Q = *(const v4u*)((const unsigned short*)(z.lds + L_Q) + br * 256 + (n >> 3) * 128 + 32 * g + 8 * (n & 3));
  }
  if constexpr ((I & 8) == 0) {
    tile_wait<W>(z.T[I]);
#pragma unroll
    for (int j = 0; j < 4; ++j) {
      const f32x4 d = mfma(z.T[I][j], z.Q, f32x4{0.f, 0.f, 0.f, 0.f});
      z.Sc[I & 7][j] = (dpp<0x00>(d.x) + dpp<0x55>(d.y)) + (dpp<0xAA>(d.z) + dpp<0xFF>(d.w));
    }
    if constexpr ((I & 7) == 7) {
      float m = z.Sc[0][0];
#pragma unroll
      for (int t = 0; t < 8; ++t)
#pragma unroll
        for (int j = 0; j < 4; ++j) m = fmax_(m, z.Sc[t][j]);
      const float m2 = xg_max(m) * C2;
      float l = 0.f;
#pragma unroll
      for (int t = 0; t < 8; ++t) {
        float p[4];
#pragma unroll
        for (int j = 0; j < 4; ++j) { p[j] = __builtin_amdgcn_exp2f(fmaf(z.Sc[t][j], C2, -m2)); z.P[t][j] = dup_bf16(p[j]); }
        l += (p[0] + p[1]) + (p[2] + p[3]);
        asm volatile("" : "+v"(z.P[t][0]), "+v"(z.P[t][1]), "+v"(z.P[t][2]), "+v"(z.P[t][3]));
      }
      l = xg_sum(l);
      if (g == 0 && (n & 7) == 0) { z.lds[L_ML + br * 32 + (n >> 3) * 8 + z.wave] = m2; z.lds[L_ML + br * 32 + 16 + (n >> 3) * 8 + z.wave] = l; }
      lds_arrive(cnt + C_ML + br, z.lane);
      z.O = f32x4{0.f, 0.f, 0.f, 0.f};
    } else {
      asm volatile("" : "+v"(z.Sc[I & 7][0]), "+v"(z.Sc[I & 7][1]), "+v"(z.Sc[I & 7][2]), "+v"(z.Sc[I & 7][3]));
    }
  } else if constexpr (W == 0) {
    v4u A[4];
#pragma unroll
    for (int j = 0; j < 4; ++j) A[j] = diag(z.P[I & 7][j], z.mask);
    unsigned da = lds_addr(z.lds + L_O + br * 2048 + z.wave * 256 + (g >> 1) * 128 + 8 * n + 4 * (g & 1));
    asm volatile("" : "+v"(A[0]), "+v"(A[1]), "+v"(A[2]), "+v"(A[3]), "+v"(da));
    piece_wait<3>(z.T[I][0]); z.O = mfma(A[0], z.T[I][0], z.O); SB();
    piece_wait<2>(z.T[I][1]); z.O = mfma(A[1], z.T[I][1], z.O); SB();
    piece_wait<1>(z.T[I][2]); z.O = mfma(A[2], z.T[I][2], z.O); SB();
    piece_wait<0>(z.T[I][3]); z.O = mfma(A[3], z.T[I][3], z.O);
    *(LAS f32x4*)(size_t)da = z.O;
  } else {
    tile_wait<W>(z.T[I]);
#pragma unroll
    for (int j = 0; j < 4; ++j) z.O = mfma(diag(z.P[I & 7][j], z.mask), z.T[I][j], z.O);
    asm volatile("" : "+v"(z.O));
    if constexpr ((I & 7) == 7) *(f32x4*)(z.lds + L_O + br * 2048 + z.wave * 256 + (g >> 1) * 128 + 8 * n + 4 * (g & 1)) = z.O;
  }
  SB();
}
template <int I, int N> struct Req { static __device__ __forceinline__ void go(St& z) { request<I>(z); Req<I + 1, N>::go(z); } };
template <int N> struct Req<N, N> { static __device__ __forceinline__ void go(St&) {} };
template <int I, int N, int LAST> struct Con {
  static __device__ __forceinline__ void go(St& z) { consume<I, 4 * (LAST - I)>(z); Con<I + 1, N, LAST>::go(z); }
};
template <int N, int LAST> struct Con<N, N, LAST> { static __device__ __forceinline__ void go(St&) {} };
template <int I, int N> struct Run {
  static __device__ __forceinline__ void go(St& z) { request<I>(z); consume<I - DEP + 1, 4 * (DEP - 1)>(z); Run<I + 1, N>::go(z); }
};
template <int N> struct Run<N, N> { static __device__ __forceinline__ void go(St&) {} };

__global__ void __launch_bounds__(NTHR, 3) gqa2_fused(Args) {
  __shared__ __attribute__((aligned(16))) float lds[L_END];
  unsigned* const cnt = (unsigned*)(lds + L_CNT);
  v16u ka; v8u kb;
  asm volatile("s_load_dwordx16 %0, %2, 0x0\n\ts_load_dwordx8 %1, %2, 0x40" : "=&s"(ka), "=&s"(kb) : "s"(__builtin_amdgcn_kernarg_segment_ptr()));
  const int tid = threadIdx.x, lane = tid & 63, wave = __builtin_amdgcn_readfirstlane(tid >> 6);
  const int g = lane >> 4, n = lane & 15;
  const int wg = blockIdx.x;
  const int ch = (wg & 7) << 5 | wg >> 3;
  const int kvh = ch >> 4, rb = (ch >> 3) & 1, s8 = ch & 7;
  {
    const v4u e = {EMPTY_O, EMPTY_O, EMPTY_O, EMPTY_O};
    const int i = tid < 512 ? tid : 0, j = tid < 48 ? tid : 0;
    *(v4u*)(lds + L_O + i * 4) = e; *(v4u*)(lds + L_O + 2048 + i * 4) = e;
    ((unsigned*)(lds + L_OB))[j] = j < 32 ? EMPTY_B : 0u;
  }
  asm volatile("s_waitcnt lgkmcnt(0)" : "+s"(ka), "+s"(kb) :: "memory");
  const v4u rk0 = mkrs(ptr64(ka[4], ka[5]) + (size_t)ch * 262144, 262144);
  unsigned probe;
  asm volatile("s_cmp_lg_u32 %2, 0\n\ts_cbranch_scc1 .Lp%=\n\ts_mov_b64 exec, 1\n\tbuffer_load_dword %0, off, %1, 0 nt\n\ts_mov_b64 exec, -1\n.Lp%=:"
               : "=v"(probe) : "s"(rk0), "s"(wave) : "scc");
  __builtin_amdgcn_s_barrier();
  asm volatile("" ::: "memory");
  unsigned char* const ws = ptr64(kb[6], kb[7]);
  const __amdgpu_buffer_rsrc_t rw = __builtin_amdgcn_make_buffer_rsrc(ws, 0, WS_END, RSRC3);
  v4u mask;
  {
    const unsigned half = (n & 1) ? 0xffff0000u : 0x0000ffffu; const int r = (n & 7) >> 1;
    mask.x = r == 0 ? half : 0u; mask.y = r == 1 ? half : 0u; mask.z = r == 2 ? half : 0u; mask.w = r == 3 ? half : 0u;
  }
  const int pidx = kvh * 8 + s8;

  if (__builtin_expect(wave >= 8, 1)) {
    __builtin_amdgcn_s_setprio(3);
    const int hw = wave - 8, cg = s8 & 3, kh = s8 >> 2, sub = 2 * g + (n >> 3);
    const __amdgpu_buffer_rsrc_t rq = __builtin_amdgcn_make_buffer_rsrc(
        (rb ? ptr64(ka[14], ka[15]) : ptr64(ka[12], ka[13])) + ((size_t)(kh * 288 + hw * 144) * NQ + kvh * 256 + cg * 64) * 2, 0, 143 * NQ * 2 + 128, RSRC3);
    const __amdgpu_buffer_rsrc_t rx = __builtin_amdgcn_make_buffer_rsrc((rb ? ptr64(ka[2], ka[3]) : ptr64(ka[0], ka[1])) + (kh * 288 + hw * 144) * 2, 0, 144 * 2, RSRC3);
    v4u wqv[18];
#pragma unroll
    for (int i = 0; i < 18; ++i) wqv[i] = __builtin_amdgcn_raw_buffer_load_b128(rq, sub * (NQ * 2) + (n & 7) * 16, i * 8 * NQ * 2, AUX_NT);
    const unsigned xa = __builtin_amdgcn_raw_buffer_load_b32(rx, n * 16 + g * 4, 0, 0), xb = __builtin_amdgcn_raw_buffer_load_b32(rx, n * 16 + g * 4 + 256, 0, 0);
    const unsigned ep = __builtin_amdgcn_raw_buffer_load_b32(rw, lane == 0 ? WS_EPOCH : OOB, 0, AUX_SC1);
    SB();
    f32x4 a0 = {0.f, 0.f, 0.f, 0.f}, a1 = {0.f, 0.f, 0.f, 0.f};
    const unsigned sel = n < 8 ? 0x01000100u : 0x03020302u;
#define QSTEP(acc, i, src, k) acc = mfma(diag(__builtin_amdgcn_perm(0u, (unsigned)dppi<BCAST + (k)>((int)(src)), sel), mask), wqv[i], acc)
    QSTEP(a0, 0, xa, 0); QSTEP(a1, 1, xa, 1); QSTEP(a0, 2, xa, 2); QSTEP(a1, 3, xa, 3); QSTEP(a0, 4, xa, 4); QSTEP(a1, 5, xa, 5);
    QSTEP(a0, 6, xa, 6); QSTEP(a1, 7, xa, 7); QSTEP(a0, 8, xa, 8); QSTEP(a1, 9, xa, 9); QSTEP(a0, 10, xa, 10); QSTEP(a1, 11, xa, 11);
    QSTEP(a0, 12, xa, 12); QSTEP(a1, 13, xa, 13); QSTEP(a0, 14, xa, 14); QSTEP(a1, 15, xa, 15); QSTEP(a0, 16, xb, 0); QSTEP(a1, 17, xb, 1);
#undef QSTEP
    if ((g >> 1) == (n >> 3)) *(f32x4*)(lds + L_QP + (hw * 2 + (n >> 3)) * 64 + 8 * (n & 7) + 4 * (g & 1)) = a0 + a1;
    const unsigned tag = __builtin_amdgcn_readfirstlane(ep) + TAG_STEP;
    lds_arrive(cnt + C_QP, lane);
    unsigned anc = 0;
    if (hw == 0) {
      lds_wait<1>(cnt + C_QP, 2u);
      const f32x2* qp = (const f32x2*)(lds + L_QP) + (lane & 31);
      const f32x2 q = (qp[0] + qp[32]) + (qp[64] + qp[96]);
      st_gran2(rw, lane < 32 ? WS_QG + ((rb * 16 + kvh) * 512 + kh * 256 + cg * 64 + 2 * lane) * 8 : OOB, tag, q.x, q.y);
      if (lane == 0) cnt[C_TAG] = tag;
      anc = lds_wait<1>(cnt + C_ANCHOR, 1u);
      if (lane == 0) __hip_atomic_store((GAS unsigned long long*)(ws + WS_AG + wg * 8), ((unsigned long long)tag << 32) | anc, RLX_AGENT);
    }
    pause<kQA>();
    if (hw) pause<kQB - kQA>();
    {
      v2u x[8];
      const v2u ag = ldg(rw, (hw == 0 && lane < 8) ? WS_AG + (wg & ~7) * 8 + lane * 8 : OOB);
      sweep<8, 512, 8>(rw, WS_QG + ((hw * 16 + kvh) * 512 + lane) * 8, tag, x);
#pragma unroll
      for (int k = 0; k < 4; ++k) ((unsigned short*)(lds + L_Q))[hw * 256 + k * 64 + lane] = f2bf(u2f(x[k].x) + u2f(x[k + 4].x));
      if (hw == 0) {
        int d = ag.y == tag ? (int)(ag.x - anc) : 0;
        d = d > 1600 ? 1600 : d < -1600 ? -1600 : d;
        d += dppi<XOR1>(d); d += dppi<XOR2>(d); d += dppi<HMIR>(d);
        if (lane == 0) cnt[C_ADJ] = (unsigned)(d / (8 * (31 - PRE)));
      }
      lds_arrive(cnt + (hw ? C_QB : C_QA), lane);
    }
#pragma nounroll
    for (int br = 0; br < 2; ++br) {
      lds_wait<8>(cnt + C_ML + br, 8u);
      const float* ml = lds + L_ML + br * 32 + hw * 8;
      const f32x4 m0 = *(const f32x4*)(ml), m1 = *(const f32x4*)(ml + 4), l0 = *(const f32x4*)(ml + 16), l1 = *(const f32x4*)(ml + 20);
      const float M = fmax_(fmax_(fmax_(m0.x, m0.y), fmax_(m0.z, m0.w)), fmax_(fmax_(m1.x, m1.y), fmax_(m1.z, m1.w)));
      float L = 0.f;
#pragma unroll
      for (int w = 0; w < 8; ++w) L = fmaf(__builtin_amdgcn_exp2f((w < 4 ? m0[w & 3] : m1[w & 3]) - M), w < 4 ? l0[w & 3] : l1[w & 3], L);
      const int mg = WS_MG + ((br * 16 + kvh) * 2 + hw) * 1024;
      st_gran2(rw, lane == 0 ? mg + (ch & 15) * 64 : OOB, tag, M, L);
      pause<kML>();
      v2u x[1];
      sweep<1, 0, 8>(rw, mg + n * 64 + (g & 1) * 8, tag, x);
      auto sw = __builtin_amdgcn_permlane16_swap(x[0].x, x[0].x, false, false);
      const float Ms = u2f(sw[0]), Ls = u2f(sw[1]);
      float Mx = fmax_(Ms, dpp<XOR1>(Ms)); Mx = fmax_(Mx, dpp<XOR2>(Mx)); Mx = fmax_(Mx, dpp<HMIR>(Mx)); Mx = fmax_(Mx, dpp<MIR>(Mx));
      float Lt = __builtin_amdgcn_exp2f(Ms - Mx) * Ls; Lt += dpp<XOR1>(Lt); Lt += dpp<XOR2>(Lt); Lt += dpp<HMIR>(Lt); Lt += dpp<MIR>(Lt);
      const float inv = 1.0f / Lt;
      float c[8];
#pragma unroll
      for (int w = 0; w < 8; ++w) c[w] = __builtin_amdgcn_exp2f((w < 4 ? m0[w & 3] : m1[w & 3]) - Mx) * inv;
      const unsigned oa = lds_addr(lds + L_O + br * 2048 + hw * 128 + 2 * lane);
      int dst = WS_PG + (br * 256 + ch) * PG_ROW + (hw * 128 + 2 * lane) * 8;
      asm volatile("" : "+v"(dst));
      float o0, o1;
      { const v2u v = lds_take2<8>(oa); o0 = c[0] * u2f(v.x); o1 = c[0] * u2f(v.y); }
#pragma unroll
      for (int w = 1; w < 8; ++w) { const v2u v = lds_take2<0>(oa + w * 1024); o0 = fmaf(c[w], u2f(v.x), o0); o1 = fmaf(c[w], u2f(v.y), o1); }
      st_gran2(rw, dst, tag, o0, o1);
      if (hw == 0 && lane == 0) __hip_atomic_store(cnt + C_PUB, (unsigned)br + 1u, RLX_WG);
    }
  } else {
    if (wave == 0) {
      unsigned long long ta;
      asm volatile("s_waitcnt vmcnt(0)\n\ts_memrealtime %0\n\ts_waitcnt lgkmcnt(0)" : "=s"(ta) : "v"(probe) : "memory");
      if (lane == 0) __hip_atomic_store(cnt + C_ANCHOR, ((unsigned)ta << 4) | 1u, RLX_WG);
    }
    St z;
    z.lds = lds; z.lane = lane; z.wave = wave; z.mask = mask;
    z.rs[0] = rk0;
    z.rs[1] = mkrs(ptr64(ka[8], ka[9]) + (size_t)ch * 262144, 262144);
    z.rs[2] = mkrs(ptr64(ka[6], ka[7]) + (size_t)ch * 262144, 262144);
    z.rs[3] = mkrs(ptr64(ka[10], ka[11]) + (size_t)ch * 262144, 262144);
    z.vk = ((lane >> 2) & 3) * 256 + g * 64 + (lane & 3) * 16; z.vv = lane * 16; z.so = wave * 1024;
    const int phase16 = (wave * kSlot16) >> 3;
    z.due = lds_wait<1>(cnt + C_ANCHOR, 1u) + (unsigned)(kOff16 + phase16);
    Req<0, PRE>::go(z);
    lds_wait<1>(cnt + C_QA, 1u);
    const unsigned tag = __builtin_amdgcn_readfirstlane(cnt[C_TAG]);
    z.adj = __builtin_amdgcn_readfirstlane(cnt[C_ADJ]);
    Con<0, PRE - DEP + 1, PRE - 1>::go(z);
    Run<PRE, 32>::go(z);
    Con<32 - DEP + 1, 32, 31>::go(z);
    const unsigned t32 = z.due - (unsigned)phase16;
    if (wave < 5) {
      const __amdgpu_buffer_rsrc_t ro = __builtin_amdgcn_make_buffer_rsrc(
          (rb ? ptr64(kb[2], kb[3]) : ptr64(kb[0], kb[1])) + (size_t)(kvh * 256 + s8 * 32) * DM * 2, 0, 32 * DM * 2, RSRC3);
      const int wcol = wave * 128 + n * 8;
      const int vwo = wcol < DM ? g * (DM * 2) + wcol * 2 : OOB;
      const unsigned oba = lds_addr(lds + L_OB + g * 8);
      v4u Wo[8];
      gate(t32 + (unsigned)(wave * kWoStep16));
#pragma unroll
      for (int i = 0; i < 8; ++i) Wo[i] = __builtin_amdgcn_raw_buffer_load_b128(ro, vwo, i * 4 * DM * 2, AUX_NT);
      int dst = wcol < DM ? WS_YG + (rb * 128 + pidx) * YG_ROW + (wcol + 4 * (g & 1) + 2 * (g >> 1)) * 8 : OOB;
      asm volatile("" : "+v"(dst), "+v"(Wo[0]), "+v"(Wo[1]), "+v"(Wo[2]), "+v"(Wo[3]), "+v"(Wo[4]), "+v"(Wo[5]), "+v"(Wo[6]), "+v"(Wo[7]));
      v4u oa, ob;
#pragma nounroll
      for (unsigned spins = 0; spins < LSPIN_CAP; ++spins) {
        asm volatile("ds_read_b128 %0, %2\n\tds_read_b128 %1, %2 offset:16\n\ts_waitcnt lgkmcnt(0)" : "=&v"(oa), "=&v"(ob) : "v"(oba) : "memory");
        if (__all(oa.x != EMPTY_B && oa.y != EMPTY_B && oa.z != EMPTY_B && oa.w != EMPTY_B && ob.x != EMPTY_B && ob.y != EMPTY_B && ob.z != EMPTY_B && ob.w != EMPTY_B)) break;
      }
      f32x4 Y0 = {0.f, 0.f, 0.f, 0.f}, Y1 = {0.f, 0.f, 0.f, 0.f};
#pragma unroll
      for (int i = 0; i < 4; ++i) { Y0 = mfma(diag(oa[i], mask), Wo[i], Y0); Y1 = mfma(diag(ob[i], mask), Wo[4 + i], Y1); }
      const f32x4 Y = Y0 + Y1;
      st_gran2(rw, dst, tag, g < 2 ? Y.x : Y.z, g < 2 ? Y.y : Y.w);
    } else if (wave == 5) {
      const int po = WS_PG + (rb * 256 + kvh * 16 + (lane >> 5)) * PG_ROW + (32 * s8 + (lane & 31)) * 8;
      v2u x[8];
      lds_wait<0>(cnt + C_PUB, (unsigned)rb + 1u);
      if (rb) pause<kHop1>();
      sweep<8, 2 * PG_ROW, 1>(rw, po, tag, x);
      const float o = x32_sum(sum8(x));
      if (lane < 32) ((unsigned*)(lds + L_OB))[(lane & 3) * 8 + (lane >> 2)] = dup_bf16(o);
    } else if (wave == 6 || pidx < 16) {
      const int col = (wave == 6 ? 0 : 512) + pidx * 4 + (lane & 3);
      GAS unsigned short* op = (GAS unsigned short*)ptr64(kb[4], kb[5]) + rb * DM + col;
      asm volatile("" : "+v"(op));
      v2u x[8];
#pragma nounroll
      for (unsigned spins = 0; spins < LSPIN_CAP; ++spins) {
        if (__builtin_amdgcn_readfirstlane(__hip_atomic_load((unsigned*)(lds + L_OB), RLX_WG)) != EMPTY_B) break;
        __builtin_amdgcn_s_sleep(1);
      }
      pause<kHop2>();
      gate(t32 + (unsigned)(4 * kWoStep16 + kHop2Gate16));
      sweep<8, 16 * YG_ROW, 1>(rw, WS_YG + (rb * 128 + (lane >> 2)) * YG_ROW + col * 8, tag, x);
      float y = sum8(x);
      y += dpp<ROR4>(y); y += dpp<ROR8>(y);
      y = xg_sum(y);
      if (lane < 4) *op = f2bf(y);
      if (rb == 0 && wave == 6 && pidx == 0 && lane == 0) __hip_atomic_store((GAS unsigned*)(ws + WS_EPOCH), tag, RLX_AGENT);
    }
  }
}

extern "C" void kernel_launch(void* const* d_in, const int* in_sizes, int n_in, void* d_out, int out_size, void* d_ws, size_t ws_size, hipStream_t stream) {
  Args a;
  a.x[0] = d_in[0]; a.k[0] = d_in[1]; a.v[0] = d_in[2]; a.wq[0] = d_in[3]; a.wo[0] = d_in[4];
  a.x[1] = d_in[5]; a.k[1] = d_in[6]; a.v[1] = d_in[7]; a.wq[1] = d_in[8]; a.wo[1] = d_in[9];
  a.out = d_out; a.ws = d_ws;
  gqa2_fused<<<NWG, NTHR, 0, stream>>>(a);
}
```

```cpp
#include <hip/hip_runtime.h>

typedef unsigned v2u __attribute__((ext_vector_type(2)));
typedef unsigned v4u __attribute__((ext_vector_type(4)));
typedef unsigned v8u __attribute__((ext_vector_type(8)));
typedef unsigned v16u __attribute__((ext_vector_type(16)));
typedef float f32x2 __attribute__((ext_vector_type(2)));
typedef float f32x4 __attribute__((ext_vector_type(4)));
typedef __bf16 bf16x2 __attribute__((ext_vector_type(2)));
typedef __bf16 bf16x8 __attribute__((ext_vector_type(8)));
#define GAS __attribute__((address_space(1)))
#define LAS __attribute__((address_space(3)))
#define RLX_AGENT __ATOMIC_RELAXED, __HIP_MEMORY_SCOPE_AGENT
#define RLX_WG __ATOMIC_RELAXED, __HIP_MEMORY_SCOPE_WORKGROUP
#define SB() __builtin_amdgcn_sched_barrier(0)

constexpr int DM = 576, NQ = 4096;
constexpr int NWG = 256, NTHR = 640;
constexpr float C2 = 0.088388347648318440f * 1.4426950408889634f;

#define DT16 1808
#define OFF16 600
constexpr int kSlot16 = DT16;
constexpr int kOff16 = OFF16;
constexpr int kPrio16 = 400;
constexpr int kWoStep16 = 600;
constexpr int kHop2Gate16 = 1700;
constexpr int kQA = 280, kQB = 600, kML = 350, kHop1 = 40, kHop2 = 50;
constexpr int PRE = 8;
constexpr int DEP = 3;
constexpr int LIM = 2;

constexpr int AUX_NT = 2, AUX_SC1 = 16, RSRC3 = 0x00020000, OOB = 0x7fff0000;
constexpr int WS_EPOCH = 0;
constexpr int WS_AG = 1024;
constexpr int WS_QG = 4096;
constexpr int WS_MG = WS_QG + 32 * 4096;
constexpr int WS_PG = WS_MG + 64 * 1024, PG_ROW = 256 * 8;
constexpr int WS_YG = WS_PG + 512 * PG_ROW, YG_ROW = DM * 8;
constexpr int WS_END = WS_YG + 256 * YG_ROW;
constexpr unsigned TAG_STEP = 0x9E3779B9u;
constexpr unsigned SPIN_CAP = 1u << 17, LSPIN_CAP = 1u << 22;
constexpr int L_O = 0;
constexpr int L_ML = L_O + 4096;
constexpr int L_QP = L_ML + 64;
constexpr int L_Q = L_QP + 256;
constexpr int L_OB = L_Q + 256;
constexpr int L_CNT = L_OB + 32, L_END = L_CNT + 16;
enum { C_QP, C_QA, C_ML  , C_PUB = 4, C_TAG, C_ANCHOR, C_QB, C_ADJ };
constexpr unsigned EMPTY_O = 0xffffffffu, EMPTY_B = 0x0000ffffu;

struct Args {
  const void* x[2]; const void* k[2]; const void* v[2]; const void* wq[2]; const void* wo[2];
  void* out; void* ws;
};

__device__ __forceinline__ f32x4 mfma(v4u a, v4u b, f32x4 c) {
  return __builtin_amdgcn_mfma_f32_16x16x32_bf16(__builtin_bit_cast(bf16x8, a), __builtin_bit_cast(bf16x8, b), c, 0, 0, 0);
}
__device__ __forceinline__ unsigned dup_bf16(float f) {
  f32x2 v = {f, f};
  return __builtin_bit_cast(unsigned, __builtin_convertvector(v, bf16x2));
}
__device__ __forceinline__ unsigned short f2bf(float f) { return __builtin_bit_cast(unsigned short, (__bf16)f); }
__device__ __forceinline__ float u2f(unsigned u) { return __uint_as_float(u); }
__device__ __forceinline__ v4u diag(unsigned pp, v4u mask) { v4u r = {pp & mask.x, pp & mask.y, pp & mask.z, pp & mask.w}; return r; }
template <int CTRL> __device__ __forceinline__ int dppi(int x) { return __builtin_amdgcn_mov_dpp(x, CTRL, 0xf, 0xf, true); }
template <int CTRL> __device__ __forceinline__ float dpp(float x) { return __builtin_bit_cast(float, dppi<CTRL>(__builtin_bit_cast(int, x))); }
constexpr int XOR1 = 0xB1, XOR2 = 0x4E, HMIR = 0x141, MIR = 0x140, ROR8 = 0x128, ROR4 = 0x124, BCAST = 0x150;
__device__ __forceinline__ float fmax_(float a, float b) { return __builtin_fmaxf(a, b); }
__device__ __forceinline__ float xg_max(float x) {
  auto s = __builtin_amdgcn_permlane16_swap(__float_as_uint(x), __float_as_uint(x), false, false);
  x = fmax_(u2f(s[0]), u2f(s[1]));
  auto t = __builtin_amdgcn_permlane32_swap(__float_as_uint(x), __float_as_uint(x), false, false);
  return fmax_(u2f(t[0]), u2f(t[1]));
}
__device__ __forceinline__ float x32_sum(float x) {
  auto t = __builtin_amdgcn_permlane32_swap(__float_as_uint(x), __float_as_uint(x), false, false);
  return u2f(t[0]) + u2f(t[1]);
}
__device__ __forceinline__ float xg_sum(float x) {
  auto s = __builtin_amdgcn_permlane16_swap(__float_as_uint(x), __float_as_uint(x), false, false);
  return x32_sum(u2f(s[0]) + u2f(s[1]));
}
__device__ __forceinline__ float sum8(const v2u (&x)[8]) {
  return ((u2f(x[0].x) + u2f(x[1].x)) + (u2f(x[2].x) + u2f(x[3].x))) + ((u2f(x[4].x) + u2f(x[5].x)) + (u2f(x[6].x) + u2f(x[7].x)));
}

__device__ __forceinline__ void st_gran2(__amdgpu_buffer_rsrc_t rs, int off, unsigned tag, float a, float b) {
  __builtin_amdgcn_raw_buffer_store_b128(v4u{__float_as_uint(a), tag, __float_as_uint(b), tag}, rs, off, 0, AUX_SC1);
}
__device__ __forceinline__ v2u ldg(__amdgpu_buffer_rsrc_t rs, int off) { return __builtin_amdgcn_raw_buffer_load_b64(rs, off, 0, AUX_SC1); }
template <int N, int STEP, int SLEEP> __device__ __forceinline__ void sweep(__amdgpu_buffer_rsrc_t rs, int off, unsigned tag, v2u (&x)[N]) {
#pragma unroll
  for (int k = 0; k < N; ++k) x[k] = ldg(rs, off + k * STEP);
  SB();
  bool ok = true;
#pragma unroll
  for (int k = 0; k < N; ++k) ok &= x[k].y == tag;
  if (__builtin_expect(__all(ok), 1)) return;
#pragma nounroll
  for (unsigned spins = 0; spins < SPIN_CAP; ++spins) {
    __builtin_amdgcn_s_sleep(SLEEP);
    v2u y[N];
#pragma unroll
    for (int k = 0; k < N; ++k) y[k] = ldg(rs, x[k].y == tag ? OOB : off + k * STEP);
    SB();
    ok = true;
#pragma unroll
    for (int k = 0; k < N; ++k) { x[k] = x[k].y == tag ? x[k] : y[k]; ok &= x[k].y == tag; }
    if (__all(ok)) break;
  }
}

__device__ __forceinline__ unsigned char* ptr64(unsigned lo, unsigned hi) { return (unsigned char*)(((unsigned long long)hi << 32) | lo); }
__device__ __forceinline__ v4u mkrs(const void* p, unsigned bytes) {
  const unsigned long long u = (unsigned long long)p;
  v4u r = {(unsigned)u, (unsigned)(u >> 32) & 0xffffu, bytes, (unsigned)RSRC3};
  return r;
}
__device__ __forceinline__ void tile_load(v4u (&d)[4], int voff, v4u rs, int soff) {
  asm volatile("buffer_load_dwordx4 %0, %4, %5, %6 offen nt\n\t"
               "buffer_load_dwordx4 %1, %4, %5, %7 offen nt\n\t"
               "buffer_load_dwordx4 %2, %4, %5, %8 offen nt\n\t"
               "buffer_load_dwordx4 %3, %4, %5, %9 offen nt"
               : "=&v"(d[0]), "=&v"(d[1]), "=&v"(d[2]), "=&v"(d[3])
               : "v"(voff), "s"(rs), "s"(soff), "s"(soff + 8192), "s"(soff + 16384), "s"(soff + 24576));
}
template <int N> __device__ __forceinline__ void tile_wait(v4u (&d)[4]) {
  asm volatile("s_waitcnt vmcnt(%4)" : "+v"(d[0]), "+v"(d[1]), "+v"(d[2]), "+v"(d[3]) : "i"(N));
}
template <int N> __device__ __forceinline__ void piece_wait(v4u& d) { asm volatile("s_waitcnt vmcnt(%1)" : "+v"(d) : "i"(N)); }

template <int TICKS> __device__ __forceinline__ void pause() {
  unsigned due;
  asm volatile("s_memrealtime vcc\n\t"
               "s_waitcnt lgkmcnt(0)\n\t"
               "s_add_i32 %0, vcc_lo, %1\n"
               ".Lg0_%=:\n\t"
               "s_sleep 1\n\t"
               "s_memrealtime vcc\n\t"
               "s_waitcnt lgkmcnt(0)\n\t"
               "s_sub_i32 vcc_lo, vcc_lo, %0\n\t"
               "s_cmp_lt_i32 vcc_lo, 0\n\t"
               "s_cbranch_scc1 .Lg0_%="
               : "=&s"(due) : "i"(TICKS) : "scc", "vcc", "memory");
}
__device__ __forceinline__ void gate(unsigned due16) {
  asm volatile("s_branch .Lg1_%=\n"
               ".Lg0_%=:\n\t"
               "s_sleep 1\n"
               ".Lg1_%=:\n\t"
               "s_memrealtime vcc\n\t"
               "s_waitcnt lgkmcnt(0)\n\t"
               "s_lshl_b32 vcc_lo, vcc_lo, 4\n\t"
               "s_sub_i32 vcc_lo, vcc_lo, %0\n\t"
               "s_cmp_lt_i32 vcc_lo, 0\n\t"
               "s_cbranch_scc1 .Lg0_%=\n\t"
               "s_setprio 0\n\t"
               "s_cmp_lt_i32 vcc_lo, %1\n\t"
               "s_cbranch_scc1 .Lg2_%=\n\t"
               "s_setprio 1\n"
               ".Lg2_%=:"
               :: "s"(due16), "i"(kPrio16) : "scc", "vcc", "memory");
}

__device__ __forceinline__ void lds_arrive(unsigned* c, int lane) {
  asm volatile("s_waitcnt lgkmcnt(0)" ::: "memory");
  if (lane == 0) __hip_atomic_fetch_add(c, 1u, RLX_WG);
}
template <int SLEEP> __device__ __forceinline__ unsigned lds_wait(unsigned* c, unsigned want) {
  unsigned v = 0;
#pragma nounroll
  for (unsigned spins = 0; spins < LSPIN_CAP; ++spins) {
    v = __builtin_amdgcn_readfirstlane(__hip_atomic_load(c, RLX_WG));
    if (v >= want) break;
    __builtin_amdgcn_s_sleep(SLEEP);
  }
  asm volatile("" ::: "memory");
  return v;
}
__device__ __forceinline__ unsigned lds_addr(const void* p) { return (unsigned)(size_t)(const LAS void*)p; }
template <int SLEEP> __device__ __forceinline__ v2u lds_take2(unsigned addr) {
  v2u v;
#pragma nounroll
  for (unsigned spins = 0; spins < LSPIN_CAP; ++spins) {
    asm volatile("ds_read_b64 %0, %1\n\ts_waitcnt lgkmcnt(0)" : "=&v"(v) : "v"(addr) : "memory");
    if (__all(v.x != EMPTY_O && v.y != EMPTY_O)) break;
    if (SLEEP) __builtin_amdgcn_s_sleep(SLEEP);
  }
  return v;
}

struct St {
  v4u T[32][4];
  float Sc[8][4];
  unsigned P[8][4];
  v4u Q, mask;
  f32x4 O;
  v4u rs[4];
  int vk, vv, so;
  unsigned due, adj;
  float* lds;
  int lane, wave;
};
template <int I> __device__ __forceinline__ void request(St& z) {
  if constexpr (I > 0) asm volatile("s_waitcnt vmcnt(%0)" :: "i"(LIM) : "memory");
  gate(z.due);
  z.due += kSlot16;
  if constexpr (I >= PRE && I < 31) z.due += z.adj;
  tile_load(z.T[I], (I & 8) ? z.vv : z.vk, z.rs[I >> 3], z.so + (I & 7) * 32768);
  SB();
}
template <int I, int W> __device__ __forceinline__ void consume(St& z) {
  const int g = z.lane >> 4, n = z.lane & 15;
  constexpr int br = I >> 4;
  unsigned* const cnt = (unsigned*)(z.lds + L_CNT);
  if constexpr ((I & 15) == 0) {
    if constexpr (br) lds_wait<1>(cnt + C_QB, 1u);
    z.Q = *(const v4u*)((const unsigned short*)(z.lds + L_Q) + br * 256 + (n >> 3) * 128 + 32 * g + 8 * (n & 3));
  }
  if constexpr ((I & 8) == 0) {
    tile_wait<W>(z.T[I]);
#pragma unroll
    for (int j = 0; j < 4; ++j) {
      const f32x4 d = mfma(z.T[I][j], z.Q, f32x4{0.f, 0.f, 0.f, 0.f});
      z.Sc[I & 7][j] = (dpp<0x00>(d.x) + dpp<0x55>(d.y)) + (dpp<0xAA>(d.z) + dpp<0xFF>(d.w));
    }
    if constexpr ((I & 7) == 7) {
      float m = z.Sc[0][0];
#pragma unroll
      for (int t = 0; t < 8; ++t)
#pragma unroll
        for (int j = 0; j < 4; ++j) m = fmax_(m, z.Sc[t][j]);
      const float m2 = xg_max(m) * C2;
      float l = 0.f;
#pragma unroll
      for (int t = 0; t < 8; ++t) {
        float p[4];
#pragma unroll
        for (int j = 0; j < 4; ++j) { p[j] = __builtin_amdgcn_exp2f(fmaf(z.Sc[t][j], C2, -m2)); z.P[t][j] = dup_bf16(p[j]); }
        l += (p[0] + p[1]) + (p[2] + p[3]);
        asm volatile("" : "+v"(z.P[t][0]), "+v"(z.P[t][1]), "+v"(z.P[t][2]), "+v"(z.P[t][3]));
      }
      l = xg_sum(l);
      if (g == 0 && (n & 7) == 0) { z.lds[L_ML + br * 32 + (n >> 3) * 8 + z.wave] = m2; z.lds[L_ML + br * 32 + 16 + (n >> 3) * 8 + z.wave] = l; }
      lds_arrive(cnt + C_ML + br, z.lane);
      z.O = f32x4{0.f, 0.f, 0.f, 0.f};
    } else {
      asm volatile("" : "+v"(z.Sc[I & 7][0]), "+v"(z.Sc[I & 7][1]), "+v"(z.Sc[I & 7][2]), "+v"(z.Sc[I & 7][3]));
    }
  } else if constexpr (W == 0) {
    v4u A[4];
#pragma unroll
    for (int j = 0; j < 4; ++j) A[j] = diag(z.P[I & 7][j], z.mask);
    unsigned da = lds_addr(z.lds + L_O + br * 2048 + z.wave * 256 + (g >> 1) * 128 + 8 * n + 4 * (g & 1));
    asm volatile("" : "+v"(A[0]), "+v"(A[1]), "+v"(A[2]), "+v"(A[3]), "+v"(da));
    piece_wait<3>(z.T[I][0]); z.O = mfma(A[0], z.T[I][0], z.O); SB();
    piece_wait<2>(z.T[I][1]); z.O = mfma(A[1], z.T[I][1], z.O); SB();
    piece_wait<1>(z.T[I][2]); z.O = mfma(A[2], z.T[I][2], z.O); SB();
    piece_wait<0>(z.T[I][3]); z.O = mfma(A[3], z.T[I][3], z.O);
    *(LAS f32x4*)(size_t)da = z.O;
  } else {
    tile_wait<W>(z.T[I]);
#pragma unroll
    for (int j = 0; j < 4; ++j) z.O = mfma(diag(z.P[I & 7][j], z.mask), z.T[I][j], z.O);
    asm volatile("" : "+v"(z.O));
    if constexpr ((I & 7) == 7) *(f32x4*)(z.lds + L_O + br * 2048 + z.wave * 256 + (g >> 1) * 128 + 8 * n + 4 * (g & 1)) = z.O;
  }
  SB();
}
template <int I, int N> struct Req { static __device__ __forceinline__ void go(St& z) { request<I>(z); Req<I + 1, N>::go(z); } };
template <int N> struct Req<N, N> { static __device__ __forceinline__ void go(St&) {} };
template <int I, int N, int LAST> struct Con {
  static __device__ __forceinline__ void go(St& z) { consume<I, 4 * (LAST - I)>(z); Con<I + 1, N, LAST>::go(z); }
};
template <int N, int LAST> struct Con<N, N, LAST> { static __device__ __forceinline__ void go(St&) {} };
template <int I, int N> struct Run {
  static __device__ __forceinline__ void go(St& z) { request<I>(z); consume<I - DEP + 1, 4 * (DEP - 1)>(z); Run<I + 1, N>::go(z); }
};
template <int N> struct Run<N, N> { static __device__ __forceinline__ void go(St&) {} };

__global__ void __launch_bounds__(NTHR, 3) gqa2_fused(Args) {
  __shared__ __attribute__((aligned(16))) float lds[L_END];
  unsigned* const cnt = (unsigned*)(lds + L_CNT);
  v16u ka; v8u kb;
  asm volatile("s_load_dwordx16 %0, %2, 0x0\n\ts_load_dwordx8 %1, %2, 0x40" : "=&s"(ka), "=&s"(kb) : "s"(__builtin_amdgcn_kernarg_segment_ptr()));
  const int tid = threadIdx.x, lane = tid & 63, wave = __builtin_amdgcn_readfirstlane(tid >> 6);
  const int g = lane >> 4, n = lane & 15;
  const int wg = blockIdx.x;
  const int ch = (wg & 7) << 5 | wg >> 3;
  const int kvh = ch >> 4, rb = (ch >> 3) & 1, s8 = ch & 7;
  {
    const v4u e = {EMPTY_O, EMPTY_O, EMPTY_O, EMPTY_O};
    const int i = tid < 512 ? tid : 0, j = tid < 48 ? tid : 0;
    *(v4u*)(lds + L_O + i * 4) = e; *(v4u*)(lds + L_O + 2048 + i * 4) = e;
    ((unsigned*)(lds + L_OB))[j] = j < 32 ? EMPTY_B : 0u;
  }
  asm volatile("s_waitcnt lgkmcnt(0)" : "+s"(ka), "+s"(kb) :: "memory");
  const v4u rk0 = mkrs(ptr64(ka[4], ka[5]) + (size_t)ch * 262144, 262144);
  unsigned probe;
  asm volatile("s_cmp_lg_u32 %2, 0\n\ts_cbranch_scc1 .Lp%=\n\ts_mov_b64 exec, 1\n\tbuffer_load_dword %0, off, %1, 0 nt\n\ts_mov_b64 exec, -1\n.Lp%=:"
               : "=v"(probe) : "s"(rk0), "s"(wave) : "scc");
  __builtin_amdgcn_s_barrier();
  asm volatile("" ::: "memory");
  unsigned char* const ws = ptr64(kb[6], kb[7]);
  const __amdgpu_buffer_rsrc_t rw = __builtin_amdgcn_make_buffer_rsrc(ws, 0, WS_END, RSRC3);
  v4u mask;
  {
    const unsigned half = (n & 1) ? 0xffff0000u : 0x0000ffffu; const int r = (n & 7) >> 1;
    mask.x = r == 0 ? half : 0u; mask.y = r == 1 ? half : 0u; mask.z = r == 2 ? half : 0u; mask.w = r == 3 ? half : 0u;
  }
  const int pidx = kvh * 8 + s8;

  if (__builtin_expect(wave >= 8, 1)) {
    __builtin_amdgcn_s_setprio(3);
    const int hw = wave - 8, cg = s8 & 3, kh = s8 >> 2, sub = 2 * g + (n >> 3);
    const __amdgpu_buffer_rsrc_t rq = __builtin_amdgcn_make_buffer_rsrc(
        (rb ? ptr64(ka[14], ka[15]) : ptr64(ka[12], ka[13])) + ((size_t)(kh * 288 + hw * 144) * NQ + kvh * 256 + cg * 64) * 2, 0, 143 * NQ * 2 + 128, RSRC3);
    const __amdgpu_buffer_rsrc_t rx = __builtin_amdgcn_make_buffer_rsrc((rb ? ptr64(ka[2], ka[3]) : ptr64(ka[0], ka[1])) + (kh * 288 + hw * 144) * 2, 0, 144 * 2, RSRC3);
    v4u wqv[18];
#pragma unroll
    for (int i = 0; i < 18; ++i) wqv[i] = __builtin_amdgcn_raw_buffer_load_b128(rq, sub * (NQ * 2) + (n & 7) * 16, i * 8 * NQ * 2, AUX_NT);
    const unsigned xa = __builtin_amdgcn_raw_buffer_load_b32(rx, n * 16 + g * 4, 0, 0), xb = __builtin_amdgcn_raw_buffer_load_b32(rx, n * 16 + g * 4 + 256, 0, 0);
    const unsigned ep = __builtin_amdgcn_raw_buffer_load_b32(rw, lane == 0 ? WS_EPOCH : OOB, 0, AUX_SC1);
    SB();
    f32x4 a0 = {0.f, 0.f, 0.f, 0.f}, a1 = {0.f, 0.f, 0.f, 0.f};
    const unsigned sel = n < 8 ? 0x01000100u : 0x03020302u;
#define QSTEP(acc, i, src, k) acc = mfma(diag(__builtin_amdgcn_perm(0u, (unsigned)dppi<BCAST + (k)>((int)(src)), sel), mask), wqv[i], acc)
    QSTEP(a0, 0, xa, 0); QSTEP(a1, 1, xa, 1); QSTEP(a0, 2, xa, 2); QSTEP(a1, 3, xa, 3); QSTEP(a0, 4, xa, 4); QSTEP(a1, 5, xa, 5);
    QSTEP(a0, 6, xa, 6); QSTEP(a1, 7, xa, 7); QSTEP(a0, 8, xa, 8); QSTEP(a1, 9, xa, 9); QSTEP(a0, 10, xa, 10); QSTEP(a1, 11, xa, 11);
    QSTEP(a0, 12, xa, 12); QSTEP(a1, 13, xa, 13); QSTEP(a0, 14, xa, 14); QSTEP(a1, 15, xa, 15); QSTEP(a0, 16, xb, 0); QSTEP(a1, 17, xb, 1);
#undef QSTEP
    if ((g >> 1) == (n >> 3)) *(f32x4*)(lds + L_QP + (hw * 2 + (n >> 3)) * 64 + 8 * (n & 7) + 4 * (g & 1)) = a0 + a1;
    const unsigned tag = __builtin_amdgcn_readfirstlane(ep) + TAG_STEP;
    lds_arrive(cnt + C_QP, lane);
    unsigned anc = 0;
    if (hw == 0) {
      lds_wait<1>(cnt + C_QP, 2u);
      const f32x2* qp = (const f32x2*)(lds + L_QP) + (lane & 31);
      const f32x2 q = (qp[0] + qp[32]) + (qp[64] + qp[96]);
      st_gran2(rw, lane < 32 ? WS_QG + ((rb * 16 + kvh) * 512 + kh * 256 + cg * 64 + 2 * lane) * 8 : OOB, tag, q.x, q.y);
      if (lane == 0) cnt[C_TAG] = tag;
      anc = lds_wait<1>(cnt + C_ANCHOR, 1u);
      if (lane == 0) __hip_atomic_store((GAS unsigned long long*)(ws + WS_AG + wg * 8), ((unsigned long long)tag << 32) | anc, RLX_AGENT);
    }
    pause<kQA>();
    if (hw) pause<kQB - kQA>();
    {
      v2u x[8];
      const v2u ag = ldg(rw, (hw == 0 && lane < 8) ? WS_AG + (wg & ~7) * 8 + lane * 8 : OOB);
      sweep<8, 512, 8>(rw, WS_QG + ((hw * 16 + kvh) * 512 + lane) * 8, tag, x);
#pragma unroll
      for (int k = 0; k < 4; ++k) ((unsigned short*)(lds + L_Q))[hw * 256 + k * 64 + lane] = f2bf(u2f(x[k].x) + u2f(x[k + 4].x));
      if (hw == 0) {
        int d = ag.y == tag ? (int)(ag.x - anc) : 0;
        d = d > 1600 ? 1600 : d < -1600 ? -1600 : d;
        d += dppi<XOR1>(d); d += dppi<XOR2>(d); d += dppi<HMIR>(d);
        if (lane == 0) cnt[C_ADJ] = (unsigned)(d / (8 * (31 - PRE)));
      }
      lds_arrive(cnt + (hw ? C_QB : C_QA), lane);
    }
#pragma nounroll
    for (int br = 0; br < 2; ++br) {
      lds_wait<8>(cnt + C_ML + br, 8u);
      const float* ml = lds + L_ML + br * 32 + hw * 8;
      const f32x4 m0 = *(const f32x4*)(ml), m1 = *(const f32x4*)(ml + 4), l0 = *(const f32x4*)(ml + 16), l1 = *(const f32x4*)(ml + 20);
      const float M = fmax_(fmax_(fmax_(m0.x, m0.y), fmax_(m0.z, m0.w)), fmax_(fmax_(m1.x, m1.y), fmax_(m1.z, m1.w)));
      float L = 0.f;
#pragma unroll
      for (int w = 0; w < 8; ++w) L = fmaf(__builtin_amdgcn_exp2f((w < 4 ? m0[w & 3] : m1[w & 3]) - M), w < 4 ? l0[w & 3] : l1[w & 3], L);
      const int mg = WS_MG + ((br * 16 + kvh) * 2 + hw) * 1024;
      st_gran2(rw, lane == 0 ? mg + (ch & 15) * 64 : OOB, tag, M, L);
      pause<kML>();
      v2u x[1];
      sweep<1, 0, 8>(rw, mg + n * 64 + (g & 1) * 8, tag, x);
      auto sw = __builtin_amdgcn_permlane16_swap(x[0].x, x[0].x, false, false);
      const float Ms = u2f(sw[0]), Ls = u2f(sw[1]);
      float Mx = fmax_(Ms, dpp<XOR1>(Ms)); Mx = fmax_(Mx, dpp<XOR2>(Mx)); Mx = fmax_(Mx, dpp<HMIR>(Mx)); Mx = fmax_(Mx, dpp<MIR>(Mx));
      float Lt = __builtin_amdgcn_exp2f(Ms - Mx) * Ls; Lt += dpp<XOR1>(Lt); Lt += dpp<XOR2>(Lt); Lt += dpp<HMIR>(Lt); Lt += dpp<MIR>(Lt);
      const float inv = 1.0f / Lt;
      float c[8];
#pragma unroll
      for (int w = 0; w < 8; ++w) c[w] = __builtin_amdgcn_exp2f((w < 4 ? m0[w & 3] : m1[w & 3]) - Mx) * inv;
      const unsigned oa = lds_addr(lds + L_O + br * 2048 + hw * 128 + 2 * lane);
      int dst = WS_PG + (br * 256 + ch) * PG_ROW + (hw * 128 + 2 * lane) * 8;
      asm volatile("" : "+v"(dst));
      float o0, o1;
      { const v2u v = lds_take2<8>(oa); o0 = c[0] * u2f(v.x); o1 = c[0] * u2f(v.y); }
#pragma unroll
      for (int w = 1; w < 8; ++w) { const v2u v = lds_take2<0>(oa + w * 1024); o0 = fmaf(c[w], u2f(v.x), o0); o1 = fmaf(c[w], u2f(v.y), o1); }
      st_gran2(rw, dst, tag, o0, o1);
      if (hw == 0 && lane == 0) __hip_atomic_store(cnt + C_PUB, (unsigned)br + 1u, RLX_WG);
    }
  } else {
    if (wave == 0) {
      unsigned long long ta;
      asm volatile("s_waitcnt vmcnt(0)\n\ts_memrealtime %0\n\ts_waitcnt lgkmcnt(0)" : "=s"(ta) : "v"(probe) : "memory");
      if (lane == 0) __hip_atomic_store(cnt + C_ANCHOR, ((unsigned)ta << 4) | 1u, RLX_WG);
    }
    St z;
    z.lds = lds; z.lane = lane; z.wave = wave; z.mask = mask;
    z.rs[0] = rk0;
    z.rs[1] = mkrs(ptr64(ka[8], ka[9]) + (size_t)ch * 262144, 262144);
    z.rs[2] = mkrs(ptr64(ka[6], ka[7]) + (size_t)ch * 262144, 262144);
    z.rs[3] = mkrs(ptr64(ka[10], ka[11]) + (size_t)ch * 262144, 262144);
    z.vk = ((lane >> 2) & 3) * 256 + g * 64 + (lane & 3) * 16; z.vv = lane * 16; z.so = wave * 1024;
    const int phase16 = (wave * kSlot16) >> 3;
    z.due = lds_wait<1>(cnt + C_ANCHOR, 1u) + (unsigned)(kOff16 + phase16);
    Req<0, PRE>::go(z);
    lds_wait<1>(cnt + C_QA, 1u);
    const unsigned tag = __builtin_amdgcn_readfirstlane(cnt[C_TAG]);
    z.adj = __builtin_amdgcn_readfirstlane(cnt[C_ADJ]);
    Con<0, PRE - DEP + 1, PRE - 1>::go(z);
    Run<PRE, 32>::go(z);
    Con<32 - DEP + 1, 32, 31>::go(z);
    const unsigned t32 = z.due - (unsigned)phase16;
    if (wave < 5) {
      const __amdgpu_buffer_rsrc_t ro = __builtin_amdgcn_make_buffer_rsrc(
          (rb ? ptr64(kb[2], kb[3]) : ptr64(kb[0], kb[1])) + (size_t)(kvh * 256 + s8 * 32) * DM * 2, 0, 32 * DM * 2, RSRC3);
      const int wcol = wave * 128 + n * 8;
      const int vwo = wcol < DM ? g * (DM * 2) + wcol * 2 : OOB;
      const unsigned oba = lds_addr(lds + L_OB + g * 8);
      v4u Wo[8];
      gate(t32 + (unsigned)(wave * kWoStep16));
#pragma unroll
      for (int i = 0; i < 8; ++i) Wo[i] = __builtin_amdgcn_raw_buffer_load_b128(ro, vwo, i * 4 * DM * 2, AUX_NT);
      int dst = wcol < DM ? WS_YG + (rb * 128 + pidx) * YG_ROW + (wcol + 4 * (g & 1) + 2 * (g >> 1)) * 8 : OOB;
      asm volatile("" : "+v"(dst), "+v"(Wo[0]), "+v"(Wo[1]), "+v"(Wo[2]), "+v"(Wo[3]), "+v"(Wo[4]), "+v"(Wo[5]), "+v"(Wo[6]), "+v"(Wo[7]));
      v4u oa, ob;
#pragma nounroll
      for (unsigned spins = 0; spins < LSPIN_CAP; ++spins) {
        asm volatile("ds_read_b128 %0, %2\n\tds_read_b128 %1, %2 offset:16\n\ts_waitcnt lgkmcnt(0)" : "=&v"(oa), "=&v"(ob) : "v"(oba) : "memory");
        if (__all(oa.x != EMPTY_B && oa.y != EMPTY_B && oa.z != EMPTY_B && oa.w != EMPTY_B && ob.x != EMPTY_B && ob.y != EMPTY_B && ob.z != EMPTY_B && ob.w != EMPTY_B)) break;
      }
      f32x4 Y0 = {0.f, 0.f, 0.f, 0.f}, Y1 = {0.f, 0.f, 0.f, 0.f};
#pragma unroll
      for (int i = 0; i < 4; ++i) { Y0 = mfma(diag(oa[i], mask), Wo[i], Y0); Y1 = mfma(diag(ob[i], mask), Wo[4 + i], Y1); }
      const f32x4 Y = Y0 + Y1;
      st_gran2(rw, dst, tag, g < 2 ? Y.x : Y.z, g < 2 ? Y.y : Y.w);
    } else if (wave == 5) {
      const int po = WS_PG + (rb * 256 + kvh * 16 + (lane >> 5)) * PG_ROW + (32 * s8 + (lane & 31)) * 8;
      v2u x[8];
      lds_wait<0>(cnt + C_PUB, (unsigned)rb + 1u);
      if (rb) pause<kHop1>();
      sweep<8, 2 * PG_ROW, 1>(rw, po, tag, x);
      const float o = x32_sum(sum8(x));
      if (lane < 32) ((unsigned*)(lds + L_OB))[(lane & 3) * 8 + (lane >> 2)] = dup_bf16(o);
    } else if (wave == 6 || pidx < 16) {
      const int col = (wave == 6 ? 0 : 512) + pidx * 4 + (lane & 3);
      GAS unsigned short* op = (GAS unsigned short*)ptr64(kb[4], kb[5]) + rb * DM + col;
      asm volatile("" : "+v"(op));
      v2u x[8];
#pragma nounroll
      for (unsigned spins = 0; spins < LSPIN_CAP; ++spins) {
        if (__builtin_amdgcn_readfirstlane(__hip_atomic_load((unsigned*)(lds + L_OB), RLX_WG)) != EMPTY_B) break;
        __builtin_amdgcn_s_sleep(1);
      }
      pause<kHop2>();
      gate(t32 + (unsigned)(4 * kWoStep16 + kHop2Gate16));
      sweep<8, 16 * YG_ROW, 1>(rw, WS_YG + (rb * 128 + (lane >> 2)) * YG_ROW + col * 8, tag, x);
      float y = sum8(x);
      y += dpp<ROR4>(y); y += dpp<ROR8>(y);
      y = xg_sum(y);
      if (lane < 4) *op = f2bf(y);
      if (rb == 0 && wave == 6 && pidx == 0 && lane == 0) __hip_atomic_store((GAS unsigned*)(ws + WS_EPOCH), tag, RLX_AGENT);
    }
  }
}

extern "C" void kernel_launch(void* const* d_in, const int* in_sizes, int n_in, void* d_out, int out_size, void* d_ws, size_t ws_size, hipStream_t stream) {
  Args a;
  a.x[0] = d_in[0]; a.k[0] = d_in[1]; a.v[0] = d_in[2]; a.wq[0] = d_in[3]; a.wo[0] = d_in[4];
  a.x[1] = d_in[5]; a.k[1] = d_in[6]; a.v[1] = d_in[7]; a.wq[1] = d_in[8]; a.wo[1] = d_in[9];
  a.out = d_out; a.ws = d_ws;
  gqa2_fused<<<NWG, NTHR, 0, stream>>>(a);
}
```

```cpp
#define OFF16 1800
#define DT16 1736
#include <hip/hip_runtime.h>

typedef unsigned v2u __attribute__((ext_vector_type(2)));
typedef unsigned v4u __attribute__((ext_vector_type(4)));
typedef unsigned v8u __attribute__((ext_vector_type(8)));
typedef unsigned v16u __attribute__((ext_vector_type(16)));
typedef float f32x2 __attribute__((ext_vector_type(2)));
typedef float f32x4 __attribute__((ext_vector_type(4)));
typedef __bf16 bf16x2 __attribute__((ext_vector_type(2)));
typedef __bf16 bf16x8 __attribute__((ext_vector_type(8)));
#define GAS __attribute__((address_space(1)))
#define LAS __attribute__((address_space(3)))
#define RLX_AGENT __ATOMIC_RELAXED, __HIP_MEMORY_SCOPE_AGENT
#define RLX_WG __ATOMIC_RELAXED, __HIP_MEMORY_SCOPE_WORKGROUP
#define SB() __builtin_amdgcn_sched_barrier(0)

constexpr int DM = 576, NQ = 4096;
constexpr int NWG = 256, NTHR = 640;
constexpr float C2 = 0.088388347648318440f * 1.4426950408889634f;

constexpr int kSlot16 = DT16;
constexpr int kOff16 = OFF16;
constexpr int kPrio16 = 400;
constexpr int kWoStep16 = 600;
constexpr int kHop2Gate16 = 2000;
constexpr int kQA = 280, kQB = 600, kML = 350, kHop1 = 40, kHop2 = 70;
constexpr int PRE = 8;
constexpr int DEP = 3;
constexpr int LIM = 2;

constexpr int AUX_NT = 2, AUX_SC1 = 16, RSRC3 = 0x00020000, OOB = 0x7fff0000;
constexpr int WS_EPOCH = 0;
constexpr int WS_AG = 1024;
constexpr int WS_QG = 4096;
constexpr int WS_MG = WS_QG + 32 * 4096;
constexpr int WS_PG = WS_MG + 64 * 1024, PG_ROW = 256 * 8;
constexpr int WS_YG = WS_PG + 512 * PG_ROW, YG_ROW = DM * 8;
constexpr int WS_END = WS_YG + 256 * YG_ROW;
constexpr unsigned TAG_STEP = 0x9E3779B9u;
constexpr unsigned SPIN_CAP = 1u << 17, LSPIN_CAP = 1u << 22;
constexpr int L_O = 0;
constexpr int L_ML = L_O + 4096;
constexpr int L_QP = L_ML + 64;
constexpr int L_Q = L_QP + 256;
constexpr int L_OB = L_Q + 256;
constexpr int L_CNT = L_OB + 32, L_END = L_CNT + 16;
enum { C_QP, C_QA, C_ML  , C_PUB = 4, C_TAG, C_ANCHOR, C_QB, C_ADJ };
constexpr unsigned EMPTY_O = 0xffffffffu, EMPTY_B = 0x0000ffffu;

struct Args {
  const void* x[2]; const void* k[2]; const void* v[2]; const void* wq[2]; const void* wo[2];
  void* out; void* ws;
};

__device__ __forceinline__ f32x4 mfma(v4u a, v4u b, f32x4 c) {
  return __builtin_amdgcn_mfma_f32_16x16x32_bf16(__builtin_bit_cast(bf16x8, a), __builtin_bit_cast(bf16x8, b), c, 0, 0, 0);
}
__device__ __forceinline__ unsigned dup_bf16(float f) {
  f32x2 v = {f, f};
  return __builtin_bit_cast(unsigned, __builtin_convertvector(v, bf16x2));
}
__device__ __forceinline__ unsigned short f2bf(float f) { return __builtin_bit_cast(unsigned short, (__bf16)f); }
__device__ __forceinline__ float u2f(unsigned u) { return __uint_as_float(u); }
__device__ __forceinline__ v4u diag(unsigned pp, v4u mask) { v4u r = {pp & mask.x, pp & mask.y, pp & mask.z, pp & mask.w}; return r; }
template <int CTRL> __device__ __forceinline__ int dppi(int x) { return __builtin_amdgcn_mov_dpp(x, CTRL, 0xf, 0xf, true); }
template <int CTRL> __device__ __forceinline__ float dpp(float x) { return __builtin_bit_cast(float, dppi<CTRL>(__builtin_bit_cast(int, x))); }
constexpr int XOR1 = 0xB1, XOR2 = 0x4E, HMIR = 0x141, MIR = 0x140, ROR8 = 0x128, ROR4 = 0x124, BCAST = 0x150;
__device__ __forceinline__ float fmax_(float a, float b) { return __builtin_fmaxf(a, b); }
__device__ __forceinline__ float xg_max(float x) {
  auto s = __builtin_amdgcn_permlane16_swap(__float_as_uint(x), __float_as_uint(x), false, false);
  x = fmax_(u2f(s[0]), u2f(s[1]));
  auto t = __builtin_amdgcn_permlane32_swap(__float_as_uint(x), __float_as_uint(x), false, false);
  return fmax_(u2f(t[0]), u2f(t[1]));
}
__device__ __forceinline__ float x32_sum(float x) {
  auto t = __builtin_amdgcn_permlane32_swap(__float_as_uint(x), __float_as_uint(x), false, false);
  return u2f(t[0]) + u2f(t[1]);
}
__device__ __forceinline__ float xg_sum(float x) {
  auto s = __builtin_amdgcn_permlane16_swap(__float_as_uint(x), __float_as_uint(x), false, false);
  return x32_sum(u2f(s[0]) + u2f(s[1]));
}
__device__ __forceinline__ float sum8(const v2u (&x)[8]) {
  return ((u2f(x[0].x) + u2f(x[1].x)) + (u2f(x[2].x) + u2f(x[3].x))) + ((u2f(x[4].x) + u2f(x[5].x)) + (u2f(x[6].x) + u2f(x[7].x)));
}

__device__ __forceinline__ void st_gran2(__amdgpu_buffer_rsrc_t rs, int off, unsigned tag, float a, float b) {
  __builtin_amdgcn_raw_buffer_store_b128(v4u{__float_as_uint(a), tag, __float_as_uint(b), tag}, rs, off, 0, AUX_SC1);
}
__device__ __forceinline__ v2u ldg(__amdgpu_buffer_rsrc_t rs, int off) { return __builtin_amdgcn_raw_buffer_load_b64(rs, off, 0, AUX_SC1); }
template <int N, int STEP, int SLEEP> __device__ __forceinline__ void sweep(__amdgpu_buffer_rsrc_t rs, int off, unsigned tag, v2u (&x)[N]) {
#pragma unroll
  for (int k = 0; k < N; ++k) x[k] = ldg(rs, off + k * STEP);
  SB();
  bool ok = true;
#pragma unroll
  for (int k = 0; k < N; ++k) ok &= x[k].y == tag;
  if (__builtin_expect(__all(ok), 1)) return;
#pragma nounroll
  for (unsigned spins = 0; spins < SPIN_CAP; ++spins) {
    __builtin_amdgcn_s_sleep(SLEEP);
    v2u y[N];
#pragma unroll
    for (int k = 0; k < N; ++k) y[k] = ldg(rs, x[k].y == tag ? OOB : off + k * STEP);
    SB();
    ok = true;
#pragma unroll
    for (int k = 0; k < N; ++k) { x[k] = x[k].y == tag ? x[k] : y[k]; ok &= x[k].y == tag; }
    if (__all(ok)) break;
  }
}

__device__ __forceinline__ unsigned char* ptr64(unsigned lo, unsigned hi) { return (unsigned char*)(((unsigned long long)hi << 32) | lo); }
__device__ __forceinline__ v4u mkrs(const void* p, unsigned bytes) {
  const unsigned long long u = (unsigned long long)p;
  v4u r = {(unsigned)u, (unsigned)(u >> 32) & 0xffffu, bytes, (unsigned)RSRC3};
  return r;
}
__device__ __forceinline__ void tile_load(v4u (&d)[4], int voff, v4u rs, int soff) {
  asm volatile("buffer_load_dwordx4 %0, %4, %5, %6 offen nt\n\t"
               "buffer_load_dwordx4 %1, %4, %5, %7 offen nt\n\t"
               "buffer_load_dwordx4 %2, %4, %5, %8 offen nt\n\t"
               "buffer_load_dwordx4 %3, %4, %5, %9 offen nt"
               : "=&v"(d[0]), "=&v"(d[1]), "=&v"(d[2]), "=&v"(d[3])
               : "v"(voff), "s"(rs), "s"(soff), "s"(soff + 8192), "s"(soff + 16384), "s"(soff + 24576));
}
template <int N> __device__ __forceinline__ void tile_wait(v4u (&d)[4]) {
  asm volatile("s_waitcnt vmcnt(%4)" : "+v"(d[0]), "+v"(d[1]), "+v"(d[2]), "+v"(d[3]) : "i"(N));
}
template <int N> __device__ __forceinline__ void piece_wait(v4u& d) { asm volatile("s_waitcnt vmcnt(%1)" : "+v"(d) : "i"(N)); }

template <int TICKS> __device__ __forceinline__ void pause() {
  unsigned due;
  asm volatile("s_memrealtime vcc\n\t"
               "s_waitcnt lgkmcnt(0)\n\t"
               "s_add_i32 %0, vcc_lo, %1\n"
               ".Lg0_%=:\n\t"
               "s_sleep 1\n\t"
               "s_memrealtime vcc\n\t"
               "s_waitcnt lgkmcnt(0)\n\t"
               "s_sub_i32 vcc_lo, vcc_lo, %0\n\t"
               "s_cmp_lt_i32 vcc_lo, 0\n\t"
               "s_cbranch_scc1 .Lg0_%="
               : "=&s"(due) : "i"(TICKS) : "scc", "vcc", "memory");
}
__device__ __forceinline__ void gate(unsigned due16) {
  asm volatile("s_branch .Lg1_%=\n"
               ".Lg0_%=:\n\t"
               "s_sleep 1\n"
               ".Lg1_%=:\n\t"
               "s_memrealtime vcc\n\t"
               "s_waitcnt lgkmcnt(0)\n\t"
               "s_lshl_b32 vcc_lo, vcc_lo, 4\n\t"
               "s_sub_i32 vcc_lo, vcc_lo, %0\n\t"
               "s_cmp_lt_i32 vcc_lo, 0\n\t"
               "s_cbranch_scc1 .Lg0_%=\n\t"
               "s_setprio 0\n\t"
               "s_cmp_lt_i32 vcc_lo, %1\n\t"
               "s_cbranch_scc1 .Lg2_%=\n\t"
               "s_setprio 1\n"
               ".Lg2_%=:"
               :: "s"(due16), "i"(kPrio16) : "scc", "vcc", "memory");
}

__device__ __forceinline__ void lds_arrive(unsigned* c, int lane) {
  asm volatile("s_waitcnt lgkmcnt(0)" ::: "memory");
  if (lane == 0) __hip_atomic_fetch_add(c, 1u, RLX_WG);
}
template <int SLEEP> __device__ __forceinline__ unsigned lds_wait(unsigned* c, unsigned want) {
  unsigned v = 0;
#pragma nounroll
  for (unsigned spins = 0; spins < LSPIN_CAP; ++spins) {
    v = __builtin_amdgcn_readfirstlane(__hip_atomic_load(c, RLX_WG));
    if (v >= want) break;
    __builtin_amdgcn_s_sleep(SLEEP);
  }
  asm volatile("" ::: "memory");
  return v;
}
__device__ __forceinline__ unsigned lds_addr(const void* p) { return (unsigned)(size_t)(const LAS void*)p; }
template <int SLEEP> __device__ __forceinline__ v2u lds_take2(unsigned addr) {
  v2u v;
#pragma nounroll
  for (unsigned spins = 0; spins < LSPIN_CAP; ++spins) {
    asm volatile("ds_read_b64 %0, %1\n\ts_waitcnt lgkmcnt(0)" : "=&v"(v) : "v"(addr) : "memory");
    if (__all(v.x != EMPTY_O && v.y != EMPTY_O)) break;
    if (SLEEP) __builtin_amdgcn_s_sleep(SLEEP);
  }
  return v;
}

struct St {
  v4u T[32][4];
  float Sc[8][4];
  unsigned P[8][4];
  v4u Q, mask;
  f32x4 O;
  v4u rs[4];
  int vk, vv, so;
  unsigned due, adj;
  float* lds;
  int lane, wave;
};
template <int I> __device__ __forceinline__ void request(St& z) {
  if constexpr (I > 0) asm volatile("s_waitcnt vmcnt(%0)" :: "i"(LIM) : "memory");
  gate(z.due);
  z.due += kSlot16;
  if constexpr (I >= PRE && I < 31) z.due += z.adj;
  tile_load(z.T[I], (I & 8) ? z.vv : z.vk, z.rs[I >> 3], z.so + (I & 7) * 32768);
  SB();
}
template <int I, int W> __device__ __forceinline__ void consume(St& z) {
  const int g = z.lane >> 4, n = z.lane & 15;
  constexpr int br = I >> 4;
  unsigned* const cnt = (unsigned*)(z.lds + L_CNT);
  if constexpr ((I & 15) == 0) {
    if constexpr (br) lds_wait<1>(cnt + C_QB, 1u);
    z.Q = *(const v4u*)((const unsigned short*)(z.lds + L_Q) + br * 256 + (n >> 3) * 128 + 32 * g + 8 * (n & 3));
  }
  if constexpr ((I & 8) == 0) {
    tile_wait<W>(z.T[I]);
#pragma unroll
    for (int j = 0; j < 4; ++j) {
      const f32x4 d = mfma(z.T[I][j], z.Q, f32x4{0.f, 0.f, 0.f, 0.f});
      z.Sc[I & 7][j] = (dpp<0x00>(d.x) + dpp<0x55>(d.y)) + (dpp<0xAA>(d.z) + dpp<0xFF>(d.w));
    }
    if constexpr ((I & 7) == 7) {
      float m = z.Sc[0][0];
#pragma unroll
      for (int t = 0; t < 8; ++t)
#pragma unroll
        for (int j = 0; j < 4; ++j) m = fmax_(m, z.Sc[t][j]);
      const float m2 = xg_max(m) * C2;
      float l = 0.f;
#pragma unroll
      for (int t = 0; t < 8; ++t) {
        float p[4];
#pragma unroll
        for (int j = 0; j < 4; ++j) { p[j] = __builtin_amdgcn_exp2f(fmaf(z.Sc[t][j], C2, -m2)); z.P[t][j] = dup_bf16(p[j]); }
        l += (p[0] + p[1]) + (p[2] + p[3]);
        asm volatile("" : "+v"(z.P[t][0]), "+v"(z.P[t][1]), "+v"(z.P[t][2]), "+v"(z.P[t][3]));
      }
      l = xg_sum(l);
      if (g == 0 && (n & 7) == 0) { z.lds[L_ML + br * 32 + (n >> 3) * 8 + z.wave] = m2; z.lds[L_ML + br * 32 + 16 + (n >> 3) * 8 + z.wave] = l; }
      lds_arrive(cnt + C_ML + br, z.lane);
      z.O = f32x4{0.f, 0.f, 0.f, 0.f};
    } else {
      asm volatile("" : "+v"(z.Sc[I & 7][0]), "+v"(z.Sc[I & 7][1]), "+v"(z.Sc[I & 7][2]), "+v"(z.Sc[I & 7][3]));
    }
  } else if constexpr (W == 0) {
    v4u A[4];
#pragma unroll
    for (int j = 0; j < 4; ++j) A[j] = diag(z.P[I & 7][j], z.mask);
    unsigned da = lds_addr(z.lds + L_O + br * 2048 + z.wave * 256 + (g >> 1) * 128 + 8 * n + 4 * (g & 1));
    asm volatile("" : "+v"(A[0]), "+v"(A[1]), "+v"(A[2]), "+v"(A[3]), "+v"(da));
    piece_wait<3>(z.T[I][0]); z.O = mfma(A[0], z.T[I][0], z.O); SB();
    piece_wait<2>(z.T[I][1]); z.O = mfma(A[1], z.T[I][1], z.O); SB();
    piece_wait<1>(z.T[I][2]); z.O = mfma(A[2], z.T[I][2], z.O); SB();
    piece_wait<0>(z.T[I][3]); z.O = mfma(A[3], z.T[I][3], z.O);
    *(LAS f32x4*)(size_t)da = z.O;
  } else {
    tile_wait<W>(z.T[I]);
#pragma unroll
    for (int j = 0; j < 4; ++j) z.O = mfma(diag(z.P[I & 7][j], z.mask), z.T[I][j], z.O);
    asm volatile("" : "+v"(z.O));
    if constexpr ((I & 7) == 7) *(f32x4*)(z.lds + L_O + br * 2048 + z.wave * 256 + (g >> 1) * 128 + 8 * n + 4 * (g & 1)) = z.O;
  }
  SB();
}
template <int I, int N> struct Req { static __device__ __forceinline__ void go(St& z) { request<I>(z); Req<I + 1, N>::go(z); } };
template <int N> struct Req<N, N> { static __device__ __forceinline__ void go(St&) {} };
template <int I, int N, int LAST> struct Con {
  static __device__ __forceinline__ void go(St& z) { consume<I, 4 * (LAST - I)>(z); Con<I + 1, N, LAST>::go(z); }
};
template <int N, int LAST> struct Con<N, N, LAST> { static __device__ __forceinline__ void go(St&) {} };
template <int I, int N> struct Run {
  static __device__ __forceinline__ void go(St& z) { request<I>(z); consume<I - DEP + 1, 4 * (DEP - 1)>(z); Run<I + 1, N>::go(z); }
};
template <int N> struct Run<N, N> { static __device__ __forceinline__ void go(St&) {} };

__global__ void __launch_bounds__(NTHR, 3) gqa2_fused(Args) {
  __shared__ __attribute__((aligned(16))) float lds[L_END];
  unsigned* const cnt = (unsigned*)(lds + L_CNT);
  v16u ka; v8u kb;
  asm volatile("s_load_dwordx16 %0, %2, 0x0\n\ts_load_dwordx8 %1, %2, 0x40" : "=&s"(ka), "=&s"(kb) : "s"(__builtin_amdgcn_kernarg_segment_ptr()));
  const int tid = threadIdx.x, lane = tid & 63, wave = __builtin_amdgcn_readfirstlane(tid >> 6);
  const int g = lane >> 4, n = lane & 15;
  const int wg = blockIdx.x;
  const int ch = (wg & 7) << 5 | wg >> 3;
  unsigned cw;
  asm volatile("s_cmp_lg_u32 %2, 0\n\ts_cbranch_scc1 .Lc%=\n\ts_cmp_gt_u32 %3, 2\n\ts_cbranch_scc1 .Lc%=\n\t"
               "s_getpc_b64 vcc\n\tglobal_load_dword %0, %1, vcc\n.Lc%=:"
               : "=v"(cw) : "v"(min((tid - 64) * 128, 20 * 1024)), "s"((((wg >> 3) ^ wg) & 7) | (wg >> 7)), "s"((unsigned)(wave - 1)) : "scc", "vcc");
  const int kvh = ch >> 4, rb = (ch >> 3) & 1, s8 = ch & 7;
  {
    const v4u e = {EMPTY_O, EMPTY_O, EMPTY_O, EMPTY_O};
    const int i = tid < 512 ? tid : 0, j = tid < 48 ? tid : 0;
    *(v4u*)(lds + L_O + i * 4) = e; *(v4u*)(lds + L_O + 2048 + i * 4) = e;
    ((unsigned*)(lds + L_OB))[j] = j < 32 ? EMPTY_B : 0u;
  }
  asm volatile("s_waitcnt lgkmcnt(0)" : "+s"(ka), "+s"(kb) :: "memory");
  const v4u rk0 = mkrs(ptr64(ka[4], ka[5]) + (size_t)ch * 262144, 262144);
  unsigned probe;
  asm volatile("s_cmp_lg_u32 %2, 0\n\ts_cbranch_scc1 .Lp%=\n\ts_mov_b64 exec, 1\n\tbuffer_load_dword %0, off, %1, 0 nt\n\ts_mov_b64 exec, -1\n.Lp%=:"
               : "=v"(probe) : "s"(rk0), "s"(wave) : "scc");
  __builtin_amdgcn_s_barrier();
  asm volatile("" ::: "memory");
  unsigned char* const ws = ptr64(kb[6], kb[7]);
  const __amdgpu_buffer_rsrc_t rw = __builtin_amdgcn_make_buffer_rsrc(ws, 0, WS_END, RSRC3);
  v4u mask;
  {
    const unsigned half = (n & 1) ? 0xffff0000u : 0x0000ffffu; const int r = (n & 7) >> 1;
    mask.x = r == 0 ? half : 0u; mask.y = r == 1 ? half : 0u; mask.z = r == 2 ? half : 0u; mask.w = r == 3 ? half : 0u;
  }
  const int pidx = kvh * 8 + s8;

  if (__builtin_expect(wave >= 8, 1)) {
    __builtin_amdgcn_s_setprio(3);
    const int hw = wave - 8, cg = s8 & 3, kh = s8 >> 2, sub = 2 * g + (n >> 3);
    const __amdgpu_buffer_rsrc_t rq = __builtin_amdgcn_make_buffer_rsrc(
        (rb ? ptr64(ka[14], ka[15]) : ptr64(ka[12], ka[13])) + ((size_t)(kh * 288 + hw * 144) * NQ + kvh * 256 + cg * 64) * 2, 0, 143 * NQ * 2 + 128, RSRC3);
    const __amdgpu_buffer_rsrc_t rx = __builtin_amdgcn_make_buffer_rsrc((rb ? ptr64(ka[2], ka[3]) : ptr64(ka[0], ka[1])) + (kh * 288 + hw * 144) * 2, 0, 144 * 2, RSRC3);
    v4u wqv[18];
#pragma unroll
    for (int i = 0; i < 18; ++i) wqv[i] = __builtin_amdgcn_raw_buffer_load_b128(rq, sub * (NQ * 2) + (n & 7) * 16, i * 8 * NQ * 2, AUX_NT);
    const unsigned xa = __builtin_amdgcn_raw_buffer_load_b32(rx, n * 16 + g * 4, 0, 0), xb = __builtin_amdgcn_raw_buffer_load_b32(rx, n * 16 + g * 4 + 256, 0, 0);
    const unsigned ep = __builtin_amdgcn_raw_buffer_load_b32(rw, lane == 0 ? WS_EPOCH : OOB, 0, AUX_SC1);
    SB();
    f32x4 a0 = {0.f, 0.f, 0.f, 0.f}, a1 = {0.f, 0.f, 0.f, 0.f};
    const unsigned sel = n < 8 ? 0x01000100u : 0x03020302u;
#define QSTEP(acc, i, src, k) acc = mfma(diag(__builtin_amdgcn_perm(0u, (unsigned)dppi<BCAST + (k)>((int)(src)), sel), mask), wqv[i], acc)
    QSTEP(a0, 0, xa, 0); QSTEP(a1, 1, xa, 1); QSTEP(a0, 2, xa, 2); QSTEP(a1, 3, xa, 3); QSTEP(a0, 4, xa, 4); QSTEP(a1, 5, xa, 5);
    QSTEP(a0, 6, xa, 6); QSTEP(a1, 7, xa, 7); QSTEP(a0, 8, xa, 8); QSTEP(a1, 9, xa, 9); QSTEP(a0, 10, xa, 10); QSTEP(a1, 11, xa, 11);
    QSTEP(a0, 12, xa, 12); QSTEP(a1, 13, xa, 13); QSTEP(a0, 14, xa, 14); QSTEP(a1, 15, xa, 15); QSTEP(a0, 16, xb, 0); QSTEP(a1, 17, xb, 1);
#undef QSTEP
    if ((g >> 1) == (n >> 3)) *(f32x4*)(lds + L_QP + (hw * 2 + (n >> 3)) * 64 + 8 * (n & 7) + 4 * (g & 1)) = a0 + a1;
    const unsigned tag = __builtin_amdgcn_readfirstlane(ep) + TAG_STEP;
    lds_arrive(cnt + C_QP, lane);
    unsigned anc = 0;
    if (hw == 0) {
      lds_wait<1>(cnt + C_QP, 2u);
      const f32x2* qp = (const f32x2*)(lds + L_QP) + (lane & 31);
      const f32x2 q = (qp[0] + qp[32]) + (qp[64] + qp[96]);
      st_gran2(rw, lane < 32 ? WS_QG + ((rb * 16 + kvh) * 512 + kh * 256 + cg * 64 + 2 * lane) * 8 : OOB, tag, q.x, q.y);
      if (lane == 0) cnt[C_TAG] = tag;
      anc = lds_wait<1>(cnt + C_ANCHOR, 1u);
      if (lane == 0) __hip_atomic_store((GAS unsigned long long*)(ws + WS_AG + wg * 8), ((unsigned long long)tag << 32) | anc, RLX_AGENT);
    }
    pause<kQA>();
    if (hw) pause<kQB - kQA>();
    {
      v2u x[8];
      const v2u ag = ldg(rw, (hw == 0 && lane < 8) ? WS_AG + (wg & ~7) * 8 + lane * 8 : OOB);
      sweep<8, 512, 8>(rw, WS_QG + ((hw * 16 + kvh) * 512 + lane) * 8, tag, x);
#pragma unroll
      for (int k = 0; k < 4; ++k) ((unsigned short*)(lds + L_Q))[hw * 256 + k * 64 + lane] = f2bf(u2f(x[k].x) + u2f(x[k + 4].x));
      if (hw == 0) {
        int d = ag.y == tag ? (int)(ag.x - anc) : 0;
        d = d > 1600 ? 1600 : d < -1600 ? -1600 : d;
        d += dppi<XOR1>(d); d += dppi<XOR2>(d); d += dppi<HMIR>(d);
        if (lane == 0) cnt[C_ADJ] = (unsigned)(d / (8 * (31 - PRE)));
      }
      lds_arrive(cnt + (hw ? C_QB : C_QA), lane);
    }
#pragma nounroll
    for (int br = 0; br < 2; ++br) {
      lds_wait<8>(cnt + C_ML + br, 8u);
      const float* ml = lds + L_ML + br * 32 + hw * 8;
      const f32x4 m0 = *(const f32x4*)(ml), m1 = *(const f32x4*)(ml + 4), l0 = *(const f32x4*)(ml + 16), l1 = *(const f32x4*)(ml + 20);
      const float M = fmax_(fmax_(fmax_(m0.x, m0.y), fmax_(m0.z, m0.w)), fmax_(fmax_(m1.x, m1.y), fmax_(m1.z, m1.w)));
      float L = 0.f;
#pragma unroll
      for (int w = 0; w < 8; ++w) L = fmaf(__builtin_amdgcn_exp2f((w < 4 ? m0[w & 3] : m1[w & 3]) - M), w < 4 ? l0[w & 3] : l1[w & 3], L);
      const int mg = WS_MG + ((br * 16 + kvh) * 2 + hw) * 1024;
      st_gran2(rw, lane == 0 ? mg + (ch & 15) * 64 : OOB, tag, M, L);
      pause<kML>();
      v2u x[1];
      sweep<1, 0, 8>(rw, mg + n * 64 + (g & 1) * 8, tag, x);
      auto sw = __builtin_amdgcn_permlane16_swap(x[0].x, x[0].x, false, false);
      const float Ms = u2f(sw[0]), Ls = u2f(sw[1]);
      float Mx = fmax_(Ms, dpp<XOR1>(Ms)); Mx = fmax_(Mx, dpp<XOR2>(Mx)); Mx = fmax_(Mx, dpp<HMIR>(Mx)); Mx = fmax_(Mx, dpp<MIR>(Mx));
      float Lt = __builtin_amdgcn_exp2f(Ms - Mx) * Ls; Lt += dpp<XOR1>(Lt); Lt += dpp<XOR2>(Lt); Lt += dpp<HMIR>(Lt); Lt += dpp<MIR>(Lt);
      const float inv = 1.0f / Lt;
      float c[8];
#pragma unroll
      for (int w = 0; w < 8; ++w) c[w] = __builtin_amdgcn_exp2f((w < 4 ? m0[w & 3] : m1[w & 3]) - Mx) * inv;
      const unsigned oa = lds_addr(lds + L_O + br * 2048 + hw * 128 + 2 * lane);
      int dst = WS_PG + (br * 256 + ch) * PG_ROW + (hw * 128 + 2 * lane) * 8;
      asm volatile("" : "+v"(dst));
      float o0, o1;
      { const v2u v = lds_take2<8>(oa); o0 = c[0] * u2f(v.x); o1 = c[0] * u2f(v.y); }
#pragma unroll
      for (int w = 1; w < 8; ++w) { const v2u v = lds_take2<0>(oa + w * 1024); o0 = fmaf(c[w], u2f(v.x), o0); o1 = fmaf(c[w], u2f(v.y), o1); }
      st_gran2(rw, dst, tag, o0, o1);
      if (hw == 0 && lane == 0) __hip_atomic_store(cnt + C_PUB, (unsigned)br + 1u, RLX_WG);
    }
  } else {
    if (wave == 0) {
      unsigned long long ta;
      asm volatile("s_waitcnt vmcnt(0)\n\ts_memrealtime %0\n\ts_waitcnt lgkmcnt(0)" : "=s"(ta) : "v"(probe) : "memory");
      if (lane == 0) __hip_atomic_store(cnt + C_ANCHOR, ((unsigned)ta << 4) | 1u, RLX_WG);
    }
    St z;
    z.lds = lds; z.lane = lane; z.wave = wave; z.mask = mask;
    z.rs[0] = rk0;
    z.rs[1] = mkrs(ptr64(ka[8], ka[9]) + (size_t)ch * 262144, 262144);
    z.rs[2] = mkrs(ptr64(ka[6], ka[7]) + (size_t)ch * 262144, 262144);
    z.rs[3] = mkrs(ptr64(ka[10], ka[11]) + (size_t)ch * 262144, 262144);
    z.vk = ((lane >> 2) & 3) * 256 + g * 64 + (lane & 3) * 16; z.vv = lane * 16; z.so = wave * 1024;
    const int phase16 = (wave * kSlot16) >> 3;
    asm volatile("s_waitcnt vmcnt(0)" :: "v"(cw) : "memory");
    z.due = lds_wait<1>(cnt + C_ANCHOR, 1u) + (unsigned)(kOff16 + phase16);
    Req<0, PRE>::go(z);
    lds_wait<1>(cnt + C_QA, 1u);
    const unsigned tag = __builtin_amdgcn_readfirstlane(cnt[C_TAG]);
    z.adj = __builtin_amdgcn_readfirstlane(cnt[C_ADJ]);
    Con<0, PRE - DEP + 1, PRE - 1>::go(z);
    Run<PRE, 32>::go(z);
    Con<32 - DEP + 1, 32, 31>::go(z);
    const unsigned t32 = z.due - (unsigned)phase16;
    if (wave < 5) {
      const __amdgpu_buffer_rsrc_t ro = __builtin_amdgcn_make_buffer_rsrc(
          (rb ? ptr64(kb[2], kb[3]) : ptr64(kb[0], kb[1])) + (size_t)(kvh * 256 + s8 * 32) * DM * 2, 0, 32 * DM * 2, RSRC3);
      const int wcol = wave * 128 + n * 8;
      const int vwo = wcol < DM ? g * (DM * 2) + wcol * 2 : OOB;
      const unsigned oba = lds_addr(lds + L_OB + g * 8);
      v4u Wo[8];
      gate(t32 + (unsigned)(wave * kWoStep16));
#pragma unroll
      for (int i = 0; i < 8; ++i) Wo[i] = __builtin_amdgcn_raw_buffer_load_b128(ro, vwo, i * 4 * DM * 2, AUX_NT);
      int dst = wcol < DM ? WS_YG + (rb * 128 + pidx) * YG_ROW + (wcol + 4 * (g & 1) + 2 * (g >> 1)) * 8 : OOB;
      asm volatile("" : "+v"(dst), "+v"(Wo[0]), "+v"(Wo[1]), "+v"(Wo[2]), "+v"(Wo[3]), "+v"(Wo[4]), "+v"(Wo[5]), "+v"(Wo[6]), "+v"(Wo[7]));
      v4u oa, ob;
#pragma nounroll
      for (unsigned spins = 0; spins < LSPIN_CAP; ++spins) {
        asm volatile("ds_read_b128 %0, %2\n\tds_read_b128 %1, %2 offset:16\n\ts_waitcnt lgkmcnt(0)" : "=&v"(oa), "=&v"(ob) : "v"(oba) : "memory");
        if (__all(oa.x != EMPTY_B && oa.y != EMPTY_B && oa.z != EMPTY_B && oa.w != EMPTY_B && ob.x != EMPTY_B && ob.y != EMPTY_B && ob.z != EMPTY_B && ob.w != EMPTY_B)) break;
      }
      f32x4 Y0 = {0.f, 0.f, 0.f, 0.f}, Y1 = {0.f, 0.f, 0.f, 0.f};
#pragma unroll
      for (int i = 0; i < 4; ++i) { Y0 = mfma(diag(oa[i], mask), Wo[i], Y0); Y1 = mfma(diag(ob[i], mask), Wo[4 + i], Y1); }
      const f32x4 Y = Y0 + Y1;
      st_gran2(rw, dst, tag, g < 2 ? Y.x : Y.z, g < 2 ? Y.y : Y.w);
    } else if (wave == 5) {
      const int po = WS_PG + (rb * 256 + kvh * 16 + (lane >> 5)) * PG_ROW + (32 * s8 + (lane & 31)) * 8;
      v2u x[8];
      lds_wait<0>(cnt + C_PUB, (unsigned)rb + 1u);
      if (rb) pause<kHop1>();
      sweep<8, 2 * PG_ROW, 1>(rw, po, tag, x);
      const float o = x32_sum(sum8(x));
      if (lane < 32) ((unsigned*)(lds + L_OB))[(lane & 3) * 8 + (lane >> 2)] = dup_bf16(o);
    } else if (wave == 6 || pidx < 16) {
      const int col = (wave == 6 ? 0 : 512) + pidx * 4 + (lane & 3);
      GAS unsigned short* op = (GAS unsigned short*)ptr64(kb[4], kb[5]) + rb * DM + col;
      asm volatile("" : "+v"(op));
      v2u x[8];
#pragma nounroll
      for (unsigned spins = 0; spins < LSPIN_CAP; ++spins) {
        if (__builtin_amdgcn_readfirstlane(__hip_atomic_load((unsigned*)(lds + L_OB), RLX_WG)) != EMPTY_B) break;
        __builtin_amdgcn_s_sleep(1);
      }
      pause<kHop2>();
      gate(t32 + (unsigned)(4 * kWoStep16 + kHop2Gate16));
      sweep<8, 16 * YG_ROW, 1>(rw, WS_YG + (rb * 128 + (lane >> 2)) * YG_ROW + col * 8, tag, x);
      float y = sum8(x);
      y += dpp<ROR4>(y); y += dpp<ROR8>(y);
      y = xg_sum(y);
      if (lane < 4) *op = f2bf(y);
      if (rb == 0 && wave == 6 && pidx == 0 && lane == 0) __hip_atomic_store((GAS unsigned*)(ws + WS_EPOCH), tag, RLX_AGENT);
    }
  }
}

extern "C" void kernel_launch(void* const* d_in, const int* in_sizes, int n_in, void* d_out, int out_size, void* d_ws, size_t ws_size, hipStream_t stream) {
  Args a;
  a.x[0] = d_in[0]; a.k[0] = d_in[1]; a.v[0] = d_in[2]; a.wq[0] = d_in[3]; a.wo[0] = d_in[4];
  a.x[1] = d_in[5]; a.k[1] = d_in[6]; a.v[1] = d_in[7]; a.wq[1] = d_in[8]; a.wo[1] = d_in[9];
  a.out = d_out; a.ws = d_ws;
  gqa2_fused<<<NWG, NTHR, 0, stream>>>(a);
}
```

```cpp
#define OFF16 1400
#define DT16 1752
#define DEFA 3
#include <hip/hip_runtime.h>

typedef unsigned v2u __attribute__((ext_vector_type(2)));
typedef unsigned v4u __attribute__((ext_vector_type(4)));
typedef unsigned v8u __attribute__((ext_vector_type(8)));
typedef unsigned v16u __attribute__((ext_vector_type(16)));
typedef float f32x2 __attribute__((ext_vector_type(2)));
typedef float f32x4 __attribute__((ext_vector_type(4)));
typedef __bf16 bf16x2 __attribute__((ext_vector_type(2)));
typedef __bf16 bf16x8 __attribute__((ext_vector_type(8)));
#define GAS __attribute__((address_space(1)))
#define LAS __attribute__((address_space(3)))
#define RLX_AGENT __ATOMIC_RELAXED, __HIP_MEMORY_SCOPE_AGENT
#define RLX_WG __ATOMIC_RELAXED, __HIP_MEMORY_SCOPE_WORKGROUP
#define SB() __builtin_amdgcn_sched_barrier(0)

constexpr int DM = 576, NQ = 4096;
constexpr int NWG = 256, NTHR = 640;
constexpr float C2 = 0.088388347648318440f * 1.4426950408889634f;

#define ABL 0
constexpr int kSlot16 = DT16;
constexpr int kOff16 = OFF16;
constexpr int kPrio16 = 400;
constexpr int kWoStep16 = 600;
constexpr int kHop2Gate16 = 2000;
constexpr int kQA = 280, kQB = 600, kML = 350, kHop1 = 40, kHop2 = 70;
constexpr int PRE = 8;
constexpr int DEP = 3;
constexpr int LIM = 2;

constexpr int AUX_NT = 2, AUX_SC1 = 16, RSRC3 = 0x00020000, OOB = 0x7fff0000;
constexpr int WS_EPOCH = 0;
constexpr int WS_AG = 1024;
constexpr int WS_QG = 4096;
constexpr int WS_MG = WS_QG + 32 * 4096;
constexpr int WS_PG = WS_MG + 64 * 1024, PG_ROW = 256 * 8;
constexpr int WS_YG = WS_PG + 512 * PG_ROW, YG_ROW = DM * 8;
constexpr int WS_END = WS_YG + 256 * YG_ROW;
constexpr unsigned TAG_STEP = 0x9E3779B9u;
constexpr unsigned SPIN_CAP = 1u << 17, LSPIN_CAP = 1u << 22;
constexpr int L_O = 0;
constexpr int L_ML = L_O + 4096;
constexpr int L_QP = L_ML + 64;
constexpr int L_Q = L_QP + 256;
constexpr int L_OB = L_Q + 256;
constexpr int L_CNT = L_OB + 32, L_END = L_CNT + 16;
enum { C_QP, C_QA, C_ML  , C_PUB = 4, C_TAG, C_ANCHOR, C_QB, C_ADJ };
constexpr unsigned EMPTY_O = 0xffffffffu, EMPTY_B = 0x0000ffffu;

struct Args {
  const void* x[2]; const void* k[2]; const void* v[2]; const void* wq[2]; const void* wo[2];
  void* out; void* ws;
};

__device__ __forceinline__ f32x4 mfma(v4u a, v4u b, f32x4 c) {
  return __builtin_amdgcn_mfma_f32_16x16x32_bf16(__builtin_bit_cast(bf16x8, a), __builtin_bit_cast(bf16x8, b), c, 0, 0, 0);
}
__device__ __forceinline__ unsigned dup_bf16(float f) {
  f32x2 v = {f, f};
  return __builtin_bit_cast(unsigned, __builtin_convertvector(v, bf16x2));
}
__device__ __forceinline__ unsigned short f2bf(float f) { return __builtin_bit_cast(unsigned short, (__bf16)f); }
__device__ __forceinline__ float u2f(unsigned u) { return __uint_as_float(u); }
__device__ __forceinline__ v4u diag(unsigned pp, v4u mask) { v4u r = {pp & mask.x, pp & mask.y, pp & mask.z, pp & mask.w}; return r; }
template <int CTRL> __device__ __forceinline__ int dppi(int x) { return __builtin_amdgcn_mov_dpp(x, CTRL, 0xf, 0xf, true); }
template <int CTRL> __device__ __forceinline__ float dpp(float x) { return __builtin_bit_cast(float, dppi<CTRL>(__builtin_bit_cast(int, x))); }
constexpr int XOR1 = 0xB1, XOR2 = 0x4E, HMIR = 0x141, MIR = 0x140, ROR8 = 0x128, ROR4 = 0x124, BCAST = 0x150;
__device__ __forceinline__ float fmax_(float a, float b) { return __builtin_fmaxf(a, b); }
__device__ __forceinline__ float xg_max(float x) {
  auto s = __builtin_amdgcn_permlane16_swap(__float_as_uint(x), __float_as_uint(x), false, false);
  x = fmax_(u2f(s[0]), u2f(s[1]));
  auto t = __builtin_amdgcn_permlane32_swap(__float_as_uint(x), __float_as_uint(x), false, false);
  return fmax_(u2f(t[0]), u2f(t[1]));
}
__device__ __forceinline__ float x32_sum(float x) {
  auto t = __builtin_amdgcn_permlane32_swap(__float_as_uint(x), __float_as_uint(x), false, false);
  return u2f(t[0]) + u2f(t[1]);
}
__device__ __forceinline__ float xg_sum(float x) {
  auto s = __builtin_amdgcn_permlane16_swap(__float_as_uint(x), __float_as_uint(x), false, false);
  return x32_sum(u2f(s[0]) + u2f(s[1]));
}
__device__ __forceinline__ float sum8(const v2u (&x)[8]) {
  return ((u2f(x[0].x) + u2f(x[1].x)) + (u2f(x[2].x) + u2f(x[3].x))) + ((u2f(x[4].x) + u2f(x[5].x)) + (u2f(x[6].x) + u2f(x[7].x)));
}

__device__ __forceinline__ void st_gran2(__amdgpu_buffer_rsrc_t rs, int off, unsigned tag, float a, float b) {
  __builtin_amdgcn_raw_buffer_store_b128(v4u{__float_as_uint(a), tag, __float_as_uint(b), tag}, rs, off, 0, AUX_SC1);
}
__device__ __forceinline__ v2u ldg(__amdgpu_buffer_rsrc_t rs, int off) { return __builtin_amdgcn_raw_buffer_load_b64(rs, off, 0, AUX_SC1); }
template <int N, int STEP, int SLEEP> __device__ __forceinline__ void sweep(__amdgpu_buffer_rsrc_t rs, int off, unsigned tag, v2u (&x)[N]) {
#pragma unroll
  for (int k = 0; k < N; ++k) x[k] = ldg(rs, off + k * STEP);
  SB();
  bool ok = true;
#pragma unroll
  for (int k = 0; k < N; ++k) ok &= x[k].y == tag;
  if (__builtin_expect(__all(ok), 1)) return;
#pragma nounroll
  for (unsigned spins = 0; spins < SPIN_CAP; ++spins) {
    __builtin_amdgcn_s_sleep(SLEEP);
    v2u y[N];
#pragma unroll
    for (int k = 0; k < N; ++k) y[k] = ldg(rs, x[k].y == tag ? OOB : off + k * STEP);
    SB();
    ok = true;
#pragma unroll
    for (int k = 0; k < N; ++k) { x[k] = x[k].y == tag ? x[k] : y[k]; ok &= x[k].y == tag; }
    if (__all(ok)) break;
  }
}

__device__ __forceinline__ unsigned char* ptr64(unsigned lo, unsigned hi) { return (unsigned char*)(((unsigned long long)hi << 32) | lo); }
__device__ __forceinline__ v4u mkrs(const void* p, unsigned bytes) {
  const unsigned long long u = (unsigned long long)p;
  v4u r = {(unsigned)u, (unsigned)(u >> 32) & 0xffffu, bytes, (unsigned)RSRC3};
  return r;
}
__device__ __forceinline__ void tile_load(v4u (&d)[4], int voff, v4u rs, int soff) {
  asm volatile("buffer_load_dwordx4 %0, %4, %5, %6 offen nt\n\t"
               "buffer_load_dwordx4 %1, %4, %5, %7 offen nt\n\t"
               "buffer_load_dwordx4 %2, %4, %5, %8 offen nt\n\t"
               "buffer_load_dwordx4 %3, %4, %5, %9 offen nt"
               : "=&v"(d[0]), "=&v"(d[1]), "=&v"(d[2]), "=&v"(d[3])
               : "v"(voff), "s"(rs), "s"(soff), "s"(soff + 8192), "s"(soff + 16384), "s"(soff + 24576));
}
template <int N> __device__ __forceinline__ void tile_wait(v4u (&d)[4]) {
  asm volatile("s_waitcnt vmcnt(%4)" : "+v"(d[0]), "+v"(d[1]), "+v"(d[2]), "+v"(d[3]) : "i"(N));
}
template <int N> __device__ __forceinline__ void piece_wait(v4u& d) { asm volatile("s_waitcnt vmcnt(%1)" : "+v"(d) : "i"(N)); }

template <int TICKS> __device__ __forceinline__ void pause() {
  unsigned due;
  asm volatile("s_memrealtime vcc\n\t"
               "s_waitcnt lgkmcnt(0)\n\t"
               "s_add_i32 %0, vcc_lo, %1\n"
               ".Lg0_%=:\n\t"
               "s_sleep 1\n\t"
               "s_memrealtime vcc\n\t"
               "s_waitcnt lgkmcnt(0)\n\t"
               "s_sub_i32 vcc_lo, vcc_lo, %0\n\t"
               "s_cmp_lt_i32 vcc_lo, 0\n\t"
               "s_cbranch_scc1 .Lg0_%="
               : "=&s"(due) : "i"(TICKS) : "scc", "vcc", "memory");
}
__device__ __forceinline__ void gate(unsigned due16) {
  asm volatile("s_branch .Lg1_%=\n"
               ".Lg0_%=:\n\t"
               "s_sleep 1\n"
               ".Lg1_%=:\n\t"
               "s_memrealtime vcc\n\t"
               "s_waitcnt lgkmcnt(0)\n\t"
               "s_lshl_b32 vcc_lo, vcc_lo, 4\n\t"
               "s_sub_i32 vcc_lo, vcc_lo, %0\n\t"
               "s_cmp_lt_i32 vcc_lo, 0\n\t"
               "s_cbranch_scc1 .Lg0_%=\n\t"
               "s_setprio 0\n\t"
               "s_cmp_lt_i32 vcc_lo, %1\n\t"
               "s_cbranch_scc1 .Lg2_%=\n\t"
               "s_setprio 1\n"
               ".Lg2_%=:"
               :: "s"(due16), "i"(kPrio16) : "scc", "vcc", "memory");
}

__device__ __forceinline__ void lds_arrive(unsigned* c, int lane) {
  asm volatile("s_waitcnt lgkmcnt(0)" ::: "memory");
  if (lane == 0) __hip_atomic_fetch_add(c, 1u, RLX_WG);
}
template <int SLEEP> __device__ __forceinline__ unsigned lds_wait(unsigned* c, unsigned want) {
  unsigned v = 0;
#pragma nounroll
  for (unsigned spins = 0; spins < LSPIN_CAP; ++spins) {
    v = __builtin_amdgcn_readfirstlane(__hip_atomic_load(c, RLX_WG));
    if (v >= want) break;
    __builtin_amdgcn_s_sleep(SLEEP);
  }
  asm volatile("" ::: "memory");
  return v;
}
__device__ __forceinline__ unsigned lds_addr(const void* p) { return (unsigned)(size_t)(const LAS void*)p; }
template <int SLEEP> __device__ __forceinline__ v2u lds_take2(unsigned addr) {
  v2u v;
#pragma nounroll
  for (unsigned spins = 0; spins < LSPIN_CAP; ++spins) {
    asm volatile("ds_read_b64 %0, %1\n\ts_waitcnt lgkmcnt(0)" : "=&v"(v) : "v"(addr) : "memory");
    if (__all(v.x != EMPTY_O && v.y != EMPTY_O)) break;
    if (SLEEP) __builtin_amdgcn_s_sleep(SLEEP);
  }
  return v;
}

struct St {
  v4u T[32][4];
  float Sc[8][4];
  unsigned P[8][4];
  v4u Q, mask;
  f32x4 O;
  v4u rs[4];
  int vk, vv, so;
  unsigned due, adj;
  float* lds;
  int lane, wave;
};
template <int I> __device__ __forceinline__ void request(St& z) {
  if constexpr (I > 0) asm volatile("s_waitcnt vmcnt(%0)" :: "i"(LIM) : "memory");
  gate(z.due);
  z.due += kSlot16;
  if constexpr (I >= PRE && I < 31) z.due += z.adj;
  tile_load(z.T[I], (I & 8) ? z.vv : z.vk, z.rs[I >> 3], z.so + (I & 7) * 32768);
  SB();
}
template <int I, int W> __device__ __forceinline__ void consume(St& z) {
  const int g = z.lane >> 4, n = z.lane & 15;
  constexpr int br = I >> 4;
  unsigned* const cnt = (unsigned*)(z.lds + L_CNT);
  if constexpr ((I & 15) == 0) {
    if constexpr (br) lds_wait<1>(cnt + C_QB, 1u);
    z.Q = *(const v4u*)((const unsigned short*)(z.lds + L_Q) + br * 256 + (n >> 3) * 128 + 32 * g + 8 * (n & 3));
  }
  if constexpr ((I & 8) == 0) {
    tile_wait<W>(z.T[I]);
#pragma unroll
    for (int j = 0; j < 4; ++j) {
      const f32x4 d = mfma(z.T[I][j], z.Q, f32x4{0.f, 0.f, 0.f, 0.f});
      z.Sc[I & 7][j] = (dpp<0x00>(d.x) + dpp<0x55>(d.y)) + (dpp<0xAA>(d.z) + dpp<0xFF>(d.w));
    }
    if constexpr ((I & 7) == 7) {
      float m = z.Sc[0][0];
#pragma unroll
      for (int t = 0; t < 8; ++t)
#pragma unroll
        for (int j = 0; j < 4; ++j) m = fmax_(m, z.Sc[t][j]);
      const float m2 = xg_max(m) * C2;
      float l = 0.f;
#pragma unroll
      for (int t = 0; t < 8; ++t) {
        float p[4];
#pragma unroll
        for (int j = 0; j < 4; ++j) { p[j] = __builtin_amdgcn_exp2f(fmaf(z.Sc[t][j], C2, -m2)); z.P[t][j] = dup_bf16(p[j]); }
        l += (p[0] + p[1]) + (p[2] + p[3]);
        asm volatile("" : "+v"(z.P[t][0]), "+v"(z.P[t][1]), "+v"(z.P[t][2]), "+v"(z.P[t][3]));
      }
      l = xg_sum(l);
      if (g == 0 && (n & 7) == 0) { z.lds[L_ML + br * 32 + (n >> 3) * 8 + z.wave] = m2; z.lds[L_ML + br * 32 + 16 + (n >> 3) * 8 + z.wave] = l; }
      lds_arrive(cnt + C_ML + br, z.lane);
      z.O = f32x4{0.f, 0.f, 0.f, 0.f};
    } else {
      asm volatile("" : "+v"(z.Sc[I & 7][0]), "+v"(z.Sc[I & 7][1]), "+v"(z.Sc[I & 7][2]), "+v"(z.Sc[I & 7][3]));
    }
  } else if constexpr (W == 0) {
    v4u A[4];
#pragma unroll
    for (int j = 0; j < 4; ++j) A[j] = diag(z.P[I & 7][j], z.mask);
    unsigned da = lds_addr(z.lds + L_O + br * 2048 + z.wave * 256 + (g >> 1) * 128 + 8 * n + 4 * (g & 1));
    asm volatile("" : "+v"(A[0]), "+v"(A[1]), "+v"(A[2]), "+v"(A[3]), "+v"(da));
    piece_wait<3>(z.T[I][0]); z.O = mfma(A[0], z.T[I][0], z.O); SB();
    piece_wait<2>(z.T[I][1]); z.O = mfma(A[1], z.T[I][1], z.O); SB();
    piece_wait<1>(z.T[I][2]); z.O = mfma(A[2], z.T[I][2], z.O); SB();
    piece_wait<0>(z.T[I][3]); z.O = mfma(A[3], z.T[I][3], z.O);
    *(LAS f32x4*)(size_t)da = z.O;
  } else {
    tile_wait<W>(z.T[I]);
#pragma unroll
    for (int j = 0; j < 4; ++j) z.O = mfma(diag(z.P[I & 7][j], z.mask), z.T[I][j], z.O);
    asm volatile("" : "+v"(z.O));
    if constexpr ((I & 7) == 7) *(f32x4*)(z.lds + L_O + br * 2048 + z.wave * 256 + (g >> 1) * 128 + 8 * n + 4 * (g & 1)) = z.O;
  }
  SB();
}
template <int I, int N> struct Req { static __device__ __forceinline__ void go(St& z) { request<I>(z); Req<I + 1, N>::go(z); } };
template <int N> struct Req<N, N> { static __device__ __forceinline__ void go(St&) {} };
template <int I, int N, int LAST> struct Con {
  static __device__ __forceinline__ void go(St& z) { consume<I, 4 * (LAST - I)>(z); Con<I + 1, N, LAST>::go(z); }
};
template <int N, int LAST> struct Con<N, N, LAST> { static __device__ __forceinline__ void go(St&) {} };
template <int I, int N> struct Run {
  static __device__ __forceinline__ void go(St& z) { request<I>(z); consume<I - DEP + 1, 4 * (DEP - 1)>(z); Run<I + 1, N>::go(z); }
};
template <int N> struct Run<N, N> { static __device__ __forceinline__ void go(St&) {} };

__global__ void __launch_bounds__(NTHR, 3) gqa2_fused(Args) {
  __shared__ __attribute__((aligned(16))) float lds[L_END];
  unsigned* const cnt = (unsigned*)(lds + L_CNT);
  v16u ka; v8u kb;
  asm volatile("s_load_dwordx16 %0, %2, 0x0\n\ts_load_dwordx8 %1, %2, 0x40" : "=&s"(ka), "=&s"(kb) : "s"(__builtin_amdgcn_kernarg_segment_ptr()));
  const int tid = threadIdx.x, lane = tid & 63, wave = __builtin_amdgcn_readfirstlane(tid >> 6);
  const int g = lane >> 4, n = lane & 15;
  const int wg = blockIdx.x;
  const int ch = (wg & 7) << 5 | wg >> 3;
  unsigned cw;
  asm volatile("s_cmp_lg_u32 %2, 0\n\ts_cbranch_scc1 .Lc%=\n\ts_cmp_gt_u32 %3, 2\n\ts_cbranch_scc1 .Lc%=\n\t"
               "s_getpc_b64 vcc\n\tglobal_load_dword %0, %1, vcc\n.Lc%=:"
               : "=v"(cw) : "v"(min((tid - 64) * 128, 20 * 1024)), "s"((((wg >> 3) ^ wg) & 7) | (wg >> 7)), "s"((unsigned)(wave - 1)) : "scc", "vcc");
  const int kvh = ch >> 4, rb = (ch >> 3) & 1, s8 = ch & 7;
  {
    const v4u e = {EMPTY_O, EMPTY_O, EMPTY_O, EMPTY_O};
    const int i = tid < 512 ? tid : 0, j = tid < 48 ? tid : 0;
    *(v4u*)(lds + L_O + i * 4) = e; *(v4u*)(lds + L_O + 2048 + i * 4) = e;
    ((unsigned*)(lds + L_OB))[j] = j < 32 ? EMPTY_B : 0u;
  }
  asm volatile("s_waitcnt lgkmcnt(0)" : "+s"(ka), "+s"(kb) :: "memory");
  const v4u rk0 = mkrs(ptr64(ka[4], ka[5]) + (size_t)ch * 262144, 262144);
  unsigned probe;
  asm volatile("s_cmp_lg_u32 %2, 0\n\ts_cbranch_scc1 .Lp%=\n\ts_mov_b64 exec, 1\n\tbuffer_load_dword %0, off, %1, 0 nt\n\ts_mov_b64 exec, -1\n.Lp%=:"
               : "=v"(probe) : "s"(rk0), "s"(wave) : "scc");
  __builtin_amdgcn_s_barrier();
  asm volatile("" ::: "memory");
  unsigned char* const ws = ptr64(kb[6], kb[7]);
  const __amdgpu_buffer_rsrc_t rw = __builtin_amdgcn_make_buffer_rsrc(ws, 0, WS_END, RSRC3);
  v4u mask;
  {
    const unsigned half = (n & 1) ? 0xffff0000u : 0x0000ffffu; const int r = (n & 7) >> 1;
    mask.x = r == 0 ? half : 0u; mask.y = r == 1 ? half : 0u; mask.z = r == 2 ? half : 0u; mask.w = r == 3 ? half : 0u;
  }
  const int pidx = kvh * 8 + s8;

  if (__builtin_expect(wave >= 8, 1)) {
    __builtin_amdgcn_s_setprio(3);
    const int hw = wave - 8, cg = s8 & 3, kh = s8 >> 2, sub = 2 * g + (n >> 3);
    const __amdgpu_buffer_rsrc_t rq = __builtin_amdgcn_make_buffer_rsrc(
        (rb ? ptr64(ka[14], ka[15]) : ptr64(ka[12], ka[13])) + ((size_t)(kh * 288 + hw * 144) * NQ + kvh * 256 + cg * 64) * 2, 0, 143 * NQ * 2 + 128, RSRC3);
    const __amdgpu_buffer_rsrc_t rx = __builtin_amdgcn_make_buffer_rsrc((rb ? ptr64(ka[2], ka[3]) : ptr64(ka[0], ka[1])) + (kh * 288 + hw * 144) * 2, 0, 144 * 2, RSRC3);
    v4u wqv[18];
#pragma unroll
    for (int i = 0; i < 18; ++i) wqv[i] = __builtin_amdgcn_raw_buffer_load_b128(rq, sub * (NQ * 2) + (n & 7) * 16, i * 8 * NQ * 2, AUX_NT);
    const unsigned xa = __builtin_amdgcn_raw_buffer_load_b32(rx, n * 16 + g * 4, 0, 0), xb = __builtin_amdgcn_raw_buffer_load_b32(rx, n * 16 + g * 4 + 256, 0, 0);
    const unsigned ep = __builtin_amdgcn_raw_buffer_load_b32(rw, lane == 0 ? WS_EPOCH : OOB, 0, AUX_SC1);
    SB();
    f32x4 a0 = {0.f, 0.f, 0.f, 0.f}, a1 = {0.f, 0.f, 0.f, 0.f};
    const unsigned sel = n < 8 ? 0x01000100u : 0x03020302u;
#define QSTEP(acc, i, src, k) acc = mfma(diag(__builtin_amdgcn_perm(0u, (unsigned)dppi<BCAST + (k)>((int)(src)), sel), mask), wqv[i], acc)
    QSTEP(a0, 0, xa, 0); QSTEP(a1, 1, xa, 1); QSTEP(a0, 2, xa, 2); QSTEP(a1, 3, xa, 3); QSTEP(a0, 4, xa, 4); QSTEP(a1, 5, xa, 5);
    QSTEP(a0, 6, xa, 6); QSTEP(a1, 7, xa, 7); QSTEP(a0, 8, xa, 8); QSTEP(a1, 9, xa, 9); QSTEP(a0, 10, xa, 10); QSTEP(a1, 11, xa, 11);
    QSTEP(a0, 12, xa, 12); QSTEP(a1, 13, xa, 13); QSTEP(a0, 14, xa, 14); QSTEP(a1, 15, xa, 15); QSTEP(a0, 16, xb, 0); QSTEP(a1, 17, xb, 1);
#undef QSTEP
    if ((g >> 1) == (n >> 3)) *(f32x4*)(lds + L_QP + (hw * 2 + (n >> 3)) * 64 + 8 * (n & 7) + 4 * (g & 1)) = a0 + a1;
    const unsigned tag = __builtin_amdgcn_readfirstlane(ep) + TAG_STEP;
    lds_arrive(cnt + C_QP, lane);
    unsigned anc = 0;
    if (hw == 0) {
      lds_wait<1>(cnt + C_QP, 2u);
      const f32x2* qp = (const f32x2*)(lds + L_QP) + (lane & 31);
      const f32x2 q = (qp[0] + qp[32]) + (qp[64] + qp[96]);
      st_gran2(rw, (!(ABL & 4) && lane < 32) ? WS_QG + ((rb * 16 + kvh) * 512 + kh * 256 + cg * 64 + 2 * lane) * 8 : OOB, tag, q.x, q.y);
      if (lane == 0) cnt[C_TAG] = tag;
      anc = lds_wait<1>(cnt + C_ANCHOR, 1u);
      if (lane == 0) __hip_atomic_store((GAS unsigned long long*)(ws + WS_AG + wg * 8), ((unsigned long long)tag << 32) | anc, RLX_AGENT);
    }
    pause<kQA>();
    if (hw) pause<kQB - kQA>();
    {
      v2u x[8];
      const v2u ag = ldg(rw, (hw == 0 && lane < 8) ? WS_AG + (wg & ~7) * 8 + lane * 8 : OOB);
      sweep<8, 512, 8>(rw, WS_QG + ((hw * 16 + kvh) * 512 + lane) * 8, tag, x);
#pragma unroll
      for (int k = 0; k < 4; ++k) ((unsigned short*)(lds + L_Q))[hw * 256 + k * 64 + lane] = f2bf(u2f(x[k].x) + u2f(x[k + 4].x));
      if (hw == 0) {
        int d = ag.y == tag ? (int)(ag.x - anc) : 0;
        d = d > 1600 ? 1600 : d < -1600 ? -1600 : d;
        d += dppi<XOR1>(d); d += dppi<XOR2>(d); d += dppi<HMIR>(d);
        if (lane == 0) cnt[C_ADJ] = (unsigned)(d / (8 * (31 - PRE)));
      }
      lds_arrive(cnt + (hw ? C_QB : C_QA), lane);
    }
    float oa0 = 0.f, oa1 = 0.f;
#pragma nounroll
    for (int br = 0; br < 2; ++br) {
      lds_wait<8>(cnt + C_ML + br, 8u);
      const float* ml = lds + L_ML + br * 32 + hw * 8;
      const f32x4 m0 = *(const f32x4*)(ml), m1 = *(const f32x4*)(ml + 4), l0 = *(const f32x4*)(ml + 16), l1 = *(const f32x4*)(ml + 20);
      const float M = fmax_(fmax_(fmax_(m0.x, m0.y), fmax_(m0.z, m0.w)), fmax_(fmax_(m1.x, m1.y), fmax_(m1.z, m1.w)));
      float L = 0.f;
#pragma unroll
      for (int w = 0; w < 8; ++w) L = fmaf(__builtin_amdgcn_exp2f((w < 4 ? m0[w & 3] : m1[w & 3]) - M), w < 4 ? l0[w & 3] : l1[w & 3], L);
      const int mg = WS_MG + ((br * 16 + kvh) * 2 + hw) * 1024;
      st_gran2(rw, (!(ABL & 2) && lane == 0) ? mg + (ch & 15) * 64 : OOB, tag, M, L);
      pause<kML>();
      v2u x[1];
      sweep<1, 0, 8>(rw, mg + n * 64 + (g & 1) * 8, tag, x);
      auto sw = __builtin_amdgcn_permlane16_swap(x[0].x, x[0].x, false, false);
      const float Ms = u2f(sw[0]), Ls = u2f(sw[1]);
      float Mx = fmax_(Ms, dpp<XOR1>(Ms)); Mx = fmax_(Mx, dpp<XOR2>(Mx)); Mx = fmax_(Mx, dpp<HMIR>(Mx)); Mx = fmax_(Mx, dpp<MIR>(Mx));
      float Lt = __builtin_amdgcn_exp2f(Ms - Mx) * Ls; Lt += dpp<XOR1>(Lt); Lt += dpp<XOR2>(Lt); Lt += dpp<HMIR>(Lt); Lt += dpp<MIR>(Lt);
      const float inv = 1.0f / Lt;
      float c[8];
#pragma unroll
      for (int w = 0; w < 8; ++w) c[w] = __builtin_amdgcn_exp2f((w < 4 ? m0[w & 3] : m1[w & 3]) - Mx) * inv;
      const unsigned oa = lds_addr(lds + L_O + br * 2048 + hw * 128 + 2 * lane);
      int dst = WS_PG + (br * 256 + ch) * PG_ROW + (hw * 128 + 2 * lane) * 8;
      asm volatile("" : "+v"(dst));
      float o0, o1;
      { const v2u v = lds_take2<8>(oa); o0 = c[0] * u2f(v.x); o1 = c[0] * u2f(v.y); }
#pragma unroll
      for (int w = 1; w < 8; ++w) {
        if (w == DEFA && br) {
          st_gran2(rw, dst - 256 * PG_ROW, tag, oa0, oa1);
          if (hw == 0 && lane == 0) __hip_atomic_store(cnt + C_PUB, 1u, RLX_WG);
        }
        const v2u v = lds_take2<0>(oa + w * 1024); o0 = fmaf(c[w], u2f(v.x), o0); o1 = fmaf(c[w], u2f(v.y), o1);
      }
      oa0 = o0; oa1 = o1;
      if (!DEFA || br) {
        st_gran2(rw, dst, tag, o0, o1);
        if (hw == 0 && lane == 0) __hip_atomic_store(cnt + C_PUB, (unsigned)br + 1u, RLX_WG);
      }
    }
  } else {
    if (wave == 0) {
      unsigned long long ta;
      asm volatile("s_waitcnt vmcnt(0)\n\ts_memrealtime %0\n\ts_waitcnt lgkmcnt(0)" : "=s"(ta) : "v"(probe) : "memory");
      if (lane == 0) __hip_atomic_store(cnt + C_ANCHOR, ((unsigned)ta << 4) | 1u, RLX_WG);
    }
    St z;
    z.lds = lds; z.lane = lane; z.wave = wave; z.mask = mask;
    z.rs[0] = rk0;
    z.rs[1] = mkrs(ptr64(ka[8], ka[9]) + (size_t)ch * 262144, 262144);
    z.rs[2] = mkrs(ptr64(ka[6], ka[7]) + (size_t)ch * 262144, 262144);
    z.rs[3] = mkrs(ptr64(ka[10], ka[11]) + (size_t)ch * 262144, 262144);
    z.vk = ((lane >> 2) & 3) * 256 + g * 64 + (lane & 3) * 16; z.vv = lane * 16; z.so = wave * 1024;
    const int phase16 = (wave * kSlot16) >> 3;
    asm volatile("s_waitcnt vmcnt(0)" :: "v"(cw) : "memory");
    z.due = lds_wait<1>(cnt + C_ANCHOR, 1u) + (unsigned)(kOff16 + phase16);
    Req<0, PRE>::go(z);
    lds_wait<1>(cnt + C_QA, 1u);
    const unsigned tag = __builtin_amdgcn_readfirstlane(cnt[C_TAG]);
    z.adj = __builtin_amdgcn_readfirstlane(cnt[C_ADJ]);
    Con<0, PRE - DEP + 1, PRE - 1>::go(z);
    Run<PRE, 32>::go(z);
    Con<32 - DEP + 1, 32, 31>::go(z);
    const unsigned t32 = z.due - (unsigned)phase16;
    if (wave < 5) {
      const __amdgpu_buffer_rsrc_t ro = __builtin_amdgcn_make_buffer_rsrc(
          (rb ? ptr64(kb[2], kb[3]) : ptr64(kb[0], kb[1])) + (size_t)(kvh * 256 + s8 * 32) * DM * 2, 0, 32 * DM * 2, RSRC3);
      const int wcol = wave * 128 + n * 8;
      const int vwo = wcol < DM ? g * (DM * 2) + wcol * 2 : OOB;
      const unsigned oba = lds_addr(lds + L_OB + g * 8);
      v4u Wo[8];
      gate(t32 + (unsigned)(wave * kWoStep16));
#pragma unroll
      for (int i = 0; i < 8; ++i) Wo[i] = __builtin_amdgcn_raw_buffer_load_b128(ro, vwo, i * 4 * DM * 2, AUX_NT);
      int dst = wcol < DM ? WS_YG + (rb * 128 + pidx) * YG_ROW + (wcol + 4 * (g & 1) + 2 * (g >> 1)) * 8 : OOB;
      asm volatile("" : "+v"(dst), "+v"(Wo[0]), "+v"(Wo[1]), "+v"(Wo[2]), "+v"(Wo[3]), "+v"(Wo[4]), "+v"(Wo[5]), "+v"(Wo[6]), "+v"(Wo[7]));
      v4u oa, ob;
#pragma nounroll
      for (unsigned spins = 0; spins < LSPIN_CAP; ++spins) {
        asm volatile("ds_read_b128 %0, %2\n\tds_read_b128 %1, %2 offset:16\n\ts_waitcnt lgkmcnt(0)" : "=&v"(oa), "=&v"(ob) : "v"(oba) : "memory");
        if (__all(oa.x != EMPTY_B && oa.y != EMPTY_B && oa.z != EMPTY_B && oa.w != EMPTY_B && ob.x != EMPTY_B && ob.y != EMPTY_B && ob.z != EMPTY_B && ob.w != EMPTY_B)) break;
      }
      f32x4 Y0 = {0.f, 0.f, 0.f, 0.f}, Y1 = {0.f, 0.f, 0.f, 0.f};
#pragma unroll
      for (int i = 0; i < 4; ++i) { Y0 = mfma(diag(oa[i], mask), Wo[i], Y0); Y1 = mfma(diag(ob[i], mask), Wo[4 + i], Y1); }
      const f32x4 Y = Y0 + Y1;
      st_gran2(rw, dst, tag, g < 2 ? Y.x : Y.z, g < 2 ? Y.y : Y.w);
    } else if (wave == 5) {
      const int po = WS_PG + (rb * 256 + kvh * 16 + (lane >> 5)) * PG_ROW + (32 * s8 + (lane & 31)) * 8;
      v2u x[8];
      lds_wait<0>(cnt + C_PUB, (unsigned)rb + 1u);
      if (rb | DEFA) pause<kHop1>();
      sweep<8, 2 * PG_ROW, 1>(rw, po, tag, x);
      const float o = x32_sum(sum8(x));
      if (lane < 32) ((unsigned*)(lds + L_OB))[(lane & 3) * 8 + (lane >> 2)] = dup_bf16(o);
    } else if (wave == 6 || pidx < 16) {
      const int col = (wave == 6 ? 0 : 512) + pidx * 4 + (lane & 3);
      GAS unsigned short* op = (GAS unsigned short*)ptr64(kb[4], kb[5]) + rb * DM + col;
      asm volatile("" : "+v"(op));
      v2u x[8];
#pragma nounroll
      for (unsigned spins = 0; spins < LSPIN_CAP; ++spins) {
        if (__builtin_amdgcn_readfirstlane(__hip_atomic_load((unsigned*)(lds + L_OB), RLX_WG)) != EMPTY_B) break;
        __builtin_amdgcn_s_sleep(1);
      }
      pause<kHop2>();
      gate(t32 + (unsigned)(4 * kWoStep16 + kHop2Gate16));
      sweep<8, 16 * YG_ROW, 1>(rw, WS_YG + (rb * 128 + (lane >> 2)) * YG_ROW + col * 8, tag, x);
      float y = sum8(x);
      y += dpp<ROR4>(y); y += dpp<ROR8>(y);
      y = xg_sum(y);
      if (lane < 4) *op = f2bf(y);
      if (rb == 0 && wave == 6 && pidx == 0 && lane == 0) __hip_atomic_store((GAS unsigned*)(ws + WS_EPOCH), tag, RLX_AGENT);
    }
  }
}

extern "C" void kernel_launch(void* const* d_in, const int* in_sizes, int n_in, void* d_out, int out_size, void* d_ws, size_t ws_size, hipStream_t stream) {
  Args a;
  a.x[0] = d_in[0]; a.k[0] = d_in[1]; a.v[0] = d_in[2]; a.wq[0] = d_in[3]; a.wo[0] = d_in[4];
  a.x[1] = d_in[5]; a.k[1] = d_in[6]; a.v[1] = d_in[7]; a.wq[1] = d_in[8]; a.wo[1] = d_in[9];
  a.out = d_out; a.ws = d_ws;
  gqa2_fused<<<NWG, NTHR, 0, stream>>>(a);
}
```

```cpp
#define OFF16 1500
#define DT16 1748
#include <hip/hip_runtime.h>

typedef unsigned v2u __attribute__((ext_vector_type(2)));
typedef unsigned v4u __attribute__((ext_vector_type(4)));
typedef unsigned v8u __attribute__((ext_vector_type(8)));
typedef unsigned v16u __attribute__((ext_vector_type(16)));
typedef float f32x2 __attribute__((ext_vector_type(2)));
typedef float f32x4 __attribute__((ext_vector_type(4)));
typedef __bf16 bf16x2 __attribute__((ext_vector_type(2)));
typedef __bf16 bf16x8 __attribute__((ext_vector_type(8)));
#define GAS __attribute__((address_space(1)))
#define LAS __attribute__((address_space(3)))
#define RLX_AGENT __ATOMIC_RELAXED, __HIP_MEMORY_SCOPE_AGENT
#define RLX_WG __ATOMIC_RELAXED, __HIP_MEMORY_SCOPE_WORKGROUP
#define SB() __builtin_amdgcn_sched_barrier(0)

constexpr int DM = 576, NQ = 4096;
constexpr int NWG = 256, NTHR = 640;
constexpr float C2 = 0.088388347648318440f * 1.4426950408889634f;

constexpr int kSlot16 = DT16;
constexpr int kOff16 = OFF16;
constexpr int kPrio16 = 400;
constexpr int kWoStep16 = 600;
constexpr int kHop2Gate16 = 2000;
constexpr int kQA = 280, kQB = 600, kML = 350, kHop1 = 40, kHop2 = 70;
constexpr int PRE = 8;
constexpr int DEP = 3;
constexpr int LIM = 2;

constexpr int AUX_NT = 2, AUX_SC1 = 16, RSRC3 = 0x00020000, OOB = 0x7fff0000;
constexpr int WS_EPOCH = 0;
constexpr int WS_AG = 1024;
constexpr int WS_QG = 4096;
constexpr int WS_MG = WS_QG + 32 * 4096;
constexpr int WS_PG = WS_MG + 64 * 1024, PG_ROW = 256 * 8;
constexpr int WS_YG = WS_PG + 512 * PG_ROW, YG_ROW = DM * 8;
constexpr int WS_END = WS_YG + 256 * YG_ROW;
constexpr unsigned TAG_STEP = 0x9E3779B9u;
constexpr unsigned SPIN_CAP = 1u << 17, LSPIN_CAP = 1u << 22;
constexpr int L_O = 0;
constexpr int L_ML = L_O + 4096;
constexpr int L_QP = L_ML + 64;
constexpr int L_Q = L_QP + 256;
constexpr int L_OB = L_Q + 256;
constexpr int L_CNT = L_OB + 32, L_END = L_CNT + 16;
enum { C_QP, C_QA, C_ML  , C_PUB = 4, C_TAG, C_ANCHOR, C_QB, C_ADJ };
constexpr unsigned EMPTY_O = 0xffffffffu, EMPTY_B = 0x0000ffffu;

struct Args {
  const void* x[2]; const void* k[2]; const void* v[2]; const void* wq[2]; const void* wo[2];
  void* out; void* ws;
};

__device__ __forceinline__ f32x4 mfma(v4u a, v4u b, f32x4 c) {
  return __builtin_amdgcn_mfma_f32_16x16x32_bf16(__builtin_bit_cast(bf16x8, a), __builtin_bit_cast(bf16x8, b), c, 0, 0, 0);
}
__device__ __forceinline__ unsigned dup_bf16(float f) {
  f32x2 v = {f, f};
  return __builtin_bit_cast(unsigned, __builtin_convertvector(v, bf16x2));
}
__device__ __forceinline__ unsigned short f2bf(float f) { return __builtin_bit_cast(unsigned short, (__bf16)f); }
__device__ __forceinline__ float u2f(unsigned u) { return __uint_as_float(u); }
__device__ __forceinline__ v4u diag(unsigned pp, v4u mask) { v4u r = {pp & mask.x, pp & mask.y, pp & mask.z, pp & mask.w}; return r; }
template <int CTRL> __device__ __forceinline__ int dppi(int x) { return __builtin_amdgcn_mov_dpp(x, CTRL, 0xf, 0xf, true); }
template <int CTRL> __device__ __forceinline__ float dpp(float x) { return __builtin_bit_cast(float, dppi<CTRL>(__builtin_bit_cast(int, x))); }
constexpr int XOR1 = 0xB1, XOR2 = 0x4E, HMIR = 0x141, MIR = 0x140, ROR8 = 0x128, ROR4 = 0x124, BCAST = 0x150;
__device__ __forceinline__ float fmax_(float a, float b) { return __builtin_fmaxf(a, b); }
__device__ __forceinline__ float xg_max(float x) {
  auto s = __builtin_amdgcn_permlane16_swap(__float_as_uint(x), __float_as_uint(x), false, false);
  x = fmax_(u2f(s[0]), u2f(s[1]));
  auto t = __builtin_amdgcn_permlane32_swap(__float_as_uint(x), __float_as_uint(x), false, false);
  return fmax_(u2f(t[0]), u2f(t[1]));
}
__device__ __forceinline__ float x32_sum(float x) {
  auto t = __builtin_amdgcn_permlane32_swap(__float_as_uint(x), __float_as_uint(x), false, false);
  return u2f(t[0]) + u2f(t[1]);
}
__device__ __forceinline__ float xg_sum(float x) {
  auto s = __builtin_amdgcn_permlane16_swap(__float_as_uint(x), __float_as_uint(x), false, false);
  return x32_sum(u2f(s[0]) + u2f(s[1]));
}
__device__ __forceinline__ float sum8(const v2u (&x)[8]) {
  return ((u2f(x[0].x) + u2f(x[1].x)) + (u2f(x[2].x) + u2f(x[3].x))) + ((u2f(x[4].x) + u2f(x[5].x)) + (u2f(x[6].x) + u2f(x[7].x)));
}

__device__ __forceinline__ void st_gran2(__amdgpu_buffer_rsrc_t rs, int off, unsigned tag, float a, float b) {
  __builtin_amdgcn_raw_buffer_store_b128(v4u{__float_as_uint(a), tag, __float_as_uint(b), tag}, rs, off, 0, AUX_SC1);
}
__device__ __forceinline__ v2u ldg(__amdgpu_buffer_rsrc_t rs, int off) { return __builtin_amdgcn_raw_buffer_load_b64(rs, off, 0, AUX_SC1); }
template <int N, int STEP, int SLEEP> __device__ __forceinline__ void sweep(__amdgpu_buffer_rsrc_t rs, int off, unsigned tag, v2u (&x)[N]) {
#pragma unroll
  for (int k = 0; k < N; ++k) x[k] = ldg(rs, off + k * STEP);
  SB();
  bool ok = true;
#pragma unroll
  for (int k = 0; k < N; ++k) ok &= x[k].y == tag;
  if (__builtin_expect(__all(ok), 1)) return;
#pragma nounroll
  for (unsigned spins = 0; spins < SPIN_CAP; ++spins) {
    __builtin_amdgcn_s_sleep(SLEEP);
    v2u y[N];
#pragma unroll
    for (int k = 0; k < N; ++k) y[k] = ldg(rs, x[k].y == tag ? OOB : off + k * STEP);
    SB();
    ok = true;
#pragma unroll
    for (int k = 0; k < N; ++k) { x[k] = x[k].y == tag ? x[k] : y[k]; ok &= x[k].y == tag; }
    if (__all(ok)) break;
  }
}

__device__ __forceinline__ unsigned char* ptr64(unsigned lo, unsigned hi) { return (unsigned char*)(((unsigned long long)hi << 32) | lo); }
__device__ __forceinline__ v4u mkrs(const void* p, unsigned bytes) {
  const unsigned long long u = (unsigned long long)p;
  v4u r = {(unsigned)u, (unsigned)(u >> 32) & 0xffffu, bytes, (unsigned)RSRC3};
  return r;
}
__device__ __forceinline__ void tile_load(v4u (&d)[4], int voff, v4u rs, int soff) {
  asm volatile("buffer_load_dwordx4 %0, %4, %5, %6 offen nt\n\t"
               "buffer_load_dwordx4 %1, %4, %5, %7 offen nt\n\t"
               "buffer_load_dwordx4 %2, %4, %5, %8 offen nt\n\t"
               "buffer_load_dwordx4 %3, %4, %5, %9 offen nt"
               : "=&v"(d[0]), "=&v"(d[1]), "=&v"(d[2]), "=&v"(d[3])
               : "v"(voff), "s"(rs), "s"(soff), "s"(soff + 8192), "s"(soff + 16384), "s"(soff + 24576));
}
template <int N> __device__ __forceinline__ void tile_wait(v4u (&d)[4]) {
  asm volatile("s_waitcnt vmcnt(%4)" : "+v"(d[0]), "+v"(d[1]), "+v"(d[2]), "+v"(d[3]) : "i"(N));
}
template <int N> __device__ __forceinline__ void piece_wait(v4u& d) { asm volatile("s_waitcnt vmcnt(%1)" : "+v"(d) : "i"(N)); }

template <int TICKS> __device__ __forceinline__ void pause() {
  unsigned due;
  asm volatile("s_memrealtime vcc\n\t"
               "s_waitcnt lgkmcnt(0)\n\t"
               "s_add_i32 %0, vcc_lo, %1\n"
               ".Lg0_%=:\n\t"
               "s_sleep 1\n\t"
               "s_memrealtime vcc\n\t"
               "s_waitcnt lgkmcnt(0)\n\t"
               "s_sub_i32 vcc_lo, vcc_lo, %0\n\t"
               "s_cmp_lt_i32 vcc_lo, 0\n\t"
               "s_cbranch_scc1 .Lg0_%="
               : "=&s"(due) : "i"(TICKS) : "scc", "vcc", "memory");
}
__device__ __forceinline__ void gate(unsigned due16) {
  asm volatile("s_branch .Lg1_%=\n"
               ".Lg0_%=:\n\t"
               "s_sleep 1\n"
               ".Lg1_%=:\n\t"
               "s_memrealtime vcc\n\t"
               "s_waitcnt lgkmcnt(0)\n\t"
               "s_lshl_b32 vcc_lo, vcc_lo, 4\n\t"
               "s_sub_i32 vcc_lo, vcc_lo, %0\n\t"
               "s_cmp_lt_i32 vcc_lo, 0\n\t"
               "s_cbranch_scc1 .Lg0_%=\n\t"
               "s_setprio 0\n\t"
               "s_cmp_lt_i32 vcc_lo, %1\n\t"
               "s_cbranch_scc1 .Lg2_%=\n\t"
               "s_setprio 1\n"
               ".Lg2_%=:"
               :: "s"(due16), "i"(kPrio16) : "scc", "vcc", "memory");
}

__device__ __forceinline__ void lds_arrive(unsigned* c, int lane) {
  asm volatile("s_waitcnt lgkmcnt(0)" ::: "memory");
  if (lane == 0) __hip_atomic_fetch_add(c, 1u, RLX_WG);
}
template <int SLEEP> __device__ __forceinline__ unsigned lds_wait(unsigned* c, unsigned want) {
  unsigned v = 0;
#pragma nounroll
  for (unsigned spins = 0; spins < LSPIN_CAP; ++spins) {
    v = __builtin_amdgcn_readfirstlane(__hip_atomic_load(c, RLX_WG));
    if (v >= want) break;
    __builtin_amdgcn_s_sleep(SLEEP);
  }
  asm volatile("" ::: "memory");
  return v;
}
__device__ __forceinline__ unsigned lds_addr(const void* p) { return (unsigned)(size_t)(const LAS void*)p; }
template <int SLEEP> __device__ __forceinline__ v2u lds_take2(unsigned addr) {
  v2u v;
#pragma nounroll
  for (unsigned spins = 0; spins < LSPIN_CAP; ++spins) {
    asm volatile("ds_read_b64 %0, %1\n\ts_waitcnt lgkmcnt(0)" : "=&v"(v) : "v"(addr) : "memory");
    if (__all(v.x != EMPTY_O && v.y != EMPTY_O)) break;
    if (SLEEP) __builtin_amdgcn_s_sleep(SLEEP);
  }
  return v;
}

struct St {
  v4u T[32][4];
  float Sc[8][4];
  unsigned P[8][4];
  v4u Q, mask;
  f32x4 O;
  v4u rs[4];
  int vk, vv, so;
  unsigned due, adj;
  float* lds;
  int lane, wave;
};
template <int I> __device__ __forceinline__ void request(St& z) {
  if constexpr (I > 0) asm volatile("s_waitcnt vmcnt(%0)" :: "i"(LIM) : "memory");
  gate(z.due);
  z.due += kSlot16;
  if constexpr (I >= PRE && I < 31) z.due += z.adj;
  tile_load(z.T[I], (I & 8) ? z.vv : z.vk, z.rs[I >> 3], z.so + (I & 7) * 32768);
  SB();
}
template <int I, int W> __device__ __forceinline__ void consume(St& z) {
  const int g = z.lane >> 4, n = z.lane & 15;
  constexpr int br = I >> 4;
  unsigned* const cnt = (unsigned*)(z.lds + L_CNT);
  if constexpr ((I & 15) == 0) {
    if constexpr (br) lds_wait<1>(cnt + C_QB, 1u);
    z.Q = *(const v4u*)((const unsigned short*)(z.lds + L_Q) + br * 256 + (n >> 3) * 128 + 32 * g + 8 * (n & 3));
  }
  if constexpr ((I & 8) == 0) {
    tile_wait<W>(z.T[I]);
#pragma unroll
    for (int j = 0; j < 4; ++j) {
      const f32x4 d = mfma(z.T[I][j], z.Q, f32x4{0.f, 0.f, 0.f, 0.f});
      z.Sc[I & 7][j] = (dpp<0x00>(d.x) + dpp<0x55>(d.y)) + (dpp<0xAA>(d.z) + dpp<0xFF>(d.w));
    }
    if constexpr ((I & 7) == 7) {
      float m = z.Sc[0][0];
#pragma unroll
      for (int t = 0; t < 8; ++t)
#pragma unroll
        for (int j = 0; j < 4; ++j) m = fmax_(m, z.Sc[t][j]);
      const float m2 = xg_max(m) * C2;
      float l = 0.f;
#pragma unroll
      for (int t = 0; t < 8; ++t) {
        float p[4];
#pragma unroll
        for (int j = 0; j < 4; ++j) { p[j] = __builtin_amdgcn_exp2f(fmaf(z.Sc[t][j], C2, -m2)); z.P[t][j] = dup_bf16(p[j]); }
        l += (p[0] + p[1]) + (p[2] + p[3]);
        asm volatile("" : "+v"(z.P[t][0]), "+v"(z.P[t][1]), "+v"(z.P[t][2]), "+v"(z.P[t][3]));
      }
      l = xg_sum(l);
      if (g == 0 && (n & 7) == 0) { z.lds[L_ML + br * 32 + (n >> 3) * 8 + z.wave] = m2; z.lds[L_ML + br * 32 + 16 + (n >> 3) * 8 + z.wave] = l; }
      lds_arrive(cnt + C_ML + br, z.lane);
      z.O = f32x4{0.f, 0.f, 0.f, 0.f};
    } else {
      asm volatile("" : "+v"(z.Sc[I & 7][0]), "+v"(z.Sc[I & 7][1]), "+v"(z.Sc[I & 7][2]), "+v"(z.Sc[I & 7][3]));
    }
  } else if constexpr (W == 0) {
    v4u A[4];
#pragma unroll
    for (int j = 0; j < 4; ++j) A[j] = diag(z.P[I & 7][j], z.mask);
    unsigned da = lds_addr(z.lds + L_O + br * 2048 + z.wave * 256 + (g >> 1) * 128 + 8 * n + 4 * (g & 1));
    asm volatile("" : "+v"(A[0]), "+v"(A[1]), "+v"(A[2]), "+v"(A[3]), "+v"(da));
    piece_wait<3>(z.T[I][0]); z.O = mfma(A[0], z.T[I][0], z.O); SB();
    piece_wait<2>(z.T[I][1]); z.O = mfma(A[1], z.T[I][1], z.O); SB();
    piece_wait<1>(z.T[I][2]); z.O = mfma(A[2], z.T[I][2], z.O); SB();
    piece_wait<0>(z.T[I][3]); z.O = mfma(A[3], z.T[I][3], z.O);
    *(LAS f32x4*)(size_t)da = z.O;
  } else {
    tile_wait<W>(z.T[I]);
#pragma unroll
    for (int j = 0; j < 4; ++j) z.O = mfma(diag(z.P[I & 7][j], z.mask), z.T[I][j], z.O);
    asm volatile("" : "+v"(z.O));
    if constexpr ((I & 7) == 7) *(f32x4*)(z.lds + L_O + br * 2048 + z.wave * 256 + (g >> 1) * 128 + 8 * n + 4 * (g & 1)) = z.O;
  }
  SB();
}
template <int I, int N> struct Req { static __device__ __forceinline__ void go(St& z) { request<I>(z); Req<I + 1, N>::go(z); } };
template <int N> struct Req<N, N> { static __device__ __forceinline__ void go(St&) {} };
template <int I, int N, int LAST> struct Con {
  static __device__ __forceinline__ void go(St& z) { consume<I, 4 * (LAST - I)>(z); Con<I + 1, N, LAST>::go(z); }
};
template <int N, int LAST> struct Con<N, N, LAST> { static __device__ __forceinline__ void go(St&) {} };
template <int I, int N> struct Run {
  static __device__ __forceinline__ void go(St& z) { request<I>(z); consume<I - DEP + 1, 4 * (DEP - 1)>(z); Run<I + 1, N>::go(z); }
};
template <int N> struct Run<N, N> { static __device__ __forceinline__ void go(St&) {} };

__global__ void __launch_bounds__(NTHR, 3) gqa2_fused(Args) {
  __shared__ __attribute__((aligned(16))) float lds[L_END];
  unsigned* const cnt = (unsigned*)(lds + L_CNT);
  v16u ka; v8u kb;
  asm volatile("s_load_dwordx16 %0, %2, 0x0\n\ts_load_dwordx8 %1, %2, 0x40" : "=&s"(ka), "=&s"(kb) : "s"(__builtin_amdgcn_kernarg_segment_ptr()));
  const int tid = threadIdx.x, lane = tid & 63, wave = __builtin_amdgcn_readfirstlane(tid >> 6);
  const int g = lane >> 4, n = lane & 15;
  const int wg = blockIdx.x;
  const int ch = (wg & 7) << 5 | wg >> 3;
  unsigned cw;
  asm volatile("s_cmp_lg_u32 %2, 0\n\ts_cbranch_scc1 .Lc%=\n\ts_cmp_gt_u32 %3, 2\n\ts_cbranch_scc1 .Lc%=\n\t"
               "s_getpc_b64 vcc\n\tglobal_load_dword %0, %1, vcc\n.Lc%=:"
               : "=v"(cw) : "v"(min((tid - 64) * 128, 20 * 1024)), "s"((((wg >> 3) ^ wg) & 7) | (wg >> 7)), "s"((unsigned)(wave - 1)) : "scc", "vcc");
  const int kvh = ch >> 4, rb = (ch >> 3) & 1, s8 = ch & 7;
  {
    const v4u e = {EMPTY_O, EMPTY_O, EMPTY_O, EMPTY_O};
    const int i = tid < 512 ? tid : 0, j = tid < 48 ? tid : 0;
    *(v4u*)(lds + L_O + i * 4) = e; *(v4u*)(lds + L_O + 2048 + i * 4) = e;
    ((unsigned*)(lds + L_OB))[j] = j < 32 ? EMPTY_B : 0u;
  }
  asm volatile("s_waitcnt lgkmcnt(0)" : "+s"(ka), "+s"(kb) :: "memory");
  const v4u rk0 = mkrs(ptr64(ka[4], ka[5]) + (size_t)ch * 262144, 262144);
  unsigned probe;
  asm volatile("s_cmp_lg_u32 %2, 0\n\ts_cbranch_scc1 .Lp%=\n\ts_mov_b64 exec, 1\n\tbuffer_load_dword %0, off, %1, 0 nt\n\ts_mov_b64 exec, -1\n.Lp%=:"
               : "=v"(probe) : "s"(rk0), "s"(wave) : "scc");
  __builtin_amdgcn_s_barrier();
  asm volatile("" ::: "memory");
  unsigned char* const ws = ptr64(kb[6], kb[7]);
  const __amdgpu_buffer_rsrc_t rw = __builtin_amdgcn_make_buffer_rsrc(ws, 0, WS_END, RSRC3);
  v4u mask;
  {
    const unsigned half = (n & 1) ? 0xffff0000u : 0x0000ffffu; const int r = (n & 7) >> 1;
    mask.x = r == 0 ? half : 0u; mask.y = r == 1 ? half : 0u; mask.z = r == 2 ? half : 0u; mask.w = r == 3 ? half : 0u;
  }
  const int pidx = kvh * 8 + s8;

  if (__builtin_expect(wave >= 8, 1)) {
    __builtin_amdgcn_s_setprio(3);
    const int hw = wave - 8, cg = s8 & 3, kh = s8 >> 2, sub = 2 * g + (n >> 3);
    const __amdgpu_buffer_rsrc_t rq = __builtin_amdgcn_make_buffer_rsrc(
        (rb ? ptr64(ka[14], ka[15]) : ptr64(ka[12], ka[13])) + ((size_t)(kh * 288 + hw * 144) * NQ + kvh * 256 + cg * 64) * 2, 0, 143 * NQ * 2 + 128, RSRC3);
    const __amdgpu_buffer_rsrc_t rx = __builtin_amdgcn_make_buffer_rsrc((rb ? ptr64(ka[2], ka[3]) : ptr64(ka[0], ka[1])) + (kh * 288 + hw * 144) * 2, 0, 144 * 2, RSRC3);
    v4u wqv[18];
#pragma unroll
    for (int i = 0; i < 18; ++i) wqv[i] = __builtin_amdgcn_raw_buffer_load_b128(rq, sub * (NQ * 2) + (n & 7) * 16, i * 8 * NQ * 2, AUX_NT);
    const unsigned xa = __builtin_amdgcn_raw_buffer_load_b32(rx, n * 16 + g * 4, 0, 0), xb = __builtin_amdgcn_raw_buffer_load_b32(rx, n * 16 + g * 4 + 256, 0, 0);
    const unsigned ep = __builtin_amdgcn_raw_buffer_load_b32(rw, lane == 0 ? WS_EPOCH : OOB, 0, AUX_SC1);
    SB();
    f32x4 a0 = {0.f, 0.f, 0.f, 0.f}, a1 = {0.f, 0.f, 0.f, 0.f};
    const unsigned sel = n < 8 ? 0x01000100u : 0x03020302u;
#define QSTEP(acc, i, src, k) acc = mfma(diag(__builtin_amdgcn_perm(0u, (unsigned)dppi<BCAST + (k)>((int)(src)), sel), mask), wqv[i], acc)
    QSTEP(a0, 0, xa, 0); QSTEP(a1, 1, xa, 1); QSTEP(a0, 2, xa, 2); QSTEP(a1, 3, xa, 3); QSTEP(a0, 4, xa, 4); QSTEP(a1, 5, xa, 5);
    QSTEP(a0, 6, xa, 6); QSTEP(a1, 7, xa, 7); QSTEP(a0, 8, xa, 8); QSTEP(a1, 9, xa, 9); QSTEP(a0, 10, xa, 10); QSTEP(a1, 11, xa, 11);
    QSTEP(a0, 12, xa, 12); QSTEP(a1, 13, xa, 13); QSTEP(a0, 14, xa, 14); QSTEP(a1, 15, xa, 15); QSTEP(a0, 16, xb, 0); QSTEP(a1, 17, xb, 1);
#undef QSTEP
    if ((g >> 1) == (n >> 3)) *(f32x4*)(lds + L_QP + (hw * 2 + (n >> 3)) * 64 + 8 * (n & 7) + 4 * (g & 1)) = a0 + a1;
    const unsigned tag = __builtin_amdgcn_readfirstlane(ep) + TAG_STEP;
    lds_arrive(cnt + C_QP, lane);
    unsigned anc = 0;
    if (hw == 0) {
      lds_wait<1>(cnt + C_QP, 2u);
      const f32x2* qp = (const f32x2*)(lds + L_QP) + (lane & 31);
      const f32x2 q = (qp[0] + qp[32]) + (qp[64] + qp[96]);
      st_gran2(rw, lane < 32 ? WS_QG + ((rb * 16 + kvh) * 512 + kh * 256 + cg * 64 + 2 * lane) * 8 : OOB, tag, q.x, q.y);
      if (lane == 0) cnt[C_TAG] = tag;
      anc = lds_wait<1>(cnt + C_ANCHOR, 1u);
      if (lane == 0) __hip_atomic_store((GAS unsigned long long*)(ws + WS_AG + wg * 8), ((unsigned long long)tag << 32) | anc, RLX_AGENT);
    }
    pause<kQA>();
    if (hw) pause<kQB - kQA>();
    {
      v2u x[8];
      const v2u ag = ldg(rw, (hw == 0 && lane < 8) ? WS_AG + (wg & ~7) * 8 + lane * 8 : OOB);
      sweep<8, 512, 8>(rw, WS_QG + ((hw * 16 + kvh) * 512 + lane) * 8, tag, x);
#pragma unroll
      for (int k = 0; k < 4; ++k) ((unsigned short*)(lds + L_Q))[hw * 256 + k * 64 + lane] = f2bf(u2f(x[k].x) + u2f(x[k + 4].x));
      if (hw == 0) {
        int d = ag.y == tag ? (int)(ag.x - anc) : 0;
        d = d > 1600 ? 1600 : d < -1600 ? -1600 : d;
        d += dppi<XOR1>(d); d += dppi<XOR2>(d); d += dppi<HMIR>(d);
        if (lane == 0) cnt[C_ADJ] = (unsigned)(d / (8 * (31 - PRE)));
      }
      lds_arrive(cnt + (hw ? C_QB : C_QA), lane);
    }
#pragma nounroll
    for (int br = 0; br < 2; ++br) {
      lds_wait<8>(cnt + C_ML + br, 8u);
      const float* ml = lds + L_ML + br * 32 + hw * 8;
      const f32x4 m0 = *(const f32x4*)(ml), m1 = *(const f32x4*)(ml + 4), l0 = *(const f32x4*)(ml + 16), l1 = *(const f32x4*)(ml + 20);
      const float M = fmax_(fmax_(fmax_(m0.x, m0.y), fmax_(m0.z, m0.w)), fmax_(fmax_(m1.x, m1.y), fmax_(m1.z, m1.w)));
      float L = 0.f;
#pragma unroll
      for (int w = 0; w < 8; ++w) L = fmaf(__builtin_amdgcn_exp2f((w < 4 ? m0[w & 3] : m1[w & 3]) - M), w < 4 ? l0[w & 3] : l1[w & 3], L);
      const int mg = WS_MG + ((br * 16 + kvh) * 2 + hw) * 1024;
      st_gran2(rw, lane == 0 ? mg + (ch & 15) * 64 : OOB, tag, M, L);
      pause<kML>();
      v2u x[1];
      sweep<1, 0, 8>(rw, mg + n * 64 + (g & 1) * 8, tag, x);
      auto sw = __builtin_amdgcn_permlane16_swap(x[0].x, x[0].x, false, false);
      const float Ms = u2f(sw[0]), Ls = u2f(sw[1]);
      float Mx = fmax_(Ms, dpp<XOR1>(Ms)); Mx = fmax_(Mx, dpp<XOR2>(Mx)); Mx = fmax_(Mx, dpp<HMIR>(Mx)); Mx = fmax_(Mx, dpp<MIR>(Mx));
      float Lt = __builtin_amdgcn_exp2f(Ms - Mx) * Ls; Lt += dpp<XOR1>(Lt); Lt += dpp<XOR2>(Lt); Lt += dpp<HMIR>(Lt); Lt += dpp<MIR>(Lt);
      const float inv = 1.0f / Lt;
      float c[8];
#pragma unroll
      for (int w = 0; w < 8; ++w) c[w] = __builtin_amdgcn_exp2f((w < 4 ? m0[w & 3] : m1[w & 3]) - Mx) * inv;
      const unsigned oa = lds_addr(lds + L_O + br * 2048 + hw * 128 + 2 * lane);
      int dst = WS_PG + (br * 256 + ch) * PG_ROW + (hw * 128 + 2 * lane) * 8;
      asm volatile("" : "+v"(dst));
      float o0, o1;
      { const v2u v = lds_take2<8>(oa); o0 = c[0] * u2f(v.x); o1 = c[0] * u2f(v.y); }
#pragma unroll
      for (int w = 1; w < 8; ++w) { const v2u v = lds_take2<0>(oa + w * 1024); o0 = fmaf(c[w], u2f(v.x), o0); o1 = fmaf(c[w], u2f(v.y), o1); }
      st_gran2(rw, dst, tag, o0, o1);
      if (hw == 0 && lane == 0) __hip_atomic_store(cnt + C_PUB, (unsigned)br + 1u, RLX_WG);
    }
  } else {
    if (wave == 0) {
      unsigned long long ta;
      asm volatile("s_waitcnt vmcnt(0)\n\ts_memrealtime %0\n\ts_waitcnt lgkmcnt(0)" : "=s"(ta) : "v"(probe) : "memory");
      if (lane == 0) __hip_atomic_store(cnt + C_ANCHOR, ((unsigned)ta << 4) | 1u, RLX_WG);
    }
    St z;
    z.lds = lds; z.lane = lane; z.wave = wave; z.mask = mask;
    z.rs[0] = rk0;
    z.rs[1] = mkrs(ptr64(ka[8], ka[9]) + (size_t)ch * 262144, 262144);
    z.rs[2] = mkrs(ptr64(ka[6], ka[7]) + (size_t)ch * 262144, 262144);
    z.rs[3] = mkrs(ptr64(ka[10], ka[11]) + (size_t)ch * 262144, 262144);
    z.vk = ((lane >> 2) & 3) * 256 + g * 64 + (lane & 3) * 16; z.vv = lane * 16; z.so = wave * 1024;
    const int phase16 = (wave * kSlot16) >> 3;
    asm volatile("s_waitcnt vmcnt(0)" :: "v"(cw) : "memory");
    z.due = lds_wait<1>(cnt + C_ANCHOR, 1u) + (unsigned)(kOff16 + phase16);
    Req<0, PRE>::go(z);
    lds_wait<1>(cnt + C_QA, 1u);
    const unsigned tag = __builtin_amdgcn_readfirstlane(cnt[C_TAG]);
    z.adj = __builtin_amdgcn_readfirstlane(cnt[C_ADJ]);
    Con<0, PRE - DEP + 1, PRE - 1>::go(z);
    Run<PRE, 32>::go(z);
    Con<32 - DEP + 1, 32, 31>::go(z);
    const unsigned t32 = z.due - (unsigned)phase16;
    if (wave < 5) {
      const __amdgpu_buffer_rsrc_t ro = __builtin_amdgcn_make_buffer_rsrc(
          (rb ? ptr64(kb[2], kb[3]) : ptr64(kb[0], kb[1])) + (size_t)(kvh * 256 + s8 * 32) * DM * 2, 0, 32 * DM * 2, RSRC3);
      const int wcol = wave * 128 + n * 8;
      const int vwo = wcol < DM ? g * (DM * 2) + wcol * 2 : OOB;
      const unsigned oba = lds_addr(lds + L_OB + g * 8);
      v4u Wo[8];
      gate(t32 + (unsigned)(wave * kWoStep16));
#pragma unroll
      for (int i = 0; i < 8; ++i) Wo[i] = __builtin_amdgcn_raw_buffer_load_b128(ro, vwo, i * 4 * DM * 2, AUX_NT);
      int dst = wcol < DM ? WS_YG + (rb * 128 + pidx) * YG_ROW + (wcol + 4 * (g & 1) + 2 * (g >> 1)) * 8 : OOB;
      asm volatile("" : "+v"(dst), "+v"(Wo[0]), "+v"(Wo[1]), "+v"(Wo[2]), "+v"(Wo[3]), "+v"(Wo[4]), "+v"(Wo[5]), "+v"(Wo[6]), "+v"(Wo[7]));
      v4u oa, ob;
#pragma nounroll
      for (unsigned spins = 0; spins < LSPIN_CAP; ++spins) {
        asm volatile("ds_read_b128 %0, %2\n\tds_read_b128 %1, %2 offset:16\n\ts_waitcnt lgkmcnt(0)" : "=&v"(oa), "=&v"(ob) : "v"(oba) : "memory");
        if (__all(oa.x != EMPTY_B && oa.y != EMPTY_B && oa.z != EMPTY_B && oa.w != EMPTY_B && ob.x != EMPTY_B && ob.y != EMPTY_B && ob.z != EMPTY_B && ob.w != EMPTY_B)) break;
      }
      f32x4 Y0 = {0.f, 0.f, 0.f, 0.f}, Y1 = {0.f, 0.f, 0.f, 0.f};
#pragma unroll
      for (int i = 0; i < 4; ++i) { Y0 = mfma(diag(oa[i], mask), Wo[i], Y0); Y1 = mfma(diag(ob[i], mask), Wo[4 + i], Y1); }
      const f32x4 Y = Y0 + Y1;
      st_gran2(rw, dst, tag, g < 2 ? Y.x : Y.z, g < 2 ? Y.y : Y.w);
    } else if (wave == 5) {
      const int po = WS_PG + (rb * 256 + kvh * 16 + (lane >> 5)) * PG_ROW + (32 * s8 + (lane & 31)) * 8;
      v2u x[8];
      lds_wait<0>(cnt + C_PUB, (unsigned)rb + 1u);
      if (rb) pause<kHop1>();
      sweep<8, 2 * PG_ROW, 1>(rw, po, tag, x);
      const float o = x32_sum(sum8(x));
      if (lane < 32) ((unsigned*)(lds + L_OB))[(lane & 3) * 8 + (lane >> 2)] = dup_bf16(o);
    } else if (wave == 6 || pidx < 16) {
      const int col = (wave == 6 ? 0 : 512) + pidx * 4 + (lane & 3);
      GAS unsigned short* op = (GAS unsigned short*)ptr64(kb[4], kb[5]) + rb * DM + col;
      asm volatile("" : "+v"(op));
      v2u x[8];
#pragma nounroll
      for (unsigned spins = 0; spins < LSPIN_CAP; ++spins) {
        if (__builtin_amdgcn_readfirstlane(__hip_atomic_load((unsigned*)(lds + L_OB), RLX_WG)) != EMPTY_B) break;
        __builtin_amdgcn_s_sleep(1);
      }
      pause<kHop2>();
      gate(t32 + (unsigned)(4 * kWoStep16 + kHop2Gate16));
      sweep<8, 16 * YG_ROW, 1>(rw, WS_YG + (rb * 128 + (lane >> 2)) * YG_ROW + col * 8, tag, x);
      float y = sum8(x);
      y += dpp<ROR4>(y); y += dpp<ROR8>(y);
      y = xg_sum(y);
      if (lane < 4) *op = f2bf(y);
      if (rb == 0 && wave == 6 && pidx == 0 && lane == 0) __hip_atomic_store((GAS unsigned*)(ws + WS_EPOCH), tag, RLX_AGENT);
    }
  }
}

extern "C" void kernel_launch(void* const* d_in, const int* in_sizes, int n_in, void* d_out, int out_size, void* d_ws, size_t ws_size, hipStream_t stream) {
  Args a;
  a.x[0] = d_in[0]; a.k[0] = d_in[1]; a.v[0] = d_in[2]; a.wq[0] = d_in[3]; a.wo[0] = d_in[4];
  a.x[1] = d_in[5]; a.k[1] = d_in[6]; a.v[1] = d_in[7]; a.wq[1] = d_in[8]; a.wo[1] = d_in[9];
  a.out = d_out; a.ws = d_ws;
  gqa2_fused<<<NWG, NTHR, 0, stream>>>(a);
}
```

```cpp
#define OFF16 1200
#define DT16 1768
#include <hip/hip_runtime.h>

typedef unsigned v2u __attribute__((ext_vector_type(2)));
typedef unsigned v4u __attribute__((ext_vector_type(4)));
typedef unsigned v8u __attribute__((ext_vector_type(8)));
typedef unsigned v16u __attribute__((ext_vector_type(16)));
typedef float f32x2 __attribute__((ext_vector_type(2)));
typedef float f32x4 __attribute__((ext_vector_type(4)));
typedef __bf16 bf16x2 __attribute__((ext_vector_type(2)));
typedef __bf16 bf16x8 __attribute__((ext_vector_type(8)));
#define GAS __attribute__((address_space(1)))
#define LAS __attribute__((address_space(3)))
#define RLX_AGENT __ATOMIC_RELAXED, __HIP_MEMORY_SCOPE_AGENT
#define RLX_WG __ATOMIC_RELAXED, __HIP_MEMORY_SCOPE_WORKGROUP
#define SB() __builtin_amdgcn_sched_barrier(0)

constexpr int DM = 576, NQ = 4096;
constexpr int NWG = 256, NTHR = 768;
constexpr float C2 = 0.088388347648318440f * 1.4426950408889634f;

constexpr int kSlot16 = DT16;
constexpr int kOff16 = OFF16;
constexpr int kPrio16 = 400;
constexpr int kWoStep16 = 600;
constexpr int kHop2Gate16 = 2000;
constexpr int kQA = 280, kQB = 600, kML = 350, kHop1 = 40, kHop2 = 70;
constexpr int kCodeBytes = 20224;
constexpr int PRE = 8;
constexpr int DEP = 3;
constexpr int LIM = 2;

constexpr int AUX_NT = 2, AUX_SC1 = 16, RSRC3 = 0x00020000, OOB = 0x7fff0000;
constexpr int WS_EPOCH = 0;
constexpr int WS_AG = 1024;
constexpr int WS_QG = 4096;
constexpr int WS_MG = WS_QG + 32 * 4096;
constexpr int WS_PG = WS_MG + 64 * 1024, PG_ROW = 256 * 8;
constexpr int WS_YG = WS_PG + 512 * PG_ROW, YG_ROW = DM * 8;
constexpr int WS_END = WS_YG + 256 * YG_ROW;
constexpr unsigned TAG_STEP = 0x9E3779B9u;
constexpr unsigned SPIN_CAP = 1u << 17, LSPIN_CAP = 1u << 22;
constexpr int L_O = 0;
constexpr int L_ML = L_O + 4096;
constexpr int L_QP = L_ML + 64;
constexpr int L_Q = L_QP + 512;
constexpr int L_OB = L_Q + 256;
constexpr int L_CNT = L_OB + 32, L_END = L_CNT + 16;
enum { C_QP, C_QA, C_ML  , C_PUB = 4, C_TAG, C_ANCHOR, C_QB, C_ADJ };
constexpr unsigned EMPTY_O = 0xffffffffu, EMPTY_B = 0x0000ffffu;

struct Args {
  const void* x[2]; const void* k[2]; const void* v[2]; const void* wq[2]; const void* wo[2];
  void* out; void* ws;
};

__device__ __forceinline__ f32x4 mfma(v4u a, v4u b, f32x4 c) {
  return __builtin_amdgcn_mfma_f32_16x16x32_bf16(__builtin_bit_cast(bf16x8, a), __builtin_bit_cast(bf16x8, b), c, 0, 0, 0);
}
__device__ __forceinline__ unsigned dup_bf16(float f) {
  f32x2 v = {f, f};
  return __builtin_bit_cast(unsigned, __builtin_convertvector(v, bf16x2));
}
__device__ __forceinline__ unsigned short f2bf(float f) { return __builtin_bit_cast(unsigned short, (__bf16)f); }
__device__ __forceinline__ float u2f(unsigned u) { return __uint_as_float(u); }
__device__ __forceinline__ v4u diag(unsigned pp, v4u mask) { v4u r = {pp & mask.x, pp & mask.y, pp & mask.z, pp & mask.w}; return r; }
template <int CTRL> __device__ __forceinline__ int dppi(int x) { return __builtin_amdgcn_mov_dpp(x, CTRL, 0xf, 0xf, true); }
template <int CTRL> __device__ __forceinline__ float dpp(float x) { return __builtin_bit_cast(float, dppi<CTRL>(__builtin_bit_cast(int, x))); }
constexpr int XOR1 = 0xB1, XOR2 = 0x4E, HMIR = 0x141, MIR = 0x140, ROR8 = 0x128, ROR4 = 0x124, BCAST = 0x150;
__device__ __forceinline__ float fmax_(float a, float b) { return __builtin_fmaxf(a, b); }
__device__ __forceinline__ float xg_max(float x) {
  auto s = __builtin_amdgcn_permlane16_swap(__float_as_uint(x), __float_as_uint(x), false, false);
  x = fmax_(u2f(s[0]), u2f(s[1]));
  auto t = __builtin_amdgcn_permlane32_swap(__float_as_uint(x), __float_as_uint(x), false, false);
  return fmax_(u2f(t[0]), u2f(t[1]));
}
__device__ __forceinline__ float x32_sum(float x) {
  auto t = __builtin_amdgcn_permlane32_swap(__float_as_uint(x), __float_as_uint(x), false, false);
  return u2f(t[0]) + u2f(t[1]);
}
__device__ __forceinline__ float xg_sum(float x) {
  auto s = __builtin_amdgcn_permlane16_swap(__float_as_uint(x), __float_as_uint(x), false, false);
  return x32_sum(u2f(s[0]) + u2f(s[1]));
}
__device__ __forceinline__ float sum8(const v2u (&x)[8]) {
  return ((u2f(x[0].x) + u2f(x[1].x)) + (u2f(x[2].x) + u2f(x[3].x))) + ((u2f(x[4].x) + u2f(x[5].x)) + (u2f(x[6].x) + u2f(x[7].x)));
}

__device__ __forceinline__ void st_gran2(__amdgpu_buffer_rsrc_t rs, int off, unsigned tag, float a, float b) {
  __builtin_amdgcn_raw_buffer_store_b128(v4u{__float_as_uint(a), tag, __float_as_uint(b), tag}, rs, off, 0, AUX_SC1);
}
__device__ __forceinline__ v2u ldg(__amdgpu_buffer_rsrc_t rs, int off) { return __builtin_amdgcn_raw_buffer_load_b64(rs, off, 0, AUX_SC1); }
template <int N, int STEP, int SLEEP> __device__ __forceinline__ void sweep(__amdgpu_buffer_rsrc_t rs, int off, unsigned tag, v2u (&x)[N]) {
#pragma unroll
  for (int k = 0; k < N; ++k) x[k] = ldg(rs, off + k * STEP);
  SB();
  bool ok = true;
#pragma unroll
  for (int k = 0; k < N; ++k) ok &= x[k].y == tag;
  if (__builtin_expect(__all(ok), 1)) return;
#pragma nounroll
  for (unsigned spins = 0; spins < SPIN_CAP; ++spins) {
    __builtin_amdgcn_s_sleep(SLEEP);
    v2u y[N];
#pragma unroll
    for (int k = 0; k < N; ++k) y[k] = ldg(rs, x[k].y == tag ? OOB : off + k * STEP);
    SB();
    ok = true;
#pragma unroll
    for (int k = 0; k < N; ++k) { x[k] = x[k].y == tag ? x[k] : y[k]; ok &= x[k].y == tag; }
    if (__all(ok)) break;
  }
}

__device__ __forceinline__ unsigned char* ptr64(unsigned lo, unsigned hi) { return (unsigned char*)(((unsigned long long)hi << 32) | lo); }
__device__ __forceinline__ v4u mkrs(const void* p, unsigned bytes) {
  const unsigned long long u = (unsigned long long)p;
  v4u r = {(unsigned)u, (unsigned)(u >> 32) & 0xffffu, bytes, (unsigned)RSRC3};
  return r;
}
__device__ __forceinline__ void tile_load(v4u (&d)[4], int voff, v4u rs, int soff) {
  asm volatile("buffer_load_dwordx4 %0, %4, %5, %6 offen nt\n\t"
               "buffer_load_dwordx4 %1, %4, %5, %7 offen nt\n\t"
               "buffer_load_dwordx4 %2, %4, %5, %8 offen nt\n\t"
               "buffer_load_dwordx4 %3, %4, %5, %9 offen nt"
               : "=&v"(d[0]), "=&v"(d[1]), "=&v"(d[2]), "=&v"(d[3])
               : "v"(voff), "s"(rs), "s"(soff), "s"(soff + 8192), "s"(soff + 16384), "s"(soff + 24576));
}
template <int N> __device__ __forceinline__ void tile_wait(v4u (&d)[4]) {
  asm volatile("s_waitcnt vmcnt(%4)" : "+v"(d[0]), "+v"(d[1]), "+v"(d[2]), "+v"(d[3]) : "i"(N));
}
template <int N> __device__ __forceinline__ void piece_wait(v4u& d) { asm volatile("s_waitcnt vmcnt(%1)" : "+v"(d) : "i"(N)); }

template <int TICKS> __device__ __forceinline__ void pause() {
  unsigned due;
  asm volatile("s_memrealtime vcc\n\t"
               "s_waitcnt lgkmcnt(0)\n\t"
               "s_add_i32 %0, vcc_lo, %1\n"
               ".Lg0_%=:\n\t"
               "s_sleep 1\n\t"
               "s_memrealtime vcc\n\t"
               "s_waitcnt lgkmcnt(0)\n\t"
               "s_sub_i32 vcc_lo, vcc_lo, %0\n\t"
               "s_cmp_lt_i32 vcc_lo, 0\n\t"
               "s_cbranch_scc1 .Lg0_%="
               : "=&s"(due) : "i"(TICKS) : "scc", "vcc", "memory");
}
__device__ __forceinline__ void gate(unsigned due16) {
  asm volatile("s_branch .Lg1_%=\n"
               ".Lg0_%=:\n\t"
               "s_sleep 1\n"
               ".Lg1_%=:\n\t"
               "s_memrealtime vcc\n\t"
               "s_waitcnt lgkmcnt(0)\n\t"
               "s_lshl_b32 vcc_lo, vcc_lo, 4\n\t"
               "s_sub_i32 vcc_lo, vcc_lo, %0\n\t"
               "s_cmp_lt_i32 vcc_lo, 0\n\t"
               "s_cbranch_scc1 .Lg0_%=\n\t"
               "s_setprio 0\n\t"
               "s_cmp_lt_i32 vcc_lo, %1\n\t"
               "s_cbranch_scc1 .Lg2_%=\n\t"
               "s_setprio 1\n"
               ".Lg2_%=:"
               :: "s"(due16), "i"(kPrio16) : "scc", "vcc", "memory");
}

__device__ __forceinline__ void lds_arrive(unsigned* c, int lane) {
  asm volatile("s_waitcnt lgkmcnt(0)" ::: "memory");
  if (lane == 0) __hip_atomic_fetch_add(c, 1u, RLX_WG);
}
template <int SLEEP> __device__ __forceinline__ unsigned lds_wait(unsigned* c, unsigned want) {
  unsigned v = 0;
#pragma nounroll
  for (unsigned spins = 0; spins < LSPIN_CAP; ++spins) {
    v = __builtin_amdgcn_readfirstlane(__hip_atomic_load(c, RLX_WG));
    if (v >= want) break;
    __builtin_amdgcn_s_sleep(SLEEP);
  }
  asm volatile("" ::: "memory");
  return v;
}
__device__ __forceinline__ unsigned lds_addr(const void* p) { return (unsigned)(size_t)(const LAS void*)p; }
template <int SLEEP> __device__ __forceinline__ v2u lds_take2(unsigned addr) {
  v2u v;
#pragma nounroll
  for (unsigned spins = 0; spins < LSPIN_CAP; ++spins) {
    asm volatile("ds_read_b64 %0, %1\n\ts_waitcnt lgkmcnt(0)" : "=&v"(v) : "v"(addr) : "memory");
    if (__all(v.x != EMPTY_O && v.y != EMPTY_O)) break;
    if (SLEEP) __builtin_amdgcn_s_sleep(SLEEP);
  }
  return v;
}

struct St {
  v4u T[32][4];
  float Sc[8][4];
  unsigned P[8][4];
  v4u Q, mask;
  f32x4 O;
  v4u rs[4];
  int vk, vv, so;
  unsigned due, adj;
  float* lds;
  int lane, wave;
};
template <int I> __device__ __forceinline__ void request(St& z) {
  if constexpr (I > 0) asm volatile("s_waitcnt vmcnt(%0)" :: "i"(LIM) : "memory");
  gate(z.due);
  z.due += kSlot16;
  if constexpr (I >= PRE && I < 31) z.due += z.adj;
  tile_load(z.T[I], (I & 8) ? z.vv : z.vk, z.rs[I >> 3], z.so + (I & 7) * 32768);
  SB();
}
template <int I, int W> __device__ __forceinline__ void consume(St& z) {
  const int g = z.lane >> 4, n = z.lane & 15;
  constexpr int br = I >> 4;
  unsigned* const cnt = (unsigned*)(z.lds + L_CNT);
  if constexpr ((I & 15) == 0) {
    if constexpr (br) lds_wait<1>(cnt + C_QB, 1u);
    z.Q = *(const v4u*)((const unsigned short*)(z.lds + L_Q) + br * 256 + (n >> 3) * 128 + 32 * g + 8 * (n & 3));
  }
  if constexpr ((I & 8) == 0) {
    tile_wait<W>(z.T[I]);
#pragma unroll
    for (int j = 0; j < 4; ++j) {
      const f32x4 d = mfma(z.T[I][j], z.Q, f32x4{0.f, 0.f, 0.f, 0.f});
      z.Sc[I & 7][j] = (dpp<0x00>(d.x) + dpp<0x55>(d.y)) + (dpp<0xAA>(d.z) + dpp<0xFF>(d.w));
    }
    if constexpr ((I & 7) == 7) {
      float m = z.Sc[0][0];
#pragma unroll
      for (int t = 0; t < 8; ++t)
#pragma unroll
        for (int j = 0; j < 4; ++j) m = fmax_(m, z.Sc[t][j]);
      const float m2 = xg_max(m) * C2;
      float l = 0.f;
#pragma unroll
      for (int t = 0; t < 8; ++t) {
        float p[4];
#pragma unroll
        for (int j = 0; j < 4; ++j) { p[j] = __builtin_amdgcn_exp2f(fmaf(z.Sc[t][j], C2, -m2)); z.P[t][j] = dup_bf16(p[j]); }
        l += (p[0] + p[1]) + (p[2] + p[3]);
        asm volatile("" : "+v"(z.P[t][0]), "+v"(z.P[t][1]), "+v"(z.P[t][2]), "+v"(z.P[t][3]));
      }
      l = xg_sum(l);
      if (g == 0 && (n & 7) == 0) { z.lds[L_ML + br * 32 + (n >> 3) * 8 + z.wave] = m2; z.lds[L_ML + br * 32 + 16 + (n >> 3) * 8 + z.wave] = l; }
      lds_arrive(cnt + C_ML + br, z.lane);
      z.O = f32x4{0.f, 0.f, 0.f, 0.f};
    } else {
      asm volatile("" : "+v"(z.Sc[I & 7][0]), "+v"(z.Sc[I & 7][1]), "+v"(z.Sc[I & 7][2]), "+v"(z.Sc[I & 7][3]));
    }
  } else if constexpr (W == 0) {
    v4u A[4];
#pragma unroll
    for (int j = 0; j < 4; ++j) A[j] = diag(z.P[I & 7][j], z.mask);
    unsigned da = lds_addr(z.lds + L_O + br * 2048 + z.wave * 256 + (g >> 1) * 128 + 8 * n + 4 * (g & 1));
    asm volatile("" : "+v"(A[0]), "+v"(A[1]), "+v"(A[2]), "+v"(A[3]), "+v"(da));
    piece_wait<3>(z.T[I][0]); z.O = mfma(A[0], z.T[I][0], z.O); SB();
    piece_wait<2>(z.T[I][1]); z.O = mfma(A[1], z.T[I][1], z.O); SB();
    piece_wait<1>(z.T[I][2]); z.O = mfma(A[2], z.T[I][2], z.O); SB();
    piece_wait<0>(z.T[I][3]); z.O = mfma(A[3], z.T[I][3], z.O);
    *(LAS f32x4*)(size_t)da = z.O;
  } else {
    tile_wait<W>(z.T[I]);
#pragma unroll
    for (int j = 0; j < 4; ++j) z.O = mfma(diag(z.P[I & 7][j], z.mask), z.T[I][j], z.O);
    asm volatile("" : "+v"(z.O));
    if constexpr ((I & 7) == 7) *(f32x4*)(z.lds + L_O + br * 2048 + z.wave * 256 + (g >> 1) * 128 + 8 * n + 4 * (g & 1)) = z.O;
  }
  SB();
}
template <int I, int N> struct Req { static __device__ __forceinline__ void go(St& z) { request<I>(z); Req<I + 1, N>::go(z); } };
template <int N> struct Req<N, N> { static __device__ __forceinline__ void go(St&) {} };
template <int I, int N, int LAST> struct Con {
  static __device__ __forceinline__ void go(St& z) { consume<I, 4 * (LAST - I)>(z); Con<I + 1, N, LAST>::go(z); }
};
template <int N, int LAST> struct Con<N, N, LAST> { static __device__ __forceinline__ void go(St&) {} };
template <int I, int N> struct Run {
  static __device__ __forceinline__ void go(St& z) { request<I>(z); consume<I - DEP + 1, 4 * (DEP - 1)>(z); Run<I + 1, N>::go(z); }
};
template <int N> struct Run<N, N> { static __device__ __forceinline__ void go(St&) {} };

__global__ void __launch_bounds__(NTHR, 3) gqa2_fused(Args) {
  __shared__ __attribute__((aligned(16))) float lds[L_END];
  unsigned* const cnt = (unsigned*)(lds + L_CNT);
  v16u ka; v8u kb;
  asm volatile("s_load_dwordx16 %0, %2, 0x0\n\ts_load_dwordx8 %1, %2, 0x40" : "=&s"(ka), "=&s"(kb) : "s"(__builtin_amdgcn_kernarg_segment_ptr()));
  const int tid = threadIdx.x, lane = tid & 63, wave = __builtin_amdgcn_readfirstlane(tid >> 6);
  const int g = lane >> 4, n = lane & 15;
  const int wg = blockIdx.x;
  const int ch = (wg & 7) << 5 | wg >> 3;
  unsigned cw;
  asm volatile("s_cmp_lg_u32 %2, 0\n\ts_cbranch_scc1 .Lc%=\n\ts_cmp_gt_u32 %3, 2\n\ts_cbranch_scc1 .Lc%=\n\t"
               "s_getpc_b64 vcc\n\tglobal_load_dword %0, %1, vcc\n.Lc%=:"
               : "=v"(cw) : "v"(min((tid - 64) * 128, kCodeBytes)), "s"((((wg >> 3) ^ wg) & 7) | (wg >> 7)), "s"((unsigned)(wave - 1)) : "scc", "vcc");
  const int kvh = ch >> 4, rb = (ch >> 3) & 1, s8 = ch & 7;
  {
    const v4u e = {EMPTY_O, EMPTY_O, EMPTY_O, EMPTY_O};
    const int i = tid < 512 ? tid : 0, j = tid < 48 ? tid : 0;
    *(v4u*)(lds + L_O + i * 4) = e; *(v4u*)(lds + L_O + 2048 + i * 4) = e;
    ((unsigned*)(lds + L_OB))[j] = j < 32 ? EMPTY_B : 0u;
  }
  asm volatile("s_waitcnt lgkmcnt(0)" : "+s"(ka), "+s"(kb) :: "memory");
  const v4u rk0 = mkrs(ptr64(ka[4], ka[5]) + (size_t)ch * 262144, 262144);
  unsigned probe;
  asm volatile("s_cmp_lg_u32 %2, 0\n\ts_cbranch_scc1 .Lp%=\n\ts_mov_b64 exec, 1\n\tbuffer_load_dword %0, off, %1, 0 nt\n\ts_mov_b64 exec, -1\n.Lp%=:"
               : "=v"(probe) : "s"(rk0), "s"(wave) : "scc");
  __builtin_amdgcn_s_barrier();
  asm volatile("" ::: "memory");
  unsigned char* const ws = ptr64(kb[6], kb[7]);
  const __amdgpu_buffer_rsrc_t rw = __builtin_amdgcn_make_buffer_rsrc(ws, 0, WS_END, RSRC3);
  v4u mask;
  {
    const unsigned half = (n & 1) ? 0xffff0000u : 0x0000ffffu; const int r = (n & 7) >> 1;
    mask.x = r == 0 ? half : 0u; mask.y = r == 1 ? half : 0u; mask.z = r == 2 ? half : 0u; mask.w = r == 3 ? half : 0u;
  }
  const int pidx = kvh * 8 + s8;

  if (__builtin_expect(wave >= 8, 1)) {
    __builtin_amdgcn_s_setprio(3);
    const int hw = wave - 8, cg = s8 & 3, kh = s8 >> 2, sub = 2 * g + (n >> 3);
    const __amdgpu_buffer_rsrc_t rq = __builtin_amdgcn_make_buffer_rsrc(
        (rb ? ptr64(ka[14], ka[15]) : ptr64(ka[12], ka[13])) + ((size_t)(kh * 288 + hw * 72) * NQ + kvh * 256 + cg * 64) * 2, 0, 71 * NQ * 2 + 128, RSRC3);
    const __amdgpu_buffer_rsrc_t rx = __builtin_amdgcn_make_buffer_rsrc((rb ? ptr64(ka[2], ka[3]) : ptr64(ka[0], ka[1])) + (kh * 288 + hw * 72) * 2, 0, 72 * 2, RSRC3);
    v4u wqv[9];
#pragma unroll
    for (int i = 0; i < 9; ++i) wqv[i] = __builtin_amdgcn_raw_buffer_load_b128(rq, sub * (NQ * 2) + (n & 7) * 16, i * 8 * NQ * 2, AUX_NT);
    const unsigned xa = __builtin_amdgcn_raw_buffer_load_b32(rx, n * 16 + g * 4, 0, 0);
    const unsigned ep = __builtin_amdgcn_raw_buffer_load_b32(rw, (lane == 0 && hw < 2) ? WS_EPOCH : OOB, 0, AUX_SC1);
    SB();
    f32x4 a0 = {0.f, 0.f, 0.f, 0.f}, a1 = {0.f, 0.f, 0.f, 0.f};
    const unsigned sel = n < 8 ? 0x01000100u : 0x03020302u;
#define QSTEP(acc, i, src, k) acc = mfma(diag(__builtin_amdgcn_perm(0u, (unsigned)dppi<BCAST + (k)>((int)(src)), sel), mask), wqv[i], acc)
    QSTEP(a0, 0, xa, 0); QSTEP(a1, 1, xa, 1); QSTEP(a0, 2, xa, 2); QSTEP(a1, 3, xa, 3); QSTEP(a0, 4, xa, 4); QSTEP(a1, 5, xa, 5);
    QSTEP(a0, 6, xa, 6); QSTEP(a1, 7, xa, 7); QSTEP(a0, 8, xa, 8);
#undef QSTEP
    if ((g >> 1) == (n >> 3)) *(f32x4*)(lds + L_QP + (hw * 2 + (n >> 3)) * 64 + 8 * (n & 7) + 4 * (g & 1)) = a0 + a1;
    const unsigned tag = __builtin_amdgcn_readfirstlane(ep) + TAG_STEP;
    lds_arrive(cnt + C_QP, lane);
    if (hw >= 2) return;
    unsigned anc = 0;
    if (hw == 0) {
      lds_wait<1>(cnt + C_QP, 4u);
      const f32x2* qp = (const f32x2*)(lds + L_QP) + (lane & 31);
      const f32x2 q = ((qp[0] + qp[32]) + (qp[64] + qp[96])) + ((qp[128] + qp[160]) + (qp[192] + qp[224]));
      st_gran2(rw, lane < 32 ? WS_QG + ((rb * 16 + kvh) * 512 + kh * 256 + cg * 64 + 2 * lane) * 8 : OOB, tag, q.x, q.y);
      if (lane == 0) cnt[C_TAG] = tag;
      anc = lds_wait<1>(cnt + C_ANCHOR, 1u);
      if (lane == 0) __hip_atomic_store((GAS unsigned long long*)(ws + WS_AG + wg * 8), ((unsigned long long)tag << 32) | anc, RLX_AGENT);
    }
    pause<kQA>();
    if (hw) pause<kQB - kQA>();
    {
      v2u x[8];
      const v2u ag = ldg(rw, (hw == 0 && lane < 8) ? WS_AG + (wg & ~7) * 8 + lane * 8 : OOB);
      sweep<8, 512, 8>(rw, WS_QG + ((hw * 16 + kvh) * 512 + lane) * 8, tag, x);
#pragma unroll
      for (int k = 0; k < 4; ++k) ((unsigned short*)(lds + L_Q))[hw * 256 + k * 64 + lane] = f2bf(u2f(x[k].x) + u2f(x[k + 4].x));
      if (hw == 0) {
        int d = ag.y == tag ? (int)(ag.x - anc) : 0;
        d = d > 1600 ? 1600 : d < -1600 ? -1600 : d;
        d += dppi<XOR1>(d); d += dppi<XOR2>(d); d += dppi<HMIR>(d);
        if (lane == 0) cnt[C_ADJ] = (unsigned)(d / (8 * (31 - PRE)));
      }
      lds_arrive(cnt + (hw ? C_QB : C_QA), lane);
    }
    float oa0 = 0.f, oa1 = 0.f;
#pragma nounroll
    for (int it = 0; it < 2; ++it) {
      int br = it;
      asm volatile("" : "+s"(br));
      lds_wait<8>(cnt + C_ML + br, 8u);
      const float* ml = lds + L_ML + br * 32 + hw * 8;
      const f32x4 m0 = *(const f32x4*)(ml), m1 = *(const f32x4*)(ml + 4), l0 = *(const f32x4*)(ml + 16), l1 = *(const f32x4*)(ml + 20);
      const float M = fmax_(fmax_(fmax_(m0.x, m0.y), fmax_(m0.z, m0.w)), fmax_(fmax_(m1.x, m1.y), fmax_(m1.z, m1.w)));
      float L = 0.f;
#pragma unroll
      for (int w = 0; w < 8; ++w) L = fmaf(__builtin_amdgcn_exp2f((w < 4 ? m0[w & 3] : m1[w & 3]) - M), w < 4 ? l0[w & 3] : l1[w & 3], L);
      const int mg = WS_MG + ((br * 16 + kvh) * 2 + hw) * 1024;
      st_gran2(rw, lane == 0 ? mg + (ch & 15) * 64 : OOB, tag, M, L);
      pause<kML>();
      v2u x[1];
      sweep<1, 0, 8>(rw, mg + n * 64 + (g & 1) * 8, tag, x);
      auto sw = __builtin_amdgcn_permlane16_swap(x[0].x, x[0].x, false, false);
      const float Ms = u2f(sw[0]), Ls = u2f(sw[1]);
      float Mx = fmax_(Ms, dpp<XOR1>(Ms)); Mx = fmax_(Mx, dpp<XOR2>(Mx)); Mx = fmax_(Mx, dpp<HMIR>(Mx)); Mx = fmax_(Mx, dpp<MIR>(Mx));
      float Lt = __builtin_amdgcn_exp2f(Ms - Mx) * Ls; Lt += dpp<XOR1>(Lt); Lt += dpp<XOR2>(Lt); Lt += dpp<HMIR>(Lt); Lt += dpp<MIR>(Lt);
      const float inv = 1.0f / Lt;
      float c[8];
#pragma unroll
      for (int w = 0; w < 8; ++w) c[w] = __builtin_amdgcn_exp2f((w < 4 ? m0[w & 3] : m1[w & 3]) - Mx) * inv;
      const unsigned oa = lds_addr(lds + L_O + br * 2048 + hw * 128 + 2 * lane);
      int dsta = br ? WS_PG + ch * PG_ROW + (hw * 128 + 2 * lane) * 8 : OOB, dstb = br ? dsta + 256 * PG_ROW : OOB;
      asm volatile("" : "+v"(dsta), "+v"(dstb));
      float o0, o1;
      { const v2u v = lds_take2<8>(oa); o0 = c[0] * u2f(v.x); o1 = c[0] * u2f(v.y); }
#pragma unroll
      for (int w = 1; w < 8; ++w) {
        if (w == 5) {
          st_gran2(rw, dsta, tag, oa0, oa1);
          if (hw == 0 && lane == 0) __hip_atomic_store(cnt + C_PUB, (unsigned)br, RLX_WG);
        }
        const v2u v = lds_take2<0>(oa + w * 1024); o0 = fmaf(c[w], u2f(v.x), o0); o1 = fmaf(c[w], u2f(v.y), o1);
      }
      st_gran2(rw, dstb, tag, o0, o1);
      if (hw == 0 && lane == 0) __hip_atomic_store(cnt + C_PUB, 2u * (unsigned)br, RLX_WG);
      oa0 = o0; oa1 = o1;
    }
  } else {
    if (wave == 0) {
      unsigned long long ta;
      asm volatile("s_waitcnt vmcnt(0)\n\ts_memrealtime %0\n\ts_waitcnt lgkmcnt(0)" : "=s"(ta) : "v"(probe) : "memory");
      if (lane == 0) __hip_atomic_store(cnt + C_ANCHOR, ((unsigned)ta << 4) | 1u, RLX_WG);
    }
    St z;
    z.lds = lds; z.lane = lane; z.wave = wave; z.mask = mask;
    z.rs[0] = rk0;
    z.rs[1] = mkrs(ptr64(ka[8], ka[9]) + (size_t)ch * 262144, 262144);
    z.rs[2] = mkrs(ptr64(ka[6], ka[7]) + (size_t)ch * 262144, 262144);
    z.rs[3] = mkrs(ptr64(ka[10], ka[11]) + (size_t)ch * 262144, 262144);
    z.vk = ((lane >> 2) & 3) * 256 + g * 64 + (lane & 3) * 16; z.vv = lane * 16; z.so = wave * 1024;
    const int phase16 = (wave * kSlot16) >> 3;
    asm volatile("s_waitcnt vmcnt(0)" :: "v"(cw) : "memory");
    z.due = lds_wait<1>(cnt + C_ANCHOR, 1u) + (unsigned)(kOff16 + phase16);
    Req<0, PRE>::go(z);
    lds_wait<1>(cnt + C_QA, 1u);
    const unsigned tag = __builtin_amdgcn_readfirstlane(cnt[C_TAG]);
    z.adj = __builtin_amdgcn_readfirstlane(cnt[C_ADJ]);
    Con<0, PRE - DEP + 1, PRE - 1>::go(z);
    Run<PRE, 32>::go(z);
    Con<32 - DEP + 1, 32, 31>::go(z);
    const unsigned t32 = z.due - (unsigned)phase16;
    if (wave < 5) {
      const __amdgpu_buffer_rsrc_t ro = __builtin_amdgcn_make_buffer_rsrc(
          (rb ? ptr64(kb[2], kb[3]) : ptr64(kb[0], kb[1])) + (size_t)(kvh * 256 + s8 * 32) * DM * 2, 0, 32 * DM * 2, RSRC3);
      const int wcol = wave * 128 + n * 8;
      const int vwo = wcol < DM ? g * (DM * 2) + wcol * 2 : OOB;
      const unsigned oba = lds_addr(lds + L_OB + g * 8);
      v4u Wo[8];
      gate(t32 + (unsigned)(wave * kWoStep16));
#pragma unroll
      for (int i = 0; i < 8; ++i) Wo[i] = __builtin_amdgcn_raw_buffer_load_b128(ro, vwo, i * 4 * DM * 2, AUX_NT);
      int dst = wcol < DM ? WS_YG + (rb * 128 + pidx) * YG_ROW + (wcol + 4 * (g & 1) + 2 * (g >> 1)) * 8 : OOB;
      asm volatile("" : "+v"(dst), "+v"(Wo[0]), "+v"(Wo[1]), "+v"(Wo[2]), "+v"(Wo[3]), "+v"(Wo[4]), "+v"(Wo[5]), "+v"(Wo[6]), "+v"(Wo[7]));
      v4u oa, ob;
#pragma nounroll
      for (unsigned spins = 0; spins < LSPIN_CAP; ++spins) {
        asm volatile("ds_read_b128 %0, %2\n\tds_read_b128 %1, %2 offset:16\n\ts_waitcnt lgkmcnt(0)" : "=&v"(oa), "=&v"(ob) : "v"(oba) : "memory");
        if (__all(oa.x != EMPTY_B && oa.y != EMPTY_B && oa.z != EMPTY_B && oa.w != EMPTY_B && ob.x != EMPTY_B && ob.y != EMPTY_B && ob.z != EMPTY_B && ob.w != EMPTY_B)) break;
      }
      f32x4 Y0 = {0.f, 0.f, 0.f, 0.f}, Y1 = {0.f, 0.f, 0.f, 0.f};
#pragma unroll
      for (int i = 0; i < 4; ++i) { Y0 = mfma(diag(oa[i], mask), Wo[i], Y0); Y1 = mfma(diag(ob[i], mask), Wo[4 + i], Y1); }
      const f32x4 Y = Y0 + Y1;
      st_gran2(rw, dst, tag, g < 2 ? Y.x : Y.z, g < 2 ? Y.y : Y.w);
    } else if (wave == 5) {
      const int po = WS_PG + (rb * 256 + kvh * 16 + (lane >> 5)) * PG_ROW + (32 * s8 + (lane & 31)) * 8;
      v2u x[8];
      lds_wait<0>(cnt + C_PUB, (unsigned)rb + 1u);
      pause<kHop1>();
      sweep<8, 2 * PG_ROW, 1>(rw, po, tag, x);
      const float o = x32_sum(sum8(x));
      if (lane < 32) ((unsigned*)(lds + L_OB))[(lane & 3) * 8 + (lane >> 2)] = dup_bf16(o);
    } else if (wave == 6 || pidx < 16) {
      const int col = (wave == 6 ? 0 : 512) + pidx * 4 + (lane & 3);
      GAS unsigned short* op = (GAS unsigned short*)ptr64(kb[4], kb[5]) + rb * DM + col;
      asm volatile("" : "+v"(op));
      v2u x[8];
#pragma nounroll
      for (unsigned spins = 0; spins < LSPIN_CAP; ++spins) {
        if (__builtin_amdgcn_readfirstlane(__hip_atomic_load((unsigned*)(lds + L_OB), RLX_WG)) != EMPTY_B) break;
        __builtin_amdgcn_s_sleep(1);
      }
      pause<kHop2>();
      gate(t32 + (unsigned)(4 * kWoStep16 + kHop2Gate16));
      sweep<8, 16 * YG_ROW, 1>(rw, WS_YG + (rb * 128 + (lane >> 2)) * YG_ROW + col * 8, tag, x);
      float y = sum8(x);
      y += dpp<ROR4>(y); y += dpp<ROR8>(y);
      y = xg_sum(y);
      if (lane < 4) *op = f2bf(y);
      if (rb == 0 && wave == 6 && pidx == 0 && lane == 0) __hip_atomic_store((GAS unsigned*)(ws + WS_EPOCH), tag, RLX_AGENT);
    }
  }
}

extern "C" void kernel_launch(void* const* d_in, const int* in_sizes, int n_in, void* d_out, int out_size, void* d_ws, size_t ws_size, hipStream_t stream) {
  Args a;
  a.x[0] = d_in[0]; a.k[0] = d_in[1]; a.v[0] = d_in[2]; a.wq[0] = d_in[3]; a.wo[0] = d_in[4];
  a.x[1] = d_in[5]; a.k[1] = d_in[6]; a.v[1] = d_in[7]; a.wq[1] = d_in[8]; a.wo[1] = d_in[9];
  a.out = d_out; a.ws = d_ws;
  gqa2_fused<<<NWG, NTHR, 0, stream>>>(a);
}
```

```cpp
#include <hip/hip_runtime.h>

typedef unsigned v2u __attribute__((ext_vector_type(2)));
typedef unsigned v4u __attribute__((ext_vector_type(4)));
typedef unsigned v8u __attribute__((ext_vector_type(8)));
typedef unsigned v16u __attribute__((ext_vector_type(16)));
typedef float f32x2 __attribute__((ext_vector_type(2)));
typedef float f32x4 __attribute__((ext_vector_type(4)));
typedef __bf16 bf16x2 __attribute__((ext_vector_type(2)));
typedef __bf16 bf16x8 __attribute__((ext_vector_type(8)));
#define GAS __attribute__((address_space(1)))
#define LAS __attribute__((address_space(3)))
#define RLX_AGENT __ATOMIC_RELAXED, __HIP_MEMORY_SCOPE_AGENT
#define RLX_WG __ATOMIC_RELAXED, __HIP_MEMORY_SCOPE_WORKGROUP
#define SB() __builtin_amdgcn_sched_barrier(0)

constexpr int DM = 576, NQ = 4096;
constexpr int NWG = 256, NTHR = 768;
constexpr float C2 = 0.088388347648318440f * 1.4426950408889634f;

#define DT16 1760
#define OFF16 1300
constexpr int kSlot16 = DT16;
constexpr int kOff16 = OFF16;
constexpr int kPrio16 = 400;
constexpr int kWoStep16 = 600;
constexpr int kHop2Gate16 = 2000;
constexpr int kQA = 280, kQB = 600, kML = 350, kHop1 = 40, kHop2 = 70;
constexpr int kCodeBytes = 20224;
constexpr int PRE = 8;
constexpr int DEP = 3;
constexpr int LIM = 2;

constexpr int AUX_NT = 2, AUX_SC1 = 16, RSRC3 = 0x00020000, OOB = 0x7fff0000;
constexpr int WS_EPOCH = 0;
constexpr int WS_AG = 1024;
constexpr int WS_QG = 4096;
constexpr int WS_MG = WS_QG + 32 * 4096;
constexpr int WS_PG = WS_MG + 64 * 1024, PG_ROW = 256 * 8;
constexpr int WS_YG = WS_PG + 512 * PG_ROW, YG_ROW = DM * 8;
constexpr int WS_END = WS_YG + 256 * YG_ROW;
constexpr unsigned TAG_STEP = 0x9E3779B9u;
constexpr unsigned SPIN_CAP = 1u << 17, LSPIN_CAP = 1u << 22;
constexpr int L_O = 0;
constexpr int L_ML = L_O + 4096;
constexpr int L_QP = L_ML + 64;
constexpr int L_Q = L_QP + 512;
constexpr int L_OB = L_Q + 256;
constexpr int L_CNT = L_OB + 32, L_END = L_CNT + 16;
enum { C_QP, C_QA, C_ML  , C_PUB = 4, C_TAG, C_ANCHOR, C_QB, C_ADJ };
constexpr unsigned EMPTY_O = 0xffffffffu, EMPTY_B = 0x0000ffffu;

struct Args {
  const void* x[2]; const void* k[2]; const void* v[2]; const void* wq[2]; const void* wo[2];
  void* out; void* ws;
};

__device__ __forceinline__ f32x4 mfma(v4u a, v4u b, f32x4 c) {
  return __builtin_amdgcn_mfma_f32_16x16x32_bf16(__builtin_bit_cast(bf16x8, a), __builtin_bit_cast(bf16x8, b), c, 0, 0, 0);
}
__device__ __forceinline__ unsigned dup_bf16(float f) {
  f32x2 v = {f, f};
  return __builtin_bit_cast(unsigned, __builtin_convertvector(v, bf16x2));
}
__device__ __forceinline__ unsigned short f2bf(float f) { return __builtin_bit_cast(unsigned short, (__bf16)f); }
__device__ __forceinline__ float u2f(unsigned u) { return __uint_as_float(u); }
__device__ __forceinline__ v4u diag(unsigned pp, v4u mask) { v4u r = {pp & mask.x, pp & mask.y, pp & mask.z, pp & mask.w}; return r; }
template <int CTRL> __device__ __forceinline__ int dppi(int x) { return __builtin_amdgcn_mov_dpp(x, CTRL, 0xf, 0xf, true); }
template <int CTRL> __device__ __forceinline__ float dpp(float x) { return __builtin_bit_cast(float, dppi<CTRL>(__builtin_bit_cast(int, x))); }
constexpr int XOR1 = 0xB1, XOR2 = 0x4E, HMIR = 0x141, MIR = 0x140, ROR8 = 0x128, ROR4 = 0x124, BCAST = 0x150;
__device__ __forceinline__ float fmax_(float a, float b) { return __builtin_fmaxf(a, b); }
__device__ __forceinline__ float xg_max(float x) {
  auto s = __builtin_amdgcn_permlane16_swap(__float_as_uint(x), __float_as_uint(x), false, false);
  x = fmax_(u2f(s[0]), u2f(s[1]));
  auto t = __builtin_amdgcn_permlane32_swap(__float_as_uint(x), __float_as_uint(x), false, false);
  return fmax_(u2f(t[0]), u2f(t[1]));
}
__device__ __forceinline__ float x32_sum(float x) {
  auto t = __builtin_amdgcn_permlane32_swap(__float_as_uint(x), __float_as_uint(x), false, false);
  return u2f(t[0]) + u2f(t[1]);
}
__device__ __forceinline__ float xg_sum(float x) {
  auto s = __builtin_amdgcn_permlane16_swap(__float_as_uint(x), __float_as_uint(x), false, false);
  return x32_sum(u2f(s[0]) + u2f(s[1]));
}
__device__ __forceinline__ float sum8(const v2u (&x)[8]) {
  return ((u2f(x[0].x) + u2f(x[1].x)) + (u2f(x[2].x) + u2f(x[3].x))) + ((u2f(x[4].x) + u2f(x[5].x)) + (u2f(x[6].x) + u2f(x[7].x)));
}

__device__ __forceinline__ void st_gran2(__amdgpu_buffer_rsrc_t rs, int off, unsigned tag, float a, float b) {
  __builtin_amdgcn_raw_buffer_store_b128(v4u{__float_as_uint(a), tag, __float_as_uint(b), tag}, rs, off, 0, AUX_SC1);
}
__device__ __forceinline__ v2u ldg(__amdgpu_buffer_rsrc_t rs, int off) { return __builtin_amdgcn_raw_buffer_load_b64(rs, off, 0, AUX_SC1); }
template <int N, int STEP, int SLEEP> __device__ __forceinline__ void sweep(__amdgpu_buffer_rsrc_t rs, int off, unsigned tag, v2u (&x)[N]) {
#pragma unroll
  for (int k = 0; k < N; ++k) x[k] = ldg(rs, off + k * STEP);
  SB();
  bool ok = true;
#pragma unroll
  for (int k = 0; k < N; ++k) ok &= x[k].y == tag;
  if (__builtin_expect(__all(ok), 1)) return;
#pragma nounroll
  for (unsigned spins = 0; spins < SPIN_CAP; ++spins) {
    __builtin_amdgcn_s_sleep(SLEEP);
    v2u y[N];
#pragma unroll
    for (int k = 0; k < N; ++k) y[k] = ldg(rs, x[k].y == tag ? OOB : off + k * STEP);
    SB();
    ok = true;
#pragma unroll
    for (int k = 0; k < N; ++k) { x[k] = x[k].y == tag ? x[k] : y[k]; ok &= x[k].y == tag; }
    if (__all(ok)) break;
  }
}

__device__ __forceinline__ unsigned char* ptr64(unsigned lo, unsigned hi) { return (unsigned char*)(((unsigned long long)hi << 32) | lo); }
__device__ __forceinline__ v4u mkrs(const void* p, unsigned bytes) {
  const unsigned long long u = (unsigned long long)p;
  v4u r = {(unsigned)u, (unsigned)(u >> 32) & 0xffffu, bytes, (unsigned)RSRC3};
  return r;
}
__device__ __forceinline__ void tile_load(v4u (&d)[4], int voff, v4u rs, int soff) {
  asm volatile("buffer_load_dwordx4 %0, %4, %5, %6 offen nt\n\t"
               "buffer_load_dwordx4 %1, %4, %5, %7 offen nt\n\t"
               "buffer_load_dwordx4 %2, %4, %5, %8 offen nt\n\t"
               "buffer_load_dwordx4 %3, %4, %5, %9 offen nt"
               : "=&v"(d[0]), "=&v"(d[1]), "=&v"(d[2]), "=&v"(d[3])
               : "v"(voff), "s"(rs), "s"(soff), "s"(soff + 8192), "s"(soff + 16384), "s"(soff + 24576));
}
template <int N> __device__ __forceinline__ void tile_wait(v4u (&d)[4]) {
  asm volatile("s_waitcnt vmcnt(%4)" : "+v"(d[0]), "+v"(d[1]), "+v"(d[2]), "+v"(d[3]) : "i"(N));
}
template <int N> __device__ __forceinline__ void piece_wait(v4u& d) { asm volatile("s_waitcnt vmcnt(%1)" : "+v"(d) : "i"(N)); }

template <int TICKS> __device__ __forceinline__ void pause() {
  unsigned due;
  asm volatile("s_memrealtime vcc\n\t"
               "s_waitcnt lgkmcnt(0)\n\t"
               "s_add_i32 %0, vcc_lo, %1\n"
               ".Lg0_%=:\n\t"
               "s_sleep 1\n\t"
               "s_memrealtime vcc\n\t"
               "s_waitcnt lgkmcnt(0)\n\t"
               "s_sub_i32 vcc_lo, vcc_lo, %0\n\t"
               "s_cmp_lt_i32 vcc_lo, 0\n\t"
               "s_cbranch_scc1 .Lg0_%="
               : "=&s"(due) : "i"(TICKS) : "scc", "vcc", "memory");
}
__device__ __forceinline__ void gate(unsigned due16) {
  asm volatile("s_branch .Lg1_%=\n"
               ".Lg0_%=:\n\t"
               "s_sleep 1\n"
               ".Lg1_%=:\n\t"
               "s_memrealtime vcc\n\t"
               "s_waitcnt lgkmcnt(0)\n\t"
               "s_lshl_b32 vcc_lo, vcc_lo, 4\n\t"
               "s_sub_i32 vcc_lo, vcc_lo, %0\n\t"
               "s_cmp_lt_i32 vcc_lo, 0\n\t"
               "s_cbranch_scc1 .Lg0_%=\n\t"
               "s_setprio 0\n\t"
               "s_cmp_lt_i32 vcc_lo, %1\n\t"
               "s_cbranch_scc1 .Lg2_%=\n\t"
               "s_setprio 1\n"
               ".Lg2_%=:"
               :: "s"(due16), "i"(kPrio16) : "scc", "vcc", "memory");
}

__device__ __forceinline__ void lds_arrive(unsigned* c, int lane) {
  asm volatile("s_waitcnt lgkmcnt(0)" ::: "memory");
  if (lane == 0) __hip_atomic_fetch_add(c, 1u, RLX_WG);
}
template <int SLEEP> __device__ __forceinline__ unsigned lds_wait(unsigned* c, unsigned want) {
  unsigned v = 0;
#pragma nounroll
  for (unsigned spins = 0; spins < LSPIN_CAP; ++spins) {
    v = __builtin_amdgcn_readfirstlane(__hip_atomic_load(c, RLX_WG));
    if (v >= want) break;
    __builtin_amdgcn_s_sleep(SLEEP);
  }
  asm volatile("" ::: "memory");
  return v;
}
__device__ __forceinline__ unsigned lds_addr(const void* p) { return (unsigned)(size_t)(const LAS void*)p; }
template <int SLEEP> __device__ __forceinline__ v2u lds_take2(unsigned addr) {
  v2u v;
#pragma nounroll
  for (unsigned spins = 0; spins < LSPIN_CAP; ++spins) {
    asm volatile("ds_read_b64 %0, %1\n\ts_waitcnt lgkmcnt(0)" : "=&v"(v) : "v"(addr) : "memory");
    if (__all(v.x != EMPTY_O && v.y != EMPTY_O)) break;
    if (SLEEP) __builtin_amdgcn_s_sleep(SLEEP);
  }
  return v;
}

struct St {
  v4u T[32][4];
  float Sc[8][4];
  unsigned P[8][4];
  v4u Q, mask;
  f32x4 O;
  v4u rs[4];
  int vk, vv, so;
  unsigned due, adj;
  float* lds;
  int lane, wave;
};
template <int I> __device__ __forceinline__ void request(St& z) {
  if constexpr (I > 0) asm volatile("s_waitcnt vmcnt(%0)" :: "i"(LIM) : "memory");
  gate(z.due);
  z.due += kSlot16;
  if constexpr (I >= PRE && I < 31) z.due += z.adj;
  tile_load(z.T[I], (I & 8) ? z.vv : z.vk, z.rs[I >> 3], z.so + (I & 7) * 32768);
  SB();
}
template <int I, int W> __device__ __forceinline__ void consume(St& z) {
  const int g = z.lane >> 4, n = z.lane & 15;
  constexpr int br = I >> 4;
  unsigned* const cnt = (unsigned*)(z.lds + L_CNT);
  if constexpr ((I & 15) == 0) {
    if constexpr (br) lds_wait<1>(cnt + C_QB, 1u);
    z.Q = *(const v4u*)((const unsigned short*)(z.lds + L_Q) + br * 256 + (n >> 3) * 128 + 32 * g + 8 * (n & 3));
  }
  if constexpr ((I & 8) == 0) {
    tile_wait<W>(z.T[I]);
#pragma unroll
    for (int j = 0; j < 4; ++j) {
      const f32x4 d = mfma(z.T[I][j], z.Q, f32x4{0.f, 0.f, 0.f, 0.f});
      z.Sc[I & 7][j] = (dpp<0x00>(d.x) + dpp<0x55>(d.y)) + (dpp<0xAA>(d.z) + dpp<0xFF>(d.w));
    }
    if constexpr ((I & 7) == 7) {
      float m = z.Sc[0][0];
#pragma unroll
      for (int t = 0; t < 8; ++t)
#pragma unroll
        for (int j = 0; j < 4; ++j) m = fmax_(m, z.Sc[t][j]);
      const float m2 = xg_max(m) * C2;
      float l = 0.f;
#pragma unroll
      for (int t = 0; t < 8; ++t) {
        float p[4];
#pragma unroll
        for (int j = 0; j < 4; ++j) { p[j] = __builtin_amdgcn_exp2f(fmaf(z.Sc[t][j], C2, -m2)); z.P[t][j] = dup_bf16(p[j]); }
        l += (p[0] + p[1]) + (p[2] + p[3]);
        asm volatile("" : "+v"(z.P[t][0]), "+v"(z.P[t][1]), "+v"(z.P[t][2]), "+v"(z.P[t][3]));
      }
      l = xg_sum(l);
      if (g == 0 && (n & 7) == 0) { z.lds[L_ML + br * 32 + (n >> 3) * 8 + z.wave] = m2; z.lds[L_ML + br * 32 + 16 + (n >> 3) * 8 + z.wave] = l; }
      lds_arrive(cnt + C_ML + br, z.lane);
      z.O = f32x4{0.f, 0.f, 0.f, 0.f};
    } else {
      asm volatile("" : "+v"(z.Sc[I & 7][0]), "+v"(z.Sc[I & 7][1]), "+v"(z.Sc[I & 7][2]), "+v"(z.Sc[I & 7][3]));
    }
  } else if constexpr (W == 0) {
    v4u A[4];
#pragma unroll
    for (int j = 0; j < 4; ++j) A[j] = diag(z.P[I & 7][j], z.mask);
    unsigned da = lds_addr(z.lds + L_O + br * 2048 + z.wave * 256 + (g >> 1) * 128 + 8 * n + 4 * (g & 1));
    asm volatile("" : "+v"(A[0]), "+v"(A[1]), "+v"(A[2]), "+v"(A[3]), "+v"(da));
    piece_wait<3>(z.T[I][0]); z.O = mfma(A[0], z.T[I][0], z.O); SB();
    piece_wait<2>(z.T[I][1]); z.O = mfma(A[1], z.T[I][1], z.O); SB();
    piece_wait<1>(z.T[I][2]); z.O = mfma(A[2], z.T[I][2], z.O); SB();
    piece_wait<0>(z.T[I][3]); z.O = mfma(A[3], z.T[I][3], z.O);
    *(LAS f32x4*)(size_t)da = z.O;
  } else {
    tile_wait<W>(z.T[I]);
#pragma unroll
    for (int j = 0; j < 4; ++j) z.O = mfma(diag(z.P[I & 7][j], z.mask), z.T[I][j], z.O);
    asm volatile("" : "+v"(z.O));
    if constexpr ((I & 7) == 7) *(f32x4*)(z.lds + L_O + br * 2048 + z.wave * 256 + (g >> 1) * 128 + 8 * n + 4 * (g & 1)) = z.O;
  }
  SB();
}
template <int I, int N> struct Req { static __device__ __forceinline__ void go(St& z) { request<I>(z); Req<I + 1, N>::go(z); } };
template <int N> struct Req<N, N> { static __device__ __forceinline__ void go(St&) {} };
template <int I, int N, int LAST> struct Con {
  static __device__ __forceinline__ void go(St& z) { consume<I, 4 * (LAST - I)>(z); Con<I + 1, N, LAST>::go(z); }
};
template <int N, int LAST> struct Con<N, N, LAST> { static __device__ __forceinline__ void go(St&) {} };
template <int I, int N> struct Run {
  static __device__ __forceinline__ void go(St& z) { request<I>(z); consume<I - DEP + 1, 4 * (DEP - 1)>(z); Run<I + 1, N>::go(z); }
};
template <int N> struct Run<N, N> { static __device__ __forceinline__ void go(St&) {} };

__global__ void __launch_bounds__(NTHR, 3) gqa2_fused(Args) {
  __shared__ __attribute__((aligned(16))) float lds[L_END];
  unsigned* const cnt = (unsigned*)(lds + L_CNT);
  v16u ka; v8u kb;
  asm volatile("s_load_dwordx16 %0, %2, 0x0\n\ts_load_dwordx8 %1, %2, 0x40" : "=&s"(ka), "=&s"(kb) : "s"(__builtin_amdgcn_kernarg_segment_ptr()));
  const int tid = threadIdx.x, lane = tid & 63, wave = __builtin_amdgcn_readfirstlane(tid >> 6);
  const int g = lane >> 4, n = lane & 15;
  const int wg = blockIdx.x;
  const int ch = (wg & 7) << 5 | wg >> 3;
  unsigned cw;
  asm volatile("s_cmp_lg_u32 %2, 0\n\ts_cbranch_scc1 .Lc%=\n\ts_cmp_gt_u32 %3, 2\n\ts_cbranch_scc1 .Lc%=\n\t"
               "s_getpc_b64 vcc\n\tglobal_load_dword %0, %1, vcc\n.Lc%=:"
               : "=v"(cw) : "v"(min((tid - 64) * 128, kCodeBytes)), "s"((((wg >> 3) ^ wg) & 7) | (wg >> 7)), "s"((unsigned)(wave - 1)) : "scc", "vcc");
  const int kvh = ch >> 4, rb = (ch >> 3) & 1, s8 = ch & 7;
  {
    const v4u e = {EMPTY_O, EMPTY_O, EMPTY_O, EMPTY_O};
    const int i = tid < 512 ? tid : 0, j = tid < 48 ? tid : 0;
    *(v4u*)(lds + L_O + i * 4) = e; *(v4u*)(lds + L_O + 2048 + i * 4) = e;
    ((unsigned*)(lds + L_OB))[j] = j < 32 ? EMPTY_B : 0u;
  }
  asm volatile("s_waitcnt lgkmcnt(0)" : "+s"(ka), "+s"(kb) :: "memory");
  const v4u rk0 = mkrs(ptr64(ka[4], ka[5]) + (size_t)ch * 262144, 262144);
  unsigned probe;
  asm volatile("s_cmp_lg_u32 %2, 0\n\ts_cbranch_scc1 .Lp%=\n\ts_mov_b64 exec, 1\n\tbuffer_load_dword %0, off, %1, 0 nt\n\ts_mov_b64 exec, -1\n.Lp%=:"
               : "=v"(probe) : "s"(rk0), "s"(wave) : "scc");
  __builtin_amdgcn_s_barrier();
  asm volatile("" ::: "memory");
  {
    unsigned av;
    asm volatile("s_cmp_lg_u32 %3, 0\n\ts_cbranch_scc1 .La%=\n\t"
                 "s_waitcnt vmcnt(0)\n\ts_memrealtime vcc\n\ts_waitcnt lgkmcnt(0)\n\t"
                 "s_lshl_b32 vcc_lo, vcc_lo, 4\n\ts_or_b32 vcc_lo, vcc_lo, 1\n\tv_mov_b32 %0, vcc_lo\n\tds_write_b32 %1, %0\n.La%=:"
                 : "=&v"(av) : "v"(lds_addr(cnt + C_ANCHOR)), "v"(probe), "s"(wave) : "scc", "vcc", "memory");
  }
  unsigned char* const ws = ptr64(kb[6], kb[7]);
  const __amdgpu_buffer_rsrc_t rw = __builtin_amdgcn_make_buffer_rsrc(ws, 0, WS_END, RSRC3);
  v4u mask;
  {
    const unsigned half = (n & 1) ? 0xffff0000u : 0x0000ffffu; const int r = (n & 7) >> 1;
    mask.x = r == 0 ? half : 0u; mask.y = r == 1 ? half : 0u; mask.z = r == 2 ? half : 0u; mask.w = r == 3 ? half : 0u;
  }
  const int pidx = kvh * 8 + s8;

  if (__builtin_expect(wave >= 8, 1)) {
    __builtin_amdgcn_s_setprio(3);
    const int hw = wave - 8, cg = s8 & 3, kh = s8 >> 2, sub = 2 * g + (n >> 3);
    const __amdgpu_buffer_rsrc_t rq = __builtin_amdgcn_make_buffer_rsrc(
        (rb ? ptr64(ka[14], ka[15]) : ptr64(ka[12], ka[13])) + ((size_t)(kh * 288 + hw * 72) * NQ + kvh * 256 + cg * 64) * 2, 0, 71 * NQ * 2 + 128, RSRC3);
    const __amdgpu_buffer_rsrc_t rx = __builtin_amdgcn_make_buffer_rsrc((rb ? ptr64(ka[2], ka[3]) : ptr64(ka[0], ka[1])) + (kh * 288 + hw * 72) * 2, 0, 72 * 2, RSRC3);
    v4u wqv[9];
#pragma unroll
    for (int i = 0; i < 9; ++i) wqv[i] = __builtin_amdgcn_raw_buffer_load_b128(rq, sub * (NQ * 2) + (n & 7) * 16, i * 8 * NQ * 2, AUX_NT);
    const unsigned xa = __builtin_amdgcn_raw_buffer_load_b32(rx, n * 16 + g * 4, 0, 0);
    const unsigned ep = __builtin_amdgcn_raw_buffer_load_b32(rw, (lane == 0 && hw < 2) ? WS_EPOCH : OOB, 0, AUX_SC1);
    SB();
    f32x4 a0 = {0.f, 0.f, 0.f, 0.f}, a1 = {0.f, 0.f, 0.f, 0.f};
    const unsigned sel = n < 8 ? 0x01000100u : 0x03020302u;
#define QSTEP(acc, i, src, k) acc = mfma(diag(__builtin_amdgcn_perm(0u, (unsigned)dppi<BCAST + (k)>((int)(src)), sel), mask), wqv[i], acc)
    QSTEP(a0, 0, xa, 0); QSTEP(a1, 1, xa, 1); QSTEP(a0, 2, xa, 2); QSTEP(a1, 3, xa, 3); QSTEP(a0, 4, xa, 4); QSTEP(a1, 5, xa, 5);
    QSTEP(a0, 6, xa, 6); QSTEP(a1, 7, xa, 7); QSTEP(a0, 8, xa, 8);
#undef QSTEP
    if ((g >> 1) == (n >> 3)) *(f32x4*)(lds + L_QP + (hw * 2 + (n >> 3)) * 64 + 8 * (n & 7) + 4 * (g & 1)) = a0 + a1;
    const unsigned tag = __builtin_amdgcn_readfirstlane(ep) + TAG_STEP;
    lds_arrive(cnt + C_QP, lane);
    if (hw >= 2) return;
    unsigned anc = 0;
    if (hw == 0) {
      lds_wait<1>(cnt + C_QP, 4u);
      const f32x2* qp = (const f32x2*)(lds + L_QP) + (lane & 31);
      const f32x2 q = ((qp[0] + qp[32]) + (qp[64] + qp[96])) + ((qp[128] + qp[160]) + (qp[192] + qp[224]));
      st_gran2(rw, lane < 32 ? WS_QG + ((rb * 16 + kvh) * 512 + kh * 256 + cg * 64 + 2 * lane) * 8 : OOB, tag, q.x, q.y);
      if (lane == 0) cnt[C_TAG] = tag;
      anc = lds_wait<1>(cnt + C_ANCHOR, 1u);
      if (lane == 0) __hip_atomic_store((GAS unsigned long long*)(ws + WS_AG + wg * 8), ((unsigned long long)tag << 32) | anc, RLX_AGENT);
    }
    pause<kQA>();
    if (hw) pause<kQB - kQA>();
    {
      v2u x[8];
      const v2u ag = ldg(rw, (hw == 0 && lane < 8) ? WS_AG + (wg & ~7) * 8 + lane * 8 : OOB);
      sweep<8, 512, 8>(rw, WS_QG + ((hw * 16 + kvh) * 512 + lane) * 8, tag, x);
#pragma unroll
      for (int k = 0; k < 4; ++k) ((unsigned short*)(lds + L_Q))[hw * 256 + k * 64 + lane] = f2bf(u2f(x[k].x) + u2f(x[k + 4].x));
      if (hw == 0) {
        int d = ag.y == tag ? (int)(ag.x - anc) : 0;
        d = d > 1600 ? 1600 : d < -1600 ? -1600 : d;
        d += dppi<XOR1>(d); d += dppi<XOR2>(d); d += dppi<HMIR>(d);
        if (lane == 0) cnt[C_ADJ] = (unsigned)(d / (8 * (31 - PRE)));
      }
      lds_arrive(cnt + (hw ? C_QB : C_QA), lane);
    }
    float oa0 = 0.f, oa1 = 0.f;
#pragma nounroll
    for (int it = 0; it < 2; ++it) {
      int br = it;
      asm volatile("" : "+s"(br));
      lds_wait<8>(cnt + C_ML + br, 8u);
      const float* ml = lds + L_ML + br * 32 + hw * 8;
      const f32x4 m0 = *(const f32x4*)(ml), m1 = *(const f32x4*)(ml + 4), l0 = *(const f32x4*)(ml + 16), l1 = *(const f32x4*)(ml + 20);
      const float M = fmax_(fmax_(fmax_(m0.x, m0.y), fmax_(m0.z, m0.w)), fmax_(fmax_(m1.x, m1.y), fmax_(m1.z, m1.w)));
      float L = 0.f;
#pragma unroll
      for (int w = 0; w < 8; ++w) L = fmaf(__builtin_amdgcn_exp2f((w < 4 ? m0[w & 3] : m1[w & 3]) - M), w < 4 ? l0[w & 3] : l1[w & 3], L);
      const int mg = WS_MG + ((br * 16 + kvh) * 2 + hw) * 1024;
      st_gran2(rw, lane == 0 ? mg + (ch & 15) * 64 : OOB, tag, M, L);
      pause<kML>();
      v2u x[1];
      sweep<1, 0, 8>(rw, mg + n * 64 + (g & 1) * 8, tag, x);
      auto sw = __builtin_amdgcn_permlane16_swap(x[0].x, x[0].x, false, false);
      const float Ms = u2f(sw[0]), Ls = u2f(sw[1]);
      float Mx = fmax_(Ms, dpp<XOR1>(Ms)); Mx = fmax_(Mx, dpp<XOR2>(Mx)); Mx = fmax_(Mx, dpp<HMIR>(Mx)); Mx = fmax_(Mx, dpp<MIR>(Mx));
      float Lt = __builtin_amdgcn_exp2f(Ms - Mx) * Ls; Lt += dpp<XOR1>(Lt); Lt += dpp<XOR2>(Lt); Lt += dpp<HMIR>(Lt); Lt += dpp<MIR>(Lt);
      const float inv = 1.0f / Lt;
      float c[8];
#pragma unroll
      for (int w = 0; w < 8; ++w) c[w] = __builtin_amdgcn_exp2f((w < 4 ? m0[w & 3] : m1[w & 3]) - Mx) * inv;
      const unsigned oa = lds_addr(lds + L_O + br * 2048 + hw * 128 + 2 * lane);
      int dsta = br ? WS_PG + ch * PG_ROW + (hw * 128 + 2 * lane) * 8 : OOB, dstb = br ? dsta + 256 * PG_ROW : OOB;
      asm volatile("" : "+v"(dsta), "+v"(dstb));
      float o0, o1;
      { const v2u v = lds_take2<8>(oa); o0 = c[0] * u2f(v.x); o1 = c[0] * u2f(v.y); }
#pragma unroll
      for (int w = 1; w < 8; ++w) {
        if (w == 5) {
          st_gran2(rw, dsta, tag, oa0, oa1);
          if (hw == 0 && lane == 0) __hip_atomic_store(cnt + C_PUB, (unsigned)br, RLX_WG);
        }
        const v2u v = lds_take2<0>(oa + w * 1024); o0 = fmaf(c[w], u2f(v.x), o0); o1 = fmaf(c[w], u2f(v.y), o1);
      }
      st_gran2(rw, dstb, tag, o0, o1);
      if (hw == 0 && lane == 0) __hip_atomic_store(cnt + C_PUB, 2u * (unsigned)br, RLX_WG);
      oa0 = o0; oa1 = o1;
    }
  } else {
    St z;
    z.lds = lds; z.lane = lane; z.wave = wave; z.mask = mask;
    z.rs[0] = rk0;
    z.rs[1] = mkrs(ptr64(ka[8], ka[9]) + (size_t)ch * 262144, 262144);
    z.rs[2] = mkrs(ptr64(ka[6], ka[7]) + (size_t)ch * 262144, 262144);
    z.rs[3] = mkrs(ptr64(ka[10], ka[11]) + (size_t)ch * 262144, 262144);
    z.vk = ((lane >> 2) & 3) * 256 + g * 64 + (lane & 3) * 16; z.vv = lane * 16; z.so = wave * 1024;
    const int phase16 = (wave * kSlot16) >> 3;
    asm volatile("s_waitcnt vmcnt(0)" :: "v"(cw) : "memory");
    z.due = lds_wait<1>(cnt + C_ANCHOR, 1u) + (unsigned)(kOff16 + phase16);
    Req<0, PRE>::go(z);
    lds_wait<1>(cnt + C_QA, 1u);
    const unsigned tag = __builtin_amdgcn_readfirstlane(cnt[C_TAG]);
    z.adj = __builtin_amdgcn_readfirstlane(cnt[C_ADJ]);
    Con<0, PRE - DEP + 1, PRE - 1>::go(z);
    Run<PRE, 32>::go(z);
    Con<32 - DEP + 1, 32, 31>::go(z);
    const unsigned t32 = z.due - (unsigned)phase16;
    if (wave < 5) {
      const __amdgpu_buffer_rsrc_t ro = __builtin_amdgcn_make_buffer_rsrc(
          (rb ? ptr64(kb[2], kb[3]) : ptr64(kb[0], kb[1])) + (size_t)(kvh * 256 + s8 * 32) * DM * 2, 0, 32 * DM * 2, RSRC3);
      const int wcol = wave * 128 + n * 8;
      const int vwo = wcol < DM ? g * (DM * 2) + wcol * 2 : OOB;
      const unsigned oba = lds_addr(lds + L_OB + g * 8);
      v4u Wo[8];
      gate(t32 + (unsigned)(wave * kWoStep16));
#pragma unroll
      for (int i = 0; i < 8; ++i) Wo[i] = __builtin_amdgcn_raw_buffer_load_b128(ro, vwo, i * 4 * DM * 2, AUX_NT);
      int dst = wcol < DM ? WS_YG + (rb * 128 + pidx) * YG_ROW + (wcol + 4 * (g & 1) + 2 * (g >> 1)) * 8 : OOB;
      asm volatile("" : "+v"(dst), "+v"(Wo[0]), "+v"(Wo[1]), "+v"(Wo[2]), "+v"(Wo[3]), "+v"(Wo[4]), "+v"(Wo[5]), "+v"(Wo[6]), "+v"(Wo[7]));
      v4u oa, ob;
#pragma nounroll
      for (unsigned spins = 0; spins < LSPIN_CAP; ++spins) {
        asm volatile("ds_read_b128 %0, %2\n\tds_read_b128 %1, %2 offset:16\n\ts_waitcnt lgkmcnt(0)" : "=&v"(oa), "=&v"(ob) : "v"(oba) : "memory");
        if (__all(oa.x != EMPTY_B && oa.y != EMPTY_B && oa.z != EMPTY_B && oa.w != EMPTY_B && ob.x != EMPTY_B && ob.y != EMPTY_B && ob.z != EMPTY_B && ob.w != EMPTY_B)) break;
      }
      f32x4 Y0 = {0.f, 0.f, 0.f, 0.f}, Y1 = {0.f, 0.f, 0.f, 0.f};
#pragma unroll
      for (int i = 0; i < 4; ++i) { Y0 = mfma(diag(oa[i], mask), Wo[i], Y0); Y1 = mfma(diag(ob[i], mask), Wo[4 + i], Y1); }
      const f32x4 Y = Y0 + Y1;
      st_gran2(rw, dst, tag, g < 2 ? Y.x : Y.z, g < 2 ? Y.y : Y.w);
    } else if (wave == 5) {
      const int po = WS_PG + (rb * 256 + kvh * 16 + (lane >> 5)) * PG_ROW + (32 * s8 + (lane & 31)) * 8;
      v2u x[8];
      lds_wait<0>(cnt + C_PUB, (unsigned)rb + 1u);
      pause<kHop1>();
      sweep<8, 2 * PG_ROW, 1>(rw, po, tag, x);
      const float o = x32_sum(sum8(x));
      if (lane < 32) ((unsigned*)(lds + L_OB))[(lane & 3) * 8 + (lane >> 2)] = dup_bf16(o);
    } else if (wave == 6 || pidx < 16) {
      const int col = (wave == 6 ? 0 : 512) + pidx * 4 + (lane & 3);
      GAS unsigned short* op = (GAS unsigned short*)ptr64(kb[4], kb[5]) + rb * DM + col;
      asm volatile("" : "+v"(op));
      v2u x[8];
#pragma nounroll
      for (unsigned spins = 0; spins < LSPIN_CAP; ++spins) {
        if (__builtin_amdgcn_readfirstlane(__hip_atomic_load((unsigned*)(lds + L_OB), RLX_WG)) != EMPTY_B) break;
        __builtin_amdgcn_s_sleep(1);
      }
      pause<kHop2>();
      gate(t32 + (unsigned)(4 * kWoStep16 + kHop2Gate16));
      sweep<8, 16 * YG_ROW, 1>(rw, WS_YG + (rb * 128 + (lane >> 2)) * YG_ROW + col * 8, tag, x);
      float y = sum8(x);
      y += dpp<ROR4>(y); y += dpp<ROR8>(y);
      y = xg_sum(y);
      if (lane < 4) *op = f2bf(y);
      if (rb == 0 && wave == 6 && pidx == 0 && lane == 0) __hip_atomic_store((GAS unsigned*)(ws + WS_EPOCH), tag, RLX_AGENT);
    }
  }
}

extern "C" void kernel_launch(void* const* d_in, const int* in_sizes, int n_in, void* d_out, int out_size, void* d_ws, size_t ws_size, hipStream_t stream) {
  Args a;
  a.x[0] = d_in[0]; a.k[0] = d_in[1]; a.v[0] = d_in[2]; a.wq[0] = d_in[3]; a.wo[0] = d_in[4];
  a.x[1] = d_in[5]; a.k[1] = d_in[6]; a.v[1] = d_in[7]; a.wq[1] = d_in[8]; a.wo[1] = d_in[9];
  a.out = d_out; a.ws = d_ws;
  gqa2_fused<<<NWG, NTHR, 0, stream>>>(a);
}
```

```cpp
#include <hip/hip_runtime.h>

typedef unsigned v2u __attribute__((ext_vector_type(2)));
typedef unsigned v4u __attribute__((ext_vector_type(4)));
typedef unsigned v8u __attribute__((ext_vector_type(8)));
typedef unsigned v16u __attribute__((ext_vector_type(16)));
typedef float f32x2 __attribute__((ext_vector_type(2)));
typedef float f32x4 __attribute__((ext_vector_type(4)));
typedef __bf16 bf16x2 __attribute__((ext_vector_type(2)));
typedef __bf16 bf16x8 __attribute__((ext_vector_type(8)));
#define GAS __attribute__((address_space(1)))
#define LAS __attribute__((address_space(3)))
#define RLX_AGENT __ATOMIC_RELAXED, __HIP_MEMORY_SCOPE_AGENT
#define RLX_WG __ATOMIC_RELAXED, __HIP_MEMORY_SCOPE_WORKGROUP
#define SB() __builtin_amdgcn_sched_barrier(0)

constexpr int DM = 576, NQ = 4096;
constexpr int NWG = 256, NTHR = 768;
constexpr float C2 = 0.088388347648318440f * 1.4426950408889634f;

constexpr int kSlot16 = 1760;
constexpr int kOff16 = 1300;
constexpr int kPrio16 = 400;
constexpr int kWoStep16 = 600;
constexpr int kHop2Gate16 = 2000;
constexpr int kQA = 280, kQB = 600, kML = 350, kHop1 = 40, kHop2 = 70;
constexpr int kCodeBytes = 20480;
constexpr int PRE = 8;
constexpr int DEP = 3;
constexpr int LIM = 2;

constexpr int AUX_NT = 2, AUX_SC1 = 16, RSRC3 = 0x00020000, OOB = 0x7fff0000;
constexpr int WS_EPOCH = 0;
constexpr int WS_AG = 1024;
constexpr int WS_QG = 4096;
constexpr int WS_MG = WS_QG + 32 * 4096;
constexpr int WS_PG = WS_MG + 64 * 1024, PG_ROW = 256 * 8;
constexpr int WS_YG = WS_PG + 512 * PG_ROW, YG_ROW = DM * 8;
constexpr int WS_END = WS_YG + 256 * YG_ROW;
constexpr unsigned TAG_STEP = 0x9E3779B9u;
constexpr unsigned SPIN_CAP = 1u << 17, LSPIN_CAP = 1u << 22;
constexpr int L_O = 0;
constexpr int L_ML = L_O + 4096;
constexpr int L_QP = L_ML + 64;
constexpr int L_Q = L_QP + 512;
constexpr int L_OB = L_Q + 256;
constexpr int L_CNT = L_OB + 32, L_END = L_CNT + 16;
enum { C_QP, C_QA, C_ML  , C_PUB = 4, C_TAG, C_ANCHOR, C_QB, C_ADJ };
constexpr unsigned EMPTY_O = 0xffffffffu, EMPTY_B = 0x0000ffffu;

struct Args {
  const void* x[2]; const void* k[2]; const void* v[2]; const void* wq[2]; const void* wo[2];
  void* out; void* ws;
};

__device__ __forceinline__ f32x4 mfma(v4u a, v4u b, f32x4 c) {
  return __builtin_amdgcn_mfma_f32_16x16x32_bf16(__builtin_bit_cast(bf16x8, a), __builtin_bit_cast(bf16x8, b), c, 0, 0, 0);
}
__device__ __forceinline__ unsigned dup_bf16(float f) {
  f32x2 v = {f, f};
  return __builtin_bit_cast(unsigned, __builtin_convertvector(v, bf16x2));
}
__device__ __forceinline__ unsigned short f2bf(float f) { return __builtin_bit_cast(unsigned short, (__bf16)f); }
__device__ __forceinline__ float u2f(unsigned u) { return __uint_as_float(u); }
__device__ __forceinline__ v4u diag(unsigned pp, v4u mask) { v4u r = {pp & mask.x, pp & mask.y, pp & mask.z, pp & mask.w}; return r; }
template <int CTRL> __device__ __forceinline__ int dppi(int x) { return __builtin_amdgcn_mov_dpp(x, CTRL, 0xf, 0xf, true); }
template <int CTRL> __device__ __forceinline__ float dpp(float x) { return __builtin_bit_cast(float, dppi<CTRL>(__builtin_bit_cast(int, x))); }
constexpr int XOR1 = 0xB1, XOR2 = 0x4E, HMIR = 0x141, MIR = 0x140, ROR8 = 0x128, ROR4 = 0x124, BCAST = 0x150;
__device__ __forceinline__ float fmax_(float a, float b) { return __builtin_fmaxf(a, b); }
__device__ __forceinline__ float xg_max(float x) {
  auto s = __builtin_amdgcn_permlane16_swap(__float_as_uint(x), __float_as_uint(x), false, false);
  x = fmax_(u2f(s[0]), u2f(s[1]));
  auto t = __builtin_amdgcn_permlane32_swap(__float_as_uint(x), __float_as_uint(x), false, false);
  return fmax_(u2f(t[0]), u2f(t[1]));
}
__device__ __forceinline__ float x32_sum(float x) {
  auto t = __builtin_amdgcn_permlane32_swap(__float_as_uint(x), __float_as_uint(x), false, false);
  return u2f(t[0]) + u2f(t[1]);
}
__device__ __forceinline__ float xg_sum(float x) {
  auto s = __builtin_amdgcn_permlane16_swap(__float_as_uint(x), __float_as_uint(x), false, false);
  return x32_sum(u2f(s[0]) + u2f(s[1]));
}
__device__ __forceinline__ float sum8(const v2u (&x)[8]) {
  return ((u2f(x[0].x) + u2f(x[1].x)) + (u2f(x[2].x) + u2f(x[3].x))) + ((u2f(x[4].x) + u2f(x[5].x)) + (u2f(x[6].x) + u2f(x[7].x)));
}

__device__ __forceinline__ void st_gran2(__amdgpu_buffer_rsrc_t rs, int off, unsigned tag, float a, float b) {
  __builtin_amdgcn_raw_buffer_store_b128(v4u{__float_as_uint(a), tag, __float_as_uint(b), tag}, rs, off, 0, AUX_SC1);
}
__device__ __forceinline__ v2u ldg(__amdgpu_buffer_rsrc_t rs, int off) { return __builtin_amdgcn_raw_buffer_load_b64(rs, off, 0, AUX_SC1); }
template <int N, int STEP, int SLEEP> __device__ __forceinline__ void sweep(__amdgpu_buffer_rsrc_t rs, int off, unsigned tag, v2u (&x)[N]) {
#pragma unroll
  for (int k = 0; k < N; ++k) x[k] = ldg(rs, off + k * STEP);
  SB();
  bool ok = true;
#pragma unroll
  for (int k = 0; k < N; ++k) ok &= x[k].y == tag;
  if (__builtin_expect(__all(ok), 1)) return;
#pragma nounroll
  for (unsigned spins = 0; spins < SPIN_CAP; ++spins) {
    __builtin_amdgcn_s_sleep(SLEEP);
    v2u y[N];
#pragma unroll
    for (int k = 0; k < N; ++k) y[k] = ldg(rs, x[k].y == tag ? OOB : off + k * STEP);
    SB();
    ok = true;
#pragma unroll
    for (int k = 0; k < N; ++k) { x[k] = x[k].y == tag ? x[k] : y[k]; ok &= x[k].y == tag; }
    if (__all(ok)) break;
  }
}

__device__ __forceinline__ unsigned char* ptr64(unsigned lo, unsigned hi) { return (unsigned char*)(((unsigned long long)hi << 32) | lo); }
__device__ __forceinline__ v4u mkrs(const void* p, unsigned bytes) {
  const unsigned long long u = (unsigned long long)p;
  v4u r = {(unsigned)u, (unsigned)(u >> 32) & 0xffffu, bytes, (unsigned)RSRC3};
  return r;
}
__device__ __forceinline__ void tile_load(v4u (&d)[4], int voff, v4u rs, int soff) {
  asm volatile("buffer_load_dwordx4 %0, %4, %5, %6 offen nt\n\t"
               "buffer_load_dwordx4 %1, %4, %5, %7 offen nt\n\t"
               "buffer_load_dwordx4 %2, %4, %5, %8 offen nt\n\t"
               "buffer_load_dwordx4 %3, %4, %5, %9 offen nt"
               : "=&v"(d[0]), "=&v"(d[1]), "=&v"(d[2]), "=&v"(d[3])
               : "v"(voff), "s"(rs), "s"(soff), "s"(soff + 8192), "s"(soff + 16384), "s"(soff + 24576));
}
template <int N> __device__ __forceinline__ void tile_wait(v4u (&d)[4]) {
  asm volatile("s_waitcnt vmcnt(%4)" : "+v"(d[0]), "+v"(d[1]), "+v"(d[2]), "+v"(d[3]) : "i"(N));
}
template <int N> __device__ __forceinline__ void piece_wait(v4u& d) { asm volatile("s_waitcnt vmcnt(%1)" : "+v"(d) : "i"(N)); }

template <int TICKS> __device__ __forceinline__ void pause() {
  unsigned due;
  asm volatile("s_memrealtime vcc\n\t"
               "s_waitcnt lgkmcnt(0)\n\t"
               "s_add_i32 %0, vcc_lo, %1\n"
               ".Lg0_%=:\n\t"
               "s_sleep 1\n\t"
               "s_memrealtime vcc\n\t"
               "s_waitcnt lgkmcnt(0)\n\t"
               "s_sub_i32 vcc_lo, vcc_lo, %0\n\t"
               "s_cmp_lt_i32 vcc_lo, 0\n\t"
               "s_cbranch_scc1 .Lg0_%="
               : "=&s"(due) : "i"(TICKS) : "scc", "vcc", "memory");
}
__device__ __forceinline__ void gate(unsigned due16) {
  asm volatile("s_branch .Lg1_%=\n"
               ".Lg0_%=:\n\t"
               "s_sleep 1\n"
               ".Lg1_%=:\n\t"
               "s_memrealtime vcc\n\t"
               "s_waitcnt lgkmcnt(0)\n\t"
               "s_lshl_b32 vcc_lo, vcc_lo, 4\n\t"
               "s_sub_i32 vcc_lo, vcc_lo, %0\n\t"
               "s_cmp_lt_i32 vcc_lo, 0\n\t"
               "s_cbranch_scc1 .Lg0_%=\n\t"
               "s_setprio 0\n\t"
               "s_cmp_lt_i32 vcc_lo, %1\n\t"
               "s_cbranch_scc1 .Lg2_%=\n\t"
               "s_setprio 1\n"
               ".Lg2_%=:"
               :: "s"(due16), "i"(kPrio16) : "scc", "vcc", "memory");
}

__device__ __forceinline__ void lds_arrive(unsigned* c, int lane) {
  asm volatile("s_waitcnt lgkmcnt(0)" ::: "memory");
  if (lane == 0) __hip_atomic_fetch_add(c, 1u, RLX_WG);
}
template <int SLEEP> __device__ __forceinline__ unsigned lds_wait(unsigned* c, unsigned want) {
  unsigned v = 0;
#pragma nounroll
  for (unsigned spins = 0; spins < LSPIN_CAP; ++spins) {
    v = __builtin_amdgcn_readfirstlane(__hip_atomic_load(c, RLX_WG));
    if (v >= want) break;
    __builtin_amdgcn_s_sleep(SLEEP);
  }
  asm volatile("" ::: "memory");
  return v;
}
__device__ __forceinline__ unsigned lds_addr(const void* p) { return (unsigned)(size_t)(const LAS void*)p; }
template <int SLEEP> __device__ __forceinline__ v2u lds_take2(unsigned addr) {
  v2u v;
#pragma nounroll
  for (unsigned spins = 0; spins < LSPIN_CAP; ++spins) {
    asm volatile("ds_read_b64 %0, %1\n\ts_waitcnt lgkmcnt(0)" : "=&v"(v) : "v"(addr) : "memory");
    if (__all(v.x != EMPTY_O && v.y != EMPTY_O)) break;
    if (SLEEP) __builtin_amdgcn_s_sleep(SLEEP);
  }
  return v;
}

struct St {
  v4u T[32][4];
  float Sc[8][4];
  unsigned P[8][4];
  v4u Q, mask;
  f32x4 O;
  v4u rs[4];
  int vk, vv, so;
  unsigned due, adj;
  float* lds;
  int lane, wave;
};
template <int I> __device__ __forceinline__ void request(St& z) {
  if constexpr (I > 0) asm volatile("s_waitcnt vmcnt(%0)" :: "i"(LIM) : "memory");
  gate(z.due);
  z.due += kSlot16;
  if constexpr (I >= PRE && I < 31) z.due += z.adj;
  tile_load(z.T[I], (I & 8) ? z.vv : z.vk, z.rs[I >> 3], z.so + (I & 7) * 32768);
  SB();
}
template <int I, int W> __device__ __forceinline__ void consume(St& z) {
  const int g = z.lane >> 4, n = z.lane & 15;
  constexpr int br = I >> 4;
  unsigned* const cnt = (unsigned*)(z.lds + L_CNT);
  if constexpr ((I & 15) == 0) {
    if constexpr (br) lds_wait<1>(cnt + C_QB, 1u);
    z.Q = *(const v4u*)((const unsigned short*)(z.lds + L_Q) + br * 256 + (n >> 3) * 128 + 32 * g + 8 * (n & 3));
  }
  if constexpr ((I & 8) == 0) {
    tile_wait<W>(z.T[I]);
#pragma unroll
    for (int j = 0; j < 4; ++j) {
      const f32x4 d = mfma(z.T[I][j], z.Q, f32x4{0.f, 0.f, 0.f, 0.f});
      z.Sc[I & 7][j] = (dpp<0x00>(d.x) + dpp<0x55>(d.y)) + (dpp<0xAA>(d.z) + dpp<0xFF>(d.w));
    }
    if constexpr ((I & 7) == 7) {
      float m = z.Sc[0][0];
#pragma unroll
      for (int t = 0; t < 8; ++t)
#pragma unroll
        for (int j = 0; j < 4; ++j) m = fmax_(m, z.Sc[t][j]);
      const float m2 = xg_max(m) * C2;
      float l = 0.f;
#pragma unroll
      for (int t = 0; t < 8; ++t) {
        float p[4];
#pragma unroll
        for (int j = 0; j < 4; ++j) { p[j] = __builtin_amdgcn_exp2f(fmaf(z.Sc[t][j], C2, -m2)); z.P[t][j] = dup_bf16(p[j]); }
        l += (p[0] + p[1]) + (p[2] + p[3]);
        asm volatile("" : "+v"(z.P[t][0]), "+v"(z.P[t][1]), "+v"(z.P[t][2]), "+v"(z.P[t][3]));
      }
      l = xg_sum(l);
      if (g == 0 && (n & 7) == 0) { z.lds[L_ML + br * 32 + (n >> 3) * 8 + z.wave] = m2; z.lds[L_ML + br * 32 + 16 + (n >> 3) * 8 + z.wave] = l; }
      lds_arrive(cnt + C_ML + br, z.lane);
      z.O = f32x4{0.f, 0.f, 0.f, 0.f};
    } else {
      asm volatile("" : "+v"(z.Sc[I & 7][0]), "+v"(z.Sc[I & 7][1]), "+v"(z.Sc[I & 7][2]), "+v"(z.Sc[I & 7][3]));
    }
  } else if constexpr (W == 0) {
    v4u A[4];
#pragma unroll
    for (int j = 0; j < 4; ++j) A[j] = diag(z.P[I & 7][j], z.mask);
    unsigned da = lds_addr(z.lds + L_O + br * 2048 + z.wave * 256 + (g >> 1) * 128 + 8 * n + 4 * (g & 1));
    asm volatile("" : "+v"(A[0]), "+v"(A[1]), "+v"(A[2]), "+v"(A[3]), "+v"(da));
    piece_wait<3>(z.T[I][0]); z.O = mfma(A[0], z.T[I][0], z.O); SB();
    piece_wait<2>(z.T[I][1]); z.O = mfma(A[1], z.T[I][1], z.O); SB();
    piece_wait<1>(z.T[I][2]); z.O = mfma(A[2], z.T[I][2], z.O); SB();
    piece_wait<0>(z.T[I][3]); z.O = mfma(A[3], z.T[I][3], z.O);
    *(LAS f32x4*)(size_t)da = z.O;
  } else {
    tile_wait<W>(z.T[I]);
#pragma unroll
    for (int j = 0; j < 4; ++j) z.O = mfma(diag(z.P[I & 7][j], z.mask), z.T[I][j], z.O);
    asm volatile("" : "+v"(z.O));
    if constexpr ((I & 7) == 7) *(f32x4*)(z.lds + L_O + br * 2048 + z.wave * 256 + (g >> 1) * 128 + 8 * n + 4 * (g & 1)) = z.O;
  }
  SB();
}
template <int I, int N> struct Req { static __device__ __forceinline__ void go(St& z) { request<I>(z); Req<I + 1, N>::go(z); } };
template <int N> struct Req<N, N> { static __device__ __forceinline__ void go(St&) {} };
template <int I, int N, int LAST> struct Con {
  static __device__ __forceinline__ void go(St& z) { consume<I, 4 * (LAST - I)>(z); Con<I + 1, N, LAST>::go(z); }
};
template <int N, int LAST> struct Con<N, N, LAST> { static __device__ __forceinline__ void go(St&) {} };
template <int I, int N> struct Run {
  static __device__ __forceinline__ void go(St& z) { request<I>(z); consume<I - DEP + 1, 4 * (DEP - 1)>(z); Run<I + 1, N>::go(z); }
};
template <int N> struct Run<N, N> { static __device__ __forceinline__ void go(St&) {} };

__global__ void __launch_bounds__(NTHR, 3) gqa2_fused(Args) {
  __shared__ __attribute__((aligned(16))) float lds[L_END];
  unsigned* const cnt = (unsigned*)(lds + L_CNT);
  v16u ka; v8u kb;
  asm volatile("s_load_dwordx16 %0, %2, 0x0\n\ts_load_dwordx8 %1, %2, 0x40" : "=&s"(ka), "=&s"(kb) : "s"(__builtin_amdgcn_kernarg_segment_ptr()));
  const int tid = threadIdx.x, lane = tid & 63, wave = __builtin_amdgcn_readfirstlane(tid >> 6);
  const int g = lane >> 4, n = lane & 15;
  const int wg = blockIdx.x;
  const int ch = (wg & 7) << 5 | wg >> 3;
  unsigned cw;
  asm volatile("s_cmp_lg_u32 %2, 0\n\ts_cbranch_scc1 .Lc%=\n\ts_cmp_gt_u32 %3, 2\n\ts_cbranch_scc1 .Lc%=\n\t"
               "s_getpc_b64 vcc\n\tglobal_load_dword %0, %1, vcc\n.Lc%=:"
               : "=v"(cw) : "v"(min((tid - 64) * 128, kCodeBytes)), "s"((((wg >> 3) ^ wg) & 7) | (wg >> 7)), "s"((unsigned)(wave - 1)) : "scc", "vcc");
  const int kvh = ch >> 4, rb = (ch >> 3) & 1, s8 = ch & 7;
  {
    const v4u e = {EMPTY_O, EMPTY_O, EMPTY_O, EMPTY_O};
    const int i = tid < 512 ? tid : 0, j = tid < 48 ? tid : 0;
    *(v4u*)(lds + L_O + i * 4) = e; *(v4u*)(lds + L_O + 2048 + i * 4) = e;
    ((unsigned*)(lds + L_OB))[j] = j < 32 ? EMPTY_B : 0u;
  }
  asm volatile("s_waitcnt lgkmcnt(0)" : "+s"(ka), "+s"(kb) :: "memory");
  const v4u rk0 = mkrs(ptr64(ka[4], ka[5]) + (size_t)ch * 262144, 262144);
  unsigned probe;
  asm volatile("s_cmp_lg_u32 %2, 0\n\ts_cbranch_scc1 .Lp%=\n\ts_mov_b64 exec, 1\n\tbuffer_load_dword %0, off, %1, 0 nt\n\ts_mov_b64 exec, -1\n.Lp%=:"
               : "=v"(probe) : "s"(rk0), "s"(wave) : "scc");
  __builtin_amdgcn_s_barrier();
  asm volatile("" ::: "memory");
  {
    unsigned av;
    asm volatile("s_cmp_lg_u32 %3, 0\n\ts_cbranch_scc1 .La%=\n\t"
                 "s_waitcnt vmcnt(0)\n\ts_memrealtime vcc\n\ts_waitcnt lgkmcnt(0)\n\t"
                 "s_lshl_b32 vcc_lo, vcc_lo, 4\n\ts_or_b32 vcc_lo, vcc_lo, 1\n\tv_mov_b32 %0, vcc_lo\n\tds_write_b32 %1, %0\n.La%=:"
                 : "=&v"(av) : "v"(lds_addr(cnt + C_ANCHOR)), "v"(probe), "s"(wave) : "scc", "vcc", "memory");
  }
  unsigned char* const ws = ptr64(kb[6], kb[7]);
  const __amdgpu_buffer_rsrc_t rw = __builtin_amdgcn_make_buffer_rsrc(ws, 0, WS_END, RSRC3);
  v4u mask;
  {
    const unsigned half = (n & 1) ? 0xffff0000u : 0x0000ffffu; const int r = (n & 7) >> 1;
    mask.x = r == 0 ? half : 0u; mask.y = r == 1 ? half : 0u; mask.z = r == 2 ? half : 0u; mask.w = r == 3 ? half : 0u;
  }
  const int pidx = kvh * 8 + s8;

  if (__builtin_expect(wave >= 8, 1)) {
    __builtin_amdgcn_s_setprio(3);
    const int hw = wave - 8, cg = s8 & 3, kh = s8 >> 2, sub = 2 * g + (n >> 3);
    const __amdgpu_buffer_rsrc_t rq = __builtin_amdgcn_make_buffer_rsrc(
        (rb ? ptr64(ka[14], ka[15]) : ptr64(ka[12], ka[13])) + ((size_t)(kh * 288 + hw * 72) * NQ + kvh * 256 + cg * 64) * 2, 0, 71 * NQ * 2 + 128, RSRC3);
    const __amdgpu_buffer_rsrc_t rx = __builtin_amdgcn_make_buffer_rsrc((rb ? ptr64(ka[2], ka[3]) : ptr64(ka[0], ka[1])) + (kh * 288 + hw * 72) * 2, 0, 72 * 2, RSRC3);
    v4u wqv[9];
#pragma unroll
    for (int i = 0; i < 9; ++i) wqv[i] = __builtin_amdgcn_raw_buffer_load_b128(rq, sub * (NQ * 2) + (n & 7) * 16, i * 8 * NQ * 2, AUX_NT);
    const unsigned xa = __builtin_amdgcn_raw_buffer_load_b32(rx, n * 16 + g * 4, 0, 0);
    const unsigned ep = __builtin_amdgcn_raw_buffer_load_b32(rw, (lane == 0 && hw < 2) ? WS_EPOCH : OOB, 0, AUX_SC1);
    SB();
    f32x4 a0 = {0.f, 0.f, 0.f, 0.f}, a1 = {0.f, 0.f, 0.f, 0.f};
    const unsigned sel = n < 8 ? 0x01000100u : 0x03020302u;
#define QSTEP(acc, i, src, k) acc = mfma(diag(__builtin_amdgcn_perm(0u, (unsigned)dppi<BCAST + (k)>((int)(src)), sel), mask), wqv[i], acc)
    QSTEP(a0, 0, xa, 0); QSTEP(a1, 1, xa, 1); QSTEP(a0, 2, xa, 2); QSTEP(a1, 3, xa, 3); QSTEP(a0, 4, xa, 4); QSTEP(a1, 5, xa, 5);
    QSTEP(a0, 6, xa, 6); QSTEP(a1, 7, xa, 7); QSTEP(a0, 8, xa, 8);
#undef QSTEP
    if ((g >> 1) == (n >> 3)) *(f32x4*)(lds + L_QP + (hw * 2 + (n >> 3)) * 64 + 8 * (n & 7) + 4 * (g & 1)) = a0 + a1;
    const unsigned tag = __builtin_amdgcn_readfirstlane(ep) + TAG_STEP;
    lds_arrive(cnt + C_QP, lane);
    if (hw >= 2) return;
    unsigned anc = 0;
    if (hw == 0) {
      lds_wait<1>(cnt + C_QP, 4u);
      const f32x2* qp = (const f32x2*)(lds + L_QP) + (lane & 31);
      const f32x2 q = ((qp[0] + qp[32]) + (qp[64] + qp[96])) + ((qp[128] + qp[160]) + (qp[192] + qp[224]));
      st_gran2(rw, lane < 32 ? WS_QG + ((rb * 16 + kvh) * 512 + kh * 256 + cg * 64 + 2 * lane) * 8 : OOB, tag, q.x, q.y);
      if (lane == 0) cnt[C_TAG] = tag;
      anc = lds_wait<1>(cnt + C_ANCHOR, 1u);
      if (lane == 0) __hip_atomic_store((GAS unsigned long long*)(ws + WS_AG + wg * 8), ((unsigned long long)tag << 32) | anc, RLX_AGENT);
    }
    pause<kQA>();
    if (hw) pause<kQB - kQA>();
    {
      v2u x[8];
      const v2u ag = ldg(rw, (hw == 0 && lane < 8) ? WS_AG + (wg & ~7) * 8 + lane * 8 : OOB);
      sweep<8, 512, 8>(rw, WS_QG + ((hw * 16 + kvh) * 512 + lane) * 8, tag, x);
#pragma unroll
      for (int k = 0; k < 4; ++k) ((unsigned short*)(lds + L_Q))[hw * 256 + k * 64 + lane] = f2bf(u2f(x[k].x) + u2f(x[k + 4].x));
      if (hw == 0) {
        int d = ag.y == tag ? (int)(ag.x - anc) : 0;
        d = d > 1600 ? 1600 : d < -1600 ? -1600 : d;
        d += dppi<XOR1>(d); d += dppi<XOR2>(d); d += dppi<HMIR>(d);
        if (lane == 0) cnt[C_ADJ] = (unsigned)(d / (8 * (31 - PRE)));
      }
      lds_arrive(cnt + (hw ? C_QB : C_QA), lane);
    }
    float oa0 = 0.f, oa1 = 0.f;
#pragma nounroll
    for (int it = 0; it < 2; ++it) {
      int br = it;
      asm volatile("" : "+s"(br));
      lds_wait<8>(cnt + C_ML + br, 8u);
      const float* ml = lds + L_ML + br * 32 + hw * 8;
      const f32x4 m0 = *(const f32x4*)(ml), m1 = *(const f32x4*)(ml + 4), l0 = *(const f32x4*)(ml + 16), l1 = *(const f32x4*)(ml + 20);
      const float M = fmax_(fmax_(fmax_(m0.x, m0.y), fmax_(m0.z, m0.w)), fmax_(fmax_(m1.x, m1.y), fmax_(m1.z, m1.w)));
      float L = 0.f;
#pragma unroll
      for (int w = 0; w < 8; ++w) L = fmaf(__builtin_amdgcn_exp2f((w < 4 ? m0[w & 3] : m1[w & 3]) - M), w < 4 ? l0[w & 3] : l1[w & 3], L);
      const int mg = WS_MG + ((br * 16 + kvh) * 2 + hw) * 1024;
      st_gran2(rw, lane == 0 ? mg + (ch & 15) * 64 : OOB, tag, M, L);
      pause<kML>();
      v2u x[1];
      sweep<1, 0, 8>(rw, mg + n * 64 + (g & 1) * 8, tag, x);
      auto sw = __builtin_amdgcn_permlane16_swap(x[0].x, x[0].x, false, false);
      const float Ms = u2f(sw[0]), Ls = u2f(sw[1]);
      float Mx = fmax_(Ms, dpp<XOR1>(Ms)); Mx = fmax_(Mx, dpp<XOR2>(Mx)); Mx = fmax_(Mx, dpp<HMIR>(Mx)); Mx = fmax_(Mx, dpp<MIR>(Mx));
      float Lt = __builtin_amdgcn_exp2f(Ms - Mx) * Ls; Lt += dpp<XOR1>(Lt); Lt += dpp<XOR2>(Lt); Lt += dpp<HMIR>(Lt); Lt += dpp<MIR>(Lt);
      const float inv = 1.0f / Lt;
      float c[8];
#pragma unroll
      for (int w = 0; w < 8; ++w) c[w] = __builtin_amdgcn_exp2f((w < 4 ? m0[w & 3] : m1[w & 3]) - Mx) * inv;
      const unsigned oa = lds_addr(lds + L_O + br * 2048 + hw * 128 + 2 * lane);
      int dsta = br ? WS_PG + ch * PG_ROW + (hw * 128 + 2 * lane) * 8 : OOB, dstb = br ? dsta + 256 * PG_ROW : OOB;
      asm volatile("" : "+v"(dsta), "+v"(dstb));
      float o0, o1;
      { const v2u v = lds_take2<8>(oa); o0 = c[0] * u2f(v.x); o1 = c[0] * u2f(v.y); }
#pragma unroll
      for (int w = 1; w < 8; ++w) {
        if (w == 5) {
          st_gran2(rw, dsta, tag, oa0, oa1);
          if (hw == 0 && lane == 0) __hip_atomic_store(cnt + C_PUB, (unsigned)br, RLX_WG);
        }
        const v2u v = lds_take2<0>(oa + w * 1024); o0 = fmaf(c[w], u2f(v.x), o0); o1 = fmaf(c[w], u2f(v.y), o1);
      }
      st_gran2(rw, dstb, tag, o0, o1);
      if (hw == 0 && lane == 0) __hip_atomic_store(cnt + C_PUB, 2u * (unsigned)br, RLX_WG);
      oa0 = o0; oa1 = o1;
    }
  } else {
    St z;
    z.lds = lds; z.lane = lane; z.wave = wave; z.mask = mask;
    z.rs[0] = rk0;
    z.rs[1] = mkrs(ptr64(ka[8], ka[9]) + (size_t)ch * 262144, 262144);
    z.rs[2] = mkrs(ptr64(ka[6], ka[7]) + (size_t)ch * 262144, 262144);
    z.rs[3] = mkrs(ptr64(ka[10], ka[11]) + (size_t)ch * 262144, 262144);
    z.vk = ((lane >> 2) & 3) * 256 + g * 64 + (lane & 3) * 16; z.vv = lane * 16; z.so = wave * 1024;
    const int phase16 = (wave * kSlot16) >> 3;
    asm volatile("s_waitcnt vmcnt(0)" :: "v"(cw) : "memory");
    z.due = lds_wait<1>(cnt + C_ANCHOR, 1u) + (unsigned)(kOff16 + phase16);
    Req<0, PRE>::go(z);
    lds_wait<1>(cnt + C_QA, 1u);
    const unsigned tag = __builtin_amdgcn_readfirstlane(cnt[C_TAG]);
    z.adj = __builtin_amdgcn_readfirstlane(cnt[C_ADJ]);
    Con<0, PRE - DEP + 1, PRE - 1>::go(z);
    Run<PRE, 32>::go(z);
    Con<32 - DEP + 1, 32, 31>::go(z);
    const unsigned t32 = z.due - (unsigned)phase16;
    if (wave < 5) {
      const __amdgpu_buffer_rsrc_t ro = __builtin_amdgcn_make_buffer_rsrc(
          (rb ? ptr64(kb[2], kb[3]) : ptr64(kb[0], kb[1])) + (size_t)(kvh * 256 + s8 * 32) * DM * 2, 0, 32 * DM * 2, RSRC3);
      const int wcol = wave * 128 + n * 8;
      const int vwo = wcol < DM ? g * (DM * 2) + wcol * 2 : OOB;
      const unsigned oba = lds_addr(lds + L_OB + g * 8);
      v4u Wo[8];
      gate(t32 + (unsigned)(wave * kWoStep16));
#pragma unroll
      for (int i = 0; i < 8; ++i) Wo[i] = __builtin_amdgcn_raw_buffer_load_b128(ro, vwo, i * 4 * DM * 2, AUX_NT);
      int dst = wcol < DM ? WS_YG + (rb * 128 + pidx) * YG_ROW + (wcol + 4 * (g & 1) + 2 * (g >> 1)) * 8 : OOB;
      asm volatile("" : "+v"(dst), "+v"(Wo[0]), "+v"(Wo[1]), "+v"(Wo[2]), "+v"(Wo[3]), "+v"(Wo[4]), "+v"(Wo[5]), "+v"(Wo[6]), "+v"(Wo[7]));
      v4u oa, ob;
#pragma nounroll
      for (unsigned spins = 0; spins < LSPIN_CAP; ++spins) {
        asm volatile("ds_read_b128 %0, %2\n\tds_read_b128 %1, %2 offset:16\n\ts_waitcnt lgkmcnt(0)" : "=&v"(oa), "=&v"(ob) : "v"(oba) : "memory");
        if (__all(oa.x != EMPTY_B && oa.y != EMPTY_B && oa.z != EMPTY_B && oa.w != EMPTY_B && ob.x != EMPTY_B && ob.y != EMPTY_B && ob.z != EMPTY_B && ob.w != EMPTY_B)) break;
      }
      f32x4 Y0 = {0.f, 0.f, 0.f, 0.f}, Y1 = {0.f, 0.f, 0.f, 0.f};
#pragma unroll
      for (int i = 0; i < 4; ++i) { Y0 = mfma(diag(oa[i], mask), Wo[i], Y0); Y1 = mfma(diag(ob[i], mask), Wo[4 + i], Y1); }
      const f32x4 Y = Y0 + Y1;
      st_gran2(rw, dst, tag, g < 2 ? Y.x : Y.z, g < 2 ? Y.y : Y.w);
    } else if (wave == 5) {
      const int po = WS_PG + (rb * 256 + kvh * 16 + (lane >> 5)) * PG_ROW + (32 * s8 + (lane & 31)) * 8;
      v2u x[8];
      lds_wait<0>(cnt + C_PUB, (unsigned)rb + 1u);
      pause<kHop1>();
      sweep<8, 2 * PG_ROW, 1>(rw, po, tag, x);
      const float o = x32_sum(sum8(x));
      if (lane < 32) ((unsigned*)(lds + L_OB))[(lane & 3) * 8 + (lane >> 2)] = dup_bf16(o);
    } else if (wave == 6 || pidx < 16) {
      const int col = (wave == 6 ? 0 : 512) + pidx * 4 + (lane & 3);
      GAS unsigned short* op = (GAS unsigned short*)ptr64(kb[4], kb[5]) + rb * DM + col;
      asm volatile("" : "+v"(op));
      v2u x[8];
#pragma nounroll
      for (unsigned spins = 0; spins < LSPIN_CAP; ++spins) {
        if (__builtin_amdgcn_readfirstlane(__hip_atomic_load((unsigned*)(lds + L_OB), RLX_WG)) != EMPTY_B) break;
        __builtin_amdgcn_s_sleep(1);
      }
      pause<kHop2>();
      gate(t32 + (unsigned)(4 * kWoStep16 + kHop2Gate16));
      sweep<8, 16 * YG_ROW, 1>(rw, WS_YG + (rb * 128 + (lane >> 2)) * YG_ROW + col * 8, tag, x);
      float y = sum8(x);
      y += dpp<ROR4>(y); y += dpp<ROR8>(y);
      y = xg_sum(y);
      if (lane < 4) *op = f2bf(y);
      if (rb == 0 && wave == 6 && pidx == 0 && lane == 0) __hip_atomic_store((GAS unsigned*)(ws + WS_EPOCH), tag, RLX_AGENT);
    }
  }
}

extern "C" void kernel_launch(void* const* d_in, const int* in_sizes, int n_in, void* d_out, int out_size, void* d_ws, size_t ws_size, hipStream_t stream) {
  Args a;
  a.x[0] = d_in[0]; a.k[0] = d_in[1]; a.v[0] = d_in[2]; a.wq[0] = d_in[3]; a.wo[0] = d_in[4];
  a.x[1] = d_in[5]; a.k[1] = d_in[6]; a.v[1] = d_in[7]; a.wq[1] = d_in[8]; a.wo[1] = d_in[9];
  a.out = d_out; a.ws = d_ws;
  gqa2_fused<<<NWG, NTHR, 0, stream>>>(a);
}
```

```cpp
#include <hip/hip_runtime.h>

typedef unsigned v2u __attribute__((ext_vector_type(2)));
typedef unsigned v4u __attribute__((ext_vector_type(4)));
typedef unsigned v8u __attribute__((ext_vector_type(8)));
typedef unsigned v16u __attribute__((ext_vector_type(16)));
typedef float f32x2 __attribute__((ext_vector_type(2)));
typedef float f32x4 __attribute__((ext_vector_type(4)));
typedef __bf16 bf16x2 __attribute__((ext_vector_type(2)));
typedef __bf16 bf16x8 __attribute__((ext_vector_type(8)));
#define GAS __attribute__((address_space(1)))
#define LAS __attribute__((address_space(3)))
#define RLX_AGENT __ATOMIC_RELAXED, __HIP_MEMORY_SCOPE_AGENT
#define RLX_WG __ATOMIC_RELAXED, __HIP_MEMORY_SCOPE_WORKGROUP
#define SB() __builtin_amdgcn_sched_barrier(0)

constexpr int DM = 576, NQ = 4096;
constexpr int NWG = 256, NTHR = 768;
constexpr float C2 = 0.088388347648318440f * 1.4426950408889634f;

constexpr int kSlot16 = 1764;
constexpr int kOff16 = 1300;
constexpr int kPrio16 = 400;
constexpr int kWoStep16 = 600;
constexpr int kHop2Gate16 = 2000;
constexpr int kQA = 280, kQB = 1100, kML = 350, kHop1 = 40, kHop2 = 70;
constexpr int kCodeBytes = 20480;
constexpr int PRE = 8;
constexpr int DEP = 3;
constexpr int LIM = 2;

constexpr int AUX_NT = 2, AUX_SC1 = 16, RSRC3 = 0x00020000, OOB = 0x7fff0000;
constexpr int WS_EPOCH = 0;
constexpr int WS_AG = 1024;
constexpr int WS_QG = 4096;
constexpr int WS_MG = WS_QG + 32 * 4096;
constexpr int WS_PG = WS_MG + 64 * 1024, PG_ROW = 256 * 8;
constexpr int WS_YG = WS_PG + 512 * PG_ROW, YG_ROW = DM * 8;
constexpr int WS_END = WS_YG + 256 * YG_ROW;
constexpr unsigned TAG_STEP = 0x9E3779B9u;
constexpr unsigned SPIN_CAP = 1u << 17, LSPIN_CAP = 1u << 22;
constexpr int L_O = 0;
constexpr int L_ML = L_O + 4096;
constexpr int L_QP = L_ML + 64;
constexpr int L_Q = L_QP + 512;
constexpr int L_OB = L_Q + 256;
constexpr int L_CNT = L_OB + 32, L_END = L_CNT + 16;
enum { C_QP, C_QA, C_ML  , C_PUB = 4, C_TAG, C_ANCHOR, C_QB, C_ADJ };
constexpr unsigned EMPTY_O = 0xffffffffu, EMPTY_B = 0x0000ffffu;

struct Args {
  const void* x[2]; const void* k[2]; const void* v[2]; const void* wq[2]; const void* wo[2];
  void* out; void* ws;
};

__device__ __forceinline__ f32x4 mfma(v4u a, v4u b, f32x4 c) {
  return __builtin_amdgcn_mfma_f32_16x16x32_bf16(__builtin_bit_cast(bf16x8, a), __builtin_bit_cast(bf16x8, b), c, 0, 0, 0);
}
__device__ __forceinline__ unsigned dup_bf16(float f) {
  f32x2 v = {f, f};
  return __builtin_bit_cast(unsigned, __builtin_convertvector(v, bf16x2));
}
__device__ __forceinline__ unsigned short f2bf(float f) { return __builtin_bit_cast(unsigned short, (__bf16)f); }
__device__ __forceinline__ float u2f(unsigned u) { return __uint_as_float(u); }
__device__ __forceinline__ v4u diag(unsigned pp, v4u mask) { v4u r = {pp & mask.x, pp & mask.y, pp & mask.z, pp & mask.w}; return r; }
template <int CTRL> __device__ __forceinline__ int dppi(int x) { return __builtin_amdgcn_mov_dpp(x, CTRL, 0xf, 0xf, true); }
template <int CTRL> __device__ __forceinline__ float dpp(float x) { return __builtin_bit_cast(float, dppi<CTRL>(__builtin_bit_cast(int, x))); }
constexpr int XOR1 = 0xB1, XOR2 = 0x4E, HMIR = 0x141, MIR = 0x140, ROR8 = 0x128, ROR4 = 0x124, BCAST = 0x150;
__device__ __forceinline__ float fmax_(float a, float b) { return __builtin_fmaxf(a, b); }
__device__ __forceinline__ float xg_max(float x) {
  auto s = __builtin_amdgcn_permlane16_swap(__float_as_uint(x), __float_as_uint(x), false, false);
  x = fmax_(u2f(s[0]), u2f(s[1]));
  auto t = __builtin_amdgcn_permlane32_swap(__float_as_uint(x), __float_as_uint(x), false, false);
  return fmax_(u2f(t[0]), u2f(t[1]));
}
__device__ __forceinline__ float x32_sum(float x) {
  auto t = __builtin_amdgcn_permlane32_swap(__float_as_uint(x), __float_as_uint(x), false, false);
  return u2f(t[0]) + u2f(t[1]);
}
__device__ __forceinline__ float xg_sum(float x) {
  auto s = __builtin_amdgcn_permlane16_swap(__float_as_uint(x), __float_as_uint(x), false, false);
  return x32_sum(u2f(s[0]) + u2f(s[1]));
}
__device__ __forceinline__ float sum8(const v2u (&x)[8]) {
  return ((u2f(x[0].x) + u2f(x[1].x)) + (u2f(x[2].x) + u2f(x[3].x))) + ((u2f(x[4].x) + u2f(x[5].x)) + (u2f(x[6].x) + u2f(x[7].x)));
}

__device__ __forceinline__ void st_gran2(__amdgpu_buffer_rsrc_t rs, int off, unsigned tag, float a, float b) {
  __builtin_amdgcn_raw_buffer_store_b128(v4u{__float_as_uint(a), tag, __float_as_uint(b), tag}, rs, off, 0, AUX_SC1);
}
__device__ __forceinline__ v2u ldg(__amdgpu_buffer_rsrc_t rs, int off) { return __builtin_amdgcn_raw_buffer_load_b64(rs, off, 0, AUX_SC1); }
template <int N, int STEP, int SLEEP> __device__ __forceinline__ void sweep(__amdgpu_buffer_rsrc_t rs, int off, unsigned tag, v2u (&x)[N]) {
#pragma unroll
  for (int k = 0; k < N; ++k) x[k] = ldg(rs, off + k * STEP);
  SB();
  bool ok = true;
#pragma unroll
  for (int k = 0; k < N; ++k) ok &= x[k].y == tag;
  if (__builtin_expect(__all(ok), 1)) return;
#pragma nounroll
  for (unsigned spins = 0; spins < SPIN_CAP; ++spins) {
    __builtin_amdgcn_s_sleep(SLEEP);
    v2u y[N];
#pragma unroll
    for (int k = 0; k < N; ++k) y[k] = ldg(rs, x[k].y == tag ? OOB : off + k * STEP);
    SB();
    ok = true;
#pragma unroll
    for (int k = 0; k < N; ++k) { x[k] = x[k].y == tag ? x[k] : y[k]; ok &= x[k].y == tag; }
    if (__all(ok)) break;
  }
}

__device__ __forceinline__ unsigned char* ptr64(unsigned lo, unsigned hi) { return (unsigned char*)(((unsigned long long)hi << 32) | lo); }
__device__ __forceinline__ v4u mkrs(const void* p, unsigned bytes) {
  const unsigned long long u = (unsigned long long)p;
  v4u r = {(unsigned)u, (unsigned)(u >> 32) & 0xffffu, bytes, (unsigned)RSRC3};
  return r;
}
__device__ __forceinline__ void tile_load(v4u (&d)[4], int voff, v4u rs, int soff) {
  asm volatile("buffer_load_dwordx4 %0, %4, %5, %6 offen nt\n\t"
               "buffer_load_dwordx4 %1, %4, %5, %7 offen nt\n\t"
               "buffer_load_dwordx4 %2, %4, %5, %8 offen nt\n\t"
               "buffer_load_dwordx4 %3, %4, %5, %9 offen nt"
               : "=&v"(d[0]), "=&v"(d[1]), "=&v"(d[2]), "=&v"(d[3])
               : "v"(voff), "s"(rs), "s"(soff), "s"(soff + 8192), "s"(soff + 16384), "s"(soff + 24576));
}
template <int N> __device__ __forceinline__ void tile_wait(v4u (&d)[4]) {
  asm volatile("s_waitcnt vmcnt(%4)" : "+v"(d[0]), "+v"(d[1]), "+v"(d[2]), "+v"(d[3]) : "i"(N));
}
template <int N> __device__ __forceinline__ void piece_wait(v4u& d) { asm volatile("s_waitcnt vmcnt(%1)" : "+v"(d) : "i"(N)); }

template <int TICKS> __device__ __forceinline__ void pause() {
  unsigned due;
  asm volatile("s_memrealtime vcc\n\t"
               "s_waitcnt lgkmcnt(0)\n\t"
               "s_add_i32 %0, vcc_lo, %1\n"
               ".Lg0_%=:\n\t"
               "s_sleep 1\n\t"
               "s_memrealtime vcc\n\t"
               "s_waitcnt lgkmcnt(0)\n\t"
               "s_sub_i32 vcc_lo, vcc_lo, %0\n\t"
               "s_cmp_lt_i32 vcc_lo, 0\n\t"
               "s_cbranch_scc1 .Lg0_%="
               : "=&s"(due) : "i"(TICKS) : "scc", "vcc", "memory");
}
__device__ __forceinline__ void gate(unsigned due16) {
  asm volatile("s_branch .Lg1_%=\n"
               ".Lg0_%=:\n\t"
               "s_sleep 1\n"
               ".Lg1_%=:\n\t"
               "s_memrealtime vcc\n\t"
               "s_waitcnt lgkmcnt(0)\n\t"
               "s_lshl_b32 vcc_lo, vcc_lo, 4\n\t"
               "s_sub_i32 vcc_lo, vcc_lo, %0\n\t"
               "s_cmp_lt_i32 vcc_lo, 0\n\t"
               "s_cbranch_scc1 .Lg0_%=\n\t"
               "s_setprio 0\n\t"
               "s_cmp_lt_i32 vcc_lo, %1\n\t"
               "s_cbranch_scc1 .Lg2_%=\n\t"
               "s_setprio 1\n"
               ".Lg2_%=:"
               :: "s"(due16), "i"(kPrio16) : "scc", "vcc", "memory");
}

__device__ __forceinline__ void lds_arrive(unsigned* c, int lane) {
  asm volatile("s_waitcnt lgkmcnt(0)" ::: "memory");
  if (lane == 0) __hip_atomic_fetch_add(c, 1u, RLX_WG);
}
template <int SLEEP> __device__ __forceinline__ unsigned lds_wait(unsigned* c, unsigned want) {
  unsigned v = 0;
#pragma nounroll
  for (unsigned spins = 0; spins < LSPIN_CAP; ++spins) {
    v = __builtin_amdgcn_readfirstlane(__hip_atomic_load(c, RLX_WG));
    if (v >= want) break;
    __builtin_amdgcn_s_sleep(SLEEP);
  }
  asm volatile("" ::: "memory");
  return v;
}
__device__ __forceinline__ unsigned lds_addr(const void* p) { return (unsigned)(size_t)(const LAS void*)p; }
template <int SLEEP> __device__ __forceinline__ v2u lds_take2(unsigned addr) {
  v2u v;
#pragma nounroll
  for (unsigned spins = 0; spins < LSPIN_CAP; ++spins) {
    asm volatile("ds_read_b64 %0, %1\n\ts_waitcnt lgkmcnt(0)" : "=&v"(v) : "v"(addr) : "memory");
    if (__all(v.x != EMPTY_O && v.y != EMPTY_O)) break;
    if (SLEEP) __builtin_amdgcn_s_sleep(SLEEP);
  }
  return v;
}

struct St {
  v4u T[32][4];
  float Sc[8][4];
  unsigned P[8][4];
  v4u Q, mask;
  f32x4 O;
  v4u rs[4];
  int vk, vv, so;
  unsigned due, adj;
  float* lds;
  int lane, wave;
};
template <int I> __device__ __forceinline__ void request(St& z) {
  if constexpr (I > 0) asm volatile("s_waitcnt vmcnt(%0)" :: "i"(LIM) : "memory");
  gate(z.due);
  z.due += kSlot16;
  if constexpr (I >= PRE && I < 31) z.due += z.adj;
  tile_load(z.T[I], (I & 8) ? z.vv : z.vk, z.rs[I >> 3], z.so + (I & 7) * 32768);
  SB();
}
template <int I, int W> __device__ __forceinline__ void consume(St& z) {
  const int g = z.lane >> 4, n = z.lane & 15;
  constexpr int br = I >> 4;
  unsigned* const cnt = (unsigned*)(z.lds + L_CNT);
  if constexpr ((I & 15) == 0) {
    if constexpr (br) lds_wait<1>(cnt + C_QB, 1u);
    z.Q = *(const v4u*)((const unsigned short*)(z.lds + L_Q) + br * 256 + (n >> 3) * 128 + 32 * g + 8 * (n & 3));
  }
  if constexpr ((I & 8) == 0) {
    tile_wait<W>(z.T[I]);
#pragma unroll
    for (int j = 0; j < 4; ++j) {
      const f32x4 d = mfma(z.T[I][j], z.Q, f32x4{0.f, 0.f, 0.f, 0.f});
      z.Sc[I & 7][j] = (dpp<0x00>(d.x) + dpp<0x55>(d.y)) + (dpp<0xAA>(d.z) + dpp<0xFF>(d.w));
    }
    if constexpr ((I & 7) == 7) {
      float m = z.Sc[0][0];
#pragma unroll
      for (int t = 0; t < 8; ++t)
#pragma unroll
        for (int j = 0; j < 4; ++j) m = fmax_(m, z.Sc[t][j]);
      const float m2 = xg_max(m) * C2;
      float l = 0.f;
#pragma unroll
      for (int t = 0; t < 8; ++t) {
        float p[4];
#pragma unroll
        for (int j = 0; j < 4; ++j) { p[j] = __builtin_amdgcn_exp2f(fmaf(z.Sc[t][j], C2, -m2)); z.P[t][j] = dup_bf16(p[j]); }
        l += (p[0] + p[1]) + (p[2] + p[3]);
        asm volatile("" : "+v"(z.P[t][0]), "+v"(z.P[t][1]), "+v"(z.P[t][2]), "+v"(z.P[t][3]));
      }
      l = xg_sum(l);
      if (g == 0 && (n & 7) == 0) { z.lds[L_ML + br * 32 + (n >> 3) * 8 + z.wave] = m2; z.lds[L_ML + br * 32 + 16 + (n >> 3) * 8 + z.wave] = l; }
      lds_arrive(cnt + C_ML + br, z.lane);
      z.O = f32x4{0.f, 0.f, 0.f, 0.f};
    } else {
      asm volatile("" : "+v"(z.Sc[I & 7][0]), "+v"(z.Sc[I & 7][1]), "+v"(z.Sc[I & 7][2]), "+v"(z.Sc[I & 7][3]));
    }
  } else if constexpr (W == 0) {
    v4u A[4];
#pragma unroll
    for (int j = 0; j < 4; ++j) A[j] = diag(z.P[I & 7][j], z.mask);
    unsigned da = lds_addr(z.lds + L_O + br * 2048 + z.wave * 256 + (g >> 1) * 128 + 8 * n + 4 * (g & 1));
    asm volatile("" : "+v"(A[0]), "+v"(A[1]), "+v"(A[2]), "+v"(A[3]), "+v"(da));
    piece_wait<3>(z.T[I][0]); z.O = mfma(A[0], z.T[I][0], z.O); SB();
    piece_wait<2>(z.T[I][1]); z.O = mfma(A[1], z.T[I][1], z.O); SB();
    piece_wait<1>(z.T[I][2]); z.O = mfma(A[2], z.T[I][2], z.O); SB();
    piece_wait<0>(z.T[I][3]); z.O = mfma(A[3], z.T[I][3], z.O);
    *(LAS f32x4*)(size_t)da = z.O;
  } else {
    tile_wait<W>(z.T[I]);
#pragma unroll
    for (int j = 0; j < 4; ++j) z.O = mfma(diag(z.P[I & 7][j], z.mask), z.T[I][j], z.O);
    asm volatile("" : "+v"(z.O));
    if constexpr ((I & 7) == 7) *(f32x4*)(z.lds + L_O + br * 2048 + z.wave * 256 + (g >> 1) * 128 + 8 * n + 4 * (g & 1)) = z.O;
  }
  SB();
}
template <int I, int N> struct Req { static __device__ __forceinline__ void go(St& z) { request<I>(z); Req<I + 1, N>::go(z); } };
template <int N> struct Req<N, N> { static __device__ __forceinline__ void go(St&) {} };
template <int I, int N, int LAST> struct Con {
  static __device__ __forceinline__ void go(St& z) { consume<I, 4 * (LAST - I)>(z); Con<I + 1, N, LAST>::go(z); }
};
template <int N, int LAST> struct Con<N, N, LAST> { static __device__ __forceinline__ void go(St&) {} };
template <int I, int N> struct Run {
  static __device__ __forceinline__ void go(St& z) { request<I>(z); consume<I - DEP + 1, 4 * (DEP - 1)>(z); Run<I + 1, N>::go(z); }
};
template <int N> struct Run<N, N> { static __device__ __forceinline__ void go(St&) {} };

__global__ void __launch_bounds__(NTHR, 3) gqa2_fused(Args) {
  __shared__ __attribute__((aligned(16))) float lds[L_END];
  unsigned* const cnt = (unsigned*)(lds + L_CNT);
  v16u ka; v8u kb;
  asm volatile("s_load_dwordx16 %0, %2, 0x0\n\ts_load_dwordx8 %1, %2, 0x40" : "=&s"(ka), "=&s"(kb) : "s"(__builtin_amdgcn_kernarg_segment_ptr()));
  const int tid = threadIdx.x, lane = tid & 63, wave = __builtin_amdgcn_readfirstlane(tid >> 6);
  const int g = lane >> 4, n = lane & 15;
  const int wg = blockIdx.x;
  const int ch = (wg & 7) << 5 | wg >> 3;
  unsigned cw;
  asm volatile("s_cmp_lg_u32 %2, 0\n\ts_cbranch_scc1 .Lc%=\n\ts_cmp_gt_u32 %3, 2\n\ts_cbranch_scc1 .Lc%=\n\t"
               "s_getpc_b64 vcc\n\tglobal_load_dword %0, %1, vcc\n.Lc%=:"
               : "=v"(cw) : "v"(min((tid - 64) * 128, kCodeBytes)), "s"((((wg >> 3) ^ wg) & 7) | (wg >> 7)), "s"((unsigned)(wave - 1)) : "scc", "vcc");
  const int kvh = ch >> 4, rb = (ch >> 3) & 1, s8 = ch & 7;
  {
    const v4u e = {EMPTY_O, EMPTY_O, EMPTY_O, EMPTY_O};
    const int i = tid < 512 ? tid : 0, j = tid < 48 ? tid : 0;
    *(v4u*)(lds + L_O + i * 4) = e; *(v4u*)(lds + L_O + 2048 + i * 4) = e;
    ((unsigned*)(lds + L_OB))[j] = j < 32 ? EMPTY_B : 0u;
  }
  asm volatile("s_waitcnt lgkmcnt(0)" : "+s"(ka), "+s"(kb) :: "memory");
  const v4u rk0 = mkrs(ptr64(ka[4], ka[5]) + (size_t)ch * 262144, 262144);
  unsigned probe;
  asm volatile("s_cmp_lg_u32 %2, 0\n\ts_cbranch_scc1 .Lp%=\n\ts_mov_b64 exec, 1\n\tbuffer_load_dword %0, off, %1, 0 nt\n\ts_mov_b64 exec, -1\n.Lp%=:"
               : "=v"(probe) : "s"(rk0), "s"(wave) : "scc");
  __builtin_amdgcn_s_barrier();
  asm volatile("" ::: "memory");
  {
    unsigned av;
    asm volatile("s_cmp_lg_u32 %3, 0\n\ts_cbranch_scc1 .La%=\n\t"
                 "s_waitcnt vmcnt(0)\n\ts_memrealtime vcc\n\ts_waitcnt lgkmcnt(0)\n\t"
                 "s_lshl_b32 vcc_lo, vcc_lo, 4\n\ts_or_b32 vcc_lo, vcc_lo, 1\n\tv_mov_b32 %0, vcc_lo\n\tds_write_b32 %1, %0\n.La%=:"
                 : "=&v"(av) : "v"(lds_addr(cnt + C_ANCHOR)), "v"(probe), "s"(wave) : "scc", "vcc", "memory");
  }
  unsigned char* const ws = ptr64(kb[6], kb[7]);
  const __amdgpu_buffer_rsrc_t rw = __builtin_amdgcn_make_buffer_rsrc(ws, 0, WS_END, RSRC3);
  v4u mask;
  {
    const unsigned half = (n & 1) ? 0xffff0000u : 0x0000ffffu; const int r = (n & 7) >> 1;
    mask.x = r == 0 ? half : 0u; mask.y = r == 1 ? half : 0u; mask.z = r == 2 ? half : 0u; mask.w = r == 3 ? half : 0u;
  }
  const int pidx = kvh * 8 + s8;

  if (__builtin_expect(wave >= 8, 1)) {
    __builtin_amdgcn_s_setprio(3);
    const int hw = wave - 8, cg = s8 & 3, kh = s8 >> 2, sub = 2 * g + (n >> 3);
    const __amdgpu_buffer_rsrc_t rq = __builtin_amdgcn_make_buffer_rsrc(
        (rb ? ptr64(ka[14], ka[15]) : ptr64(ka[12], ka[13])) + ((size_t)(kh * 288 + hw * 72) * NQ + kvh * 256 + cg * 64) * 2, 0, 71 * NQ * 2 + 128, RSRC3);
    const __amdgpu_buffer_rsrc_t rx = __builtin_amdgcn_make_buffer_rsrc((rb ? ptr64(ka[2], ka[3]) : ptr64(ka[0], ka[1])) + (kh * 288 + hw * 72) * 2, 0, 72 * 2, RSRC3);
    v4u wqv[9];
#pragma unroll
    for (int i = 0; i < 9; ++i) wqv[i] = __builtin_amdgcn_raw_buffer_load_b128(rq, sub * (NQ * 2) + (n & 7) * 16, i * 8 * NQ * 2, AUX_NT);
    const unsigned xa = __builtin_amdgcn_raw_buffer_load_b32(rx, n * 16 + g * 4, 0, 0);
    const unsigned ep = __builtin_amdgcn_raw_buffer_load_b32(rw, (lane == 0 && hw < 2) ? WS_EPOCH : OOB, 0, AUX_SC1);
    SB();
    f32x4 a0 = {0.f, 0.f, 0.f, 0.f}, a1 = {0.f, 0.f, 0.f, 0.f};
    const unsigned sel = n < 8 ? 0x01000100u : 0x03020302u;
#define QSTEP(acc, i, src, k) acc = mfma(diag(__builtin_amdgcn_perm(0u, (unsigned)dppi<BCAST + (k)>((int)(src)), sel), mask), wqv[i], acc)
    QSTEP(a0, 0, xa, 0); QSTEP(a1, 1, xa, 1); QSTEP(a0, 2, xa, 2); QSTEP(a1, 3, xa, 3); QSTEP(a0, 4, xa, 4); QSTEP(a1, 5, xa, 5);
    QSTEP(a0, 6, xa, 6); QSTEP(a1, 7, xa, 7); QSTEP(a0, 8, xa, 8);
#undef QSTEP
    if ((g >> 1) == (n >> 3)) *(f32x4*)(lds + L_QP + (hw * 2 + (n >> 3)) * 64 + 8 * (n & 7) + 4 * (g & 1)) = a0 + a1;
    const unsigned tag = __builtin_amdgcn_readfirstlane(ep) + TAG_STEP;
    lds_arrive(cnt + C_QP, lane);
    if (hw >= 2) return;
    unsigned anc = 0;
    if (hw == 0) {
      lds_wait<1>(cnt + C_QP, 4u);
      const f32x2* qp = (const f32x2*)(lds + L_QP) + (lane & 31);
      const f32x2 q = ((qp[0] + qp[32]) + (qp[64] + qp[96])) + ((qp[128] + qp[160]) + (qp[192] + qp[224]));
      st_gran2(rw, lane < 32 ? WS_QG + ((rb * 16 + kvh) * 512 + kh * 256 + cg * 64 + 2 * lane) * 8 : OOB, tag, q.x, q.y);
      if (lane == 0) cnt[C_TAG] = tag;
      anc = lds_wait<1>(cnt + C_ANCHOR, 1u);
      if (lane == 0) __hip_atomic_store((GAS unsigned long long*)(ws + WS_AG + wg * 8), ((unsigned long long)tag << 32) | anc, RLX_AGENT);
    }
    pause<kQA>();
    if (hw) pause<kQB - kQA>();
    {
      v2u x[8];
      const v2u ag = ldg(rw, (hw == 0 && lane < 8) ? WS_AG + ((wg & ~7) + lane) * 8 : OOB);
      sweep<8, 512, 8>(rw, WS_QG + ((hw * 16 + kvh) * 512 + lane) * 8, tag, x);
#pragma unroll
      for (int k = 0; k < 4; ++k) ((unsigned short*)(lds + L_Q))[hw * 256 + k * 64 + lane] = f2bf(u2f(x[k].x) + u2f(x[k + 4].x));
      if (hw == 0) {
        int d = ag.y == tag ? (int)(ag.x - anc) : 0;
        d = d > 1600 ? 1600 : d < -1600 ? -1600 : d;
        d += dppi<XOR1>(d); d += dppi<XOR2>(d); d += dppi<HMIR>(d);
        if (lane == 0) cnt[C_ADJ] = (unsigned)(d / (8 * (31 - PRE)));
      }
      lds_arrive(cnt + (hw ? C_QB : C_QA), lane);
    }
    lds_wait<8>(cnt + C_ML + 1, 8u);
    const int mg = WS_MG + (kvh * 2 + hw) * 1024;
    {
      const float* ml = lds + L_ML + (lane & 1) * 32 + hw * 8;
      const f32x4 m0 = *(const f32x4*)(ml), m1 = *(const f32x4*)(ml + 4), l0 = *(const f32x4*)(ml + 16), l1 = *(const f32x4*)(ml + 20);
      const float M = fmax_(fmax_(fmax_(m0.x, m0.y), fmax_(m0.z, m0.w)), fmax_(fmax_(m1.x, m1.y), fmax_(m1.z, m1.w)));
      float L = 0.f;
#pragma unroll
      for (int w = 0; w < 8; ++w) L = fmaf(__builtin_amdgcn_exp2f((w < 4 ? m0[w & 3] : m1[w & 3]) - M), w < 4 ? l0[w & 3] : l1[w & 3], L);
      st_gran2(rw, lane < 2 ? mg + (ch & 15) * 64 + lane * 16 : OOB, tag, M, L);
    }
    pause<kML>();
    float Mx, inv;
    {
      v2u x[1];
      sweep<1, 0, 8>(rw, mg + n * 64 + g * 8, tag, x);
      auto sw = __builtin_amdgcn_permlane16_swap(x[0].x, x[0].x, false, false);
      const float Ms = u2f(sw[0]), Ls = u2f(sw[1]);
      Mx = fmax_(Ms, dpp<XOR1>(Ms)); Mx = fmax_(Mx, dpp<XOR2>(Mx)); Mx = fmax_(Mx, dpp<HMIR>(Mx)); Mx = fmax_(Mx, dpp<MIR>(Mx));
      float Lt = __builtin_amdgcn_exp2f(Ms - Mx) * Ls; Lt += dpp<XOR1>(Lt); Lt += dpp<XOR2>(Lt); Lt += dpp<HMIR>(Lt); Lt += dpp<MIR>(Lt);
      inv = 1.0f / Lt;
    }
    float oa0 = 0.f, oa1 = 0.f;
#pragma nounroll
    for (int it = 0; it < 2; ++it) {
      int br = it;
      asm volatile("" : "+s"(br));
      const float* ml = lds + L_ML + br * 32 + hw * 8;
      const f32x4 m0 = *(const f32x4*)(ml), m1 = *(const f32x4*)(ml + 4);
      const float Mb = u2f(__builtin_amdgcn_readlane(__float_as_uint(Mx), br * 32)), ib = u2f(__builtin_amdgcn_readlane(__float_as_uint(inv), br * 32));
      float c[8];
#pragma unroll
      for (int w = 0; w < 8; ++w) c[w] = __builtin_amdgcn_exp2f((w < 4 ? m0[w & 3] : m1[w & 3]) - Mb) * ib;
      const unsigned oa = lds_addr(lds + L_O + br * 2048 + hw * 128 + 2 * lane);
      int dsta = br ? WS_PG + ch * PG_ROW + (hw * 128 + 2 * lane) * 8 : OOB, dstb = br ? dsta + 256 * PG_ROW : OOB;
      asm volatile("" : "+v"(dsta), "+v"(dstb));
      float o0, o1;
      { const v2u v = lds_take2<8>(oa); o0 = c[0] * u2f(v.x); o1 = c[0] * u2f(v.y); }
#pragma unroll
      for (int w = 1; w < 8; ++w) {
        if (w == 5) {
          st_gran2(rw, dsta, tag, oa0, oa1);
          if (hw == 0 && lane == 0) __hip_atomic_store(cnt + C_PUB, (unsigned)br, RLX_WG);
        }
        const v2u v = lds_take2<0>(oa + w * 1024); o0 = fmaf(c[w], u2f(v.x), o0); o1 = fmaf(c[w], u2f(v.y), o1);
      }
      st_gran2(rw, dstb, tag, o0, o1);
      if (hw == 0 && lane == 0) __hip_atomic_store(cnt + C_PUB, 2u * (unsigned)br, RLX_WG);
      oa0 = o0; oa1 = o1;
    }
  } else {
    St z;
    z.lds = lds; z.lane = lane; z.wave = wave; z.mask = mask;
    z.rs[0] = rk0;
    z.rs[1] = mkrs(ptr64(ka[8], ka[9]) + (size_t)ch * 262144, 262144);
    z.rs[2] = mkrs(ptr64(ka[6], ka[7]) + (size_t)ch * 262144, 262144);
    z.rs[3] = mkrs(ptr64(ka[10], ka[11]) + (size_t)ch * 262144, 262144);
    z.vk = ((lane >> 2) & 3) * 256 + g * 64 + (lane & 3) * 16; z.vv = lane * 16; z.so = wave * 1024;
    const int phase16 = (wave * kSlot16) >> 3;
    asm volatile("s_waitcnt vmcnt(0)" :: "v"(cw) : "memory");
    z.due = lds_wait<1>(cnt + C_ANCHOR, 1u) + (unsigned)(kOff16 + phase16);
    Req<0, PRE>::go(z);
    lds_wait<1>(cnt + C_QA, 1u);
    const unsigned tag = __builtin_amdgcn_readfirstlane(cnt[C_TAG]);
    z.adj = __builtin_amdgcn_readfirstlane(cnt[C_ADJ]);
    Con<0, PRE - DEP + 1, PRE - 1>::go(z);
    Run<PRE, 32>::go(z);
    Con<32 - DEP + 1, 32, 31>::go(z);
    const unsigned t32 = z.due - (unsigned)phase16;
    if (wave < 5) {
      const __amdgpu_buffer_rsrc_t ro = __builtin_amdgcn_make_buffer_rsrc(
          (rb ? ptr64(kb[2], kb[3]) : ptr64(kb[0], kb[1])) + (size_t)(kvh * 256 + s8 * 32) * DM * 2, 0, 32 * DM * 2, RSRC3);
      const int wcol = wave * 128 + n * 8;
      const int vwo = wcol < DM ? g * (DM * 2) + wcol * 2 : OOB;
      const unsigned oba = lds_addr(lds + L_OB + g * 8);
      v4u Wo[8];
      gate(t32 + (unsigned)(wave * kWoStep16));
#pragma unroll
      for (int i = 0; i < 8; ++i) Wo[i] = __builtin_amdgcn_raw_buffer_load_b128(ro, vwo, i * 4 * DM * 2, AUX_NT);
      int dst = wcol < DM ? WS_YG + (rb * 128 + pidx) * YG_ROW + (wcol + 4 * (g & 1) + 2 * (g >> 1)) * 8 : OOB;
      asm volatile("" : "+v"(dst), "+v"(Wo[0]), "+v"(Wo[1]), "+v"(Wo[2]), "+v"(Wo[3]), "+v"(Wo[4]), "+v"(Wo[5]), "+v"(Wo[6]), "+v"(Wo[7]));
      v4u oa, ob;
#pragma nounroll
      for (unsigned spins = 0; spins < LSPIN_CAP; ++spins) {
        asm volatile("ds_read_b128 %0, %2\n\tds_read_b128 %1, %2 offset:16\n\ts_waitcnt lgkmcnt(0)" : "=&v"(oa), "=&v"(ob) : "v"(oba) : "memory");
        if (__all(oa.x != EMPTY_B && oa.y != EMPTY_B && oa.z != EMPTY_B && oa.w != EMPTY_B && ob.x != EMPTY_B && ob.y != EMPTY_B && ob.z != EMPTY_B && ob.w != EMPTY_B)) break;
      }
      f32x4 Y0 = {0.f, 0.f, 0.f, 0.f}, Y1 = {0.f, 0.f, 0.f, 0.f};
#pragma unroll
      for (int i = 0; i < 4; ++i) { Y0 = mfma(diag(oa[i], mask), Wo[i], Y0); Y1 = mfma(diag(ob[i], mask), Wo[4 + i], Y1); }
      const f32x4 Y = Y0 + Y1;
      st_gran2(rw, dst, tag, g < 2 ? Y.x : Y.z, g < 2 ? Y.y : Y.w);
    } else if (wave == 5) {
      const int po = WS_PG + (rb * 256 + kvh * 16 + (lane >> 5)) * PG_ROW + (32 * s8 + (lane & 31)) * 8;
      v2u x[8];
      lds_wait<0>(cnt + C_PUB, (unsigned)rb + 1u);
      pause<kHop1>();
      sweep<8, 2 * PG_ROW, 1>(rw, po, tag, x);
      const float o = x32_sum(sum8(x));
      if (lane < 32) ((unsigned*)(lds + L_OB))[(lane & 3) * 8 + (lane >> 2)] = dup_bf16(o);
    } else if (wave == 6 || pidx < 16) {
      const int col = (wave == 6 ? 0 : 512) + pidx * 4 + (lane & 3);
      GAS unsigned short* op = (GAS unsigned short*)ptr64(kb[4], kb[5]) + rb * DM + col;
      asm volatile("" : "+v"(op));
      v2u x[8];
#pragma nounroll
      for (unsigned spins = 0; spins < LSPIN_CAP; ++spins) {
        if (__builtin_amdgcn_readfirstlane(__hip_atomic_load((unsigned*)(lds + L_OB), RLX_WG)) != EMPTY_B) break;
        __builtin_amdgcn_s_sleep(1);
      }
      pause<kHop2>();
      gate(t32 + (unsigned)(4 * kWoStep16 + kHop2Gate16));
      sweep<8, 16 * YG_ROW, 1>(rw, WS_YG + (rb * 128 + (lane >> 2)) * YG_ROW + col * 8, tag, x);
      float y = sum8(x);
      y += dpp<ROR4>(y); y += dpp<ROR8>(y);
      y = xg_sum(y);
      if (lane < 4) *op = f2bf(y);
      if (rb == 0 && wave == 6 && pidx == 0 && lane == 0) __hip_atomic_store((GAS unsigned*)(ws + WS_EPOCH), tag, RLX_AGENT);
    }
  }
}

extern "C" void kernel_launch(void* const* d_in, const int* in_sizes, int n_in, void* d_out, int out_size, void* d_ws, size_t ws_size, hipStream_t stream) {
  Args a;
  a.x[0] = d_in[0]; a.k[0] = d_in[1]; a.v[0] = d_in[2]; a.wq[0] = d_in[3]; a.wo[0] = d_in[4];
  a.x[1] = d_in[5]; a.k[1] = d_in[6]; a.v[1] = d_in[7]; a.wq[1] = d_in[8]; a.wo[1] = d_in[9];
  a.out = d_out; a.ws = d_ws;
  gqa2_fused<<<NWG, NTHR, 0, stream>>>(a);
}
```
